```python
import torch

def _softmax(x, axis=-1):
    return torch.softmax(x, dim=axis)

def _gelu(x):
    return torch.nn.functional.gelu(x, approximate='tanh')

def _silu(x):
    return torch.nn.functional.silu(x)

def _sigmoid(x):
    return torch.sigmoid(x)

def _layernorm(x, axis=-1, eps=1e-05):
    m = x.mean(dim=axis, keepdim=True)
    v = x.var(dim=axis, keepdim=True, unbiased=False)
    return (x - m) / torch.sqrt(v + eps)

def _scatter_add(tgt, idx, src, axis=0):
    return tgt.clone().index_add_(axis, idx, src)

def _segment_sum(src, seg, n):
    out = torch.zeros((n,) + src.shape[1:], dtype=src.dtype)
    return out.index_add_(0, seg, src)

def _pad(x, pw):
    flat = tuple((v for pair in reversed(pw) for v in pair))
    return torch.nn.functional.pad(x, flat)
kernel_name = 'adaLN_cond__ssm_conv_s4d__moe_arch_mod__prompt'

def setup_inputs():

    _w = lambda a: (lambda t: t.to(torch.bfloat16) if t.is_floating_point() else t)(torch.as_tensor(a))

    def _A_setup():
        Bsz, L, D, N = (1, 2048, 576, 128)
        return {'u': _w((lambda *s: torch.randn(*s, dtype=torch.bfloat16) * 0.1)(Bsz, L, D)), 'log_A_real': _w((lambda *s: torch.randn(*s, dtype=torch.bfloat16) * 3.2)(D, N) * 0.5), 'A_imag': _w((lambda *s: torch.randn(*s, dtype=torch.bfloat16) * 0.4)(D, N) * 3.14), 'C_real': _w((lambda *s: torch.randn(*s, dtype=torch.bfloat16) * 0.8)(D, N) / N ** 0.5), 'C_imag': _w((lambda *s: torch.randn(*s, dtype=torch.bfloat16) * 0.8)(D, N) / N ** 0.5), 'log_dt': _w((lambda *s: torch.randn(*s, dtype=torch.bfloat16) * 0.4)(D) * 0.5 - 4.0), 'D_skip': _w((lambda *s: torch.randn(*s, dtype=torch.bfloat16) * 0.4)(D))}
    _A_in = _A_setup()

    def _B_setup():
        B, S, D, F, C = (1, 2048, 576, 4608, 8)
        return {'x': _w((lambda *s: torch.randn(*s, dtype=torch.bfloat16) * 0.1)(B, S, D)), 'w_r': _w((lambda *s: torch.randn(*s, dtype=torch.bfloat16) * 0.4)(D)), 'w_gate': _w((lambda *s: torch.randn(*s, dtype=torch.bfloat16) * 0.4)(D, F)), 'w_up': _w((lambda *s: torch.randn(*s, dtype=torch.bfloat16) * 0.4)(D, F)), 'w_down': _w((lambda *s: torch.randn(*s, dtype=torch.bfloat16) * 0.4)(F, D))}
    _B_in = _B_setup()
    return {'A_u': _A_in['u'], 'A_log_A_real': _A_in['log_A_real'], 'A_A_imag': _A_in['A_imag'], 'A_C_real': _A_in['C_real'], 'A_C_imag': _A_in['C_imag'], 'A_log_dt': _A_in['log_dt'], 'A_D_skip': _A_in['D_skip'], 'B_x': _B_in['x'], 'B_w_r': _B_in['w_r'], 'B_w_gate': _B_in['w_gate'], 'B_w_up': _B_in['w_up'], 'B_w_down': _B_in['w_down'], 'c': _w((lambda *s: torch.randn(*s, dtype=torch.bfloat16) * 0.2)(1, 576) * 8.0), 'w_mod': _w((lambda *s: torch.randn(*s, dtype=torch.bfloat16) * 0.2)(576, 6 * 576) * 576 ** (-0.5) * 8.0)}

def reference(A_u, A_log_A_real, A_A_imag, A_C_real, A_C_imag, A_log_dt, A_D_skip, B_x, B_w_r, B_w_gate, B_w_up, B_w_down, c, w_mod):

    def _A_ref(u, log_A_real, A_imag, C_real, C_imag, log_dt, D_skip):
        L = u.shape[1]
        dt = torch.exp(log_dt)
        A = -torch.exp(log_A_real) + 1j * A_imag
        C = (C_real + 1j * C_imag) * dt[:, None]
        pos = (lambda x, dt: x.to(dt))(torch.arange(L), log_dt.dtype)
        P = torch.exp((A * dt[:, None])[:, :, None] * pos[None, None, :])
        K = 2.0 * torch.einsum('dn,dnl->dl', C, P).real
        L2 = 2 * L
        u_t = (lambda x, *d: x.permute(*d))(u, 0, 2, 1)
        Kf = (lambda x, *a, **k: torch.fft.rfft(x.float(), *a, **k))(K, L2)
        Uf = (lambda x, *a, **k: torch.fft.rfft(x.float(), *a, **k))(u_t, L2)
        y = (lambda x, *a, **k: torch.fft.irfft(x, *a, **k).to(torch.bfloat16))(Uf * Kf[None, :, :], L2)[:, :, :L]
        y = (lambda x, *d: x.permute(*d))(y, 0, 2, 1)
        return y + u * D_skip[None, None, :]

    def _B_ref(x, w_r, w_gate, w_up, w_down):
        B, S, D = x.shape
        C = 8
        r = x @ w_r
        idx = torch.argsort(-r, axis=-1)[:, :C]
        idx_e = (lambda x, n, axis: torch.repeat_interleave(x, n, dim=axis))(idx.reshape(B, C, 1), D, axis=2)
        x_sel = (lambda x, i, axis: torch.take_along_dim(x, i, dim=axis))(x, idx_e, axis=1)
        g = _silu(x_sel @ w_gate)
        u = x_sel @ w_up
        y = g * u @ w_down
        r_sel = _sigmoid((lambda x, i, axis: torch.take_along_dim(x, i, dim=axis))(r, idx, axis=1))
        y = y * r_sel.reshape(B, C, 1)
        b_off = (torch.arange(B) * S).reshape(B, 1)
        flat_idx = (idx + b_off).reshape(B * C)
        zero = (x * 0.0).reshape(B * S, D)
        upd = _scatter_add(zero, flat_idx, y.reshape(B * C, D), axis=0)
        return x + upd.reshape(B, S, D)
    x = A_u
    mod = (_silu(c) @ w_mod).reshape(c.shape[0], 6, 1, -1)
    sa, ca, ga, sb, cb, gb = (mod[:, 0].clone(), mod[:, 1].clone(), mod[:, 2].clone(), mod[:, 3].clone(), mod[:, 4].clone(), mod[:, 5].clone())
    h = _layernorm(x, axis=-1) * (1 + ca) + sa
    x = x + ga * _A_ref(h, A_log_A_real, A_A_imag, A_C_real, A_C_imag, A_log_dt, A_D_skip)
    h = _layernorm(x, axis=-1) * (1 + cb) + sb
    x = x + gb * _B_ref(h, B_w_r, B_w_gate, B_w_up, B_w_down)
    return x
```

```cpp
#include <hip/hip_runtime.h>
#include <stdint.h>

typedef unsigned short u16;
typedef unsigned int u32;
typedef unsigned long long u64;
typedef _Float16 f16;
typedef f16 f16x8 __attribute__((ext_vector_type(8)));
typedef f16 f16x2 __attribute__((ext_vector_type(2)));
typedef __bf16 bf16x8 __attribute__((ext_vector_type(8)));
typedef __bf16 bf16x4 __attribute__((ext_vector_type(4)));
typedef __bf16 bf16x2 __attribute__((ext_vector_type(2)));
typedef float f32x2 __attribute__((ext_vector_type(2)));
typedef float f32x4 __attribute__((ext_vector_type(4)));
typedef float f32x16 __attribute__((ext_vector_type(16)));
typedef u16 u16x2 __attribute__((ext_vector_type(2)));
typedef u32 u32x2 __attribute__((ext_vector_type(2)));
typedef u32 u32x3 __attribute__((ext_vector_type(3)));
typedef u32 u32x4 __attribute__((ext_vector_type(4)));
#define LAS __attribute__((address_space(3)))
#define CAS __attribute__((address_space(4)))
#define GAS __attribute__((address_space(1)))
typedef LAS char lchar;

#define DEV __device__ __forceinline__
#define CBAR() asm volatile("" ::: "memory")
#define VKEEP(x) asm volatile("" : "+v"(x))
constexpr int D_ = 576, N_ = 128, F_ = 4608;
constexpr u32 SENT = 0xffffffffu;
constexpr int SPIN_CAP = 1 << 15;
#define THR 63
#define CPRIO 1
#define WNT 1
#define WPREF 1
#define TW 9
#define MSLP 8
#define NLO 3
#define HSLP 4
#define PF1 200
#define PFLAG ""
#define XSLP 2
#define DSLP 32
#define PSLP 38
#define GTHR 4
#define GCNT 9
#define ESLP 4
#define ETHR 1
#define WSEND 0
#define COOP 1
#define MNT ""
#define PADS 128
#define HPRIO 1
#define MFIRST 0
#define OSLP 0
#define HSL2 0
#define FSLP 20
#define X1VM 1
#define X1SLP 15
#define KDLY 0
#define RSLP 0
#define B0LATE 1
#define LW 8
#define PMEARLY 0
#define PA 128
#define PDLY 2
#define OAUX 16
#define XN 0
#define XNT ""
#define KW 16
#define EARLYW 6
#define KHI 36
#define UNG 0
#define SLOWP 32
#define LOWRB 48
#define PSL2 24
#define MPF 2
#define MPRIO 1
#define PUB11 1
#define KSLP 4
#define PREF_L 232

constexpr int WS_EPOCH = 0;
constexpr int WS_MODG = 4096 + 2048 * 8;
constexpr int WS_KEY = WS_MODG + 3456 * 24;
constexpr int WS_ODONE = WS_KEY + 2048 * 8;
constexpr int WS_X1 = 131072;
constexpr int X1S = 16384 + PADS, XPS = 12288 + PADS;
constexpr int WS_H2 = WS_X1 + 192 * X1S;
constexpr int WS_PART = WS_H2 + 576 * 2048 * 2;
constexpr int WS_XP = WS_PART + 64 * 72 * 384;
constexpr int WS_END = WS_XP + 256 * XPS;
constexpr long WS_TIM = WSEND ? -(176l << 20) : (64l << 20);

constexpr int GROW = 544, GTAB = 53 * GROW;
constexpr int L_Y = 0;
constexpr int KCS = 1344, KCH = 4 * KCS;
constexpr int L_KN = 61440;
constexpr int KAB = 2304;
constexpr int KA_E3 = 1280 + 64, KA_E5 = 1536 + 64, KA_E9 = 1792 + 64, KA_DUM = 2176;
constexpr int L_KA = 86528;
constexpr int L_ONE = L_KA + 3 * KAB;
constexpr int L_KO = L_ONE + 5 * 1024;
constexpr int L_U = L_KO + 4096;
constexpr int UB = 4576;
constexpr int IMS = 2304 + 16;
constexpr int L_IMX = L_KN;
constexpr int L_IMH = L_IMX + 8 * IMS;
constexpr int L_RED = L_U + 3 * UB + 64;
constexpr int L_RED2 = L_RED + 2048;
constexpr int L_PRM = L_RED2 + 768;
constexpr int L_GB = L_PRM + 576 * 16;
constexpr int L_MISC = L_GB + 576 * 4;
constexpr int SXS = 304;
constexpr int L_SX = L_PRM;
constexpr int L_SP = L_MISC + 256;
constexpr int L_CE = L_SP + 512 + 24;
constexpr int L_DUM = L_SP + 512 + 32;
constexpr int L_XR = L_DUM + 256;
constexpr int LDS_BYTES = L_XR + 20480;
static_assert(3 * GTAB <= L_KA && L_KN + 3 * KCH <= L_KA && L_IMH + 8 * IMS <= L_RED && LDS_BYTES <= 163840 && LDS_BYTES > 81920, "lds");
constexpr int L_EB = L_KN;
constexpr int L_MP = L_EB;
constexpr int L_GP = 0;
constexpr int L_AS = L_EB + 4608;
constexpr int L_LIST = L_AS + 1152;
constexpr int L_WM = L_LIST + 2112 * 4;
constexpr int L_SEL = L_WM + 512;
constexpr int L_FIN = L_SEL + 64;
static_assert(L_FIN + 18 * 288 <= L_SX, "lds");

struct CTab { u32 v[9 * 256]; };
constexpr CTab make_ctab() {
  CTab t = {};
  const int c0[5] = {18, 26, 21, 20, 19}, wd[5] = {3, 3, 5, 7, 9};
  for (int f = 0; f < 5; f++)
    for (int l = 0; l < 64; l++)
      for (int j = 0; j < 8; j++) {
        const int u = (l & 15) + 8 * (l >> 4) + j - c0[f];
        if (u >= 0 && u < wd[f]) t.v[f * 256 + l * 4 + j / 2] |= 0x3c00u << (16 * (j & 1));
      }
  for (int i = 0; i < 256; i++)
    for (int r = 0; r < 4; r++) {
      const int row = (i >> 7) * 16 + ((i >> 4) & 3) * 4 + r, cg = ((i >> 6) & 1) * 16 + (i & 15);
      int o = KA_DUM + 2 * cg;
      if (row < 8) o = 32 + 2 * (row + 8 * cg);
      else if (row == 8) o = 32 + 2 * (256 + 8 * cg);
      else if (row == 9) o = cg ? KA_E5 + 2 * (cg - 1) : 32 + 2 * 512;
      else if (row == 10) o = KA_E5 + 2 * (31 + cg);
      else if (row == 11) o = cg ? KA_E9 + 2 * (cg - 1) : KA_E5 + 2 * 63;
      else if (row < 16) o = KA_E9 + 2 * (32 * (row - 11) + cg - 1);
      else if (row < 19) o = 32 + 2 * (258 + 2 * (row - 16) + 8 * cg);
      else if (row < 21) o = KA_E3 + 2 * (32 * (row - 19) + cg);
      t.v[5 * 256 + i * 4 + r] = (u32)o;
    }
  return t;
}
__device__ __attribute__((aligned(128))) const CTab ctab = make_ctab();

#define TS(i) do {} while (0)
#define TA(i, ...) do {} while (0)
#define TB(i, ...) do {} while (0)

DEV float bf2f(u16 v) { return __uint_as_float(((u32)v) << 16); }
DEV float bflo(u32 v) { return __uint_as_float(v << 16); }
DEV float bfhi(u32 v) { return __uint_as_float(v & 0xffff0000u); }
DEV u32 pkbf(float lo, float hi) { f32x2 v = {lo, hi}; return __builtin_bit_cast(u32, __builtin_convertvector(v, bf16x2)); }
DEV float rbf(float x) { return __uint_as_float(pkbf(0.f, x)); }
DEV u32 pkh(float lo, float hi) { return __builtin_bit_cast(u32, __builtin_amdgcn_cvt_pkrtz(lo, hi)); }
DEV float hlo(u32 v) { return (float)__builtin_bit_cast(f16x2, v)[0]; }
DEV float hhi(u32 v) { return (float)__builtin_bit_cast(f16x2, v)[1]; }
DEV float pow2i(int e) { return __uint_as_float((u32)(e + 127) << 23); }
DEV float fexp(float x) { return __builtin_amdgcn_exp2f(x * 1.44269504088896341f); }
DEV float frcp(float x) { return __builtin_amdgcn_rcpf(x); }
DEV float dot2(u32 a, u32 b, float c) { return __builtin_amdgcn_fdot2_f32_bf16(__builtin_bit_cast(bf16x2, a), __builtin_bit_cast(bf16x2, b), c, false); }
DEV u32 selu(u32 m, u32 a, u32 b) { return (m & a) | (~m & b); }
DEV float self(u32 m, float a, float b) { return __uint_as_float(selu(m, __float_as_uint(a), __float_as_uint(b))); }
DEV u32 perm_lo(u32 a, u32 b) { return __builtin_amdgcn_perm(b, a, 0x05040100u); }
DEV u32 perm_hi(u32 a, u32 b) { return __builtin_amdgcn_perm(b, a, 0x07060302u); }
template <int CTRL> DEV float dppf(float v) { return __int_as_float(__builtin_amdgcn_update_dpp(0, __float_as_int(v), CTRL, 0xf, 0xf, true)); }
template <int CTRL> DEV u32 dppu(u32 v) { return (u32)__builtin_amdgcn_update_dpp(0, (int)v, CTRL, 0xf, 0xf, true); }
DEV float row_sum(float v) { v += dppf<0xB1>(v); v += dppf<0x4E>(v); v += dppf<0x141>(v); v += dppf<0x140>(v); return v; }
DEV float swap32_add(float a, float b) { auto r = __builtin_amdgcn_permlane32_swap(__float_as_uint(a), __float_as_uint(b), false, false); return __uint_as_float(r[0]) + __uint_as_float(r[1]); }
DEV float swap16_add(float a, float b) { auto r = __builtin_amdgcn_permlane16_swap(__float_as_uint(a), __float_as_uint(b), false, false); return __uint_as_float(r[0]) + __uint_as_float(r[1]); }
DEV u32 wave_max_u32(u32 v) {
  v = max(v, dppu<0xB1>(v)); v = max(v, dppu<0x4E>(v)); v = max(v, dppu<0x141>(v)); v = max(v, dppu<0x140>(v));
  u32 a = __builtin_amdgcn_readlane(v, 0), b = __builtin_amdgcn_readlane(v, 16), c = __builtin_amdgcn_readlane(v, 32), d = __builtin_amdgcn_readlane(v, 48);
  return max(max(a, b), max(c, d));
}
template <typename T> DEV T lds_ld(lchar* p) { return *(LAS T*)p; }
template <typename T> DEV void lds_st(lchar* p, T v) { *(LAS T*)p = v; }
DEV void vm0() { asm volatile("s_waitcnt vmcnt(0)" ::: "memory"); }
DEV u32 ln_stats(float s1, float s2) {
  float mu = s1 * (1.f / 576.f);
  float var = fmaxf(__builtin_fmaf(-mu, mu, s2 * (1.f / 576.f)), 0.f);
  return pkbf(mu, __builtin_amdgcn_sqrtf(rbf(rbf(var) + 1e-5f)));
}

DEV f32x2 cmul(f32x2 a, f32x2 b) {
  f32x2 t, r;
  asm("v_pk_mul_f32 %0, %2, %3 op_sel:[1,1] op_sel_hi:[1,0] neg_lo:[0,1]\n\tv_pk_fma_f32 %1, %2, %3, %0 op_sel_hi:[0,1,1]" : "=&v"(t), "=v"(r) : "v"(a), "v"(b));
  return r;
}
DEV u32 pkh_rne(f32x2 v) { return __builtin_bit_cast(u32, __builtin_convertvector(v, f16x2)); }
DEV f32x2 cpow(float p, float e1, float th, float tl) {
  const float m = __builtin_amdgcn_exp2f(p * e1), a = __builtin_fmaf(p, tl, __builtin_amdgcn_fractf(p * th));
  return f32x2{m * __builtin_amdgcn_cosf(a), m * __builtin_amdgcn_sinf(a)};
}

#define BAR() do { __builtin_amdgcn_sched_barrier(0); asm volatile("s_waitcnt lgkmcnt(0)\n\ts_barrier" ::: "memory"); __builtin_amdgcn_sched_barrier(0); } while (0)
DEV bool quad_any(bool b) { u32 v = b; v |= dppu<0xB1>(v); v |= dppu<0x4E>(v); return v != 0; }
DEV void touch(const void* p, lchar* sm) {
  u32 keep;
  asm volatile("s_mov_b32 %0, m0\n\ts_mov_b32 m0, %2\n\ts_nop 0\n\tglobal_load_lds_dword %1, off" PFLAG "\n\ts_mov_b32 m0, %0" : "=&s"(keep) : "v"(p), "s"((u32)(uintptr_t)(sm + L_DUM)));
}
DEV void dma16(const void* p, u32 lds) {
  u32 keep;
  asm volatile("s_mov_b32 %0, m0\n\ts_mov_b32 m0, %2\n\ts_nop 0\n\tglobal_load_lds_dwordx4 %1, off\n\ts_mov_b32 m0, %0" : "=&s"(keep) : "v"(p), "s"(lds) : "memory");
}
#define DSR64(dst, addr, off) asm volatile("ds_read_b64 %0, %1 offset:%2" : "=v"(dst) : "v"(addr), "i"(off))
#define DSR128(dst, addr, off) asm volatile("ds_read_b128 %0, %1 offset:%2" : "=v"(dst) : "v"(addr), "i"(off))
DEV f32x4 mm16(u32x4 a, u32x4 b, f32x4 c) { return __builtin_amdgcn_mfma_f32_16x16x32_f16(__builtin_bit_cast(f16x8, a), __builtin_bit_cast(f16x8, b), c, 0, 0, 0); }
struct NSet { u32x2 a[4]; u32x4 b[6]; };
#define SB() __builtin_amdgcn_sched_barrier(0)
template <int NB, int OFF, int I> DEV void near_ld1(NSet& s, u32 ka, u32 ua) {
  if constexpr (I == 0) DSR64(s.a[0], ka, OFF);
  else if constexpr (I == 1) DSR64(s.a[1], ka, OFF + 8);
  else if constexpr (I < 2 + NB) DSR128(s.b[NB + 1 - I], ua, OFF + (I - 2) * 512);
  else if constexpr (I == 2 + NB) DSR64(s.a[2], ka, OFF + 512);
  else if constexpr (I == 3 + NB) DSR64(s.a[3], ka, OFF + 520);
}
template <int NB, int OFF, int I = 0> DEV void near_load(NSet& s, u32 ka, u32 ua) {
  if constexpr (I < 4 + NB) { near_ld1<NB, OFF, I>(s, ka, ua); near_load<NB, OFF, I + 1>(s, ka, ua); }
}
template <int NB, int CNT> DEV void near_wait(NSet& s) {
  asm volatile("s_waitcnt lgkmcnt(%7)" : "+v"(s.a[0]), "+v"(s.a[1]), "+v"(s.a[2]), "+v"(s.a[3]), "+v"(s.b[0]), "+v"(s.b[1]), "+v"(s.b[2]) : "i"(CNT));
  if constexpr (NB > 3) VKEEP(s.b[3]);
  if constexpr (NB > 4) VKEEP(s.b[4]);
  if constexpr (NB > 5) VKEEP(s.b[5]);
}
template <int NT, int NB, int OFF, int I = 0> DEV void near_mm(NSet& c, NSet& n, u32 ka, u32 ua, f32x4 (&acc)[5]) {
  if constexpr (I < 2 * NT) {
    constexpr int i = NT - 1 - I % NT, bi = i + NB - NT - I / NT;
    if constexpr (I == 0) near_wait<NB, 0>(c);
    if constexpr (bi >= 0) acc[i] = mm16(I < NT ? u32x4{c.a[0][0], c.a[0][1], c.a[1][0], c.a[1][1]} : u32x4{c.a[2][0], c.a[2][1], c.a[3][0], c.a[3][1]}, c.b[bi], acc[i]);
    SB();
    near_ld1<NB, OFF, 2 * I>(n, ka, ua); near_ld1<NB, OFF, 2 * I + 1>(n, ka, ua);
    SB();
    near_mm<NT, NB, OFF, I + 1>(c, n, ka, ua, acc);
  }
}
template <int C0, int NT> DEV void near_conv(u32 ka, u32 ua, lchar* yp) {
  constexpr int NB = C0 == 0 ? NT : NT + 1;
  f32x4 acc[5] = {};
  NSet s0, s1;
  near_load<NB, 0>(s0, ka, ua);
#pragma unroll 1
  for (int it = 0; it < 4; it++) {
    near_mm<NT, NB, 64>(s0, s1, ka, ua, acc);
    near_mm<NT, NB, 128>(s1, s0, ka, ua, acc);
    ka += 128; ua += 128;
  }
  if (C0 > 0) { DSR64(s1.a[0], ka, 640); DSR64(s1.a[1], ka, 648); DSR128(s1.b[0], ua, 6 * 512); DSR128(s1.b[1], ua, 5 * 512); }
  near_wait<NB, (C0 > 0 ? 4 : 0)>(s0);
  {
    const u32x4 A1 = {s0.a[2][0], s0.a[2][1], s0.a[3][0], s0.a[3][1]};
#pragma unroll
    for (int i = 0; i < NT; i++) if (C0 + i >= 2) acc[i] = mm16(A1, s0.b[i + NB - NT - 1], acc[i]);
  }
  if (C0 > 0) {
    asm volatile("s_waitcnt lgkmcnt(0)" : "+v"(s1.a[0]), "+v"(s1.a[1]), "+v"(s1.b[0]), "+v"(s1.b[1]));
    const u32x4 A = {s1.a[0][0], s1.a[0][1], s1.a[1][0], s1.a[1][1]};
#pragma unroll
    for (int c = 4; c < 8; c++) acc[c - C0] = mm16(A, c < 6 ? s1.b[c - 4] : s0.b[c - 2 - C0], acc[c - C0]);
  }
#pragma unroll
  for (int i = 0; i < NT; i++) lds_st<f32x4>(yp + (C0 + i) * 1280, acc[i]);
}
DEV u32x4 pack2(f32x4 a, f32x4 b) { return u32x4{pkh_rne(f32x2{a[3], b[3]}), pkh_rne(f32x2{a[2], b[2]}), pkh_rne(f32x2{a[1], b[1]}), pkh_rne(f32x2{a[0], b[0]})}; }
DEV u32x4 tapf(lchar* p) { return u32x4{lds_ld<u32>(p), lds_ld<u32>(p + 4), lds_ld<u32>(p + 8), lds_ld<u32>(p + 12)}; }
struct FarPre { u32x4 T3[3], T5[3], O3a, O3b, O5; };
DEV void far_pre(FarPre& f, lchar* kt, lchar* one) {
  f.O3a = lds_ld<u32x4>(one); f.O3b = lds_ld<u32x4>(one + 1024); f.O5 = lds_ld<u32x4>(one + 2048);
#pragma unroll
  for (int e = 0; e < 3; e++) { f.T3[e] = tapf(kt + 64 * e); f.T5[e] = tapf(kt + 256 + 64 * e); }
}
DEV void far_conv(const FarPre& fp, lchar* kt, lchar* up, lchar* one, lchar* yp, lchar* ce, int lane) {
  const f32x4 z = {0.f, 0.f, 0.f, 0.f};
  f32x4 acc[8] = {};
  u32x4 bu = lds_ld<u32x4>(up), bv = lds_ld<u32x4>(up + 16);
  {
    const u32x4 O3a = fp.O3a, O3b = fp.O3b, O5 = fp.O5;
    const u32x4* T3 = fp.T3; const u32x4* T5 = fp.T5;
    u32x4 bu1 = lds_ld<u32x4>(up - 512), bv1 = lds_ld<u32x4>(up - 512 + 16);
    SB();
    f32x4 p0 = mm16(bu, O3a, z), p1 = mm16(bu, O3b, z), p2 = mm16(bu, O5, z), p3 = mm16(bv, O5, z);
#pragma unroll
    for (int n = 0; n < 6; n++) {
      const u32x4 f3 = pack2(p0, p1), f5 = pack2(p2, p3);
      bu = bu1; bv = bv1;
      const int nn = n + 2 < 6 ? n + 2 : n - 4;
      bu1 = lds_ld<u32x4>(up - 512 * nn); bv1 = lds_ld<u32x4>(up - 512 * nn + 16);
      SB();
      if (n < 5) { p0 = mm16(bu, O3a, z); p1 = mm16(bu, O3b, z); p2 = mm16(bu, O5, z); p3 = mm16(bv, O5, z); }
#pragma unroll
      for (int e = 0; e < 3; e++) if (n + 2 + e < 8) acc[n + 2 + e] = mm16(f3, T3[e], acc[n + 2 + e]);
#pragma unroll
      for (int e = 0; e < 3; e++) if (n + 2 + e < 8) acc[n + 2 + e] = mm16(f5, T5[e], acc[n + 2 + e]);
      SB();
      if (n == 1) { lds_st<f32x4>(yp + 2 * 1280, acc[2]); CBAR(); if (lane == 0) __hip_atomic_fetch_add((LAS u32*)ce, 256u, __ATOMIC_RELAXED, __HIP_MEMORY_SCOPE_WORKGROUP); }
      if (n == 2) lds_st<f32x4>(yp + 3 * 1280, acc[3]);
    }
    const u32x4 O7 = lds_ld<u32x4>(one + 3072), O9 = lds_ld<u32x4>(one + 4096);
    const u32x4 bu2 = lds_ld<u32x4>(up - 1024), bv2 = lds_ld<u32x4>(up - 1024 + 16), bu3 = lds_ld<u32x4>(up - 1536), bv3 = lds_ld<u32x4>(up - 1536 + 16);
    u32x4 T9[4];
#pragma unroll
    for (int e = 0; e < 4; e++) T9[e] = tapf(kt + 512 + 64 * e);
    SB();
    const f32x4 q0 = mm16(bu, O7, z), q1 = mm16(bv, O9, z), q2 = mm16(bu1, O7, z), q3 = mm16(bv1, O9, z);
    const f32x4 q4 = mm16(bu2, O7, z), q5 = mm16(bv2, O9, z), q6 = mm16(bu3, O7, z), q7 = mm16(bv3, O9, z);
    const u32x4 f[4] = {pack2(q0, q1), pack2(q2, q3), pack2(q4, q5), pack2(q6, q7)};
#pragma unroll
    for (int n = 0; n < 4; n++) {
#pragma unroll
      for (int e = 0; e < 4; e++) if (n + 4 + e < 8) acc[n + 4 + e] = mm16(f[n], T9[e], acc[n + 4 + e]);
      lds_st<f32x4>(yp + (n + 4) * 1280, acc[n + 4]);
    }
  }
}

DEV u32 make_key(float r, int l) {
  u32 u = __float_as_uint(r);
  u |= (u & 0x7ffu) ? 0x800u : 0u;
  u = (u & 0x80000000u) ? ~u : (u | 0x80000000u);
  return (u & 0xfffff800u) | (u32)(2047 - l);
}
DEV float key_score(u32 k) { return rbf(__uint_as_float(((k & 0x80000000u) ? (k & 0x7fffffffu) : ~k) & 0xfffff800u)); }
#define LD128(off) __builtin_amdgcn_raw_buffer_load_b128(rs, (off), 0, 16)
#define LD64(off) __builtin_amdgcn_raw_buffer_load_b64(rs, (off), 0, 16)
#define LD32(off) __builtin_amdgcn_raw_buffer_load_b32(rs, (off), 0, 16)
#define ST128(v, off) __builtin_amdgcn_raw_buffer_store_b128((v), rs, (off), 0, 16)
#define ST96(v, off) __builtin_amdgcn_raw_buffer_store_b96((v), rs, (off), 0, 16)
#define ST64(v, off) __builtin_amdgcn_raw_buffer_store_b64((v), rs, (off), 0, 16)
#define ST32(v, off) __builtin_amdgcn_raw_buffer_store_b32((v), rs, (off), 0, 16)
DEV bool has_sent(u32x4 v) { return max(max(v[0], v[1]), max(v[2], v[3])) == SENT; }
DEV u32 nosent(u32 v) { return min(v, SENT - 1u); }

#define LD96(off) __builtin_amdgcn_raw_buffer_load_b96(rs, (off), 0, 16)
#define VMW_(n) asm volatile("s_waitcnt vmcnt(" #n ")" ::: "memory")
#define VMW(n) VMW_(n)
DEV float ld_bf_s(const u16* p, int i) { u32 v = *(const CAS u32*)(p + (i & ~1)); return __uint_as_float((i & 1) ? (v & 0xffff0000u) : (v << 16)); }
template <typename T> DEV T ldnt(const void* p) { return __builtin_nontemporal_load((const T*)p); }

template <typename T> DEV T LDW(const void* p) { return __builtin_nontemporal_load((const T*)p); }

__global__ void __launch_bounds__(1024) mega(const u16* __restrict__ x, const u16* __restrict__ lar, const u16* __restrict__ aim,
    const u16* __restrict__ cre, const u16* __restrict__ cim, const u16* __restrict__ ldt, const u16* __restrict__ dsk,
    const u16* __restrict__ wr, const u16* __restrict__ wg, const u16* __restrict__ wu, const u16* __restrict__ wd,
    const u16* __restrict__ cvec, const u16* __restrict__ wmod, u16* __restrict__ out, char* __restrict__ ws) {
  extern __shared__ __attribute__((aligned(16))) char smem_[];
  lchar* sm = (lchar*)smem_;
  u64 t00; asm volatile("s_memrealtime %0\n\ts_waitcnt lgkmcnt(0)" : "=s"(t00) :: "memory");
  asm volatile("s_load_dword s90, %0, 0x0\n\ts_load_dword s91, %0, 0x40" :: "s"(__builtin_amdgcn_kernarg_segment_ptr()) : "s90", "s91");
  u64 pc0;
  asm volatile("s_getpc_b64 %0" : "=s"(pc0));
  const int tid = threadIdx.x, lane = tid & 63, wave = __builtin_amdgcn_readfirstlane(tid >> 6), blk = blockIdx.x;
  if (wave >= 12 && (blk < 192 || !MFIRST)) __builtin_amdgcn_s_setprio(3);
  if (wave < 9 && blk >= 192 && MFIRST) __builtin_amdgcn_s_setprio(3);
  if (blk >= 248 && tid < PREF_L) touch((const void*)((pc0 & ~127ull) + tid * 128), sm);
  const bool isC = blk < 192;
  const u32x4 sent4 = {SENT, SENT, SENT, SENT}, zero4 = {0u, 0u, 0u, 0u};
  int i16 = lane & 15, g4 = lane >> 4;
  VKEEP(i16); VKEEP(g4);
  u32 tag = 0;
  const int grp = (blk & 7) * 24 + (blk >> 3);
  const int eb = (blk & 7) * 8 + ((blk - 192) >> 3);
  const int mu = (blk - 192) >> 1, mth = blk & 1;
  u32 o0, o1;
  if (wave >= 12) { o1 = ((blk * 8 + (lane >> 3)) * D_ + (wave - 12) * 144) * 2; o0 = o1 + (lane & 7) * 16; o1 += min(lane & 7, 1) * 16; }
  else if (isC) { o1 = ((grp * 3 + (wave >> 2)) * 64 + (lane & 31)) * 4; o0 = o1 + ((wave + (wave >> 2)) & 1) * 128; }
  else { o1 = mth * 288 + (tid >> 3) * 4; o0 = (o1 * 3456 + mu * 64 + (lane & 7) * 8) * 2; o1 *= 2; }
  VKEEP(o0); VKEEP(o1);
  if (tid >= 960 && tid < 994) lds_st<u32>(sm + L_SP + (tid - 960) * 16 + 8, 0u);
  if (!B0LATE) BAR();
  asm volatile("s_waitcnt lgkmcnt(0)" :: "s"(x), "s"(lar), "s"(aim), "s"(cre), "s"(cim), "s"(ldt), "s"(dsk), "s"(wr), "s"(wg), "s"(wu), "s"(wd), "s"(cvec), "s"(wmod), "s"(out), "s"(ws) : "memory");
  const auto rs = __builtin_amdgcn_make_buffer_rsrc(ws, 0, WS_END, 0x00020000);
#define PRE_CLOB "v108", "v109", "v110", "v111", "v112", "v113", "v114", "v115", "v116", "v117", "v118", "v119", "v120", "v121", "v122", "v123", "v124", "v125"
  if (wave >= 12)
    asm volatile("global_load_dwordx4 v[108:111], %0, %2" XNT " ; PRE_ISSUE\n\tglobal_load_dwordx4 v[112:115], %0, %2 offset:128" XNT " ; PRE_ISSUE\n\tglobal_load_dwordx4 v[116:119], %1, %2 offset:256" XNT " ; PRE_ISSUE\n\tbuffer_load_dword v124, off, %3, 0 sc1 ; PRE_ISSUE"
                 :: "v"(o0), "v"(o1), "s"(x), "s"(rs) : PRE_CLOB);
  if (wave < 12 && isC) {
    asm volatile("global_load_dword v108, %0, off ; PRE_ISSUE\n\ts_load_dwordx2 s[92:93], %1, %3\n\ts_load_dwordx2 s[94:95], %2, %3"
                 :: "v"((const char*)(lane < 32 ? lar : aim) + o0), "s"(ldt), "s"(dsk), "s"((grp * 6) & ~3) : PRE_CLOB, "s92", "s93", "s94", "s95");
    if (((wave + (wave >> 2)) & 2) == 0) asm volatile("global_load_dword v109, %0, off ; PRE_ISSUE\n\tglobal_load_dword v110, %0, off offset:128 ; PRE_ISSUE" :: "v"((const char*)(lane < 32 ? cre : cim) + o1) : PRE_CLOB);
  }
  const bool hasM = !isC && blk < 192 + 54;
  if (wave < 9 && hasM)
    asm volatile("global_load_dwordx2 v[124:125], %4, %6 ; PRE_ISSUE\n\tglobal_load_dwordx4 v[108:111], %0, %5" MNT " ; PRE_ISSUE\n\tglobal_load_dwordx4 v[112:115], %1, %5" MNT " ; PRE_ISSUE\n\tglobal_load_dwordx4 v[116:119], %2, %5" MNT " ; PRE_ISSUE\n\tglobal_load_dwordx4 v[120:123], %3, %5" MNT " ; PRE_ISSUE"
                 :: "v"(o0), "v"(o0 + 6912), "v"(o0 + 2 * 6912), "v"(o0 + 3 * 6912), "v"(o1), "s"(wmod), "s"(cvec) : PRE_CLOB);
  if (B0LATE) { SB(); asm volatile("s_barrier ; 0: the flags are zero (their writer has waited for the arguments, on the same counter)" ::: "memory"); SB(); }
  auto unit = [&](int j) __attribute__((always_inline)) { const int js = (3 - blk - (int)((uintptr_t)wg >> 7)) & 7, q = js + 16 - j; return (blk & 7) * 9 + (q >= 18 ? q - 18 : (q >= 9 ? q - 9 : q)); };
  TA(1);

  auto t_dump = [&]() __attribute__((always_inline)) {
  };
  auto ln1_rows = [&](bool cinit) __attribute__((always_inline)) {
    const int sw = wave - 12, row = lane >> 3, j8 = lane & 7, tp = tid - 768;
    if (cinit) {
      if (MPF) {
        const int t = (blk & 7) + 8 * (lane & 7), r = 288 - 96 * MPF + ((blk >> 3) * 4 + sw) * MPF + (lane >> 3);
        if (t < 54 && (lane >> 3) < MPF) touch(wmod + (size_t)((t & 1) * 288 + r) * 3456 + (t >> 1) * 64, sm);
      }
#pragma unroll
      for (int i = 0; i < 2; i++) dma16(ctab.v + (sw + 4 * i) * 256 + lane * 4, (u32)(uintptr_t)(sm + L_ONE) + (sw + 4 * i) * 1024);
      if (sw < 3 && lane < 30) lds_st<u32x4>(sm + L_U + sw * UB + 4096 + lane * 16, zero4);
      lds_st<u32x4>(sm + L_KA + tp * 16, zero4);
      if (tp < 176) lds_st<u32x4>(sm + L_KA + 4096 + tp * 16, zero4);
      SB();
    }
    TA(2);
    u32x4 a0, a1, a2; u32 ep;
    if (cinit && MPF) asm volatile("s_waitcnt vmcnt(3) ; PRE_WAIT" : "={v[108:111]}"(a0), "={v[112:115]}"(a1), "={v[116:119]}"(a2), "={v124}"(ep));
    else if (cinit) asm volatile("s_waitcnt vmcnt(2) ; PRE_WAIT" : "={v[108:111]}"(a0), "={v[112:115]}"(a1), "={v[116:119]}"(a2), "={v124}"(ep));
    else asm volatile("s_waitcnt vmcnt(0) ; PRE_WAIT" : "={v[108:111]}"(a0), "={v[112:115]}"(a1), "={v[116:119]}"(a2), "={v124}"(ep));
    TA(3);
    lchar* im = sm + L_SX + sw * (8 * SXS);
    lds_st<u32x4>(im + row * SXS + j8 * 16, a0); lds_st<u32x4>(im + row * SXS + 128 + j8 * 16, a1); lds_st<u32x4>(im + row * SXS + 256 + min(j8, 1) * 16, a2);
    CBAR();
    lchar* rp_ = im + (lane & 3) * 2 * SXS + (((lane >> 2) * 6) & ~3);
    u32 qa0[3], qa1[3], qb0[3], qb1[3];
#pragma unroll
    for (int i = 0; i < 3; i++) { qa0[i] = lds_ld<u32>(rp_ + 96 * i); qa1[i] = lds_ld<u32>(rp_ + 96 * i + 4); qb0[i] = lds_ld<u32>(rp_ + SXS + 96 * i); qb1[i] = lds_ld<u32>(rp_ + SXS + 96 * i + 4); }
    float s1 = 0.f, s2 = 0.f, s3 = 0.f, s4 = 0.f, s5 = 0.f, s6 = 0.f;
#pragma unroll
    for (int j = 0; j < 4; j++) {
      s1 = dot2(a0[j], 0x3f803f80u, s1); s2 = dot2(a0[j], a0[j], s2); s3 = dot2(a1[j], 0x3f803f80u, s3); s4 = dot2(a1[j], a1[j], s4);
      s5 = dot2(a2[j], 0x3f803f80u, s5); s6 = dot2(a2[j], a2[j], s6);
    }
    const float m2 = j8 < 2 ? 1.f : 0.f;
    s1 = __builtin_fmaf(s5, m2, s1 + s3); s2 = __builtin_fmaf(s6, m2, s2 + s4);
    s1 += dppf<0xB1>(s1); s2 += dppf<0xB1>(s2); s1 += dppf<0x4E>(s1); s2 += dppf<0x4E>(s2); s1 += dppf<0x141>(s1); s2 += dppf<0x141>(s2);
    if (j8 == 0) lds_st<u32x4>(sm + L_SP + (row * 4 + sw) * 16, u32x4{__float_as_uint(s1), __float_as_uint(s2), 1u, 1u});
    if (cinit && sw == 0) dma16(ctab.v + 8 * 256 + lane * 4, (u32)(uintptr_t)(sm + L_ONE) + 8 * 1024);
    tag = __builtin_amdgcn_readfirstlane(ep) + 1u;
    if (tp == 0) lds_st<u32>(sm + L_MISC, tag);
    TA(4);
    const u32 sh16 = (lane & 4) * 4;
    float xa[9], xb[9];
#pragma unroll
    for (int i = 0; i < 3; i++) {
      const u32 u0 = __builtin_amdgcn_alignbit(qa1[i], qa0[i], sh16), u1 = qa1[i] >> sh16, v0 = __builtin_amdgcn_alignbit(qb1[i], qb0[i], sh16), v1 = qb1[i] >> sh16;
      xa[3 * i] = bflo(u0); xa[3 * i + 1] = bfhi(u0); xa[3 * i + 2] = bflo(u1); xb[3 * i] = bflo(v0); xb[3 * i + 1] = bfhi(v0); xb[3 * i + 2] = bflo(v1);
#pragma unroll
      for (int c = 0; c < 3; c++) { VKEEP(xa[3 * i + c]); VKEEP(xb[3 * i + c]); }
    }
    u32x4 pv;
#pragma unroll 1
    for (int it = 0; it < SPIN_CAP; it++) {
      CBAR();
      pv = lds_ld<u32x4>(sm + L_SP + (lane & 31) * 16);
      if (__all(pv[2] == 1u)) break;
    }
    float t1 = __uint_as_float(pv[0]), t2 = __uint_as_float(pv[1]);
    t1 += dppf<0xB1>(t1); t2 += dppf<0xB1>(t2); t1 += dppf<0x4E>(t1); t2 += dppf<0x4E>(t2);
    const int st = (int)ln_stats(t1, t2);
    const u32 sA = __builtin_amdgcn_ds_bpermute((lane & 3) * 32, st), sB = __builtin_amdgcn_ds_bpermute((lane & 3) * 32 + 16, st);
    const float mA = bflo(sA), rA = frcp(bfhi(sA)), mB = bflo(sB), rB = frcp(bfhi(sB));
    TS(1); TA(5);
#pragma unroll
    for (int i = 0; i < 3; i++) {
      u32x4 o;
      o[3] = tag;
#pragma unroll
      for (int c = 0; c < 3; c++) o[c] = pkbf(rbf(xa[3 * i + c] - mA) * rA, rbf(xb[3 * i + c] - mB) * rB);
      ST128(o, WS_XP + blk * XPS + sw * 3072 + i * 1024 + lane * 16);
    }
    TS(2); TA(6);
    __builtin_amdgcn_s_setprio(0);
    if (cinit) VMW(3);
  };
  u32x4 v[6]; u32 w;
  auto prm_issue = [&](int pr) __attribute__((always_inline)) {
#pragma unroll
    for (int k = 0; k < 6; k++) v[k] = LD128(WS_MODG + (k & 1) * 27648 + (3 + (k >> 1)) * 4608 + pr * 16);
    w = ((const u32*)wr)[pr];
  };
  auto prm_finish = [&](int pr) __attribute__((always_inline)) {
#pragma unroll 1
    for (int it = 0; it < SPIN_CAP; it++) {
      bool ok = true;
#pragma unroll
      for (int k = 0; k < 6; k++) ok &= v[k][1] == tag && v[k][3] == tag;
      if (__all(ok)) break;
      __builtin_amdgcn_s_sleep(4);
      CBAR();
#pragma unroll
      for (int k = 0; k < 6; k++) v[k] = LD128(WS_MODG + (k & 1) * 27648 + (3 + (k >> 1)) * 4608 + pr * 16);
    }
    auto sum = [&](int k, int i) { return rbf(__uint_as_float(v[k][i]) + __uint_as_float(v[k + 1][i])); };
    lds_st<f32x4>(sm + L_PRM + pr * 32, f32x4{rbf(1.f + sum(2, 0)), sum(0, 0), bflo(w), 0.f});
    lds_st<f32x4>(sm + L_PRM + pr * 32 + 16, f32x4{rbf(1.f + sum(2, 2)), sum(0, 2), bfhi(w), 0.f});
    lds_st<f32x2>(sm + L_GB + pr * 8, f32x2{sum(4, 0), sum(4, 2)});
  };

  if (__builtin_expect(isC, 1)) {
    const int tp = tid - 768;
    const bool isL = blk < 128;
    const int rb = (blk & 7) * 16 + (blk >> 3);
    const int pw = (int)((0x3210FE54DCBA9876ull >> (4 * wave)) & 15u), pt = pw * 64 + lane;
    const int xoff = WS_XP + (pt >> 2) * XPS + grp * 64 + (pt & 3) * 16;
    u32x4 xp; u32 hd[3]; float fy[3];
    auto x_issue = [&]() __attribute__((always_inline)) {
      asm volatile("buffer_load_dwordx4 %0, %1, %2, 0 offen sc1" : "=&v"(xp) : "v"(xoff), "s"(rs));
    };
    auto stage_h = [&]() __attribute__((always_inline)) {
      f32x4 kc[3];
#pragma unroll 1
      for (int it = 0; it < (SPIN_CAP << 6); it++) {
        CBAR();
        const u32 fl = lds_ld<u32>(sm + L_SP + 512 + 8);
        CBAR();
        kc[0] = lds_ld<f32x4>(sm + L_MISC + 64); kc[1] = lds_ld<f32x4>(sm + L_MISC + 80); kc[2] = lds_ld<f32x4>(sm + L_MISC + 96);
        if (__builtin_amdgcn_readfirstlane(fl) == 1u) break;
        __builtin_amdgcn_s_sleep(2);
      }
      asm volatile("s_waitcnt vmcnt(0)" : "+v"(xp));
#pragma unroll 1
      for (int it = 0; it < SPIN_CAP; it++) {
        const bool bad = quad_any(xp[3] != tag);
        if (!__any(bad)) break;
        __builtin_amdgcn_s_sleep(XSLP);
        CBAR();
        if (bad) xp = LD128(xoff);
      }
      TS(5); TA(12);
      float hA[3], hB[3];
#pragma unroll
      for (int c = 0; c < 3; c++) {
        hA[c] = rbf(rbf(bflo(xp[c]) * kc[c][0]) + kc[c][1]); hB[c] = rbf(rbf(bfhi(xp[c]) * kc[c][0]) + kc[c][1]);
        lds_st<u32>(sm + L_U + c * UB + (2046 - 2 * pt) * 2, pkh(hB[c], hA[c]));
      }
#pragma unroll
      for (int c = 0; c < 3; c++) { hd[c] = pkbf(hA[c] * kc[c][2], hB[c] * kc[c][2]); VKEEP(hd[c]); fy[c] = kc[c][3]; }
      TS(6); TA(13);
      BAR();
      TA(14);
    };
    auto w_pref = [&](int u) __attribute__((always_inline)) { touch(wd + (size_t)((blk & 7) * 576) * D_ + min(u, 5183) * 64, sm); };
    auto g_pref = [&](int half, int u) __attribute__((always_inline)) {
      const int q = (u * 57) >> 9, r = (blk >> 3) * 12 + (q >> 1);
      touch(((q & 1) ? wu : wg) + (size_t)((r >> 5) * 64 + half * 32 + (r & 31)) * F_ + (blk & 7) * 576 + (u - q * 9) * 64, sm);
    };
    if (wave < 12) {
    const int ch = wave >> 2, role = (wave + ch) & 3, nh = role & 1;
    TA(2);
    u32 r0, r1, r2;
    u64 lw, dw;
    asm volatile("s_waitcnt vmcnt(0) lgkmcnt(0) ; PRE_WAIT" : "={v108}"(r0), "={v109}"(r1), "={v110}"(r2), "={s[92:93]}"(lw), "={s[94:95]}"(dw));
    TA(3);
    const float ldtv = bflo((u32)(lw >> (16 * (ch + (grp & 1)))));
    const u32 ps = lane < 32 ? 0x01000c0cu : 0x03020c0cu;
    const float dt = rbf(fexp(ldtv));
    const auto q0 = __builtin_amdgcn_permlane32_swap(r0, r0, false, false);
    const float e1 = rbf(fexp(__uint_as_float(__builtin_amdgcn_perm(0u, q0[0], ps)))) * dt * -1.44269504088896341f, ai = __uint_as_float(__builtin_amdgcn_perm(0u, q0[1], ps)) * dt;
    TS(1);
    const float ch_ = 0.15915494309189535f, cl_ = (float)(0.15915494309189533577 - (double)0.15915494309189535f);
    const float th = ai * ch_, tl = __builtin_fmaf(ai, cl_, __builtin_fmaf(ai, ch_, -th));
    lchar* pw = sm + ch * GTAB + nh * 256 + lane * 4;
    if (role < 2) {
      const u16x2 mp = __builtin_elementwise_max(__builtin_bit_cast(u16x2, r1 & 0x7fff7fffu), __builtin_bit_cast(u16x2, r2 & 0x7fff7fffu));
      const u32 mx = wave_max_u32(max((u32)mp[0], (u32)mp[1]));
      const int eC = min(max((int)(mx >> 7) - 126, -100), 100);
      if (role == 0 && lane == 0) lds_st<float>(sm + L_MISC + 16 + ch * 4, 2.f * dt * pow2i(eC - 7));
      const u32 rc = nh ? r2 : r1;
      const auto q1 = __builtin_amdgcn_permlane32_swap(rc, rc, false, false);
      const f32x2 w1 = cpow(1.f, e1, th, tl), w256 = cpow(256.f, e1, th, tl);
      const f32x2 C = f32x2{__uint_as_float(__builtin_amdgcn_perm(0u, q1[0], ps)), __uint_as_float(__builtin_amdgcn_perm(0u, q1[1], ps))} * pow2i(13 - eC);
      const f32x2 w2 = cmul(w1, w1), w4 = cmul(w2, w2);
      TA(4, "v"(w4));
      f32x2 z = C, y = C;
#pragma unroll
      for (int k = 0; k < 8; k++) {
        y = cmul(y, w256);
        lds_st<u32>(pw + k * GROW, pkh_rne(z)); lds_st<u32>(pw + (8 + k) * GROW, pkh_rne(y));
        if (k < 7) z = cmul(z, w1);
        if (k == 0) { f32x2 v = y;
#pragma unroll
          for (int q = 0; q < 3; q++) { v = cmul(v, w2); lds_st<u32>(pw + (16 + q) * GROW, pkh_rne(v)); } }
        if (k == 1 || k == 2) lds_st<u32>(pw + (18 + k) * GROW, pkh_rne(cmul(y, w4)));
      }
    } else {
      const f32x2 w8 = cpow(8.f, e1, -th, -tl);
      f32x2 z = {16384.f, 0.f}, y = cpow(128.f, e1, -th, -tl) * 16384.f;
      TA(4, "v"(y));
      pw += 21 * GROW;
#pragma unroll 4
      for (int k = 0; k < 16; k++) {
        lds_st<u32>(pw + k * GROW, pkh_rne(z)); lds_st<u32>(pw + (16 + k) * GROW, pkh_rne(y));
        z = cmul(z, w8); y = cmul(y, w8);
      }
    }
    TS(2);
      if (wave == 0 && lane == 0) lds_st<u64>(sm + L_MISC + 32, dw);
      TA(7);
      BAR();
      TS(3); TA(8);
      tag = __builtin_amdgcn_readfirstlane(lds_ld<u32>(sm + L_MISC));
      lchar* pa = sm + ch * GTAB + ((wave & 2) * 8 + i16) * GROW + g4 * 16;
      lchar* pb = sm + ch * GTAB + (21 + (wave & 1) * 16 + i16) * GROW + g4 * 16;
      const u32x4 ko = lds_ld<u32x4>(sm + L_KO + (tid & 255) * 16);
      u32x4 fa[8], fb[8];
#pragma unroll
      for (int q = 0; q < 8; q++) { fa[q] = lds_ld<u32x4>(pa + 64 * q); fb[q] = lds_ld<u32x4>(pb + 64 * q); }
      SB();
      x_issue();
      const int sd = wave & 3, cc = sd == 3 ? ch : sd, x0 = 1 + i16 + 8 * g4;
      u32 ka = (u32)(uintptr_t)(sm + L_KN + cc * KCH + (x0 & 3) * KCS + (x0 & ~3) * 2);
      lchar* ub = sm + L_U + cc * UB + (2032 - 16 * i16 + 8 * g4) * 2 - 7 * 512;
      lchar* yp = sm + L_Y + cc * 20480 + i16 * 16 + g4 * 320;
      lchar* kt = sm + L_KA + cc * KAB + KA_E3 + (i16 - 4 * g4 - 3) * 4;
      lchar* one = sm + L_ONE + lane * 16;
      VKEEP(ka); VKEEP(ub); VKEEP(yp); VKEEP(kt); VKEEP(one);
      SB();
      f32x4 a0 = {}, a1 = {};
#pragma unroll
      for (int q = 0; q < 4; q++) { a0 = mm16(fa[2 * q], fb[2 * q], a0); a1 = mm16(fa[2 * q + 1], fb[2 * q + 1], a1); }
      a0 = (a0 + a1) * 9.5367431640625e-07f;
      lchar* T = sm + L_KA + ch * KAB;
      const u32 h01 = pkh_rne(f32x2{a0[0], a0[1]}), h23 = pkh_rne(f32x2{a0[2], a0[3]});
      lds_st<u16>(T + ko[0], (u16)h01); lds_st<u16>(T + ko[1], (u16)(h01 >> 16)); lds_st<u16>(T + ko[2], (u16)h23); lds_st<u16>(T + ko[3], (u16)(h23 >> 16));
      if ((tid & 255) == 32) {
        lds_st<u16>(T + 32 + 1026, (u16)(h01 >> 16)); CBAR(); lds_st<u16>(T + 32 + 1028, (u16)(h01 >> 16));
        lds_st<u16>(T + 32 + 1158, (u16)(h23 >> 16)); CBAR(); lds_st<u16>(T + 32 + 1160, (u16)(h23 >> 16));
      }
      TA(9);
      BAR();
      TS(4); TA(10);
      TA(11);
      FarPre fp;
      if (wave < 3) far_pre(fp, kt, one);
      stage_h();
      if (wave < 8 || wave == 11) {
        if (sd == 3) {
          near_conv<0, NLO>(ka, (u32)(uintptr_t)ub + (8 - NLO) * 512, yp);
          CBAR();
          if (lane == 0) __hip_atomic_fetch_add((LAS u32*)(sm + L_CE), 1u, __ATOMIC_RELAXED, __HIP_MEMORY_SCOPE_WORKGROUP);
        }
        else if (ch == 1) { __builtin_amdgcn_s_setprio(HPRIO); near_conv<NLO, 8 - NLO>(ka, (u32)(uintptr_t)ub, yp); __builtin_amdgcn_s_setprio(0); }
        else far_conv(fp, kt, ub + 7 * 512, one, yp + 10240, sm + L_CE, lane);
      } else {
        if (isL) {
          if (wave < 10) {
#pragma unroll
            for (int i = 0; i < 10; i++) {
              const int e = i * 128 + tid - 512;
              if (i < 9 || wave == 8) dma16((const char*)x + rb * 18432 + min(e - ((e * 898) >> 16), 1151) * 16, (u32)(uintptr_t)(sm + L_XR) + (i * 2 + wave - 8) * 1024);
            }
#pragma unroll
            for (int i = 0; i < 9; i++) ST128(sent4, WS_H2 + rb * 18432 + (i * 128 + tid - 512) * 16);
            VMW(9);
          } else {
            __builtin_amdgcn_s_sleep(PSLP); prm_issue(256 + (lane & 31));
          }
        } else {
          if (SLOWP && wave == 10) {
            const int r = 576 - SLOWP * 8 + ((blk - 128) >> 3) * SLOWP + min(lane >> 1, SLOWP - 1);
            touch(((lane & 1) ? wu : wg) + (size_t)r * F_ + unit(7) * 64, sm);
          }
          if (tid - 512 < PA) w_pref(((blk - 128) >> 3) * PA + tid - 512);
        }
      }
      TS(7);
    } else {
      ln1_rows(true);
      if ((WPREF & 2) && tp < 216) g_pref(0, tp);
      if ((WPREF & 8) && tp < 216) g_pref(1, tp);
      TA(7);
      BAR();
      TS(3); TA(8);
      const int cl = min(lane >> 3, 2), md = WS_MODG + (lane & 1) * 27648 + (min((lane >> 1) & 3, 2) * 576 + grp * 3 + cl) * 8;
      u32x2 mg;
      auto consts = [&]() __attribute__((always_inline)) {
        const float dk = bflo((u32)(lds_ld<u64>(sm + L_MISC + 32) >> (16 * (cl + (grp & 1))))), t = __uint_as_float(mg[0]);
        const float sa = rbf(dppf<0xA0>(t) + dppf<0xF5>(t));
        const float g1 = rbf(1.f + dppf<0xAA>(sa));
        if ((lane & 7) == 4) lds_st<float>(sm + L_MISC + 48 + cl * 4, sa);
        u32 eH = min(max(__float_as_uint(__builtin_fmaf(32.f, fabsf(g1), fabsf(sa))) >> 23, 26u), 226u);
        const float sh = __uint_as_float((264u - eH) << 23), ish = __uint_as_float((eH - 10u) << 23);
        if ((lane & 7) == 0) lds_st<f32x4>(sm + L_MISC + 64 + cl * 16, f32x4{g1 * sh, sa * sh, dk * ish, ish * lds_ld<float>(sm + L_MISC + 16 + cl * 4)});
        CBAR();
        lds_st<u32>(sm + L_SP + 512 + 8, 1u);
      };
      x_issue();
      bool done = true;
      if (wave == 15) {
        __builtin_amdgcn_s_setprio(3);
        __builtin_amdgcn_s_sleep(MSLP);
        mg = LD64(md);
        done = __all(mg[1] == tag);
        if (done) consts();
        else asm volatile("buffer_load_dwordx2 %0, %1, %2, 0 offen sc1" : "=&v"(mg) : "v"(md), "s"(rs));
      }
      TA(9);
      BAR();
      TS(4); TA(10);
      if (!done) {
        asm volatile("s_waitcnt vmcnt(0)" : "+v"(mg));
#pragma unroll 1
        for (int it = 0; it < SPIN_CAP; it++) {
          if (__all(mg[1] == tag)) break;
          __builtin_amdgcn_s_sleep(2);
          CBAR();
          mg = LD64(md);
        }
        consts();
      }
      __builtin_amdgcn_s_setprio(1);
      if (tp < 234) {
        const int ch = (tp * 841) >> 16, pi = tp - ch * 78;
        lchar* T = sm + L_KA + ch * KAB + pi * 16;
        const u32x4 in = lds_ld<u32x4>(T);
        const u32x2 nx = lds_ld<u32x2>(T + 16);
        const bool b1 = (u32)(pi - 34) < 32u, n1 = (u32)(pi - 33) < 32u;
        const u32 p0 = b1 ? 0x01000100u : 0x03020100u, p1 = b1 ? 0x05040100u : 0x03020100u, p4 = n1 ? 0x01000100u : 0x03020100u;
        const u32 d0 = __builtin_amdgcn_perm(0u, in[0], p0), d1 = __builtin_amdgcn_perm(in[2], in[1], p1), d2 = __builtin_amdgcn_perm(0u, in[2], p0),
                  d3 = __builtin_amdgcn_perm(nx[0], in[3], p1), d4 = __builtin_amdgcn_perm(0u, nx[0], p4);
        const u32 s0 = __builtin_amdgcn_alignbit(d1, d0, 16), s1 = __builtin_amdgcn_alignbit(d2, d1, 16), s2 = __builtin_amdgcn_alignbit(d3, d2, 16),
                  s3 = __builtin_amdgcn_alignbit(d4, d3, 16), s4 = __builtin_amdgcn_alignbit(nx[1], d4, 16);
        lchar* p = sm + L_KN + ch * KCH + pi * 16;
        lds_st<u32x4>(p, u32x4{d0, d1, d2, d3});
        lds_st<u32x4>(p + KCS, u32x4{s0, s1, s2, s3});
        lds_st<u32x4>(p + 2 * KCS, u32x4{d1, d2, d3, d4});
        lds_st<u32x4>(p + 3 * KCS, u32x4{s1, s2, s3, s4});
      }
      TA(11);
      stage_h();
      __builtin_amdgcn_s_setprio(0);
      if ((WPREF & 16) && tp < 216) g_pref(0, tp);
      if ((WPREF & 4) && tp < 216) g_pref(1, tp);
      if (!isL) {
        if (tp < 144) touch(((tp & 1) ? wu : wg) + (size_t)(((blk - 128) >> 3) * 72 + (tp >> 1)) * F_ + unit(8) * 64, sm);
        if (tp >= 184) w_pref((unit(8) % 9) * 576 + ((blk - 128) >> 3) * 72 + tp - 184);
        if (192 + tp < PA) w_pref(((blk - 128) >> 3) * PA + 192 + tp);
      }
      if (isL) { __builtin_amdgcn_s_sleep(PSLP); prm_issue(tp); }
      TS(7);
    }
    {
      u32 ya = (u32)(uintptr_t)(sm + L_Y + (pt >> 7) * 1280 + ((pt >> 3) & 15) * 16 + ((pt >> 1) & 3) * 320 + (pt & 1) * 8), ga_a = (u32)(uintptr_t)(sm + L_MISC + 48);
      float hl[3], hh[3];
#pragma unroll
      for (int c = 0; c < 3; c++) { hl[c] = bflo(hd[c]); hh[c] = bfhi(hd[c]); VKEEP(hl[c]); VKEEP(hh[c]); }
      VKEEP(ya); VKEEP(ga_a);
      if (pw >= EARLYW) BAR();
      else {
#pragma unroll 1
        for (int it = 0; it < (SPIN_CAP << 6); it++) {
          CBAR();
          if (__builtin_amdgcn_readfirstlane(lds_ld<u32>(sm + L_CE)) == 0x303u) break;
          __builtin_amdgcn_s_sleep(2);
        }
        CBAR();
      }
      f32x2 yn[3], yf[3]; f32x4 gav;
      DSR64(yn[0], ya, 0); DSR64(yf[0], ya, 10240); DSR64(yn[1], ya, 20480); DSR64(yf[1], ya, 30720); DSR64(yn[2], ya, 40960); DSR64(yf[2], ya, 51200);
      DSR128(gav, ga_a, 0);
      asm volatile("s_waitcnt lgkmcnt(0)" : "+v"(yn[0]), "+v"(yf[0]), "+v"(yn[1]), "+v"(yf[1]), "+v"(yn[2]), "+v"(yf[2]), "+v"(gav));
      if (pw >= 4) {
#pragma unroll
        for (int c = 0; c < 3; c++) yn[c] += yf[c];
      }
      u32x4 o;
      o[3] = tag;
#pragma unroll
      for (int c = 0; c < 3; c++) o[c] = pkbf(gav[c] * rbf(rbf(yn[c][0] * fy[c]) + hl[c]), gav[c] * rbf(rbf(yn[c][1] * fy[c]) + hh[c]));
      ST128(o, WS_X1 + grp * X1S + pt * 16);
      if (pw < EARLYW) BAR();
    }
    TS(8); TB(1);
    if (isL) {
      if (wave < LW) {
        constexpr int NG = 24 / LW, GS = 8 * LW, NC = 3 * NG;
        const int rp = lane & 7, gl = tid >> 3, xo = WS_X1 + gl * X1S + rb * 128 + rp * 16;
        if (rb >= LOWRB) { vm0(); if (X1SLP) __builtin_amdgcn_s_sleep(X1SLP); }
        TB(2);
        u32x4 xi[NG];
#pragma unroll
        for (int k = 0; k < NG; k++) xi[k] = LD128(xo + k * GS * X1S);
        SB();
        float xa[NC], xb[NC];
#pragma unroll
        for (int k = 0; k < 2 * NG; k++) {
          lchar* xr = sm + L_XR + (rp * 2 + (k & 1)) * 1168 + (k >> 1) * GS * 6 + ((gl * 6) & ~3);
          const u32 w0 = lds_ld<u32>(xr), w1 = lds_ld<u32>(xr + 4), sh16 = (gl & 1) * 16;
          const u32 u0 = __builtin_amdgcn_alignbit(w1, w0, sh16);
          float* d = ((k & 1) ? xb : xa) + (k >> 1) * 3;
          d[0] = bflo(u0); d[1] = bfhi(u0); d[2] = bflo(w1 >> sh16);
          VKEEP(d[0]); VKEEP(d[1]); VKEEP(d[2]);
        }
        lchar* im = sm + L_IMX + rp * IMS + gl * 12;
        lchar* rw = sm + L_RED + (wave * 8 + rp) * 16 + g4 * 4;
        VKEEP(im); VKEEP(rw);
        TB(3);
#pragma unroll 1
        for (int it = 0; it < SPIN_CAP; it++) {
          bool b_ = false;
#pragma unroll
          for (int k = 0; k < NG; k++) b_ |= xi[k][3] != tag;
          const bool bad = quad_any(b_);
          if (!__any(bad)) break;
          CBAR();
          if (bad) {
#pragma unroll
            for (int k = 0; k < NG; k++) xi[k] = LD128(xo + k * GS * X1S);
          }
        }
        TS(9); TB(4);
        auto red4 = [](float a, float b, float c, float d) { float v = swap16_add(swap32_add(a, c), swap32_add(b, d)); v += dppf<0x128>(v); return v; };
        {
          float s1a = 0.f, s2a = 0.f, s1b = 0.f, s2b = 0.f;
#pragma unroll
          for (int c = 0; c < NC; c++) {
            const u32 t = xi[c / 3][c % 3];
            xa[c] = rbf(xa[c] + bflo(t)); xb[c] = rbf(xb[c] + bfhi(t));
            s1a += xa[c]; s2a = __builtin_fmaf(xa[c], xa[c], s2a); s1b += xb[c]; s2b = __builtin_fmaf(xb[c], xb[c], s2b);
          }
          const float u = red4(s1a, s2a, s1b, s2b);
          if ((lane & 8) == 0) lds_st<float>(rw, u);
        }
        f32x4 pm[NC];
        if (PMEARLY) {
#pragma unroll 1
          for (int it = 0; it < (SPIN_CAP << 6); it++) {
            CBAR();
            const u32 fl = lds_ld<u32>(sm + L_SP + 512 + 8);
            CBAR();
#pragma unroll
            for (int c = 0; c < NC; c++) pm[c] = lds_ld<f32x4>(sm + L_PRM + (gl * 3 + (c / 3) * GS * 3 + c % 3) * 16);
            if (__builtin_amdgcn_readfirstlane(fl) == 6u) break;
            __builtin_amdgcn_s_sleep(2);
          }
        }
        TB(5);
        BAR();
        TB(6);
        f32x4 t1 = lds_ld<f32x4>(sm + L_RED + lane * 16);
        if (LW > 8) t1 += lds_ld<f32x4>(sm + L_RED + 1024 + lane * 16);
        if (!PMEARLY) {
#pragma unroll
          for (int c = 0; c < NC; c++) pm[c] = lds_ld<f32x4>(sm + L_PRM + (gl * 3 + (c / 3) * GS * 3 + c % 3) * 16);
          SB();
        }
        float mA, mB, rA, rB;
        {
          const float u = red4(t1[0], t1[1], t1[2], t1[3]);
          const auto q = __builtin_amdgcn_permlane16_swap(__float_as_uint(u), __float_as_uint(u), false, false);
          const u32 st = ln_stats(__uint_as_float(q[0]), __uint_as_float(q[1]));
          const auto z = __builtin_amdgcn_permlane32_swap(st, st, false, false);
          mA = bflo(z[0]); rA = frcp(bfhi(z[0])); mB = bflo(z[1]); rB = frcp(bfhi(z[1]));
        }
        TB(7, "v"(rA), "v"(rB));
        float ra = 0.f, rbv = 0.f, ha[NC], hb[NC];
#pragma unroll
        for (int c = 0; c < NC; c++) {
          ha[c] = rbf(rbf(rbf(rbf(xa[c] - mA) * rA) * pm[c][0]) + pm[c][1]); hb[c] = rbf(rbf(rbf(rbf(xb[c] - mB) * rB) * pm[c][0]) + pm[c][1]);
          ra = __builtin_fmaf(ha[c], pm[c][2], ra); rbv = __builtin_fmaf(hb[c], pm[c][2], rbv);
        }
        float v2 = swap32_add(ra, rbv);
        v2 = swap16_add(v2, v2); v2 += dppf<0x128>(v2);
        if ((lane & 24) == 0) lds_st<float>(sm + L_RED2 + (rp * 2 + (lane >> 5)) * 48 + wave * 4, v2);
        TB(8);
        BAR();
        TB(9);
#pragma unroll
        for (int c = 0; c < NC; c++) {
          lds_st<u32>(im + 8 * IMS + (c / 3) * GS * 12 + (c % 3) * 4, perm_hi(__float_as_uint(ha[c]), __float_as_uint(hb[c])));
          lds_st<u32>(im + (c / 3) * GS * 12 + (c % 3) * 4, perm_hi(__float_as_uint(xa[c]), __float_as_uint(xb[c])));
        }
      } else {
        if (wave == 15) __builtin_amdgcn_s_setprio(3);
        if (wave >= 12 || wave == 10) {
          prm_finish(wave == 10 ? 256 + (lane & 31) : tp);
          CBAR();
          if (lane == 0) __hip_atomic_fetch_add((LAS u32*)(sm + L_SP + 512 + 8), 1u, __ATOMIC_RELAXED, __HIP_MEMORY_SCOPE_WORKGROUP);
        } else vm0();
        if (LW > 8 && tp < 32) lds_st<u32x4>(sm + L_RED + 1536 + tp * 16, zero4);
        TB(5);
        BAR();
        BAR();
        if (wave == 15 && lane < 16) {
          f32x4 t0 = lds_ld<f32x4>(sm + L_RED2 + lane * 48) + lds_ld<f32x4>(sm + L_RED2 + lane * 48 + 16);
          if (LW > 8) t0 += lds_ld<f32x4>(sm + L_RED2 + lane * 48 + 32);
          ST64((u32x2{make_key((t0[0] + t0[1]) + (t0[2] + t0[3]), rb * 16 + lane), tag}), WS_KEY + rb * 128 + lane * 8);
        }
      }
      TS(10); TB(10);
      BAR();
      if (HSL2) __builtin_amdgcn_s_sleep(HSL2);
      if (wave < 9) {
        const int rp = (tid * 911) >> 16, cb = (tid - rp * 72) * 8, oo = (rb * 16 + rp * 2) * 1152 + cb * 2;
        lchar* ip = sm + L_IMX + rp * IMS + cb * 4;
        const u32x4 h0 = lds_ld<u32x4>(ip + 8 * IMS), h1 = lds_ld<u32x4>(ip + 8 * IMS + 16), x0 = lds_ld<u32x4>(ip), x1 = lds_ld<u32x4>(ip + 16);
        const f32x4 gA = lds_ld<f32x4>(sm + L_GB + cb * 4), gB = lds_ld<f32x4>(sm + L_GB + cb * 4 + 16);
        ST128((u32x4{nosent(perm_lo(h0[0], h0[1])), nosent(perm_lo(h0[2], h0[3])), nosent(perm_lo(h1[0], h1[1])), nosent(perm_lo(h1[2], h1[3]))}), WS_H2 + oo);
        ST128((u32x4{nosent(perm_hi(h0[0], h0[1])), nosent(perm_hi(h0[2], h0[3])), nosent(perm_hi(h1[0], h1[1])), nosent(perm_hi(h1[2], h1[3]))}), WS_H2 + oo + 1152);
        SB();
        if (OSLP) __builtin_amdgcn_s_sleep(OSLP);
        auto lo = [](u32 xx, u32 hh, float gg) { return bflo(xx) + rbf(gg * bflo(hh)); };
        auto hi = [](u32 xx, u32 hh, float gg) { return bfhi(xx) + rbf(gg * bfhi(hh)); };
        const auto ro = __builtin_amdgcn_make_buffer_rsrc(out, 0, 2048 * 1152, 0x00020000);
        __builtin_amdgcn_raw_buffer_store_b128(u32x4{pkbf(lo(x0[0], h0[0], gA[0]), lo(x0[1], h0[1], gA[1])), pkbf(lo(x0[2], h0[2], gA[2]), lo(x0[3], h0[3], gA[3])),
                                                     pkbf(lo(x1[0], h1[0], gB[0]), lo(x1[1], h1[1], gB[1])), pkbf(lo(x1[2], h1[2], gB[2]), lo(x1[3], h1[3], gB[3]))}, ro, oo, 0, OAUX);
        __builtin_amdgcn_raw_buffer_store_b128(u32x4{pkbf(hi(x0[0], h0[0], gA[0]), hi(x0[1], h0[1], gA[1])), pkbf(hi(x0[2], h0[2], gA[2]), hi(x0[3], h0[3], gA[3])),
                                                     pkbf(hi(x1[0], h1[0], gB[0]), hi(x1[1], h1[1], gB[1])), pkbf(hi(x1[2], h1[2], gB[2]), hi(x1[3], h1[3], gB[3]))}, ro, oo + 1152, 0, OAUX);
      }
      vm0();
      BAR();
      if (tid == 1023) ST32(tag, WS_ODONE + rb * 4);
      TS(11);
    }
    if (!isL && blk < 184) {
      constexpr int PB = (5184 - 8 * PA + 6) / 7;
      if (PDLY & 1) vm0();
      if (PDLY & 2) BAR();
      if (PSL2) __builtin_amdgcn_s_sleep(PSL2);
      if (tid < PB) w_pref(8 * PA + ((blk - 128) >> 3) * PB + tid);
    }
    vm0();
  } else {
    auto m_publish = [&](int p) __attribute__((always_inline)) {
      float s = 0.f;
#pragma unroll
      for (int w = 0; w < 9; w++) s += lds_ld<float>(sm + L_MP + p * 2304 + (w * 64 + lane) * 4);
      ST64((u32x2{__float_as_uint(s), tag}), WS_MODG + mth * 27648 + ((mu + 27 * p) * 64 + lane) * 8);
    };
    if (wave < 9 && hasM) {
      if (MPRIO) __builtin_amdgcn_s_setprio(3);
      TA(2);
      u32x2 cv; u32x4 wv[2][4];
      asm volatile("s_waitcnt vmcnt(4)" : "={v[124:125]}"(cv) :: "v108", "v109", "v110", "v111", "v112", "v113", "v114", "v115", "v116", "v117", "v118", "v119", "v120", "v121", "v122", "v123");
      TA(3);
      float sv[4];
      {
        const float c = __uint_as_float(__builtin_amdgcn_perm(cv[1], cv[0], 0x01000c0cu + 0x02020000u * (lane & 3))), sl = rbf(c * frcp(1.f + fexp(-c)));
        sv[0] = dppf<0x00>(sl); sv[1] = dppf<0x55>(sl); sv[2] = dppf<0xAA>(sl); sv[3] = dppf<0xFF>(sl);
      }
      SB();
      asm volatile("s_waitcnt vmcnt(0) ; PRE_WAIT" : "={v[108:111]}"(wv[0][0]), "={v[112:115]}"(wv[0][1]), "={v[116:119]}"(wv[0][2]), "={v[120:123]}"(wv[0][3]));
      TA(4);
#pragma unroll
      for (int p = 0; p < 2; p++) {
        float a[8] = {};
#pragma unroll
        for (int i = 0; i < 4; i++)
#pragma unroll
          for (int j = 0; j < 4; j++) { a[2 * j] = __builtin_fmaf(sv[i], bflo(wv[p][i][j]), a[2 * j]); a[2 * j + 1] = __builtin_fmaf(sv[i], bfhi(wv[p][i][j]), a[2 * j + 1]); }
        float b[4], c2[2];
#pragma unroll
        for (int k = 0; k < 4; k++) b[k] = swap32_add(a[k], a[k + 4]);
#pragma unroll
        for (int k = 0; k < 2; k++) { c2[k] = swap16_add(b[k], b[k + 2]); c2[k] += dppf<0x128>(c2[k]); }
        if ((lane & 8) == 0) lds_st<f32x2>(sm + L_MP + p * 2304 + (wave * 64 + (lane & 7) * 8 + 2 * (g4 & 1) + 4 * (g4 >> 1)) * 4, f32x2{c2[0], c2[1]});
        CBAR();
        if (lane == 0) __hip_atomic_fetch_add((LAS u32*)(sm + L_SP + 512 + 8), 1u << (8 * p), __ATOMIC_RELAXED, __HIP_MEMORY_SCOPE_WORKGROUP);
        if (p == 0) {
          TA(7);
          __builtin_amdgcn_s_setprio(0);
          BAR();
#pragma unroll
          for (int i = 0; i < 4; i++) wv[1][i] = LDW<u32x4>((const char*)wmod + o0 + i * 6912 + 27 * 128);
          SB();
          TS(3); TA(8);
        }
      }
      TS(4); TA(10);
    } else {
      if (MPRIO) __builtin_amdgcn_s_setprio(MPRIO - 1);
      if (wave >= 12) ln1_rows(false);
      asm volatile("; PRE_WAIT (no loads of its own)");
      auto m_wait = [&](u32 n) __attribute__((always_inline)) {
#pragma unroll 1
        for (int it = 0; it < (SPIN_CAP << 6); it++) {
          CBAR();
          if (((__builtin_amdgcn_readfirstlane(lds_ld<u32>(sm + L_SP + 512 + 8)) >> n) & 255u) == 9u) break;
          __builtin_amdgcn_s_sleep(1);
        }
        CBAR();
      };
      if (wave == 15) __builtin_amdgcn_s_setprio(3);
      if (wave == (PUB11 ? 11 : 15) && hasM) {
        if (PUB11) { __builtin_amdgcn_s_setprio(3); tag = __builtin_amdgcn_readfirstlane(LD32(WS_EPOCH)) + 1u; }
        m_wait(0); m_publish(0);
        if (PUB11) __builtin_amdgcn_s_setprio(0);
      }
      TS(3); TA(8);
      BAR();
      if (wave == 15 && hasM) { m_wait(8); m_publish(1); TS(4); TA(10); }
    }
  }
  if (blk >= 184) {
    const int ub = unit(isC ? 8 : (blk - 192) >> 3);
    u32x4 W[16];
    auto w_gate = [&]() __attribute__((always_inline)) {
#pragma unroll 1
      for (int it = 0; it < (SPIN_CAP << 4); it++) {
        CBAR();
        if ((__builtin_amdgcn_readfirstlane(lds_ld<u32>(sm + L_SP + 512 + 8)) >> 24) >= GCNT) break;
        __builtin_amdgcn_s_sleep(4);
      }
      CBAR();
    };
    if (wave < 9) {
      const u16* pw = ((lane & 8) ? wu : wg) + (size_t)(wave * 64 + g4 * 8) * F_ + ub * 64 + (lane & 7) * 8;
      u32x4 R[16];
#pragma unroll
      for (int j = 0; j < 16; j++) { R[j] = LDW<u32x4>(pw + (size_t)((j >> 3) * 32 + (j & 7)) * F_); CBAR(); }
      VMW(GTHR);
      if (lane == 0) __hip_atomic_fetch_add((LAS u32*)(sm + L_SP + 512 + 8), 1u << 24, __ATOMIC_RELAXED, __HIP_MEMORY_SCOPE_WORKGROUP);
      SB();
#pragma unroll
      for (int s = 0; s < 2; s++)
#pragma unroll
        for (int h = 0; h < 4; h++)
#pragma unroll
          for (int j = 0; j < 4; j++) { W[s * 8 + 2 * h][j] = perm_lo(R[s * 8 + 2 * j][h], R[s * 8 + 2 * j + 1][h]); W[s * 8 + 2 * h + 1][j] = perm_hi(R[s * 8 + 2 * j][h], R[s * 8 + 2 * j + 1][h]); }
    } else if (wave < 15) {
      const u16* pd = wd + (size_t)(ub * 64 + g4 * 16) * D_ + (wave - 9) * 96 + 6 * i16;
      u32x3 R[16];
      if (UNG ? hasM : !isC) w_gate();
#pragma unroll
      for (int j = 0; j < 16; j++) { R[j] = (WNT & 2) ? ldnt<u32x3>(pd + j * D_) : *(const u32x3*)(pd + j * D_); CBAR(); }
#pragma unroll
      for (int h = 0; h < 3; h++)
#pragma unroll
        for (int j = 0; j < 8; j++) { W[(j >> 2) * 6 + 2 * h][j & 3] = perm_lo(R[2 * j][h], R[2 * j + 1][h]); W[(j >> 2) * 6 + 2 * h + 1][j & 3] = perm_hi(R[2 * j][h], R[2 * j + 1][h]); }
    } else {
      lds_st<u32>(sm + L_LIST + lane * 4, 0u);
      lds_st<u32>(sm + L_WM + lane * 4, 0u);
      if (lane == 0) lds_st<u32>(sm + L_SEL + 32, 0u);
#pragma unroll 1
      for (int it = 0; it < SPIN_CAP; it++) {
        if (LD32(WS_KEY + (KHI ? 127 - ub % KHI : ub) * 128 + 4) == tag) break;
        __builtin_amdgcn_s_sleep(KSLP);
        CBAR();
      }
      if (KDLY) __builtin_amdgcn_s_sleep(KDLY);
    }
    TS(7);
    BAR();
    tag = __builtin_amdgcn_readfirstlane(lds_ld<u32>(sm + L_MISC));
    TS(11); TB(1);
    constexpr int KN_ = 16 / KW;
    u32x4 kv[KN_];
    if (wave < KW) {
#pragma unroll
      for (int k = 0; k < KN_; k++) kv[k] = LD128(WS_KEY + (k * KW * 64 + tid) * 16);
#pragma unroll 1
      for (int it = 0; it < SPIN_CAP; it++) {
        bool b_ = false;
#pragma unroll
        for (int k = 0; k < KN_; k++) b_ |= kv[k][1] != tag || kv[k][3] != tag;
        const bool bad = quad_any(b_);
        if (!__any(bad)) break;
        CBAR();
        if (bad) {
#pragma unroll
          for (int k = 0; k < KN_; k++) kv[k] = LD128(WS_KEY + (k * KW * 64 + tid) * 16);
        }
      }
      TS(12); TB(2);
      u32 v = 0;
#pragma unroll
      for (int k = 0; k < KN_; k++) v = max(v, max(kv[k][0], kv[k][2]));
      v = max(v, dppu<0x128>(v));
      const auto a = __builtin_amdgcn_permlane16_swap(v, v, false, false); v = max(a[0], a[1]);
      if ((lane & 24) == 0) __hip_atomic_fetch_max((LAS u32*)(sm + L_WM + (lane & 7) * 4), v, __ATOMIC_RELAXED, __HIP_MEMORY_SCOPE_WORKGROUP);
    }
    BAR();
    TB(3);
    if (wave < KW) {
      u32 th = lds_ld<u32>(sm + L_WM + (lane & 7) * 4);
      th = min(th, dppu<0xB1>(th)); th = min(th, dppu<0x4E>(th)); th = min(th, dppu<0x141>(th));
      u32 cnt = 0;
#pragma unroll
      for (int k = 0; k < 2 * KN_; k++) cnt += kv[k >> 1][(k & 1) * 2] >= th;
      if (cnt) {
        u32 pos;
        asm volatile("ds_add_rtn_u32 %0, %1, %2\n\ts_waitcnt lgkmcnt(0)" : "=v"(pos) : "v"((u32)(uintptr_t)(sm + L_SEL + 32)), "v"(cnt) : "memory");
#pragma unroll
        for (int k = 0; k < 2 * KN_; k++) if (kv[k >> 1][(k & 1) * 2] >= th) { lds_st<u32>(sm + L_LIST + pos * 4, kv[k >> 1][(k & 1) * 2]); pos++; }
      }
    }
    BAR();
    TB(4);
    {
      const u32 nv = lds_ld<u32>(sm + L_SEL + 32), mine = lds_ld<u32>(sm + L_LIST + (tid >> 4) * 4);
      const u32x4 o = lds_ld<u32x4>(sm + L_LIST + i16 * 16);
      SB();
      const int n = __builtin_amdgcn_readfirstlane(nv);
      if (n <= 64) {
        if (wave * 4 < n) {
          u32 rank = (o[0] > mine) + (o[1] > mine) + (o[2] > mine) + (o[3] > mine);
          rank += dppu<0xB1>(rank); rank += dppu<0x4E>(rank); rank += dppu<0x141>(rank); rank += dppu<0x140>(rank);
          if (i16 == 0 && rank < 8) lds_st<u32>(sm + L_SEL + rank * 4, mine);
        }
      } else {
#pragma unroll 1
        for (int i = tid; i < n; i += 1024) {
          const u32 me = lds_ld<u32>(sm + L_LIST + i * 4);
          u32 rank = 0;
#pragma unroll 1
          for (int j = 0; j < n; j++) rank += lds_ld<u32>(sm + L_LIST + j * 4) > me;
          if (rank < 8) lds_st<u32>(sm + L_SEL + rank * 4, me);
        }
      }
    }
    BAR();
    TS(13); TB(5);
    const int dcol = (eb & 7) * 72 + min(lane, 35) * 2, d3 = (dcol * 683) >> 11, dr = dcol - d3 * 3;
    u32 key = 0, hw, od, xr; int lrow = 0; u32x4 ta, tb, gw, gx;
    auto fin_load = [&]() __attribute__((always_inline)) {
      ta = LD128(WS_X1 + d3 * X1S + (lrow >> 1) * 16); tb = LD128(WS_X1 + (d3 + (dr >> 1)) * X1S + (lrow >> 1) * 16);
      hw = LD32(WS_H2 + lrow * 1152 + dcol * 2); od = LD32(WS_ODONE + (lrow >> 4) * 4);
      gw = LD128(WS_MODG + (5 * 576 + dcol) * 8); gx = LD128(WS_MODG + 27648 + (5 * 576 + dcol) * 8);
    };
    if (wave < 9) {
      const int l = 2047 - (int)(lds_ld<u32>(sm + L_SEL + (lane & 7) * 4) & 2047u);
      u32x4 a;
#pragma unroll 1
      for (int it = 0; it < SPIN_CAP; it++) {
        a = LD128(WS_H2 + l * 1152 + (wave * 64 + (i16 >> 3) * 32 + g4 * 8) * 2);
        if (!__any(has_sent(a))) break;
        CBAR();
      }
      TB(6);
      u32x4 a0, a1;
#pragma unroll
      for (int j = 0; j < 4; j++) { a0[j] = __builtin_amdgcn_update_dpp(a[j], a[j], 0x128, 0xf, 0xc, false); a1[j] = __builtin_amdgcn_update_dpp(a[j], a[j], 0x128, 0xf, 0x3, false); }
      const bf16x8 A0 = __builtin_bit_cast(bf16x8, a0), A1 = __builtin_bit_cast(bf16x8, a1);
      f32x4 z[8];
#pragma unroll
      for (int e = 0; e < 8; e++) z[e] = __builtin_amdgcn_mfma_f32_16x16x32_bf16(A0, __builtin_bit_cast(bf16x8, W[e]), f32x4{0.f, 0.f, 0.f, 0.f}, 0, 0, 0);
#pragma unroll
      for (int e = 0; e < 8; e++) z[e] = __builtin_amdgcn_mfma_f32_16x16x32_bf16(A1, __builtin_bit_cast(bf16x8, W[8 + e]), z[e], 0, 0, 0);
      u32 um = g4 >= 2 ? ~0u : 0u; VKEEP(um);
#pragma unroll
      for (int e = 0; e < 4; e++)
        lds_st<f32x4>(sm + L_GP + wave * 4096 + (2 * e + (g4 >> 1)) * 512 + i16 * 32 + (g4 & 1) * 16,
                      f32x4{self(um, z[2 * e + 1][0], z[2 * e][0]), self(um, z[2 * e + 1][1], z[2 * e][1]), self(um, z[2 * e + 1][2], z[2 * e][2]), self(um, z[2 * e + 1][3], z[2 * e][3])});
    } else if (wave == 15 && !isC) {
      key = lds_ld<u32>(sm + L_SEL + (eb >> 3) * 4);
      lrow = 2047 - (int)(key & 2047u);
      xr = *(const u32*)(x + lrow * D_ + dcol);
      fin_load();
    }
    TB(7);
    BAR();
    TB(8);
    if (wave < 8) {
      float gs = 0.f, us = 0.f;
#pragma unroll
      for (int w = 0; w < 9; w++) { gs += lds_ld<float>(sm + L_GP + w * 4096 + wave * 512 + lane * 4); us += lds_ld<float>(sm + L_GP + w * 4096 + wave * 512 + 256 + lane * 4); }
      gs = rbf(gs); us = rbf(us);
      const float sg = rbf(gs * frcp(1.f + fexp(-gs)));
      lds_st<u16>(sm + L_AS + (lane & 7) * 144 + ((lane & 56) + wave) * 2, (u16)(pkbf(sg * us, 0.f)));
    }
    BAR();
    TB(9);
    if (wave >= 9 && wave < 15) {
      lchar* ap = sm + L_AS + (i16 & 7) * 144 + g4 * 32;
      const bf16x8 A0 = lds_ld<bf16x8>(ap), A1 = lds_ld<bf16x8>(ap + 16);
      f32x4 y[6];
#pragma unroll
      for (int e = 0; e < 6; e++) y[e] = __builtin_amdgcn_mfma_f32_16x16x32_bf16(A0, __builtin_bit_cast(bf16x8, W[e]), f32x4{0.f, 0.f, 0.f, 0.f}, 0, 0, 0);
#pragma unroll
      for (int e = 0; e < 6; e++) y[e] = __builtin_amdgcn_mfma_f32_16x16x32_bf16(A1, __builtin_bit_cast(bf16x8, W[6 + e]), y[e], 0, 0, 0);
      const int q = (wave - 9) * 16 + i16, cg = (q * 43) >> 9;
      u32 um = g4 >= 2 ? ~0u : 0u; VKEEP(um);
      const int off = WS_PART + (((g4 & 1) * 32 + cg) * 72 + ub) * 384 + (q - cg * 12) * 32 + (um & 16);
#pragma unroll
      for (int r = 0; r < 4; r++)
        ST128((u32x4{__float_as_uint(self(um, y[3][r], y[0][r])), __float_as_uint(self(um, y[4][r], y[1][r])), __float_as_uint(self(um, y[5][r], y[2][r])), tag}), off + r * 8 * 72 * 384);
      TB(10);
      if (!FSLP) vm0();
    }
    TS(14);
    if (FSLP) __builtin_amdgcn_s_sleep(FSLP); else
    BAR();
    TB(11);
    float xa = 0.f, xb = 0.f, ga_ = 0.f, gb_ = 0.f, rsel = 0.f;
    if (!isC) {
      if (wave < 7) {
        const int po = WS_PART + eb * 27648 + min(tid, 431) * 16;
        u32x4 pv[4];
#pragma unroll
        for (int k = 0; k < 4; k++) pv[k] = LD128(po + k * 6912);
#pragma unroll 1
        for (int it = 0; it < SPIN_CAP; it++) {
          const bool bad = quad_any(pv[0][3] != tag || pv[1][3] != tag || pv[2][3] != tag || pv[3][3] != tag);
          if (!__any(bad)) break;
          CBAR();
          if (bad) {
#pragma unroll
            for (int k = 0; k < 4; k++) pv[k] = LD128(po + k * 6912);
          }
        }
#pragma unroll
        for (int c = 0; c < 3; c++) lds_st<float>(sm + L_FIN + min(tid, 431) * 12 + c * 4, (__uint_as_float(pv[0][c]) + __uint_as_float(pv[1][c])) + (__uint_as_float(pv[2][c]) + __uint_as_float(pv[3][c])));
      } else if (wave == 15) {
#pragma unroll 1
        for (int it = 0; it < SPIN_CAP; it++) {
          if (__all(ta[3] == tag && tb[3] == tag && hw != SENT && od == tag && gw[1] == tag && gw[3] == tag && gx[1] == tag && gx[3] == tag)) break;
          CBAR();
          fin_load();
        }
        const u32 t0 = dr == 0 ? ta[0] : (dr == 1 ? ta[1] : ta[2]), t1 = dr == 0 ? ta[1] : (dr == 1 ? ta[2] : tb[0]);
        xa = rbf(bflo(xr) + ((lrow & 1) ? bfhi(t0) : bflo(t0))); xb = rbf(bfhi(xr) + ((lrow & 1) ? bfhi(t1) : bflo(t1)));
        rsel = rbf(frcp(1.f + fexp(-key_score(key))));
        ga_ = rbf(__uint_as_float(gw[0]) + __uint_as_float(gx[0])); gb_ = rbf(__uint_as_float(gw[2]) + __uint_as_float(gx[2]));
      }
      TB(12);
      BAR();
      TB(13);
      if (wave == 15 && lane < 36) {
        f32x2 y = {0.f, 0.f};
#pragma unroll
        for (int k = 0; k < 18; k++) y += lds_ld<f32x2>(sm + L_FIN + k * 288 + lane * 8);
        *(u32*)(out + lrow * D_ + dcol) = pkbf(xa + rbf(ga_ * rbf(bflo(hw) + rbf(rbf(y[0]) * rsel))), xb + rbf(gb_ * rbf(bfhi(hw) + rbf(rbf(y[1]) * rsel))));
      }
    }
    if (blk == 192 && tid == 0) ST32(tag, WS_EPOCH);
    TS(15); TB(14);
  }
  if (blk == 0 && tid == 0) *(volatile u64*)(ws + (32 << 20)) = t00;
}

__global__ void __launch_bounds__(1024) pfirst(char* ws) {
  u64 t; asm volatile("s_memrealtime %0\n\ts_waitcnt lgkmcnt(0)" : "=s"(t) :: "memory");
  if (blockIdx.x == 0 && threadIdx.x == 0) *(volatile u64*)(ws + (32 << 20)) = t;
}
__global__ void __launch_bounds__(1024) plast(char* ws) {
  if (blockIdx.x == 0 && threadIdx.x < 64) { const u64 goal = *(volatile u64*)(ws + (32 << 20)) + 6000; for (int it = 0; it < 200000; it++) { if (__builtin_amdgcn_s_memrealtime() >= goal) break; __builtin_amdgcn_s_sleep(1); } }
}
extern "C" void kernel_launch(void* const* d_in, const int* in_sizes, int n_in, void* d_out, int out_size, void* d_ws, size_t ws_size, hipStream_t stream) {
  (void)hipFuncSetAttribute((const void*)mega, hipFuncAttributeMaxDynamicSharedMemorySize, LDS_BYTES);
  mega<<<256, 1024, LDS_BYTES, stream>>>((const u16*)d_in[0], (const u16*)d_in[1], (const u16*)d_in[2], (const u16*)d_in[3], (const u16*)d_in[4],
                                         (const u16*)d_in[5], (const u16*)d_in[6], (const u16*)d_in[8], (const u16*)d_in[9], (const u16*)d_in[10],
                                         (const u16*)d_in[11], (const u16*)d_in[12], (const u16*)d_in[13], (u16*)d_out, (char*)d_ws + (WSEND ? ((ws_size - (16u << 20)) & ~(size_t)4095) : 0));
  (void)hipFuncSetAttribute((const void*)plast, hipFuncAttributeMaxDynamicSharedMemorySize, 163840);
  plast<<<256, 1024, 150000, stream>>>((char*)d_ws);
}
```

```cpp
#include <hip/hip_runtime.h>
#include <stdint.h>

typedef unsigned short u16;
typedef unsigned int u32;
typedef unsigned long long u64;
typedef _Float16 f16;
typedef f16 f16x8 __attribute__((ext_vector_type(8)));
typedef f16 f16x2 __attribute__((ext_vector_type(2)));
typedef __bf16 bf16x8 __attribute__((ext_vector_type(8)));
typedef __bf16 bf16x4 __attribute__((ext_vector_type(4)));
typedef __bf16 bf16x2 __attribute__((ext_vector_type(2)));
typedef float f32x2 __attribute__((ext_vector_type(2)));
typedef float f32x4 __attribute__((ext_vector_type(4)));
typedef float f32x16 __attribute__((ext_vector_type(16)));
typedef u16 u16x2 __attribute__((ext_vector_type(2)));
typedef u32 u32x2 __attribute__((ext_vector_type(2)));
typedef u32 u32x3 __attribute__((ext_vector_type(3)));
typedef u32 u32x4 __attribute__((ext_vector_type(4)));
#define LAS __attribute__((address_space(3)))
#define CAS __attribute__((address_space(4)))
#define GAS __attribute__((address_space(1)))
typedef LAS char lchar;

#define DEV __device__ __forceinline__
#define CBAR() asm volatile("" ::: "memory")
#define VKEEP(x) asm volatile("" : "+v"(x))
constexpr int D_ = 576, N_ = 128, F_ = 4608;
constexpr u32 SENT = 0xffffffffu;
constexpr int SPIN_CAP = 1 << 15;
#define THR 63
#define CPRIO 1
#define WNT 1
#define WPREF 1
#define TW 9
#define MSLP 8
#define NLO 3
#define HSLP 4
#define PF1 200
#define PFLAG ""
#define XSLP 2
#define DSLP 32
#define PSLP 38
#define GTHR 4
#define GCNT 9
#define ESLP 4
#define ETHR 1
#define WSEND 0
#define COOP 1
#define MNT ""
#define PADS 128
#define HPRIO 1
#define MFIRST 0
#define OSLP 0
#define HSL2 0
#define FSLP 20
#define X1VM 1
#define X1SLP 15
#define KDLY 0
#define RSLP 0
#define B0LATE 1
#define LW 8
#define PMEARLY 0
#define PA 128
#define PDLY 2
#define OAUX 16
#define XN 0
#define XNT ""
#define KW 16
#define EARLYW 6
#define KHI 36
#define UNG 0
#define SLOWP 32
#define LOWRB 48
#define PSL2 24
#define MPF 2
#define MPRIO 1
#define PUB11 1
#define KSLP 4
#define PREF_L 232

constexpr int WS_EPOCH = 0;
constexpr int WS_MODG = 4096 + 2048 * 8;
constexpr int WS_KEY = WS_MODG + 3456 * 24;
constexpr int WS_ODONE = WS_KEY + 2048 * 8;
constexpr int WS_X1 = 131072;
constexpr int X1S = 16384 + PADS, XPS = 12288 + PADS;
constexpr int WS_H2 = WS_X1 + 192 * X1S;
constexpr int WS_PART = WS_H2 + 576 * 2048 * 2;
constexpr int WS_XP = WS_PART + 64 * 72 * 384;
constexpr int WS_END = WS_XP + 256 * XPS;
constexpr long WS_TIM = WSEND ? -(176l << 20) : (64l << 20);

constexpr int GROW = 544, GTAB = 53 * GROW;
constexpr int L_Y = 0;
constexpr int KCS = 1344, KCH = 4 * KCS;
constexpr int L_KN = 61440;
constexpr int KAB = 2304;
constexpr int KA_E3 = 1280 + 64, KA_E5 = 1536 + 64, KA_E9 = 1792 + 64, KA_DUM = 2176;
constexpr int L_KA = 86528;
constexpr int L_ONE = L_KA + 3 * KAB;
constexpr int L_KO = L_ONE + 5 * 1024;
constexpr int L_U = L_KO + 4096;
constexpr int UB = 4576;
constexpr int IMS = 2304 + 16;
constexpr int L_IMX = L_KN;
constexpr int L_IMH = L_IMX + 8 * IMS;
constexpr int L_RED = L_U + 3 * UB + 64;
constexpr int L_RED2 = L_RED + 2048;
constexpr int L_PRM = L_RED2 + 768;
constexpr int L_GB = L_PRM + 576 * 16;
constexpr int L_MISC = L_GB + 576 * 4;
constexpr int SXS = 304;
constexpr int L_SX = L_PRM;
constexpr int L_SP = L_MISC + 256;
constexpr int L_CE = L_SP + 512 + 24;
constexpr int L_DUM = L_SP + 512 + 32;
constexpr int L_XR = L_DUM + 256;
constexpr int LDS_BYTES = L_XR + 20480;
static_assert(3 * GTAB <= L_KA && L_KN + 3 * KCH <= L_KA && L_IMH + 8 * IMS <= L_RED && LDS_BYTES <= 163840 && LDS_BYTES > 81920, "lds");
constexpr int L_EB = L_KN;
constexpr int L_MP = L_EB;
constexpr int L_GP = 0;
constexpr int L_AS = L_EB + 4608;
constexpr int L_LIST = L_AS + 1152;
constexpr int L_WM = L_LIST + 2112 * 4;
constexpr int L_SEL = L_WM + 512;
constexpr int L_FIN = L_SEL + 64;
static_assert(L_FIN + 18 * 288 <= L_SX, "lds");

struct CTab { u32 v[9 * 256]; };
constexpr CTab make_ctab() {
  CTab t = {};
  const int c0[5] = {18, 26, 21, 20, 19}, wd[5] = {3, 3, 5, 7, 9};
  for (int f = 0; f < 5; f++)
    for (int l = 0; l < 64; l++)
      for (int j = 0; j < 8; j++) {
        const int u = (l & 15) + 8 * (l >> 4) + j - c0[f];
        if (u >= 0 && u < wd[f]) t.v[f * 256 + l * 4 + j / 2] |= 0x3c00u << (16 * (j & 1));
      }
  for (int i = 0; i < 256; i++)
    for (int r = 0; r < 4; r++) {
      const int row = (i >> 7) * 16 + ((i >> 4) & 3) * 4 + r, cg = ((i >> 6) & 1) * 16 + (i & 15);
      int o = KA_DUM + 2 * cg;
      if (row < 8) o = 32 + 2 * (row + 8 * cg);
      else if (row == 8) o = 32 + 2 * (256 + 8 * cg);
      else if (row == 9) o = cg ? KA_E5 + 2 * (cg - 1) : 32 + 2 * 512;
      else if (row == 10) o = KA_E5 + 2 * (31 + cg);
      else if (row == 11) o = cg ? KA_E9 + 2 * (cg - 1) : KA_E5 + 2 * 63;
      else if (row < 16) o = KA_E9 + 2 * (32 * (row - 11) + cg - 1);
      else if (row < 19) o = 32 + 2 * (258 + 2 * (row - 16) + 8 * cg);
      else if (row < 21) o = KA_E3 + 2 * (32 * (row - 19) + cg);
      t.v[5 * 256 + i * 4 + r] = (u32)o;
    }
  return t;
}
__device__ __attribute__((aligned(128))) const CTab ctab = make_ctab();

#define TS(i) do {} while (0)
#define TA(i, ...) do {} while (0)
#define TB(i, ...) do {} while (0)

DEV float bf2f(u16 v) { return __uint_as_float(((u32)v) << 16); }
DEV float bflo(u32 v) { return __uint_as_float(v << 16); }
DEV float bfhi(u32 v) { return __uint_as_float(v & 0xffff0000u); }
DEV u32 pkbf(float lo, float hi) { f32x2 v = {lo, hi}; return __builtin_bit_cast(u32, __builtin_convertvector(v, bf16x2)); }
DEV float rbf(float x) { return __uint_as_float(pkbf(0.f, x)); }
DEV u32 pkh(float lo, float hi) { return __builtin_bit_cast(u32, __builtin_amdgcn_cvt_pkrtz(lo, hi)); }
DEV float hlo(u32 v) { return (float)__builtin_bit_cast(f16x2, v)[0]; }
DEV float hhi(u32 v) { return (float)__builtin_bit_cast(f16x2, v)[1]; }
DEV float pow2i(int e) { return __uint_as_float((u32)(e + 127) << 23); }
DEV float fexp(float x) { return __builtin_amdgcn_exp2f(x * 1.44269504088896341f); }
DEV float frcp(float x) { return __builtin_amdgcn_rcpf(x); }
DEV float dot2(u32 a, u32 b, float c) { return __builtin_amdgcn_fdot2_f32_bf16(__builtin_bit_cast(bf16x2, a), __builtin_bit_cast(bf16x2, b), c, false); }
DEV u32 selu(u32 m, u32 a, u32 b) { return (m & a) | (~m & b); }
DEV float self(u32 m, float a, float b) { return __uint_as_float(selu(m, __float_as_uint(a), __float_as_uint(b))); }
DEV u32 perm_lo(u32 a, u32 b) { return __builtin_amdgcn_perm(b, a, 0x05040100u); }
DEV u32 perm_hi(u32 a, u32 b) { return __builtin_amdgcn_perm(b, a, 0x07060302u); }
template <int CTRL> DEV float dppf(float v) { return __int_as_float(__builtin_amdgcn_update_dpp(0, __float_as_int(v), CTRL, 0xf, 0xf, true)); }
template <int CTRL> DEV u32 dppu(u32 v) { return (u32)__builtin_amdgcn_update_dpp(0, (int)v, CTRL, 0xf, 0xf, true); }
DEV float row_sum(float v) { v += dppf<0xB1>(v); v += dppf<0x4E>(v); v += dppf<0x141>(v); v += dppf<0x140>(v); return v; }
DEV float swap32_add(float a, float b) { auto r = __builtin_amdgcn_permlane32_swap(__float_as_uint(a), __float_as_uint(b), false, false); return __uint_as_float(r[0]) + __uint_as_float(r[1]); }
DEV float swap16_add(float a, float b) { auto r = __builtin_amdgcn_permlane16_swap(__float_as_uint(a), __float_as_uint(b), false, false); return __uint_as_float(r[0]) + __uint_as_float(r[1]); }
DEV u32 wave_max_u32(u32 v) {
  v = max(v, dppu<0xB1>(v)); v = max(v, dppu<0x4E>(v)); v = max(v, dppu<0x141>(v)); v = max(v, dppu<0x140>(v));
  u32 a = __builtin_amdgcn_readlane(v, 0), b = __builtin_amdgcn_readlane(v, 16), c = __builtin_amdgcn_readlane(v, 32), d = __builtin_amdgcn_readlane(v, 48);
  return max(max(a, b), max(c, d));
}
template <typename T> DEV T lds_ld(lchar* p) { return *(LAS T*)p; }
template <typename T> DEV void lds_st(lchar* p, T v) { *(LAS T*)p = v; }
DEV void vm0() { asm volatile("s_waitcnt vmcnt(0)" ::: "memory"); }
DEV u32 ln_stats(float s1, float s2) {
  float mu = s1 * (1.f / 576.f);
  float var = fmaxf(__builtin_fmaf(-mu, mu, s2 * (1.f / 576.f)), 0.f);
  return pkbf(mu, __builtin_amdgcn_sqrtf(rbf(rbf(var) + 1e-5f)));
}

DEV f32x2 cmul(f32x2 a, f32x2 b) {
  f32x2 t, r;
  asm("v_pk_mul_f32 %0, %2, %3 op_sel:[1,1] op_sel_hi:[1,0] neg_lo:[0,1]\n\tv_pk_fma_f32 %1, %2, %3, %0 op_sel_hi:[0,1,1]" : "=&v"(t), "=v"(r) : "v"(a), "v"(b));
  return r;
}
DEV u32 pkh_rne(f32x2 v) { return __builtin_bit_cast(u32, __builtin_convertvector(v, f16x2)); }
DEV f32x2 cpow(float p, float e1, float th, float tl) {
  const float m = __builtin_amdgcn_exp2f(p * e1), a = __builtin_fmaf(p, tl, __builtin_amdgcn_fractf(p * th));
  return f32x2{m * __builtin_amdgcn_cosf(a), m * __builtin_amdgcn_sinf(a)};
}

#define BAR() do { __builtin_amdgcn_sched_barrier(0); asm volatile("s_waitcnt lgkmcnt(0)\n\ts_barrier" ::: "memory"); __builtin_amdgcn_sched_barrier(0); } while (0)
DEV bool quad_any(bool b) { u32 v = b; v |= dppu<0xB1>(v); v |= dppu<0x4E>(v); return v != 0; }
DEV void touch(const void* p, lchar* sm) {
  u32 keep;
  asm volatile("s_mov_b32 %0, m0\n\ts_mov_b32 m0, %2\n\ts_nop 0\n\tglobal_load_lds_dword %1, off" PFLAG "\n\ts_mov_b32 m0, %0" : "=&s"(keep) : "v"(p), "s"((u32)(uintptr_t)(sm + L_DUM)));
}
DEV void dma16(const void* p, u32 lds) {
  u32 keep;
  asm volatile("s_mov_b32 %0, m0\n\ts_mov_b32 m0, %2\n\ts_nop 0\n\tglobal_load_lds_dwordx4 %1, off\n\ts_mov_b32 m0, %0" : "=&s"(keep) : "v"(p), "s"(lds) : "memory");
}
#define DSR64(dst, addr, off) asm volatile("ds_read_b64 %0, %1 offset:%2" : "=v"(dst) : "v"(addr), "i"(off))
#define DSR128(dst, addr, off) asm volatile("ds_read_b128 %0, %1 offset:%2" : "=v"(dst) : "v"(addr), "i"(off))
DEV f32x4 mm16(u32x4 a, u32x4 b, f32x4 c) { return __builtin_amdgcn_mfma_f32_16x16x32_f16(__builtin_bit_cast(f16x8, a), __builtin_bit_cast(f16x8, b), c, 0, 0, 0); }
struct NSet { u32x2 a[4]; u32x4 b[6]; };
#define SB() __builtin_amdgcn_sched_barrier(0)
template <int NB, int OFF, int I> DEV void near_ld1(NSet& s, u32 ka, u32 ua) {
  if constexpr (I == 0) DSR64(s.a[0], ka, OFF);
  else if constexpr (I == 1) DSR64(s.a[1], ka, OFF + 8);
  else if constexpr (I < 2 + NB) DSR128(s.b[NB + 1 - I], ua, OFF + (I - 2) * 512);
  else if constexpr (I == 2 + NB) DSR64(s.a[2], ka, OFF + 512);
  else if constexpr (I == 3 + NB) DSR64(s.a[3], ka, OFF + 520);
}
template <int NB, int OFF, int I = 0> DEV void near_load(NSet& s, u32 ka, u32 ua) {
  if constexpr (I < 4 + NB) { near_ld1<NB, OFF, I>(s, ka, ua); near_load<NB, OFF, I + 1>(s, ka, ua); }
}
template <int NB, int CNT> DEV void near_wait(NSet& s) {
  asm volatile("s_waitcnt lgkmcnt(%7)" : "+v"(s.a[0]), "+v"(s.a[1]), "+v"(s.a[2]), "+v"(s.a[3]), "+v"(s.b[0]), "+v"(s.b[1]), "+v"(s.b[2]) : "i"(CNT));
  if constexpr (NB > 3) VKEEP(s.b[3]);
  if constexpr (NB > 4) VKEEP(s.b[4]);
  if constexpr (NB > 5) VKEEP(s.b[5]);
}
template <int NT, int NB, int OFF, int I = 0> DEV void near_mm(NSet& c, NSet& n, u32 ka, u32 ua, f32x4 (&acc)[5]) {
  if constexpr (I < 2 * NT) {
    constexpr int i = NT - 1 - I % NT, bi = i + NB - NT - I / NT;
    if constexpr (I == 0) near_wait<NB, 0>(c);
    if constexpr (bi >= 0) acc[i] = mm16(I < NT ? u32x4{c.a[0][0], c.a[0][1], c.a[1][0], c.a[1][1]} : u32x4{c.a[2][0], c.a[2][1], c.a[3][0], c.a[3][1]}, c.b[bi], acc[i]);
    SB();
    near_ld1<NB, OFF, 2 * I>(n, ka, ua); near_ld1<NB, OFF, 2 * I + 1>(n, ka, ua);
    SB();
    near_mm<NT, NB, OFF, I + 1>(c, n, ka, ua, acc);
  }
}
template <int C0, int NT> DEV void near_conv(u32 ka, u32 ua, lchar* yp) {
  constexpr int NB = C0 == 0 ? NT : NT + 1;
  f32x4 acc[5] = {};
  NSet s0, s1;
  near_load<NB, 0>(s0, ka, ua);
#pragma unroll 1
  for (int it = 0; it < 4; it++) {
    near_mm<NT, NB, 64>(s0, s1, ka, ua, acc);
    near_mm<NT, NB, 128>(s1, s0, ka, ua, acc);
    ka += 128; ua += 128;
  }
  if (C0 > 0) { DSR64(s1.a[0], ka, 640); DSR64(s1.a[1], ka, 648); DSR128(s1.b[0], ua, 6 * 512); DSR128(s1.b[1], ua, 5 * 512); }
  near_wait<NB, (C0 > 0 ? 4 : 0)>(s0);
  {
    const u32x4 A1 = {s0.a[2][0], s0.a[2][1], s0.a[3][0], s0.a[3][1]};
#pragma unroll
    for (int i = 0; i < NT; i++) if (C0 + i >= 2) acc[i] = mm16(A1, s0.b[i + NB - NT - 1], acc[i]);
  }
  if (C0 > 0) {
    asm volatile("s_waitcnt lgkmcnt(0)" : "+v"(s1.a[0]), "+v"(s1.a[1]), "+v"(s1.b[0]), "+v"(s1.b[1]));
    const u32x4 A = {s1.a[0][0], s1.a[0][1], s1.a[1][0], s1.a[1][1]};
#pragma unroll
    for (int c = 4; c < 8; c++) acc[c - C0] = mm16(A, c < 6 ? s1.b[c - 4] : s0.b[c - 2 - C0], acc[c - C0]);
  }
#pragma unroll
  for (int i = 0; i < NT; i++) lds_st<f32x4>(yp + (C0 + i) * 1280, acc[i]);
}
DEV u32x4 pack2(f32x4 a, f32x4 b) { return u32x4{pkh_rne(f32x2{a[3], b[3]}), pkh_rne(f32x2{a[2], b[2]}), pkh_rne(f32x2{a[1], b[1]}), pkh_rne(f32x2{a[0], b[0]})}; }
DEV u32x4 tapf(lchar* p) { return u32x4{lds_ld<u32>(p), lds_ld<u32>(p + 4), lds_ld<u32>(p + 8), lds_ld<u32>(p + 12)}; }
struct FarPre { u32x4 T3[3], T5[3], O3a, O3b, O5; };
DEV void far_pre(FarPre& f, lchar* kt, lchar* one) {
  f.O3a = lds_ld<u32x4>(one); f.O3b = lds_ld<u32x4>(one + 1024); f.O5 = lds_ld<u32x4>(one + 2048);
#pragma unroll
  for (int e = 0; e < 3; e++) { f.T3[e] = tapf(kt + 64 * e); f.T5[e] = tapf(kt + 256 + 64 * e); }
}
DEV void far_conv(const FarPre& fp, lchar* kt, lchar* up, lchar* one, lchar* yp, lchar* ce, int lane) {
  const f32x4 z = {0.f, 0.f, 0.f, 0.f};
  f32x4 acc[8] = {};
  u32x4 bu = lds_ld<u32x4>(up), bv = lds_ld<u32x4>(up + 16);
  {
    const u32x4 O3a = fp.O3a, O3b = fp.O3b, O5 = fp.O5;
    const u32x4* T3 = fp.T3; const u32x4* T5 = fp.T5;
    u32x4 bu1 = lds_ld<u32x4>(up - 512), bv1 = lds_ld<u32x4>(up - 512 + 16);
    SB();
    f32x4 p0 = mm16(bu, O3a, z), p1 = mm16(bu, O3b, z), p2 = mm16(bu, O5, z), p3 = mm16(bv, O5, z);
#pragma unroll
    for (int n = 0; n < 6; n++) {
      const u32x4 f3 = pack2(p0, p1), f5 = pack2(p2, p3);
      bu = bu1; bv = bv1;
      const int nn = n + 2 < 6 ? n + 2 : n - 4;
      bu1 = lds_ld<u32x4>(up - 512 * nn); bv1 = lds_ld<u32x4>(up - 512 * nn + 16);
      SB();
      if (n < 5) { p0 = mm16(bu, O3a, z); p1 = mm16(bu, O3b, z); p2 = mm16(bu, O5, z); p3 = mm16(bv, O5, z); }
#pragma unroll
      for (int e = 0; e < 3; e++) if (n + 2 + e < 8) acc[n + 2 + e] = mm16(f3, T3[e], acc[n + 2 + e]);
#pragma unroll
      for (int e = 0; e < 3; e++) if (n + 2 + e < 8) acc[n + 2 + e] = mm16(f5, T5[e], acc[n + 2 + e]);
      SB();
      if (n == 1) { lds_st<f32x4>(yp + 2 * 1280, acc[2]); CBAR(); if (lane == 0) __hip_atomic_fetch_add((LAS u32*)ce, 256u, __ATOMIC_RELAXED, __HIP_MEMORY_SCOPE_WORKGROUP); }
      if (n == 2) lds_st<f32x4>(yp + 3 * 1280, acc[3]);
    }
    const u32x4 O7 = lds_ld<u32x4>(one + 3072), O9 = lds_ld<u32x4>(one + 4096);
    const u32x4 bu2 = lds_ld<u32x4>(up - 1024), bv2 = lds_ld<u32x4>(up - 1024 + 16), bu3 = lds_ld<u32x4>(up - 1536), bv3 = lds_ld<u32x4>(up - 1536 + 16);
    u32x4 T9[4];
#pragma unroll
    for (int e = 0; e < 4; e++) T9[e] = tapf(kt + 512 + 64 * e);
    SB();
    const f32x4 q0 = mm16(bu, O7, z), q1 = mm16(bv, O9, z), q2 = mm16(bu1, O7, z), q3 = mm16(bv1, O9, z);
    const f32x4 q4 = mm16(bu2, O7, z), q5 = mm16(bv2, O9, z), q6 = mm16(bu3, O7, z), q7 = mm16(bv3, O9, z);
    const u32x4 f[4] = {pack2(q0, q1), pack2(q2, q3), pack2(q4, q5), pack2(q6, q7)};
#pragma unroll
    for (int n = 0; n < 4; n++) {
#pragma unroll
      for (int e = 0; e < 4; e++) if (n + 4 + e < 8) acc[n + 4 + e] = mm16(f[n], T9[e], acc[n + 4 + e]);
      lds_st<f32x4>(yp + (n + 4) * 1280, acc[n + 4]);
    }
  }
}

DEV u32 make_key(float r, int l) {
  u32 u = __float_as_uint(r);
  u |= (u & 0x7ffu) ? 0x800u : 0u;
  u = (u & 0x80000000u) ? ~u : (u | 0x80000000u);
  return (u & 0xfffff800u) | (u32)(2047 - l);
}
DEV float key_score(u32 k) { return rbf(__uint_as_float(((k & 0x80000000u) ? (k & 0x7fffffffu) : ~k) & 0xfffff800u)); }
#define LD128(off) __builtin_amdgcn_raw_buffer_load_b128(rs, (off), 0, 16)
#define LD64(off) __builtin_amdgcn_raw_buffer_load_b64(rs, (off), 0, 16)
#define LD32(off) __builtin_amdgcn_raw_buffer_load_b32(rs, (off), 0, 16)
#define ST128(v, off) __builtin_amdgcn_raw_buffer_store_b128((v), rs, (off), 0, 16)
#define ST96(v, off) __builtin_amdgcn_raw_buffer_store_b96((v), rs, (off), 0, 16)
#define ST64(v, off) __builtin_amdgcn_raw_buffer_store_b64((v), rs, (off), 0, 16)
#define ST32(v, off) __builtin_amdgcn_raw_buffer_store_b32((v), rs, (off), 0, 16)
DEV bool has_sent(u32x4 v) { return max(max(v[0], v[1]), max(v[2], v[3])) == SENT; }
DEV u32 nosent(u32 v) { return min(v, SENT - 1u); }

#define LD96(off) __builtin_amdgcn_raw_buffer_load_b96(rs, (off), 0, 16)
#define VMW_(n) asm volatile("s_waitcnt vmcnt(" #n ")" ::: "memory")
#define VMW(n) VMW_(n)
DEV float ld_bf_s(const u16* p, int i) { u32 v = *(const CAS u32*)(p + (i & ~1)); return __uint_as_float((i & 1) ? (v & 0xffff0000u) : (v << 16)); }
template <typename T> DEV T ldnt(const void* p) { return __builtin_nontemporal_load((const T*)p); }

template <typename T> DEV T LDW(const void* p) { return __builtin_nontemporal_load((const T*)p); }

__global__ void __launch_bounds__(1024) mega(const u16* __restrict__ x, const u16* __restrict__ lar, const u16* __restrict__ aim,
    const u16* __restrict__ cre, const u16* __restrict__ cim, const u16* __restrict__ ldt, const u16* __restrict__ dsk,
    const u16* __restrict__ wr, const u16* __restrict__ wg, const u16* __restrict__ wu, const u16* __restrict__ wd,
    const u16* __restrict__ cvec, const u16* __restrict__ wmod, u16* __restrict__ out, char* __restrict__ ws) {
  extern __shared__ __attribute__((aligned(16))) char smem_[];
  lchar* sm = (lchar*)smem_;
  u64 pt0, pt1 = 0; asm volatile("s_memrealtime %0\n\ts_waitcnt lgkmcnt(0)" : "=s"(pt0) :: "memory");
  asm volatile("s_load_dword s90, %0, 0x0\n\ts_load_dword s91, %0, 0x40" :: "s"(__builtin_amdgcn_kernarg_segment_ptr()) : "s90", "s91");
  u64 pc0;
  asm volatile("s_getpc_b64 %0" : "=s"(pc0));
  const int tid = threadIdx.x, lane = tid & 63, wave = __builtin_amdgcn_readfirstlane(tid >> 6), blk = blockIdx.x;
  if (wave >= 12 && (blk < 192 || !MFIRST)) __builtin_amdgcn_s_setprio(3);
  if (wave < 9 && blk >= 192 && MFIRST) __builtin_amdgcn_s_setprio(3);
  if (blk >= 248 && tid < PREF_L) touch((const void*)((pc0 & ~127ull) + tid * 128), sm);
  const bool isC = blk < 192;
  const u32x4 sent4 = {SENT, SENT, SENT, SENT}, zero4 = {0u, 0u, 0u, 0u};
  int i16 = lane & 15, g4 = lane >> 4;
  VKEEP(i16); VKEEP(g4);
  u32 tag = 0;
  const int grp = (blk & 7) * 24 + (blk >> 3);
  const int eb = (blk & 7) * 8 + ((blk - 192) >> 3);
  const int mu = (blk - 192) >> 1, mth = blk & 1;
  u32 o0, o1;
  if (wave >= 12) { o1 = ((blk * 8 + (lane >> 3)) * D_ + (wave - 12) * 144) * 2; o0 = o1 + (lane & 7) * 16; o1 += min(lane & 7, 1) * 16; }
  else if (isC) { o1 = ((grp * 3 + (wave >> 2)) * 64 + (lane & 31)) * 4; o0 = o1 + ((wave + (wave >> 2)) & 1) * 128; }
  else { o1 = mth * 288 + (tid >> 3) * 4; o0 = (o1 * 3456 + mu * 64 + (lane & 7) * 8) * 2; o1 *= 2; }
  VKEEP(o0); VKEEP(o1);
  if (tid >= 960 && tid < 994) lds_st<u32>(sm + L_SP + (tid - 960) * 16 + 8, 0u);
  if (!B0LATE) BAR();
  asm volatile("s_waitcnt lgkmcnt(0)" :: "s"(x), "s"(lar), "s"(aim), "s"(cre), "s"(cim), "s"(ldt), "s"(dsk), "s"(wr), "s"(wg), "s"(wu), "s"(wd), "s"(cvec), "s"(wmod), "s"(out), "s"(ws) : "memory");
  asm volatile("s_memrealtime %0\n\ts_waitcnt lgkmcnt(0)" : "=s"(pt1) :: "memory");
  const auto rs = __builtin_amdgcn_make_buffer_rsrc(ws, 0, WS_END, 0x00020000);
#define PRE_CLOB "v108", "v109", "v110", "v111", "v112", "v113", "v114", "v115", "v116", "v117", "v118", "v119", "v120", "v121", "v122", "v123", "v124", "v125"
  if (wave >= 12)
    asm volatile("global_load_dwordx4 v[108:111], %0, %2" XNT " ; PRE_ISSUE\n\tglobal_load_dwordx4 v[112:115], %0, %2 offset:128" XNT " ; PRE_ISSUE\n\tglobal_load_dwordx4 v[116:119], %1, %2 offset:256" XNT " ; PRE_ISSUE\n\tbuffer_load_dword v124, off, %3, 0 sc1 ; PRE_ISSUE"
                 :: "v"(o0), "v"(o1), "s"(x), "s"(rs) : PRE_CLOB);
  if (wave < 12 && isC) {
    asm volatile("global_load_dword v108, %0, off ; PRE_ISSUE\n\ts_load_dwordx2 s[92:93], %1, %3\n\ts_load_dwordx2 s[94:95], %2, %3"
                 :: "v"((const char*)(lane < 32 ? lar : aim) + o0), "s"(ldt), "s"(dsk), "s"((grp * 6) & ~3) : PRE_CLOB, "s92", "s93", "s94", "s95");
    if (((wave + (wave >> 2)) & 2) == 0) asm volatile("global_load_dword v109, %0, off ; PRE_ISSUE\n\tglobal_load_dword v110, %0, off offset:128 ; PRE_ISSUE" :: "v"((const char*)(lane < 32 ? cre : cim) + o1) : PRE_CLOB);
  }
  const bool hasM = !isC && blk < 192 + 54;
  if (wave < 9 && hasM)
    asm volatile("global_load_dwordx2 v[124:125], %4, %6 ; PRE_ISSUE\n\tglobal_load_dwordx4 v[108:111], %0, %5" MNT " ; PRE_ISSUE\n\tglobal_load_dwordx4 v[112:115], %1, %5" MNT " ; PRE_ISSUE\n\tglobal_load_dwordx4 v[116:119], %2, %5" MNT " ; PRE_ISSUE\n\tglobal_load_dwordx4 v[120:123], %3, %5" MNT " ; PRE_ISSUE"
                 :: "v"(o0), "v"(o0 + 6912), "v"(o0 + 2 * 6912), "v"(o0 + 3 * 6912), "v"(o1), "s"(wmod), "s"(cvec) : PRE_CLOB);
  if (B0LATE) { SB(); asm volatile("s_barrier ; 0: the flags are zero (their writer has waited for the arguments, on the same counter)" ::: "memory"); SB(); }
  auto unit = [&](int j) __attribute__((always_inline)) { const int js = (3 - blk - (int)((uintptr_t)wg >> 7)) & 7, q = js + 16 - j; return (blk & 7) * 9 + (q >= 18 ? q - 18 : (q >= 9 ? q - 9 : q)); };
  TA(1);

  auto t_dump = [&]() __attribute__((always_inline)) {
  };
  auto ln1_rows = [&](bool cinit) __attribute__((always_inline)) {
    const int sw = wave - 12, row = lane >> 3, j8 = lane & 7, tp = tid - 768;
    if (cinit) {
      if (MPF) {
        const int t = (blk & 7) + 8 * (lane & 7), r = 288 - 96 * MPF + ((blk >> 3) * 4 + sw) * MPF + (lane >> 3);
        if (t < 54 && (lane >> 3) < MPF) touch(wmod + (size_t)((t & 1) * 288 + r) * 3456 + (t >> 1) * 64, sm);
      }
#pragma unroll
      for (int i = 0; i < 2; i++) dma16(ctab.v + (sw + 4 * i) * 256 + lane * 4, (u32)(uintptr_t)(sm + L_ONE) + (sw + 4 * i) * 1024);
      if (sw < 3 && lane < 30) lds_st<u32x4>(sm + L_U + sw * UB + 4096 + lane * 16, zero4);
      lds_st<u32x4>(sm + L_KA + tp * 16, zero4);
      if (tp < 176) lds_st<u32x4>(sm + L_KA + 4096 + tp * 16, zero4);
      SB();
    }
    TA(2);
    u32x4 a0, a1, a2; u32 ep;
    if (cinit && MPF) asm volatile("s_waitcnt vmcnt(3) ; PRE_WAIT" : "={v[108:111]}"(a0), "={v[112:115]}"(a1), "={v[116:119]}"(a2), "={v124}"(ep));
    else if (cinit) asm volatile("s_waitcnt vmcnt(2) ; PRE_WAIT" : "={v[108:111]}"(a0), "={v[112:115]}"(a1), "={v[116:119]}"(a2), "={v124}"(ep));
    else asm volatile("s_waitcnt vmcnt(0) ; PRE_WAIT" : "={v[108:111]}"(a0), "={v[112:115]}"(a1), "={v[116:119]}"(a2), "={v124}"(ep));
    TA(3);
    lchar* im = sm + L_SX + sw * (8 * SXS);
    lds_st<u32x4>(im + row * SXS + j8 * 16, a0); lds_st<u32x4>(im + row * SXS + 128 + j8 * 16, a1); lds_st<u32x4>(im + row * SXS + 256 + min(j8, 1) * 16, a2);
    CBAR();
    lchar* rp_ = im + (lane & 3) * 2 * SXS + (((lane >> 2) * 6) & ~3);
    u32 qa0[3], qa1[3], qb0[3], qb1[3];
#pragma unroll
    for (int i = 0; i < 3; i++) { qa0[i] = lds_ld<u32>(rp_ + 96 * i); qa1[i] = lds_ld<u32>(rp_ + 96 * i + 4); qb0[i] = lds_ld<u32>(rp_ + SXS + 96 * i); qb1[i] = lds_ld<u32>(rp_ + SXS + 96 * i + 4); }
    float s1 = 0.f, s2 = 0.f, s3 = 0.f, s4 = 0.f, s5 = 0.f, s6 = 0.f;
#pragma unroll
    for (int j = 0; j < 4; j++) {
      s1 = dot2(a0[j], 0x3f803f80u, s1); s2 = dot2(a0[j], a0[j], s2); s3 = dot2(a1[j], 0x3f803f80u, s3); s4 = dot2(a1[j], a1[j], s4);
      s5 = dot2(a2[j], 0x3f803f80u, s5); s6 = dot2(a2[j], a2[j], s6);
    }
    const float m2 = j8 < 2 ? 1.f : 0.f;
    s1 = __builtin_fmaf(s5, m2, s1 + s3); s2 = __builtin_fmaf(s6, m2, s2 + s4);
    s1 += dppf<0xB1>(s1); s2 += dppf<0xB1>(s2); s1 += dppf<0x4E>(s1); s2 += dppf<0x4E>(s2); s1 += dppf<0x141>(s1); s2 += dppf<0x141>(s2);
    if (j8 == 0) lds_st<u32x4>(sm + L_SP + (row * 4 + sw) * 16, u32x4{__float_as_uint(s1), __float_as_uint(s2), 1u, 1u});
    if (cinit && sw == 0) dma16(ctab.v + 8 * 256 + lane * 4, (u32)(uintptr_t)(sm + L_ONE) + 8 * 1024);
    tag = __builtin_amdgcn_readfirstlane(ep) + 1u;
    if (tp == 0) lds_st<u32>(sm + L_MISC, tag);
    TA(4);
    const u32 sh16 = (lane & 4) * 4;
    float xa[9], xb[9];
#pragma unroll
    for (int i = 0; i < 3; i++) {
      const u32 u0 = __builtin_amdgcn_alignbit(qa1[i], qa0[i], sh16), u1 = qa1[i] >> sh16, v0 = __builtin_amdgcn_alignbit(qb1[i], qb0[i], sh16), v1 = qb1[i] >> sh16;
      xa[3 * i] = bflo(u0); xa[3 * i + 1] = bfhi(u0); xa[3 * i + 2] = bflo(u1); xb[3 * i] = bflo(v0); xb[3 * i + 1] = bfhi(v0); xb[3 * i + 2] = bflo(v1);
#pragma unroll
      for (int c = 0; c < 3; c++) { VKEEP(xa[3 * i + c]); VKEEP(xb[3 * i + c]); }
    }
    u32x4 pv;
#pragma unroll 1
    for (int it = 0; it < SPIN_CAP; it++) {
      CBAR();
      pv = lds_ld<u32x4>(sm + L_SP + (lane & 31) * 16);
      if (__all(pv[2] == 1u)) break;
    }
    float t1 = __uint_as_float(pv[0]), t2 = __uint_as_float(pv[1]);
    t1 += dppf<0xB1>(t1); t2 += dppf<0xB1>(t2); t1 += dppf<0x4E>(t1); t2 += dppf<0x4E>(t2);
    const int st = (int)ln_stats(t1, t2);
    const u32 sA = __builtin_amdgcn_ds_bpermute((lane & 3) * 32, st), sB = __builtin_amdgcn_ds_bpermute((lane & 3) * 32 + 16, st);
    const float mA = bflo(sA), rA = frcp(bfhi(sA)), mB = bflo(sB), rB = frcp(bfhi(sB));
    TS(1); TA(5);
#pragma unroll
    for (int i = 0; i < 3; i++) {
      u32x4 o;
      o[3] = tag;
#pragma unroll
      for (int c = 0; c < 3; c++) o[c] = pkbf(rbf(xa[3 * i + c] - mA) * rA, rbf(xb[3 * i + c] - mB) * rB);
      ST128(o, WS_XP + blk * XPS + sw * 3072 + i * 1024 + lane * 16);
    }
    TS(2); TA(6);
    __builtin_amdgcn_s_setprio(0);
    if (cinit) VMW(3);
  };
  u32x4 v[6]; u32 w;
  auto prm_issue = [&](int pr) __attribute__((always_inline)) {
#pragma unroll
    for (int k = 0; k < 6; k++) v[k] = LD128(WS_MODG + (k & 1) * 27648 + (3 + (k >> 1)) * 4608 + pr * 16);
    w = ((const u32*)wr)[pr];
  };
  auto prm_finish = [&](int pr) __attribute__((always_inline)) {
#pragma unroll 1
    for (int it = 0; it < SPIN_CAP; it++) {
      bool ok = true;
#pragma unroll
      for (int k = 0; k < 6; k++) ok &= v[k][1] == tag && v[k][3] == tag;
      if (__all(ok)) break;
      __builtin_amdgcn_s_sleep(4);
      CBAR();
#pragma unroll
      for (int k = 0; k < 6; k++) v[k] = LD128(WS_MODG + (k & 1) * 27648 + (3 + (k >> 1)) * 4608 + pr * 16);
    }
    auto sum = [&](int k, int i) { return rbf(__uint_as_float(v[k][i]) + __uint_as_float(v[k + 1][i])); };
    lds_st<f32x4>(sm + L_PRM + pr * 32, f32x4{rbf(1.f + sum(2, 0)), sum(0, 0), bflo(w), 0.f});
    lds_st<f32x4>(sm + L_PRM + pr * 32 + 16, f32x4{rbf(1.f + sum(2, 2)), sum(0, 2), bfhi(w), 0.f});
    lds_st<f32x2>(sm + L_GB + pr * 8, f32x2{sum(4, 0), sum(4, 2)});
  };

  if (__builtin_expect(isC, 1)) {
    const int tp = tid - 768;
    const bool isL = blk < 128;
    const int rb = (blk & 7) * 16 + (blk >> 3);
    const int pw = (int)((0x3210FE54DCBA9876ull >> (4 * wave)) & 15u), pt = pw * 64 + lane;
    const int xoff = WS_XP + (pt >> 2) * XPS + grp * 64 + (pt & 3) * 16;
    u32x4 xp; u32 hd[3]; float fy[3];
    auto x_issue = [&]() __attribute__((always_inline)) {
      asm volatile("buffer_load_dwordx4 %0, %1, %2, 0 offen sc1" : "=&v"(xp) : "v"(xoff), "s"(rs));
    };
    auto stage_h = [&]() __attribute__((always_inline)) {
      f32x4 kc[3];
#pragma unroll 1
      for (int it = 0; it < (SPIN_CAP << 6); it++) {
        CBAR();
        const u32 fl = lds_ld<u32>(sm + L_SP + 512 + 8);
        CBAR();
        kc[0] = lds_ld<f32x4>(sm + L_MISC + 64); kc[1] = lds_ld<f32x4>(sm + L_MISC + 80); kc[2] = lds_ld<f32x4>(sm + L_MISC + 96);
        if (__builtin_amdgcn_readfirstlane(fl) == 1u) break;
        __builtin_amdgcn_s_sleep(2);
      }
      asm volatile("s_waitcnt vmcnt(0)" : "+v"(xp));
#pragma unroll 1
      for (int it = 0; it < SPIN_CAP; it++) {
        const bool bad = quad_any(xp[3] != tag);
        if (!__any(bad)) break;
        __builtin_amdgcn_s_sleep(XSLP);
        CBAR();
        if (bad) xp = LD128(xoff);
      }
      TS(5); TA(12);
      float hA[3], hB[3];
#pragma unroll
      for (int c = 0; c < 3; c++) {
        hA[c] = rbf(rbf(bflo(xp[c]) * kc[c][0]) + kc[c][1]); hB[c] = rbf(rbf(bfhi(xp[c]) * kc[c][0]) + kc[c][1]);
        lds_st<u32>(sm + L_U + c * UB + (2046 - 2 * pt) * 2, pkh(hB[c], hA[c]));
      }
#pragma unroll
      for (int c = 0; c < 3; c++) { hd[c] = pkbf(hA[c] * kc[c][2], hB[c] * kc[c][2]); VKEEP(hd[c]); fy[c] = kc[c][3]; }
      TS(6); TA(13);
      BAR();
      TA(14);
    };
    auto w_pref = [&](int u) __attribute__((always_inline)) { touch(wd + (size_t)((blk & 7) * 576) * D_ + min(u, 5183) * 64, sm); };
    auto g_pref = [&](int half, int u) __attribute__((always_inline)) {
      const int q = (u * 57) >> 9, r = (blk >> 3) * 12 + (q >> 1);
      touch(((q & 1) ? wu : wg) + (size_t)((r >> 5) * 64 + half * 32 + (r & 31)) * F_ + (blk & 7) * 576 + (u - q * 9) * 64, sm);
    };
    if (wave < 12) {
    const int ch = wave >> 2, role = (wave + ch) & 3, nh = role & 1;
    TA(2);
    u32 r0, r1, r2;
    u64 lw, dw;
    asm volatile("s_waitcnt vmcnt(0) lgkmcnt(0) ; PRE_WAIT" : "={v108}"(r0), "={v109}"(r1), "={v110}"(r2), "={s[92:93]}"(lw), "={s[94:95]}"(dw));
    TA(3);
    const float ldtv = bflo((u32)(lw >> (16 * (ch + (grp & 1)))));
    const u32 ps = lane < 32 ? 0x01000c0cu : 0x03020c0cu;
    const float dt = rbf(fexp(ldtv));
    const auto q0 = __builtin_amdgcn_permlane32_swap(r0, r0, false, false);
    const float e1 = rbf(fexp(__uint_as_float(__builtin_amdgcn_perm(0u, q0[0], ps)))) * dt * -1.44269504088896341f, ai = __uint_as_float(__builtin_amdgcn_perm(0u, q0[1], ps)) * dt;
    TS(1);
    const float ch_ = 0.15915494309189535f, cl_ = (float)(0.15915494309189533577 - (double)0.15915494309189535f);
    const float th = ai * ch_, tl = __builtin_fmaf(ai, cl_, __builtin_fmaf(ai, ch_, -th));
    lchar* pw = sm + ch * GTAB + nh * 256 + lane * 4;
    if (role < 2) {
      const u16x2 mp = __builtin_elementwise_max(__builtin_bit_cast(u16x2, r1 & 0x7fff7fffu), __builtin_bit_cast(u16x2, r2 & 0x7fff7fffu));
      const u32 mx = wave_max_u32(max((u32)mp[0], (u32)mp[1]));
      const int eC = min(max((int)(mx >> 7) - 126, -100), 100);
      if (role == 0 && lane == 0) lds_st<float>(sm + L_MISC + 16 + ch * 4, 2.f * dt * pow2i(eC - 7));
      const u32 rc = nh ? r2 : r1;
      const auto q1 = __builtin_amdgcn_permlane32_swap(rc, rc, false, false);
      const f32x2 w1 = cpow(1.f, e1, th, tl), w256 = cpow(256.f, e1, th, tl);
      const f32x2 C = f32x2{__uint_as_float(__builtin_amdgcn_perm(0u, q1[0], ps)), __uint_as_float(__builtin_amdgcn_perm(0u, q1[1], ps))} * pow2i(13 - eC);
      const f32x2 w2 = cmul(w1, w1), w4 = cmul(w2, w2);
      TA(4, "v"(w4));
      f32x2 z = C, y = C;
#pragma unroll
      for (int k = 0; k < 8; k++) {
        y = cmul(y, w256);
        lds_st<u32>(pw + k * GROW, pkh_rne(z)); lds_st<u32>(pw + (8 + k) * GROW, pkh_rne(y));
        if (k < 7) z = cmul(z, w1);
        if (k == 0) { f32x2 v = y;
#pragma unroll
          for (int q = 0; q < 3; q++) { v = cmul(v, w2); lds_st<u32>(pw + (16 + q) * GROW, pkh_rne(v)); } }
        if (k == 1 || k == 2) lds_st<u32>(pw + (18 + k) * GROW, pkh_rne(cmul(y, w4)));
      }
    } else {
      const f32x2 w8 = cpow(8.f, e1, -th, -tl);
      f32x2 z = {16384.f, 0.f}, y = cpow(128.f, e1, -th, -tl) * 16384.f;
      TA(4, "v"(y));
      pw += 21 * GROW;
#pragma unroll 4
      for (int k = 0; k < 16; k++) {
        lds_st<u32>(pw + k * GROW, pkh_rne(z)); lds_st<u32>(pw + (16 + k) * GROW, pkh_rne(y));
        z = cmul(z, w8); y = cmul(y, w8);
      }
    }
    TS(2);
      if (wave == 0 && lane == 0) lds_st<u64>(sm + L_MISC + 32, dw);
      TA(7);
      BAR();
      TS(3); TA(8);
      tag = __builtin_amdgcn_readfirstlane(lds_ld<u32>(sm + L_MISC));
      lchar* pa = sm + ch * GTAB + ((wave & 2) * 8 + i16) * GROW + g4 * 16;
      lchar* pb = sm + ch * GTAB + (21 + (wave & 1) * 16 + i16) * GROW + g4 * 16;
      const u32x4 ko = lds_ld<u32x4>(sm + L_KO + (tid & 255) * 16);
      u32x4 fa[8], fb[8];
#pragma unroll
      for (int q = 0; q < 8; q++) { fa[q] = lds_ld<u32x4>(pa + 64 * q); fb[q] = lds_ld<u32x4>(pb + 64 * q); }
      SB();
      x_issue();
      const int sd = wave & 3, cc = sd == 3 ? ch : sd, x0 = 1 + i16 + 8 * g4;
      u32 ka = (u32)(uintptr_t)(sm + L_KN + cc * KCH + (x0 & 3) * KCS + (x0 & ~3) * 2);
      lchar* ub = sm + L_U + cc * UB + (2032 - 16 * i16 + 8 * g4) * 2 - 7 * 512;
      lchar* yp = sm + L_Y + cc * 20480 + i16 * 16 + g4 * 320;
      lchar* kt = sm + L_KA + cc * KAB + KA_E3 + (i16 - 4 * g4 - 3) * 4;
      lchar* one = sm + L_ONE + lane * 16;
      VKEEP(ka); VKEEP(ub); VKEEP(yp); VKEEP(kt); VKEEP(one);
      SB();
      f32x4 a0 = {}, a1 = {};
#pragma unroll
      for (int q = 0; q < 4; q++) { a0 = mm16(fa[2 * q], fb[2 * q], a0); a1 = mm16(fa[2 * q + 1], fb[2 * q + 1], a1); }
      a0 = (a0 + a1) * 9.5367431640625e-07f;
      lchar* T = sm + L_KA + ch * KAB;
      const u32 h01 = pkh_rne(f32x2{a0[0], a0[1]}), h23 = pkh_rne(f32x2{a0[2], a0[3]});
      lds_st<u16>(T + ko[0], (u16)h01); lds_st<u16>(T + ko[1], (u16)(h01 >> 16)); lds_st<u16>(T + ko[2], (u16)h23); lds_st<u16>(T + ko[3], (u16)(h23 >> 16));
      if ((tid & 255) == 32) {
        lds_st<u16>(T + 32 + 1026, (u16)(h01 >> 16)); CBAR(); lds_st<u16>(T + 32 + 1028, (u16)(h01 >> 16));
        lds_st<u16>(T + 32 + 1158, (u16)(h23 >> 16)); CBAR(); lds_st<u16>(T + 32 + 1160, (u16)(h23 >> 16));
      }
      TA(9);
      BAR();
      TS(4); TA(10);
      TA(11);
      FarPre fp;
      if (wave < 3) far_pre(fp, kt, one);
      stage_h();
      if (wave < 8 || wave == 11) {
        if (sd == 3) {
          near_conv<0, NLO>(ka, (u32)(uintptr_t)ub + (8 - NLO) * 512, yp);
          CBAR();
          if (lane == 0) __hip_atomic_fetch_add((LAS u32*)(sm + L_CE), 1u, __ATOMIC_RELAXED, __HIP_MEMORY_SCOPE_WORKGROUP);
        }
        else if (ch == 1) { __builtin_amdgcn_s_setprio(HPRIO); near_conv<NLO, 8 - NLO>(ka, (u32)(uintptr_t)ub, yp); __builtin_amdgcn_s_setprio(0); }
        else far_conv(fp, kt, ub + 7 * 512, one, yp + 10240, sm + L_CE, lane);
      } else {
        if (isL) {
          if (wave < 10) {
#pragma unroll
            for (int i = 0; i < 10; i++) {
              const int e = i * 128 + tid - 512;
              if (i < 9 || wave == 8) dma16((const char*)x + rb * 18432 + min(e - ((e * 898) >> 16), 1151) * 16, (u32)(uintptr_t)(sm + L_XR) + (i * 2 + wave - 8) * 1024);
            }
#pragma unroll
            for (int i = 0; i < 9; i++) ST128(sent4, WS_H2 + rb * 18432 + (i * 128 + tid - 512) * 16);
            VMW(9);
          } else {
            __builtin_amdgcn_s_sleep(PSLP); prm_issue(256 + (lane & 31));
          }
        } else {
          if (SLOWP && wave == 10) {
            const int r = 576 - SLOWP * 8 + ((blk - 128) >> 3) * SLOWP + min(lane >> 1, SLOWP - 1);
            touch(((lane & 1) ? wu : wg) + (size_t)r * F_ + unit(7) * 64, sm);
          }
          if (tid - 512 < PA) w_pref(((blk - 128) >> 3) * PA + tid - 512);
        }
      }
      TS(7);
    } else {
      ln1_rows(true);
      if ((WPREF & 2) && tp < 216) g_pref(0, tp);
      if ((WPREF & 8) && tp < 216) g_pref(1, tp);
      TA(7);
      BAR();
      TS(3); TA(8);
      const int cl = min(lane >> 3, 2), md = WS_MODG + (lane & 1) * 27648 + (min((lane >> 1) & 3, 2) * 576 + grp * 3 + cl) * 8;
      u32x2 mg;
      auto consts = [&]() __attribute__((always_inline)) {
        const float dk = bflo((u32)(lds_ld<u64>(sm + L_MISC + 32) >> (16 * (cl + (grp & 1))))), t = __uint_as_float(mg[0]);
        const float sa = rbf(dppf<0xA0>(t) + dppf<0xF5>(t));
        const float g1 = rbf(1.f + dppf<0xAA>(sa));
        if ((lane & 7) == 4) lds_st<float>(sm + L_MISC + 48 + cl * 4, sa);
        u32 eH = min(max(__float_as_uint(__builtin_fmaf(32.f, fabsf(g1), fabsf(sa))) >> 23, 26u), 226u);
        const float sh = __uint_as_float((264u - eH) << 23), ish = __uint_as_float((eH - 10u) << 23);
        if ((lane & 7) == 0) lds_st<f32x4>(sm + L_MISC + 64 + cl * 16, f32x4{g1 * sh, sa * sh, dk * ish, ish * lds_ld<float>(sm + L_MISC + 16 + cl * 4)});
        CBAR();
        lds_st<u32>(sm + L_SP + 512 + 8, 1u);
      };
      x_issue();
      bool done = true;
      if (wave == 15) {
        __builtin_amdgcn_s_setprio(3);
        __builtin_amdgcn_s_sleep(MSLP);
        mg = LD64(md);
        done = __all(mg[1] == tag);
        if (done) consts();
        else asm volatile("buffer_load_dwordx2 %0, %1, %2, 0 offen sc1" : "=&v"(mg) : "v"(md), "s"(rs));
      }
      TA(9);
      BAR();
      TS(4); TA(10);
      if (!done) {
        asm volatile("s_waitcnt vmcnt(0)" : "+v"(mg));
#pragma unroll 1
        for (int it = 0; it < SPIN_CAP; it++) {
          if (__all(mg[1] == tag)) break;
          __builtin_amdgcn_s_sleep(2);
          CBAR();
          mg = LD64(md);
        }
        consts();
      }
      __builtin_amdgcn_s_setprio(1);
      if (tp < 234) {
        const int ch = (tp * 841) >> 16, pi = tp - ch * 78;
        lchar* T = sm + L_KA + ch * KAB + pi * 16;
        const u32x4 in = lds_ld<u32x4>(T);
        const u32x2 nx = lds_ld<u32x2>(T + 16);
        const bool b1 = (u32)(pi - 34) < 32u, n1 = (u32)(pi - 33) < 32u;
        const u32 p0 = b1 ? 0x01000100u : 0x03020100u, p1 = b1 ? 0x05040100u : 0x03020100u, p4 = n1 ? 0x01000100u : 0x03020100u;
        const u32 d0 = __builtin_amdgcn_perm(0u, in[0], p0), d1 = __builtin_amdgcn_perm(in[2], in[1], p1), d2 = __builtin_amdgcn_perm(0u, in[2], p0),
                  d3 = __builtin_amdgcn_perm(nx[0], in[3], p1), d4 = __builtin_amdgcn_perm(0u, nx[0], p4);
        const u32 s0 = __builtin_amdgcn_alignbit(d1, d0, 16), s1 = __builtin_amdgcn_alignbit(d2, d1, 16), s2 = __builtin_amdgcn_alignbit(d3, d2, 16),
                  s3 = __builtin_amdgcn_alignbit(d4, d3, 16), s4 = __builtin_amdgcn_alignbit(nx[1], d4, 16);
        lchar* p = sm + L_KN + ch * KCH + pi * 16;
        lds_st<u32x4>(p, u32x4{d0, d1, d2, d3});
        lds_st<u32x4>(p + KCS, u32x4{s0, s1, s2, s3});
        lds_st<u32x4>(p + 2 * KCS, u32x4{d1, d2, d3, d4});
        lds_st<u32x4>(p + 3 * KCS, u32x4{s1, s2, s3, s4});
      }
      TA(11);
      stage_h();
      __builtin_amdgcn_s_setprio(0);
      if ((WPREF & 16) && tp < 216) g_pref(0, tp);
      if ((WPREF & 4) && tp < 216) g_pref(1, tp);
      if (!isL) {
        if (tp < 144) touch(((tp & 1) ? wu : wg) + (size_t)(((blk - 128) >> 3) * 72 + (tp >> 1)) * F_ + unit(8) * 64, sm);
        if (tp >= 184) w_pref((unit(8) % 9) * 576 + ((blk - 128) >> 3) * 72 + tp - 184);
        if (192 + tp < PA) w_pref(((blk - 128) >> 3) * PA + 192 + tp);
      }
      if (isL) { __builtin_amdgcn_s_sleep(PSLP); prm_issue(tp); }
      TS(7);
    }
    {
      u32 ya = (u32)(uintptr_t)(sm + L_Y + (pt >> 7) * 1280 + ((pt >> 3) & 15) * 16 + ((pt >> 1) & 3) * 320 + (pt & 1) * 8), ga_a = (u32)(uintptr_t)(sm + L_MISC + 48);
      float hl[3], hh[3];
#pragma unroll
      for (int c = 0; c < 3; c++) { hl[c] = bflo(hd[c]); hh[c] = bfhi(hd[c]); VKEEP(hl[c]); VKEEP(hh[c]); }
      VKEEP(ya); VKEEP(ga_a);
      if (pw >= EARLYW) BAR();
      else {
#pragma unroll 1
        for (int it = 0; it < (SPIN_CAP << 6); it++) {
          CBAR();
          if (__builtin_amdgcn_readfirstlane(lds_ld<u32>(sm + L_CE)) == 0x303u) break;
          __builtin_amdgcn_s_sleep(2);
        }
        CBAR();
      }
      f32x2 yn[3], yf[3]; f32x4 gav;
      DSR64(yn[0], ya, 0); DSR64(yf[0], ya, 10240); DSR64(yn[1], ya, 20480); DSR64(yf[1], ya, 30720); DSR64(yn[2], ya, 40960); DSR64(yf[2], ya, 51200);
      DSR128(gav, ga_a, 0);
      asm volatile("s_waitcnt lgkmcnt(0)" : "+v"(yn[0]), "+v"(yf[0]), "+v"(yn[1]), "+v"(yf[1]), "+v"(yn[2]), "+v"(yf[2]), "+v"(gav));
      if (pw >= 4) {
#pragma unroll
        for (int c = 0; c < 3; c++) yn[c] += yf[c];
      }
      u32x4 o;
      o[3] = tag;
#pragma unroll
      for (int c = 0; c < 3; c++) o[c] = pkbf(gav[c] * rbf(rbf(yn[c][0] * fy[c]) + hl[c]), gav[c] * rbf(rbf(yn[c][1] * fy[c]) + hh[c]));
      ST128(o, WS_X1 + grp * X1S + pt * 16);
      if (pw < EARLYW) BAR();
    }
    TS(8); TB(1);
    if (isL) {
      if (wave < LW) {
        constexpr int NG = 24 / LW, GS = 8 * LW, NC = 3 * NG;
        const int rp = lane & 7, gl = tid >> 3, xo = WS_X1 + gl * X1S + rb * 128 + rp * 16;
        if (rb >= LOWRB) { vm0(); if (X1SLP) __builtin_amdgcn_s_sleep(X1SLP); }
        TB(2);
        u32x4 xi[NG];
#pragma unroll
        for (int k = 0; k < NG; k++) xi[k] = LD128(xo + k * GS * X1S);
        SB();
        float xa[NC], xb[NC];
#pragma unroll
        for (int k = 0; k < 2 * NG; k++) {
          lchar* xr = sm + L_XR + (rp * 2 + (k & 1)) * 1168 + (k >> 1) * GS * 6 + ((gl * 6) & ~3);
          const u32 w0 = lds_ld<u32>(xr), w1 = lds_ld<u32>(xr + 4), sh16 = (gl & 1) * 16;
          const u32 u0 = __builtin_amdgcn_alignbit(w1, w0, sh16);
          float* d = ((k & 1) ? xb : xa) + (k >> 1) * 3;
          d[0] = bflo(u0); d[1] = bfhi(u0); d[2] = bflo(w1 >> sh16);
          VKEEP(d[0]); VKEEP(d[1]); VKEEP(d[2]);
        }
        lchar* im = sm + L_IMX + rp * IMS + gl * 12;
        lchar* rw = sm + L_RED + (wave * 8 + rp) * 16 + g4 * 4;
        VKEEP(im); VKEEP(rw);
        TB(3);
#pragma unroll 1
        for (int it = 0; it < SPIN_CAP; it++) {
          bool b_ = false;
#pragma unroll
          for (int k = 0; k < NG; k++) b_ |= xi[k][3] != tag;
          const bool bad = quad_any(b_);
          if (!__any(bad)) break;
          CBAR();
          if (bad) {
#pragma unroll
            for (int k = 0; k < NG; k++) xi[k] = LD128(xo + k * GS * X1S);
          }
        }
        TS(9); TB(4);
        auto red4 = [](float a, float b, float c, float d) { float v = swap16_add(swap32_add(a, c), swap32_add(b, d)); v += dppf<0x128>(v); return v; };
        {
          float s1a = 0.f, s2a = 0.f, s1b = 0.f, s2b = 0.f;
#pragma unroll
          for (int c = 0; c < NC; c++) {
            const u32 t = xi[c / 3][c % 3];
            xa[c] = rbf(xa[c] + bflo(t)); xb[c] = rbf(xb[c] + bfhi(t));
            s1a += xa[c]; s2a = __builtin_fmaf(xa[c], xa[c], s2a); s1b += xb[c]; s2b = __builtin_fmaf(xb[c], xb[c], s2b);
          }
          const float u = red4(s1a, s2a, s1b, s2b);
          if ((lane & 8) == 0) lds_st<float>(rw, u);
        }
        f32x4 pm[NC];
        if (PMEARLY) {
#pragma unroll 1
          for (int it = 0; it < (SPIN_CAP << 6); it++) {
            CBAR();
            const u32 fl = lds_ld<u32>(sm + L_SP + 512 + 8);
            CBAR();
#pragma unroll
            for (int c = 0; c < NC; c++) pm[c] = lds_ld<f32x4>(sm + L_PRM + (gl * 3 + (c / 3) * GS * 3 + c % 3) * 16);
            if (__builtin_amdgcn_readfirstlane(fl) == 6u) break;
            __builtin_amdgcn_s_sleep(2);
          }
        }
        TB(5);
        BAR();
        TB(6);
        f32x4 t1 = lds_ld<f32x4>(sm + L_RED + lane * 16);
        if (LW > 8) t1 += lds_ld<f32x4>(sm + L_RED + 1024 + lane * 16);
        if (!PMEARLY) {
#pragma unroll
          for (int c = 0; c < NC; c++) pm[c] = lds_ld<f32x4>(sm + L_PRM + (gl * 3 + (c / 3) * GS * 3 + c % 3) * 16);
          SB();
        }
        float mA, mB, rA, rB;
        {
          const float u = red4(t1[0], t1[1], t1[2], t1[3]);
          const auto q = __builtin_amdgcn_permlane16_swap(__float_as_uint(u), __float_as_uint(u), false, false);
          const u32 st = ln_stats(__uint_as_float(q[0]), __uint_as_float(q[1]));
          const auto z = __builtin_amdgcn_permlane32_swap(st, st, false, false);
          mA = bflo(z[0]); rA = frcp(bfhi(z[0])); mB = bflo(z[1]); rB = frcp(bfhi(z[1]));
        }
        TB(7, "v"(rA), "v"(rB));
        float ra = 0.f, rbv = 0.f, ha[NC], hb[NC];
#pragma unroll
        for (int c = 0; c < NC; c++) {
          ha[c] = rbf(rbf(rbf(rbf(xa[c] - mA) * rA) * pm[c][0]) + pm[c][1]); hb[c] = rbf(rbf(rbf(rbf(xb[c] - mB) * rB) * pm[c][0]) + pm[c][1]);
          ra = __builtin_fmaf(ha[c], pm[c][2], ra); rbv = __builtin_fmaf(hb[c], pm[c][2], rbv);
        }
        float v2 = swap32_add(ra, rbv);
        v2 = swap16_add(v2, v2); v2 += dppf<0x128>(v2);
        if ((lane & 24) == 0) lds_st<float>(sm + L_RED2 + (rp * 2 + (lane >> 5)) * 48 + wave * 4, v2);
        TB(8);
        BAR();
        TB(9);
#pragma unroll
        for (int c = 0; c < NC; c++) {
          lds_st<u32>(im + 8 * IMS + (c / 3) * GS * 12 + (c % 3) * 4, perm_hi(__float_as_uint(ha[c]), __float_as_uint(hb[c])));
          lds_st<u32>(im + (c / 3) * GS * 12 + (c % 3) * 4, perm_hi(__float_as_uint(xa[c]), __float_as_uint(xb[c])));
        }
      } else {
        if (wave == 15) __builtin_amdgcn_s_setprio(3);
        if (wave >= 12 || wave == 10) {
          prm_finish(wave == 10 ? 256 + (lane & 31) : tp);
          CBAR();
          if (lane == 0) __hip_atomic_fetch_add((LAS u32*)(sm + L_SP + 512 + 8), 1u, __ATOMIC_RELAXED, __HIP_MEMORY_SCOPE_WORKGROUP);
        } else vm0();
        if (LW > 8 && tp < 32) lds_st<u32x4>(sm + L_RED + 1536 + tp * 16, zero4);
        TB(5);
        BAR();
        BAR();
        if (wave == 15 && lane < 16) {
          f32x4 t0 = lds_ld<f32x4>(sm + L_RED2 + lane * 48) + lds_ld<f32x4>(sm + L_RED2 + lane * 48 + 16);
          if (LW > 8) t0 += lds_ld<f32x4>(sm + L_RED2 + lane * 48 + 32);
          ST64((u32x2{make_key((t0[0] + t0[1]) + (t0[2] + t0[3]), rb * 16 + lane), tag}), WS_KEY + rb * 128 + lane * 8);
        }
      }
      TS(10); TB(10);
      BAR();
      if (HSL2) __builtin_amdgcn_s_sleep(HSL2);
      if (wave < 9) {
        const int rp = (tid * 911) >> 16, cb = (tid - rp * 72) * 8, oo = (rb * 16 + rp * 2) * 1152 + cb * 2;
        lchar* ip = sm + L_IMX + rp * IMS + cb * 4;
        const u32x4 h0 = lds_ld<u32x4>(ip + 8 * IMS), h1 = lds_ld<u32x4>(ip + 8 * IMS + 16), x0 = lds_ld<u32x4>(ip), x1 = lds_ld<u32x4>(ip + 16);
        const f32x4 gA = lds_ld<f32x4>(sm + L_GB + cb * 4), gB = lds_ld<f32x4>(sm + L_GB + cb * 4 + 16);
        ST128((u32x4{nosent(perm_lo(h0[0], h0[1])), nosent(perm_lo(h0[2], h0[3])), nosent(perm_lo(h1[0], h1[1])), nosent(perm_lo(h1[2], h1[3]))}), WS_H2 + oo);
        ST128((u32x4{nosent(perm_hi(h0[0], h0[1])), nosent(perm_hi(h0[2], h0[3])), nosent(perm_hi(h1[0], h1[1])), nosent(perm_hi(h1[2], h1[3]))}), WS_H2 + oo + 1152);
        SB();
        if (OSLP) __builtin_amdgcn_s_sleep(OSLP);
        auto lo = [](u32 xx, u32 hh, float gg) { return bflo(xx) + rbf(gg * bflo(hh)); };
        auto hi = [](u32 xx, u32 hh, float gg) { return bfhi(xx) + rbf(gg * bfhi(hh)); };
        const auto ro = __builtin_amdgcn_make_buffer_rsrc(out, 0, 2048 * 1152, 0x00020000);
        __builtin_amdgcn_raw_buffer_store_b128(u32x4{pkbf(lo(x0[0], h0[0], gA[0]), lo(x0[1], h0[1], gA[1])), pkbf(lo(x0[2], h0[2], gA[2]), lo(x0[3], h0[3], gA[3])),
                                                     pkbf(lo(x1[0], h1[0], gB[0]), lo(x1[1], h1[1], gB[1])), pkbf(lo(x1[2], h1[2], gB[2]), lo(x1[3], h1[3], gB[3]))}, ro, oo, 0, OAUX);
        __builtin_amdgcn_raw_buffer_store_b128(u32x4{pkbf(hi(x0[0], h0[0], gA[0]), hi(x0[1], h0[1], gA[1])), pkbf(hi(x0[2], h0[2], gA[2]), hi(x0[3], h0[3], gA[3])),
                                                     pkbf(hi(x1[0], h1[0], gB[0]), hi(x1[1], h1[1], gB[1])), pkbf(hi(x1[2], h1[2], gB[2]), hi(x1[3], h1[3], gB[3]))}, ro, oo + 1152, 0, OAUX);
      }
      vm0();
      BAR();
      if (tid == 1023) ST32(tag, WS_ODONE + rb * 4);
      TS(11);
    }
    if (!isL && blk < 184) {
      constexpr int PB = (5184 - 8 * PA + 6) / 7;
      if (PDLY & 1) vm0();
      if (PDLY & 2) BAR();
      if (PSL2) __builtin_amdgcn_s_sleep(PSL2);
      if (tid < PB) w_pref(8 * PA + ((blk - 128) >> 3) * PB + tid);
    }
    vm0();
  } else {
    auto m_publish = [&](int p) __attribute__((always_inline)) {
      float s = 0.f;
#pragma unroll
      for (int w = 0; w < 9; w++) s += lds_ld<float>(sm + L_MP + p * 2304 + (w * 64 + lane) * 4);
      ST64((u32x2{__float_as_uint(s), tag}), WS_MODG + mth * 27648 + ((mu + 27 * p) * 64 + lane) * 8);
    };
    if (wave < 9 && hasM) {
      if (MPRIO) __builtin_amdgcn_s_setprio(3);
      TA(2);
      u32x2 cv; u32x4 wv[2][4];
      asm volatile("s_waitcnt vmcnt(4)" : "={v[124:125]}"(cv) :: "v108", "v109", "v110", "v111", "v112", "v113", "v114", "v115", "v116", "v117", "v118", "v119", "v120", "v121", "v122", "v123");
      TA(3);
      float sv[4];
      {
        const float c = __uint_as_float(__builtin_amdgcn_perm(cv[1], cv[0], 0x01000c0cu + 0x02020000u * (lane & 3))), sl = rbf(c * frcp(1.f + fexp(-c)));
        sv[0] = dppf<0x00>(sl); sv[1] = dppf<0x55>(sl); sv[2] = dppf<0xAA>(sl); sv[3] = dppf<0xFF>(sl);
      }
      SB();
      asm volatile("s_waitcnt vmcnt(0) ; PRE_WAIT" : "={v[108:111]}"(wv[0][0]), "={v[112:115]}"(wv[0][1]), "={v[116:119]}"(wv[0][2]), "={v[120:123]}"(wv[0][3]));
      TA(4);
#pragma unroll
      for (int p = 0; p < 2; p++) {
        float a[8] = {};
#pragma unroll
        for (int i = 0; i < 4; i++)
#pragma unroll
          for (int j = 0; j < 4; j++) { a[2 * j] = __builtin_fmaf(sv[i], bflo(wv[p][i][j]), a[2 * j]); a[2 * j + 1] = __builtin_fmaf(sv[i], bfhi(wv[p][i][j]), a[2 * j + 1]); }
        float b[4], c2[2];
#pragma unroll
        for (int k = 0; k < 4; k++) b[k] = swap32_add(a[k], a[k + 4]);
#pragma unroll
        for (int k = 0; k < 2; k++) { c2[k] = swap16_add(b[k], b[k + 2]); c2[k] += dppf<0x128>(c2[k]); }
        if ((lane & 8) == 0) lds_st<f32x2>(sm + L_MP + p * 2304 + (wave * 64 + (lane & 7) * 8 + 2 * (g4 & 1) + 4 * (g4 >> 1)) * 4, f32x2{c2[0], c2[1]});
        CBAR();
        if (lane == 0) __hip_atomic_fetch_add((LAS u32*)(sm + L_SP + 512 + 8), 1u << (8 * p), __ATOMIC_RELAXED, __HIP_MEMORY_SCOPE_WORKGROUP);
        if (p == 0) {
          TA(7);
          __builtin_amdgcn_s_setprio(0);
          BAR();
#pragma unroll
          for (int i = 0; i < 4; i++) wv[1][i] = LDW<u32x4>((const char*)wmod + o0 + i * 6912 + 27 * 128);
          SB();
          TS(3); TA(8);
        }
      }
      TS(4); TA(10);
    } else {
      if (MPRIO) __builtin_amdgcn_s_setprio(MPRIO - 1);
      if (wave >= 12) ln1_rows(false);
      asm volatile("; PRE_WAIT (no loads of its own)");
      auto m_wait = [&](u32 n) __attribute__((always_inline)) {
#pragma unroll 1
        for (int it = 0; it < (SPIN_CAP << 6); it++) {
          CBAR();
          if (((__builtin_amdgcn_readfirstlane(lds_ld<u32>(sm + L_SP + 512 + 8)) >> n) & 255u) == 9u) break;
          __builtin_amdgcn_s_sleep(1);
        }
        CBAR();
      };
      if (wave == 15) __builtin_amdgcn_s_setprio(3);
      if (wave == (PUB11 ? 11 : 15) && hasM) {
        if (PUB11) { __builtin_amdgcn_s_setprio(3); tag = __builtin_amdgcn_readfirstlane(LD32(WS_EPOCH)) + 1u; }
        m_wait(0); m_publish(0);
        if (PUB11) __builtin_amdgcn_s_setprio(0);
      }
      TS(3); TA(8);
      BAR();
      if (wave == 15 && hasM) { m_wait(8); m_publish(1); TS(4); TA(10); }
    }
  }
  if (blk >= 184) {
    const int ub = unit(isC ? 8 : (blk - 192) >> 3);
    u32x4 W[16];
    auto w_gate = [&]() __attribute__((always_inline)) {
#pragma unroll 1
      for (int it = 0; it < (SPIN_CAP << 4); it++) {
        CBAR();
        if ((__builtin_amdgcn_readfirstlane(lds_ld<u32>(sm + L_SP + 512 + 8)) >> 24) >= GCNT) break;
        __builtin_amdgcn_s_sleep(4);
      }
      CBAR();
    };
    if (wave < 9) {
      const u16* pw = ((lane & 8) ? wu : wg) + (size_t)(wave * 64 + g4 * 8) * F_ + ub * 64 + (lane & 7) * 8;
      u32x4 R[16];
#pragma unroll
      for (int j = 0; j < 16; j++) { R[j] = LDW<u32x4>(pw + (size_t)((j >> 3) * 32 + (j & 7)) * F_); CBAR(); }
      VMW(GTHR);
      if (lane == 0) __hip_atomic_fetch_add((LAS u32*)(sm + L_SP + 512 + 8), 1u << 24, __ATOMIC_RELAXED, __HIP_MEMORY_SCOPE_WORKGROUP);
      SB();
#pragma unroll
      for (int s = 0; s < 2; s++)
#pragma unroll
        for (int h = 0; h < 4; h++)
#pragma unroll
          for (int j = 0; j < 4; j++) { W[s * 8 + 2 * h][j] = perm_lo(R[s * 8 + 2 * j][h], R[s * 8 + 2 * j + 1][h]); W[s * 8 + 2 * h + 1][j] = perm_hi(R[s * 8 + 2 * j][h], R[s * 8 + 2 * j + 1][h]); }
    } else if (wave < 15) {
      const u16* pd = wd + (size_t)(ub * 64 + g4 * 16) * D_ + (wave - 9) * 96 + 6 * i16;
      u32x3 R[16];
      if (UNG ? hasM : !isC) w_gate();
#pragma unroll
      for (int j = 0; j < 16; j++) { R[j] = (WNT & 2) ? ldnt<u32x3>(pd + j * D_) : *(const u32x3*)(pd + j * D_); CBAR(); }
#pragma unroll
      for (int h = 0; h < 3; h++)
#pragma unroll
        for (int j = 0; j < 8; j++) { W[(j >> 2) * 6 + 2 * h][j & 3] = perm_lo(R[2 * j][h], R[2 * j + 1][h]); W[(j >> 2) * 6 + 2 * h + 1][j & 3] = perm_hi(R[2 * j][h], R[2 * j + 1][h]); }
    } else {
      lds_st<u32>(sm + L_LIST + lane * 4, 0u);
      lds_st<u32>(sm + L_WM + lane * 4, 0u);
      if (lane == 0) lds_st<u32>(sm + L_SEL + 32, 0u);
#pragma unroll 1
      for (int it = 0; it < SPIN_CAP; it++) {
        if (LD32(WS_KEY + (KHI ? 127 - ub % KHI : ub) * 128 + 4) == tag) break;
        __builtin_amdgcn_s_sleep(KSLP);
        CBAR();
      }
      if (KDLY) __builtin_amdgcn_s_sleep(KDLY);
    }
    TS(7);
    BAR();
    tag = __builtin_amdgcn_readfirstlane(lds_ld<u32>(sm + L_MISC));
    TS(11); TB(1);
    constexpr int KN_ = 16 / KW;
    u32x4 kv[KN_];
    if (wave < KW) {
#pragma unroll
      for (int k = 0; k < KN_; k++) kv[k] = LD128(WS_KEY + (k * KW * 64 + tid) * 16);
#pragma unroll 1
      for (int it = 0; it < SPIN_CAP; it++) {
        bool b_ = false;
#pragma unroll
        for (int k = 0; k < KN_; k++) b_ |= kv[k][1] != tag || kv[k][3] != tag;
        const bool bad = quad_any(b_);
        if (!__any(bad)) break;
        CBAR();
        if (bad) {
#pragma unroll
          for (int k = 0; k < KN_; k++) kv[k] = LD128(WS_KEY + (k * KW * 64 + tid) * 16);
        }
      }
      TS(12); TB(2);
      u32 v = 0;
#pragma unroll
      for (int k = 0; k < KN_; k++) v = max(v, max(kv[k][0], kv[k][2]));
      v = max(v, dppu<0x128>(v));
      const auto a = __builtin_amdgcn_permlane16_swap(v, v, false, false); v = max(a[0], a[1]);
      if ((lane & 24) == 0) __hip_atomic_fetch_max((LAS u32*)(sm + L_WM + (lane & 7) * 4), v, __ATOMIC_RELAXED, __HIP_MEMORY_SCOPE_WORKGROUP);
    }
    BAR();
    TB(3);
    if (wave < KW) {
      u32 th = lds_ld<u32>(sm + L_WM + (lane & 7) * 4);
      th = min(th, dppu<0xB1>(th)); th = min(th, dppu<0x4E>(th)); th = min(th, dppu<0x141>(th));
      u32 cnt = 0;
#pragma unroll
      for (int k = 0; k < 2 * KN_; k++) cnt += kv[k >> 1][(k & 1) * 2] >= th;
      if (cnt) {
        u32 pos;
        asm volatile("ds_add_rtn_u32 %0, %1, %2\n\ts_waitcnt lgkmcnt(0)" : "=v"(pos) : "v"((u32)(uintptr_t)(sm + L_SEL + 32)), "v"(cnt) : "memory");
#pragma unroll
        for (int k = 0; k < 2 * KN_; k++) if (kv[k >> 1][(k & 1) * 2] >= th) { lds_st<u32>(sm + L_LIST + pos * 4, kv[k >> 1][(k & 1) * 2]); pos++; }
      }
    }
    BAR();
    TB(4);
    {
      const u32 nv = lds_ld<u32>(sm + L_SEL + 32), mine = lds_ld<u32>(sm + L_LIST + (tid >> 4) * 4);
      const u32x4 o = lds_ld<u32x4>(sm + L_LIST + i16 * 16);
      SB();
      const int n = __builtin_amdgcn_readfirstlane(nv);
      if (n <= 64) {
        if (wave * 4 < n) {
          u32 rank = (o[0] > mine) + (o[1] > mine) + (o[2] > mine) + (o[3] > mine);
          rank += dppu<0xB1>(rank); rank += dppu<0x4E>(rank); rank += dppu<0x141>(rank); rank += dppu<0x140>(rank);
          if (i16 == 0 && rank < 8) lds_st<u32>(sm + L_SEL + rank * 4, mine);
        }
      } else {
#pragma unroll 1
        for (int i = tid; i < n; i += 1024) {
          const u32 me = lds_ld<u32>(sm + L_LIST + i * 4);
          u32 rank = 0;
#pragma unroll 1
          for (int j = 0; j < n; j++) rank += lds_ld<u32>(sm + L_LIST + j * 4) > me;
          if (rank < 8) lds_st<u32>(sm + L_SEL + rank * 4, me);
        }
      }
    }
    BAR();
    TS(13); TB(5);
    const int dcol = (eb & 7) * 72 + min(lane, 35) * 2, d3 = (dcol * 683) >> 11, dr = dcol - d3 * 3;
    u32 key = 0, hw, od, xr; int lrow = 0; u32x4 ta, tb, gw, gx;
    auto fin_load = [&]() __attribute__((always_inline)) {
      ta = LD128(WS_X1 + d3 * X1S + (lrow >> 1) * 16); tb = LD128(WS_X1 + (d3 + (dr >> 1)) * X1S + (lrow >> 1) * 16);
      hw = LD32(WS_H2 + lrow * 1152 + dcol * 2); od = LD32(WS_ODONE + (lrow >> 4) * 4);
      gw = LD128(WS_MODG + (5 * 576 + dcol) * 8); gx = LD128(WS_MODG + 27648 + (5 * 576 + dcol) * 8);
    };
    if (wave < 9) {
      const int l = 2047 - (int)(lds_ld<u32>(sm + L_SEL + (lane & 7) * 4) & 2047u);
      u32x4 a;
#pragma unroll 1
      for (int it = 0; it < SPIN_CAP; it++) {
        a = LD128(WS_H2 + l * 1152 + (wave * 64 + (i16 >> 3) * 32 + g4 * 8) * 2);
        if (!__any(has_sent(a))) break;
        CBAR();
      }
      TB(6);
      u32x4 a0, a1;
#pragma unroll
      for (int j = 0; j < 4; j++) { a0[j] = __builtin_amdgcn_update_dpp(a[j], a[j], 0x128, 0xf, 0xc, false); a1[j] = __builtin_amdgcn_update_dpp(a[j], a[j], 0x128, 0xf, 0x3, false); }
      const bf16x8 A0 = __builtin_bit_cast(bf16x8, a0), A1 = __builtin_bit_cast(bf16x8, a1);
      f32x4 z[8];
#pragma unroll
      for (int e = 0; e < 8; e++) z[e] = __builtin_amdgcn_mfma_f32_16x16x32_bf16(A0, __builtin_bit_cast(bf16x8, W[e]), f32x4{0.f, 0.f, 0.f, 0.f}, 0, 0, 0);
#pragma unroll
      for (int e = 0; e < 8; e++) z[e] = __builtin_amdgcn_mfma_f32_16x16x32_bf16(A1, __builtin_bit_cast(bf16x8, W[8 + e]), z[e], 0, 0, 0);
      u32 um = g4 >= 2 ? ~0u : 0u; VKEEP(um);
#pragma unroll
      for (int e = 0; e < 4; e++)
        lds_st<f32x4>(sm + L_GP + wave * 4096 + (2 * e + (g4 >> 1)) * 512 + i16 * 32 + (g4 & 1) * 16,
                      f32x4{self(um, z[2 * e + 1][0], z[2 * e][0]), self(um, z[2 * e + 1][1], z[2 * e][1]), self(um, z[2 * e + 1][2], z[2 * e][2]), self(um, z[2 * e + 1][3], z[2 * e][3])});
    } else if (wave == 15 && !isC) {
      key = lds_ld<u32>(sm + L_SEL + (eb >> 3) * 4);
      lrow = 2047 - (int)(key & 2047u);
      xr = *(const u32*)(x + lrow * D_ + dcol);
      fin_load();
    }
    TB(7);
    BAR();
    TB(8);
    if (wave < 8) {
      float gs = 0.f, us = 0.f;
#pragma unroll
      for (int w = 0; w < 9; w++) { gs += lds_ld<float>(sm + L_GP + w * 4096 + wave * 512 + lane * 4); us += lds_ld<float>(sm + L_GP + w * 4096 + wave * 512 + 256 + lane * 4); }
      gs = rbf(gs); us = rbf(us);
      const float sg = rbf(gs * frcp(1.f + fexp(-gs)));
      lds_st<u16>(sm + L_AS + (lane & 7) * 144 + ((lane & 56) + wave) * 2, (u16)(pkbf(sg * us, 0.f)));
    }
    BAR();
    TB(9);
    if (wave >= 9 && wave < 15) {
      lchar* ap = sm + L_AS + (i16 & 7) * 144 + g4 * 32;
      const bf16x8 A0 = lds_ld<bf16x8>(ap), A1 = lds_ld<bf16x8>(ap + 16);
      f32x4 y[6];
#pragma unroll
      for (int e = 0; e < 6; e++) y[e] = __builtin_amdgcn_mfma_f32_16x16x32_bf16(A0, __builtin_bit_cast(bf16x8, W[e]), f32x4{0.f, 0.f, 0.f, 0.f}, 0, 0, 0);
#pragma unroll
      for (int e = 0; e < 6; e++) y[e] = __builtin_amdgcn_mfma_f32_16x16x32_bf16(A1, __builtin_bit_cast(bf16x8, W[6 + e]), y[e], 0, 0, 0);
      const int q = (wave - 9) * 16 + i16, cg = (q * 43) >> 9;
      u32 um = g4 >= 2 ? ~0u : 0u; VKEEP(um);
      const int off = WS_PART + (((g4 & 1) * 32 + cg) * 72 + ub) * 384 + (q - cg * 12) * 32 + (um & 16);
#pragma unroll
      for (int r = 0; r < 4; r++)
        ST128((u32x4{__float_as_uint(self(um, y[3][r], y[0][r])), __float_as_uint(self(um, y[4][r], y[1][r])), __float_as_uint(self(um, y[5][r], y[2][r])), tag}), off + r * 8 * 72 * 384);
      TB(10);
      if (!FSLP) vm0();
    }
    TS(14);
    if (FSLP) __builtin_amdgcn_s_sleep(FSLP); else
    BAR();
    TB(11);
    float xa = 0.f, xb = 0.f, ga_ = 0.f, gb_ = 0.f, rsel = 0.f;
    if (!isC) {
      if (wave < 7) {
        const int po = WS_PART + eb * 27648 + min(tid, 431) * 16;
        u32x4 pv[4];
#pragma unroll
        for (int k = 0; k < 4; k++) pv[k] = LD128(po + k * 6912);
#pragma unroll 1
        for (int it = 0; it < SPIN_CAP; it++) {
          const bool bad = quad_any(pv[0][3] != tag || pv[1][3] != tag || pv[2][3] != tag || pv[3][3] != tag);
          if (!__any(bad)) break;
          CBAR();
          if (bad) {
#pragma unroll
            for (int k = 0; k < 4; k++) pv[k] = LD128(po + k * 6912);
          }
        }
#pragma unroll
        for (int c = 0; c < 3; c++) lds_st<float>(sm + L_FIN + min(tid, 431) * 12 + c * 4, (__uint_as_float(pv[0][c]) + __uint_as_float(pv[1][c])) + (__uint_as_float(pv[2][c]) + __uint_as_float(pv[3][c])));
      } else if (wave == 15) {
#pragma unroll 1
        for (int it = 0; it < SPIN_CAP; it++) {
          if (__all(ta[3] == tag && tb[3] == tag && hw != SENT && od == tag && gw[1] == tag && gw[3] == tag && gx[1] == tag && gx[3] == tag)) break;
          CBAR();
          fin_load();
        }
        const u32 t0 = dr == 0 ? ta[0] : (dr == 1 ? ta[1] : ta[2]), t1 = dr == 0 ? ta[1] : (dr == 1 ? ta[2] : tb[0]);
        xa = rbf(bflo(xr) + ((lrow & 1) ? bfhi(t0) : bflo(t0))); xb = rbf(bfhi(xr) + ((lrow & 1) ? bfhi(t1) : bflo(t1)));
        rsel = rbf(frcp(1.f + fexp(-key_score(key))));
        ga_ = rbf(__uint_as_float(gw[0]) + __uint_as_float(gx[0])); gb_ = rbf(__uint_as_float(gw[2]) + __uint_as_float(gx[2]));
      }
      TB(12);
      BAR();
      TB(13);
      if (wave == 15 && lane < 36) {
        f32x2 y = {0.f, 0.f};
#pragma unroll
        for (int k = 0; k < 18; k++) y += lds_ld<f32x2>(sm + L_FIN + k * 288 + lane * 8);
        *(u32*)(out + lrow * D_ + dcol) = pkbf(xa + rbf(ga_ * rbf(bflo(hw) + rbf(rbf(y[0]) * rsel))), xb + rbf(gb_ * rbf(bfhi(hw) + rbf(rbf(y[1]) * rsel))));
      }
    }
    if (blk == 192 && tid == 0) ST32(tag, WS_EPOCH);
    TS(15); TB(14);
  }
  if (blk < 128 && wave == 0) { const u64 goal = pt0 + 4000 + 10 * (pt1 - pt0); for (int it = 0; it < 200000; it++) { if (__builtin_amdgcn_s_memrealtime() >= goal) break; __builtin_amdgcn_s_sleep(1); } }
}

extern "C" void kernel_launch(void* const* d_in, const int* in_sizes, int n_in, void* d_out, int out_size, void* d_ws, size_t ws_size, hipStream_t stream) {
  (void)hipFuncSetAttribute((const void*)mega, hipFuncAttributeMaxDynamicSharedMemorySize, LDS_BYTES);
  mega<<<256, 1024, LDS_BYTES, stream>>>((const u16*)d_in[0], (const u16*)d_in[1], (const u16*)d_in[2], (const u16*)d_in[3], (const u16*)d_in[4],
                                         (const u16*)d_in[5], (const u16*)d_in[6], (const u16*)d_in[8], (const u16*)d_in[9], (const u16*)d_in[10],
                                         (const u16*)d_in[11], (const u16*)d_in[12], (const u16*)d_in[13], (u16*)d_out, (char*)d_ws + (WSEND ? ((ws_size - (16u << 20)) & ~(size_t)4095) : 0));
}
```

```cpp
#include <hip/hip_runtime.h>
#include <stdint.h>

typedef unsigned short u16;
typedef unsigned int u32;
typedef unsigned long long u64;

#define WNT " nt"
#define DNTF ""
#define PRN 3
#define PRF 2
#define PRG 2
#define PRH 3
#define PRE_ 1
#define GPRIO 1
#define XDLY 0
#define X1T 4
#define X1SLP 6
#define PSLP 8
#define KSLP 4
#define FSLP 16
#define OFLAG " sc1"
#define WARM 0
#define PAD(k) do { if (WARM) asm volatile("Lpad" #k ":\n\ts_cbranch_execz Lwret"); } while (0)
#define PADS(k) ""
#define STR_(x) #x
#define STR(x) STR_(x)
#define PB(k) "s_branch Lpad" #k "\n\t"
#define PN "s_branch Lwret\n\t"
#define NPADC 24
#define PADTAB PB(0) PB(1) PB(2) PB(3) PB(4) PB(5) PB(6) PB(7) PB(8) PB(9) PB(10) PB(11) PB(12) PB(13) PB(14) PB(15) \
               PB(16) PB(17) PB(18) PB(19) PB(20) PB(21) PB(22) PB(23) PN PN PN PN PN PN PN PN \
               PB(32) PB(33) PB(34) PB(35) PB(36) PB(37) PB(38) PB(39) PB(40) PB(41) PB(42) PB(43) PB(44) PB(45) PB(46) PB(47)
#define CODE_LINES 218
#define OLATE 1
#define MSLP 1
#define GSLP 10
#define XT 3800
#define JDLY 30
#define DT 9600
#define PFG 0x10
#define PFT ""
#define PFD 1
#define P1D 8
#define GUW 8
#define GAE 2
#define KBAR 1
#define NOC 0
#define GED 6
#define XD2 0
#define WD2 0
#define WDLY 80
#define PFM 0
#define PFW 3

constexpr int LDS_BYTES = 163840;

#define TS(i) do {} while (0)
#define CNT(i) do {} while (0)

#define STA(i, r) ""
#define STAMP_IO
#define LSTAMP_IO
#define GSTAMP1 STA(1, "s[60:61]")
#define SSTAMP1 GSTAMP1
#define GSTAMP2 STA(2, "s[62:63]")
#define SSTAMP2 GSTAMP2
#define GSTAMP3 STA(3, "s[64:65]")
#define SSTAMP3 GSTAMP3
#define GSTAMP4 STA(4, "s[66:67]")
#define SSTAMP4 GSTAMP4
#define GSTAMP5 STA(5, "s[68:69]")
#define SSTAMP5 GSTAMP5
#define GSTAMP6 STA(6, "s[70:71]")
#define SSTAMP6 GSTAMP6
#define GSTAMP7 STA(7, "s[72:73]")
#define SSTAMP7 GSTAMP7
#define GSTAMP8 STA(8, "s[74:75]")
#define SSTAMP8 GSTAMP8
#define GSTAMP9 STA(9, "s[76:77]")
#define SSTAMP9 GSTAMP9
#define GSTAMP10 STA(10, "s[78:79]")
#define SSTAMP10 GSTAMP10
#define GSTAMP11 STA(11, "s[80:81]")
#define SSTAMP11 GSTAMP11
#define STC(r) ""
#define SSTAMPCm STC("s60")
#define SSTAMPCu0 STC("s62")
#define SSTAMPCu1 STC("s62")
#define SSTAMPCu2 STC("s66")
#define SSTAMPCu3 STC("s66")
#define SSTAMPCa STC("s64")
#define LSTAMPC STC("s38")
#define LSTAMP8 GSTAMP8
#define LSTAMP9 GSTAMP9
#define LSTAMP10 GSTAMP10
#define LSTAMP11 GSTAMP11
#define LSTAMP13 STA(13, "s[36:37]")
#define LSTAMP14 STA(14, "s[38:39]")
#define GSTAMP12 SSTAMPE1
#define GSTAMP13 LSTAMP13
#define GSTAMPC LSTAMPC
#define SSTAMPE0 GSTAMP6
#define SSTAMPE1 STA(12, "s[82:83]")
#define SSTAMPE2 ""
#define GW_HEAD \
  "s_load_dwordx8 s[40:47], %[k], 0x8\n" \
  "s_load_dwordx2 s[48:49], %[k], 0x28\n" \
  "s_setprio 1\n" \
  "s_lshr_b32 s50, %[w], 2\n" \
  "s_and_b32 s54, %[b], 1\n" \
  "s_add_u32 s54, s54, s50\n" \
  "s_cmp_eq_u32 s54, 3\n" \
  "s_cselect_b32 s54, 0, s54\n" \
  "s_mul_i32 s53, %[b], 3\n" \
  "s_add_u32 s53, s53, s54\n" \
  "s_add_u32 s52, %[w], s50\n" \
  "s_lshl_b32 s55, s52, 7\n" \
  "s_and_b32 s55, s55, 128\n" \
  "s_lshl_b32 s54, s53, 8\n" \
  "s_add_u32 s54, s54, s55\n" \
  "v_and_b32 v9, 63, %[t]\n" \
  "v_lshl_add_u32 v5, v9, 1, s54\n" \
  "v_mov_b64 v[108:109], 0\n" \
  "v_mov_b64 v[110:111], 0\n" \
  "s_mul_i32 s57, s50, 32096\n" \
  "s_lshl1_add_u32 m0, s55, s57\n" \
  "s_lshl_b32 s56, s53, 1\n" \
  "s_and_b32 s56, s56, -4\n" \
  "s_bitcmp1_b32 s52, 1\n" \
  GSTAMP1 \
  "s_waitcnt lgkmcnt(0)\n" \
  "global_load_short_d16_hi v108, v5, s[40:41]\n" \
  "global_load_short_d16_hi v109, v5, s[42:43]\n" \
  "s_load_dword s98, s[48:49], s56\n" \
  "s_cbranch_scc1 Lnc%=\n" \
  "global_load_short_d16_hi v110, v5, s[44:45]\n" \
  "global_load_short_d16_hi v111, v5, s[46:47]\n" \
  "Lnc%=:\n" \
  "s_barrier\n"

#define GW_BODY \
  "s_and_b32 s51, %[w], 3\n" \
  "s_lshl_b32 s58, s50, 13\n" \
  "s_add_u32 s58, s58, 96288\n" \
  "v_and_b32 v1, 15, %[t]\n" \
  "v_and_b32 v2, 48, %[t]\n" \
  "v_mov_b64 v[12:13], 0\n" \
  "v_mov_b64 v[14:15], 0\n" \
  "v_and_b32 v5, 255, %[t]\n" \
  "v_lshl_add_u32 v5, v5, 4, s58\n" \
  "ds_write_b128 v5, v[12:15] offset:4096\n" \
  "v_min_u32 v4, 1, v9\n" \
  "v_mul_u32_u24 v4, 40, v4\n" \
  "v_add_u32 v4, v4, v9\n" \
  "v_mov_b32 v5, 159488\n" \
  "v_lshl_add_u32 v4, v4, 2, v5\n" \
  "s_movk_i32 s54, 544\n" \
  "v_mad_u32_u24 v5, v1, s54, v2\n" \
  "s_and_b32 s54, s51, 1\n" \
  "s_mul_i32 s55, s54, 8704\n" \
  "s_add_u32 s55, s55, 14688\n" \
  "s_cmp_lt_u32 s51, 2\n" \
  "s_cselect_b32 s56, 0, s55\n" \
  "s_cselect_b32 s55, s55, 8704\n" \
  "s_cselect_b32 s40, 16, 64\n" \
  "s_cselect_b32 s41, 512, 16\n" \
  "s_cselect_b32 s42, 8, 5\n" \
  "s_cselect_b32 s43, -2912, 0\n" \
  "v_add_u32 v5, s57, v5\n" \
  "v_add_u32 v6, s55, v5\n" \
  "v_add_u32 v5, s56, v5\n" \
  "s_lshl_b32 s54, s54, s42\n" \
  "s_add_u32 s54, s54, s43\n" \
  "s_add_u32 s54, s54, s58\n" \
  "v_lshrrev_b32 v7, 1, v2\n" \
  "v_and_b32 v7, 8, v7\n" \
  "v_mad_u32_u24 v7, v1, s40, v7\n" \
  "v_lshrrev_b32 v9, 5, v9\n" \
  "v_mad_u32_u24 v7, v9, s41, v7\n" \
  "v_add_u32 v7, s54, v7\n" \
  "s_lshl_b32 s59, s50, 3\n" \
  "s_lshl_b32 s59, 1, s59\n" \
  "v_mov_b32 v16, s59\n" \
  "s_not_b32 s53, s53\n" \
  "s_lshl_b32 s53, s53, 4\n" \
  ".if " STR(PFM) "\n" \
  "s_cmp_lg_u32 s50, 2\n" \
  "s_cbranch_scc1 Lf9%=\n" \
  "s_cmp_eq_u32 s51, 3\n" \
  "s_cbranch_scc1 Lf9%=\n" \
  "s_load_dwordx4 s[44:47], %[k], 0x40\n" \
  "s_load_dwordx2 s[48:49], %[k], 0x60\n" \
  "s_and_b32 s40, %[b], 7\n" \
  "s_lshr_b32 s41, %[b], 3\n" \
  "v_and_b32 v20, 63, %[t]\n" \
  "s_cmp_eq_u32 s51, 2\n" \
  "s_cbranch_scc1 Lf8%=\n" \
  "v_lshrrev_b32 v21, 3, v20\n" \
  "v_lshl_add_u32 v21, v21, 3, s40\n" \
  "v_min_u32 v21, 53, v21\n" \
  "v_mul_u32_u24 v22, 43, v21\n" \
  "v_lshrrev_b32 v22, 7, v22\n" \
  "v_mul_u32_u24 v23, 3, v22\n" \
  "v_sub_u32 v21, v21, v23\n" \
  "v_mul_u32_u24 v21, 192, v21\n" \
  "v_and_b32 v20, 7, v20\n" \
  "s_lshl_b32 s41, s41, 3\n" \
  "v_add3_u32 v21, v21, v20, s41\n" \
  "v_mul_u32_u24 v21, 6912, v21\n" \
  "v_lshl_add_u32 v21, v22, 7, v21\n" \
  "v_cmp_le_u32 vcc, 9, v22\n" \
  "s_mul_i32 s41, s51, 1152\n" \
  "s_add_u32 s41, s41, 2304\n" \
  "v_cndmask_b32 v22, 0, 9, vcc\n" \
  "v_lshl_add_u32 v21, v22, 7, v21\n" \
  "v_add_u32 v21, s41, v21\n" \
  "s_waitcnt vmcnt(0) lgkmcnt(0)\n" \
  ".if " STR(PFM) " & 1\n" \
  "global_load_dword v127, v21, s[48:49]\n" \
  ".endif\n" \
  "s_branch Lf9%=\n" \
  "Lf8%=:\n" \
  "s_mul_i32 s41, s41, 24\n" \
  "v_min_u32 v20, 23, v20\n" \
  "v_add_u32 v20, s41, v20\n" \
  "v_mul_u32_u24 v20, 9216, v20\n" \
  "s_waitcnt vmcnt(0) lgkmcnt(0)\n" \
  "s_lshr_b32 s41, s44, 7\n" \
  "s_add_u32 s41, s41, s40\n" \
  "s_sub_u32 s41, 3, s41\n" \
  "s_and_b32 s41, s41, 7\n" \
  "s_mul_i32 s40, s40, 9\n" \
  "s_add_u32 s41, s41, s40\n" \
  "s_lshl_b32 s41, s41, 7\n" \
  "v_add_u32 v20, s41, v20\n" \
  ".if " STR(PFM) " & 2\n" \
  "global_load_dword v127, v20, s[44:45]\n" \
  "global_load_dword v127, v20, s[46:47]\n" \
  ".endif\n" \
  "Lf9%=:\n" \
  ".endif\n" \
  "s_cmp_eq_u32 s50, 0\n" \
  "s_cbranch_scc0 Lp1%=\n" \
  "s_setprio 2\n" \
  "Lp1%=:\n" \
  "s_bitcmp1_b32 s52, 1\n" \
  GSTAMP2 \
  "s_cbranch_scc1 Lgb%=\n" \
  ".if " STR(PFM) "\n" \
  "s_sub_u32 s40, %[w], 8\n" \
  "s_cmp_lt_u32 s40, 3\n" \
  "s_cbranch_scc1 Lw141%=\n" \
  ".endif\n" \
  "s_waitcnt vmcnt(0) lgkmcnt(0)\n" \
  "Lw141%=:\n" \
  "s_lshl_b32 s98, s98, s53\n" \
  "s_and_b32 s98, s98, 0xffff0000\n" \
  "v_mul_f32 v108, 0x3fb8aa3b, v108\n" \
  "v_mov_b32 v112, s98\n" \
  "v_mul_f32 v112, 0x3fb8aa3b, v112\n" \
  "v_exp_f32 v108, v108\n" \
  "v_exp_f32 v112, v112\n" \
  "v_max_f32 v122, |v110|, |v111|\n" \
  "v_cvt_pk_bf16_f32 v108, 0, v108\n" \
  "v_cvt_pk_bf16_f32 v112, 0, v112\n" \
  "v_max_u32_dpp v122, v122, v122 quad_perm:[1,0,3,2] row_mask:0xf bank_mask:0xf\n" \
  "v_pk_mul_f32 v[108:109], v[108:109], v[112:113] op_sel_hi:[1,0]\n" \
  "v_mul_f32 v115, 0x3e22f983, v109\n" \
  "v_max_u32_dpp v122, v122, v122 quad_perm:[2,3,0,1] row_mask:0xf bank_mask:0xf\n" \
  "v_mul_f32 v117, 0x4222f983, v109\n" \
  "v_mul_f32 v114, 0xbfb8aa3b, v108\n" \
  "v_max_u32_dpp v122, v122, v122 row_half_mirror row_mask:0xf bank_mask:0xf\n" \
  "v_mul_f32 v116, 0xc3b8aa3b, v108\n" \
  "v_fract_f32 v115, v115\n" \
  "v_max_u32_dpp v122, v122, v122 row_mirror row_mask:0xf bank_mask:0xf\n" \
  "v_fract_f32 v117, v117\n" \
  "v_exp_f32 v114, v114\n" \
  "v_max_u32_dpp v122, v122, v122 row_bcast:15 row_mask:0xa bank_mask:0xf\n" \
  "v_cos_f32 v118, v115\n" \
  "v_sin_f32 v119, v115\n" \
  "v_max_u32_dpp v122, v122, v122 row_bcast:31 row_mask:0xc bank_mask:0xf\n" \
  "v_exp_f32 v116, v116\n" \
  "v_cos_f32 v120, v117\n" \
  "v_readlane_b32 s94, v122, 63\n" \
  "v_sin_f32 v121, v117\n" \
  "v_pk_mul_f32 v[118:119], v[118:119], v[114:115] op_sel_hi:[1,0]\n" \
  "s_lshr_b32 s94, s94, 23\n" \
  "v_pk_mul_f32 v[120:121], v[120:121], v[116:117] op_sel_hi:[1,0]\n" \
  "s_max_u32 s94, s94, 26\n" \
  "s_min_u32 s94, s94, 226\n" \
  "v_mov_b32 v113, s94\n" \
  "s_sub_u32 s94, 266, s94\n" \
  "s_lshl_b32 s94, s94, 23\n" \
  "ds_write_addtid_b32 v113 offset:8704\n" \
  "ds_write_addtid_b32 v112 offset:10336\n" \
  "v_pk_mul_f32 v[110:111], v[110:111], s[94:95] op_sel_hi:[1,0]\n" \
  "v_pk_mul_f32 v[104:105], v[110:111], v[118:119] op_sel:[1,1] op_sel_hi:[1,0] neg_lo:[0,1]\n" \
  "v_pk_mul_f32 v[106:107], v[110:111], v[120:121] op_sel:[1,1] op_sel_hi:[1,0] neg_lo:[0,1]\n" \
  "v_cvt_pk_f16_f32 v112, v110, v111\n" \
  "v_pk_fma_f32 v[114:115], v[110:111], v[118:119], v[104:105] op_sel_hi:[0,1,1]\n" \
  "v_pk_fma_f32 v[116:117], v[110:111], v[120:121], v[106:107] op_sel_hi:[0,1,1]\n" \
  "ds_write_addtid_b32 v112 offset:0\n" \
  "v_pk_mul_f32 v[104:105], v[114:115], v[118:119] op_sel:[1,1] op_sel_hi:[1,0] neg_lo:[0,1]\n" \
  "v_cvt_pk_f16_f32 v112, v114, v115\n" \
  "v_cvt_pk_f16_f32 v103, v116, v117\n" \
  "v_pk_fma_f32 v[114:115], v[114:115], v[118:119], v[104:105] op_sel_hi:[0,1,1]\n" \
  "ds_write_addtid_b32 v112 offset:544\n" \
  "ds_write_addtid_b32 v103 offset:4352\n" \
  "ds_write_addtid_b32 v103 offset:4896\n" \
  "v_pk_mul_f32 v[104:105], v[114:115], v[118:119] op_sel:[1,1] op_sel_hi:[1,0] neg_lo:[0,1]\n" \
  "v_pk_mul_f32 v[106:107], v[114:115], v[120:121] op_sel:[1,1] op_sel_hi:[1,0] neg_lo:[0,1]\n" \
  "v_cvt_pk_f16_f32 v112, v114, v115\n" \
  "v_pk_fma_f32 v[122:123], v[114:115], v[118:119], v[104:105] op_sel_hi:[0,1,1]\n" \
  "v_pk_fma_f32 v[116:117], v[114:115], v[120:121], v[106:107] op_sel_hi:[0,1,1]\n" \
  "ds_write_addtid_b32 v112 offset:1088\n" \
  "v_pk_mul_f32 v[104:105], v[122:123], v[118:119] op_sel:[1,1] op_sel_hi:[1,0] neg_lo:[0,1]\n" \
  "v_cvt_pk_f16_f32 v112, v122, v123\n" \
  "v_cvt_pk_f16_f32 v103, v116, v117\n" \
  "v_pk_fma_f32 v[114:115], v[122:123], v[118:119], v[104:105] op_sel_hi:[0,1,1]\n" \
  "ds_write_addtid_b32 v112 offset:1632\n" \
  "ds_write_addtid_b32 v103 offset:5440\n" \
  "v_pk_mul_f32 v[104:105], v[114:115], v[118:119] op_sel:[1,1] op_sel_hi:[1,0] neg_lo:[0,1]\n" \
  "v_pk_mul_f32 v[106:107], v[114:115], v[120:121] op_sel:[1,1] op_sel_hi:[1,0] neg_lo:[0,1]\n" \
  "v_cvt_pk_f16_f32 v112, v114, v115\n" \
  "v_pk_fma_f32 v[122:123], v[114:115], v[118:119], v[104:105] op_sel_hi:[0,1,1]\n" \
  "v_pk_fma_f32 v[116:117], v[114:115], v[120:121], v[106:107] op_sel_hi:[0,1,1]\n" \
  "ds_write_addtid_b32 v112 offset:2176\n" \
  "v_pk_mul_f32 v[104:105], v[122:123], v[118:119] op_sel:[1,1] op_sel_hi:[1,0] neg_lo:[0,1]\n" \
  "v_pk_mul_f32 v[106:107], v[116:117], v[120:121] op_sel:[1,1] op_sel_hi:[1,0] neg_lo:[0,1]\n" \
  "v_cvt_pk_f16_f32 v112, v122, v123\n" \
  "v_cvt_pk_f16_f32 v103, v116, v117\n" \
  "v_pk_fma_f32 v[114:115], v[122:123], v[118:119], v[104:105] op_sel_hi:[0,1,1]\n" \
  "v_pk_fma_f32 v[116:117], v[116:117], v[120:121], v[106:107] op_sel_hi:[0,1,1]\n" \
  "ds_write_addtid_b32 v112 offset:2720\n" \
  "ds_write_addtid_b32 v103 offset:5984\n" \
  "ds_write_addtid_b32 v103 offset:6528\n" \
  "ds_write_addtid_b32 v103 offset:7072\n" \
  "v_pk_mul_f32 v[104:105], v[114:115], v[118:119] op_sel:[1,1] op_sel_hi:[1,0] neg_lo:[0,1]\n" \
  "v_pk_mul_f32 v[106:107], v[116:117], v[120:121] op_sel:[1,1] op_sel_hi:[1,0] neg_lo:[0,1]\n" \
  "v_cvt_pk_f16_f32 v112, v114, v115\n" \
  "v_cvt_pk_f16_f32 v103, v116, v117\n" \
  "v_pk_fma_f32 v[122:123], v[114:115], v[118:119], v[104:105] op_sel_hi:[0,1,1]\n" \
  "v_pk_fma_f32 v[116:117], v[116:117], v[120:121], v[106:107] op_sel_hi:[0,1,1]\n" \
  "ds_write_addtid_b32 v112 offset:3264\n" \
  "ds_write_addtid_b32 v103 offset:9248\n" \
  "v_pk_mul_f32 v[104:105], v[122:123], v[118:119] op_sel:[1,1] op_sel_hi:[1,0] neg_lo:[0,1]\n" \
  "v_pk_mul_f32 v[106:107], v[114:115], v[120:121] op_sel:[1,1] op_sel_hi:[1,0] neg_lo:[0,1]\n" \
  "v_cvt_pk_f16_f32 v112, v122, v123\n" \
  "v_cvt_pk_f16_f32 v103, v116, v117\n" \
  "v_pk_fma_f32 v[122:123], v[122:123], v[118:119], v[104:105] op_sel_hi:[0,1,1]\n" \
  "v_pk_fma_f32 v[116:117], v[114:115], v[120:121], v[106:107] op_sel_hi:[0,1,1]\n" \
  "ds_write_addtid_b32 v112 offset:3808\n" \
  "ds_write_addtid_b32 v103 offset:9792\n" \
  "v_pk_mul_f32 v[104:105], v[122:123], v[120:121] op_sel:[1,1] op_sel_hi:[1,0] neg_lo:[0,1]\n" \
  "v_pk_mul_f32 v[106:107], v[120:121], v[120:121] op_sel:[1,1] op_sel_hi:[1,0] neg_lo:[0,1]\n" \
  "v_cvt_pk_f16_f32 v112, v116, v117\n" \
  "v_pk_fma_f32 v[114:115], v[122:123], v[120:121], v[104:105] op_sel_hi:[0,1,1]\n" \
  "v_pk_fma_f32 v[116:117], v[120:121], v[120:121], v[106:107] op_sel_hi:[0,1,1]\n" \
  "ds_write_addtid_b32 v112 offset:7616\n" \
  "v_pk_mul_f32 v[104:105], v[122:123], v[116:117] op_sel:[1,1] op_sel_hi:[1,0] neg_lo:[0,1]\n" \
  "v_pk_mul_f32 v[106:107], v[114:115], v[116:117] op_sel:[1,1] op_sel_hi:[1,0] neg_lo:[0,1]\n" \
  "v_cvt_pk_f16_f32 v112, v114, v115\n" \
  "v_pk_fma_f32 v[122:123], v[122:123], v[116:117], v[104:105] op_sel_hi:[0,1,1]\n" \
  "v_pk_fma_f32 v[114:115], v[114:115], v[116:117], v[106:107] op_sel_hi:[0,1,1]\n" \
  "ds_write_addtid_b32 v112 offset:8160\n" \
  "v_pk_mul_f32 v[104:105], v[122:123], v[116:117] op_sel:[1,1] op_sel_hi:[1,0] neg_lo:[0,1]\n" \
  "v_pk_mul_f32 v[106:107], v[114:115], v[116:117] op_sel:[1,1] op_sel_hi:[1,0] neg_lo:[0,1]\n" \
  "v_cvt_pk_f16_f32 v112, v122, v123\n" \
  "v_cvt_pk_f16_f32 v103, v114, v115\n" \
  "v_pk_fma_f32 v[122:123], v[122:123], v[116:117], v[104:105] op_sel_hi:[0,1,1]\n" \
  "v_pk_fma_f32 v[114:115], v[114:115], v[116:117], v[106:107] op_sel_hi:[0,1,1]\n" \
  "ds_write_addtid_b32 v112 offset:10880\n" \
  "ds_write_addtid_b32 v103 offset:11424\n" \
  "v_pk_mul_f32 v[104:105], v[122:123], v[116:117] op_sel:[1,1] op_sel_hi:[1,0] neg_lo:[0,1]\n" \
  "v_pk_mul_f32 v[106:107], v[114:115], v[116:117] op_sel:[1,1] op_sel_hi:[1,0] neg_lo:[0,1]\n" \
  "v_cvt_pk_f16_f32 v112, v122, v123\n" \
  "v_cvt_pk_f16_f32 v103, v114, v115\n" \
  "v_pk_fma_f32 v[122:123], v[122:123], v[116:117], v[104:105] op_sel_hi:[0,1,1]\n" \
  "v_pk_fma_f32 v[114:115], v[114:115], v[116:117], v[106:107] op_sel_hi:[0,1,1]\n" \
  "ds_write_addtid_b32 v112 offset:12512\n" \
  "ds_write_addtid_b32 v103 offset:13056\n" \
  "v_cvt_pk_f16_f32 v112, v122, v123\n" \
  "v_cvt_pk_f16_f32 v103, v114, v115\n" \
  "ds_write_addtid_b32 v112 offset:13600\n" \
  "ds_write_addtid_b32 v103 offset:14144\n" \
  "s_branch Lgk%=\n" \
  "Lgb%=:\n" \
  ".if " STR(PFM) "\n" \
  "s_sub_u32 s40, %[w], 8\n" \
  "s_cmp_lt_u32 s40, 3\n" \
  "s_cbranch_scc1 Lw274%=\n" \
  ".endif\n" \
  "s_waitcnt vmcnt(0) lgkmcnt(0)\n" \
  "Lw274%=:\n" \
  "s_lshl_b32 s98, s98, s53\n" \
  "s_and_b32 s98, s98, 0xffff0000\n" \
  "v_mul_f32 v108, 0x3fb8aa3b, v108\n" \
  "v_mov_b32 v112, s98\n" \
  "v_mul_f32 v112, 0x3fb8aa3b, v112\n" \
  "v_exp_f32 v108, v108\n" \
  "v_exp_f32 v112, v112\n" \
  "v_mov_b32 v103, 0x3c00\n" \
  "v_cvt_pk_bf16_f32 v108, 0, v108\n" \
  "v_cvt_pk_bf16_f32 v112, 0, v112\n" \
  "v_pk_mul_f32 v[108:109], v[108:109], v[112:113] op_sel_hi:[1,0]\n" \
  "v_mul_f32 v115, 0xbfa2f983, v109\n" \
  "v_mul_f32 v117, 0xc1a2f983, v109\n" \
  "v_mul_f32 v114, 0xc138aa3b, v108\n" \
  "v_mul_f32 v116, 0xc338aa3b, v108\n" \
  "v_fract_f32 v115, v115\n" \
  "v_fract_f32 v117, v117\n" \
  "v_exp_f32 v114, v114\n" \
  "v_cos_f32 v118, v115\n" \
  "v_sin_f32 v119, v115\n" \
  "v_exp_f32 v116, v116\n" \
  "v_cos_f32 v120, v117\n" \
  "v_sin_f32 v121, v117\n" \
  "v_pk_mul_f32 v[118:119], v[118:119], v[114:115] op_sel_hi:[1,0]\n" \
  "v_pk_mul_f32 v[120:121], v[120:121], v[116:117] op_sel_hi:[1,0]\n" \
  "ds_write_addtid_b32 v103 offset:14688\n" \
  "v_pk_mul_f32 v[104:105], v[118:119], v[118:119] op_sel:[1,1] op_sel_hi:[1,0] neg_lo:[0,1]\n" \
  "v_pk_mul_f32 v[106:107], v[120:121], v[118:119] op_sel:[1,1] op_sel_hi:[1,0] neg_lo:[0,1]\n" \
  "v_cvt_pk_f16_f32 v112, v118, v119\n" \
  "v_cvt_pk_f16_f32 v103, v120, v121\n" \
  "v_pk_fma_f32 v[110:111], v[118:119], v[118:119], v[104:105] op_sel_hi:[0,1,1]\n" \
  "v_pk_fma_f32 v[122:123], v[120:121], v[118:119], v[106:107] op_sel_hi:[0,1,1]\n" \
  "ds_write_addtid_b32 v112 offset:15232\n" \
  "ds_write_addtid_b32 v103 offset:23392\n" \
  "v_pk_mul_f32 v[104:105], v[110:111], v[118:119] op_sel:[1,1] op_sel_hi:[1,0] neg_lo:[0,1]\n" \
  "v_pk_mul_f32 v[106:107], v[122:123], v[118:119] op_sel:[1,1] op_sel_hi:[1,0] neg_lo:[0,1]\n" \
  "v_cvt_pk_f16_f32 v112, v110, v111\n" \
  "v_cvt_pk_f16_f32 v103, v122, v123\n" \
  "v_pk_fma_f32 v[110:111], v[110:111], v[118:119], v[104:105] op_sel_hi:[0,1,1]\n" \
  "v_pk_fma_f32 v[122:123], v[122:123], v[118:119], v[106:107] op_sel_hi:[0,1,1]\n" \
  "ds_write_addtid_b32 v112 offset:15776\n" \
  "ds_write_addtid_b32 v103 offset:23936\n" \
  "v_pk_mul_f32 v[104:105], v[110:111], v[118:119] op_sel:[1,1] op_sel_hi:[1,0] neg_lo:[0,1]\n" \
  "v_pk_mul_f32 v[106:107], v[122:123], v[118:119] op_sel:[1,1] op_sel_hi:[1,0] neg_lo:[0,1]\n" \
  "v_cvt_pk_f16_f32 v112, v110, v111\n" \
  "v_cvt_pk_f16_f32 v103, v122, v123\n" \
  "v_pk_fma_f32 v[110:111], v[110:111], v[118:119], v[104:105] op_sel_hi:[0,1,1]\n" \
  "v_pk_fma_f32 v[122:123], v[122:123], v[118:119], v[106:107] op_sel_hi:[0,1,1]\n" \
  "ds_write_addtid_b32 v112 offset:16320\n" \
  "ds_write_addtid_b32 v103 offset:24480\n" \
  "v_pk_mul_f32 v[104:105], v[110:111], v[118:119] op_sel:[1,1] op_sel_hi:[1,0] neg_lo:[0,1]\n" \
  "v_pk_mul_f32 v[106:107], v[122:123], v[118:119] op_sel:[1,1] op_sel_hi:[1,0] neg_lo:[0,1]\n" \
  "v_cvt_pk_f16_f32 v112, v110, v111\n" \
  "v_cvt_pk_f16_f32 v103, v122, v123\n" \
  "v_pk_fma_f32 v[110:111], v[110:111], v[118:119], v[104:105] op_sel_hi:[0,1,1]\n" \
  "v_pk_fma_f32 v[122:123], v[122:123], v[118:119], v[106:107] op_sel_hi:[0,1,1]\n" \
  "ds_write_addtid_b32 v112 offset:16864\n" \
  "ds_write_addtid_b32 v103 offset:25024\n" \
  "v_pk_mul_f32 v[104:105], v[110:111], v[118:119] op_sel:[1,1] op_sel_hi:[1,0] neg_lo:[0,1]\n" \
  "v_pk_mul_f32 v[106:107], v[122:123], v[118:119] op_sel:[1,1] op_sel_hi:[1,0] neg_lo:[0,1]\n" \
  "v_cvt_pk_f16_f32 v112, v110, v111\n" \
  "v_cvt_pk_f16_f32 v103, v122, v123\n" \
  "v_pk_fma_f32 v[110:111], v[110:111], v[118:119], v[104:105] op_sel_hi:[0,1,1]\n" \
  "v_pk_fma_f32 v[122:123], v[122:123], v[118:119], v[106:107] op_sel_hi:[0,1,1]\n" \
  "ds_write_addtid_b32 v112 offset:17408\n" \
  "ds_write_addtid_b32 v103 offset:25568\n" \
  "v_pk_mul_f32 v[104:105], v[110:111], v[118:119] op_sel:[1,1] op_sel_hi:[1,0] neg_lo:[0,1]\n" \
  "v_pk_mul_f32 v[106:107], v[122:123], v[118:119] op_sel:[1,1] op_sel_hi:[1,0] neg_lo:[0,1]\n" \
  "v_cvt_pk_f16_f32 v112, v110, v111\n" \
  "v_cvt_pk_f16_f32 v103, v122, v123\n" \
  "v_pk_fma_f32 v[110:111], v[110:111], v[118:119], v[104:105] op_sel_hi:[0,1,1]\n" \
  "v_pk_fma_f32 v[122:123], v[122:123], v[118:119], v[106:107] op_sel_hi:[0,1,1]\n" \
  "ds_write_addtid_b32 v112 offset:17952\n" \
  "ds_write_addtid_b32 v103 offset:26112\n" \
  "v_pk_mul_f32 v[104:105], v[110:111], v[118:119] op_sel:[1,1] op_sel_hi:[1,0] neg_lo:[0,1]\n" \
  "v_pk_mul_f32 v[106:107], v[122:123], v[118:119] op_sel:[1,1] op_sel_hi:[1,0] neg_lo:[0,1]\n" \
  "v_cvt_pk_f16_f32 v112, v110, v111\n" \
  "v_cvt_pk_f16_f32 v103, v122, v123\n" \
  "v_pk_fma_f32 v[110:111], v[110:111], v[118:119], v[104:105] op_sel_hi:[0,1,1]\n" \
  "v_pk_fma_f32 v[122:123], v[122:123], v[118:119], v[106:107] op_sel_hi:[0,1,1]\n" \
  "ds_write_addtid_b32 v112 offset:18496\n" \
  "ds_write_addtid_b32 v103 offset:26656\n" \
  "v_pk_mul_f32 v[104:105], v[110:111], v[118:119] op_sel:[1,1] op_sel_hi:[1,0] neg_lo:[0,1]\n" \
  "v_pk_mul_f32 v[106:107], v[122:123], v[118:119] op_sel:[1,1] op_sel_hi:[1,0] neg_lo:[0,1]\n" \
  "v_cvt_pk_f16_f32 v112, v110, v111\n" \
  "v_cvt_pk_f16_f32 v103, v122, v123\n" \
  "v_pk_fma_f32 v[110:111], v[110:111], v[118:119], v[104:105] op_sel_hi:[0,1,1]\n" \
  "v_pk_fma_f32 v[122:123], v[122:123], v[118:119], v[106:107] op_sel_hi:[0,1,1]\n" \
  "ds_write_addtid_b32 v112 offset:19040\n" \
  "ds_write_addtid_b32 v103 offset:27200\n" \
  "v_pk_mul_f32 v[104:105], v[110:111], v[118:119] op_sel:[1,1] op_sel_hi:[1,0] neg_lo:[0,1]\n" \
  "v_pk_mul_f32 v[106:107], v[122:123], v[118:119] op_sel:[1,1] op_sel_hi:[1,0] neg_lo:[0,1]\n" \
  "v_cvt_pk_f16_f32 v112, v110, v111\n" \
  "v_cvt_pk_f16_f32 v103, v122, v123\n" \
  "v_pk_fma_f32 v[110:111], v[110:111], v[118:119], v[104:105] op_sel_hi:[0,1,1]\n" \
  "v_pk_fma_f32 v[122:123], v[122:123], v[118:119], v[106:107] op_sel_hi:[0,1,1]\n" \
  "ds_write_addtid_b32 v112 offset:19584\n" \
  "ds_write_addtid_b32 v103 offset:27744\n" \
  "v_pk_mul_f32 v[104:105], v[110:111], v[118:119] op_sel:[1,1] op_sel_hi:[1,0] neg_lo:[0,1]\n" \
  "v_pk_mul_f32 v[106:107], v[122:123], v[118:119] op_sel:[1,1] op_sel_hi:[1,0] neg_lo:[0,1]\n" \
  "v_cvt_pk_f16_f32 v112, v110, v111\n" \
  "v_cvt_pk_f16_f32 v103, v122, v123\n" \
  "v_pk_fma_f32 v[110:111], v[110:111], v[118:119], v[104:105] op_sel_hi:[0,1,1]\n" \
  "v_pk_fma_f32 v[122:123], v[122:123], v[118:119], v[106:107] op_sel_hi:[0,1,1]\n" \
  "ds_write_addtid_b32 v112 offset:20128\n" \
  "ds_write_addtid_b32 v103 offset:28288\n" \
  "v_pk_mul_f32 v[104:105], v[110:111], v[118:119] op_sel:[1,1] op_sel_hi:[1,0] neg_lo:[0,1]\n" \
  "v_pk_mul_f32 v[106:107], v[122:123], v[118:119] op_sel:[1,1] op_sel_hi:[1,0] neg_lo:[0,1]\n" \
  "v_cvt_pk_f16_f32 v112, v110, v111\n" \
  "v_cvt_pk_f16_f32 v103, v122, v123\n" \
  "v_pk_fma_f32 v[110:111], v[110:111], v[118:119], v[104:105] op_sel_hi:[0,1,1]\n" \
  "v_pk_fma_f32 v[122:123], v[122:123], v[118:119], v[106:107] op_sel_hi:[0,1,1]\n" \
  "ds_write_addtid_b32 v112 offset:20672\n" \
  "ds_write_addtid_b32 v103 offset:28832\n" \
  "v_pk_mul_f32 v[104:105], v[110:111], v[118:119] op_sel:[1,1] op_sel_hi:[1,0] neg_lo:[0,1]\n" \
  "v_pk_mul_f32 v[106:107], v[122:123], v[118:119] op_sel:[1,1] op_sel_hi:[1,0] neg_lo:[0,1]\n" \
  "v_cvt_pk_f16_f32 v112, v110, v111\n" \
  "v_cvt_pk_f16_f32 v103, v122, v123\n" \
  "v_pk_fma_f32 v[110:111], v[110:111], v[118:119], v[104:105] op_sel_hi:[0,1,1]\n" \
  "v_pk_fma_f32 v[122:123], v[122:123], v[118:119], v[106:107] op_sel_hi:[0,1,1]\n" \
  "ds_write_addtid_b32 v112 offset:21216\n" \
  "ds_write_addtid_b32 v103 offset:29376\n" \
  "v_pk_mul_f32 v[104:105], v[110:111], v[118:119] op_sel:[1,1] op_sel_hi:[1,0] neg_lo:[0,1]\n" \
  "v_pk_mul_f32 v[106:107], v[122:123], v[118:119] op_sel:[1,1] op_sel_hi:[1,0] neg_lo:[0,1]\n" \
  "v_cvt_pk_f16_f32 v112, v110, v111\n" \
  "v_cvt_pk_f16_f32 v103, v122, v123\n" \
  "v_pk_fma_f32 v[110:111], v[110:111], v[118:119], v[104:105] op_sel_hi:[0,1,1]\n" \
  "v_pk_fma_f32 v[122:123], v[122:123], v[118:119], v[106:107] op_sel_hi:[0,1,1]\n" \
  "ds_write_addtid_b32 v112 offset:21760\n" \
  "ds_write_addtid_b32 v103 offset:29920\n" \
  "v_pk_mul_f32 v[104:105], v[110:111], v[118:119] op_sel:[1,1] op_sel_hi:[1,0] neg_lo:[0,1]\n" \
  "v_pk_mul_f32 v[106:107], v[122:123], v[118:119] op_sel:[1,1] op_sel_hi:[1,0] neg_lo:[0,1]\n" \
  "v_cvt_pk_f16_f32 v112, v110, v111\n" \
  "v_cvt_pk_f16_f32 v103, v122, v123\n" \
  "v_pk_fma_f32 v[110:111], v[110:111], v[118:119], v[104:105] op_sel_hi:[0,1,1]\n" \
  "v_pk_fma_f32 v[122:123], v[122:123], v[118:119], v[106:107] op_sel_hi:[0,1,1]\n" \
  "ds_write_addtid_b32 v112 offset:22304\n" \
  "ds_write_addtid_b32 v103 offset:30464\n" \
  "v_pk_mul_f32 v[104:105], v[122:123], v[118:119] op_sel:[1,1] op_sel_hi:[1,0] neg_lo:[0,1]\n" \
  "v_cvt_pk_f16_f32 v112, v110, v111\n" \
  "v_cvt_pk_f16_f32 v103, v122, v123\n" \
  "v_pk_fma_f32 v[122:123], v[122:123], v[118:119], v[104:105] op_sel_hi:[0,1,1]\n" \
  "ds_write_addtid_b32 v112 offset:22848\n" \
  "ds_write_addtid_b32 v103 offset:31008\n" \
  "v_cvt_pk_f16_f32 v112, v122, v123\n" \
  "ds_write_addtid_b32 v112 offset:31552\n" \
  "Lgk%=:\n" \
  "ds_add_u32 v4, v16 offset:8\n" \
  GSTAMP3 \
  "s_lshl_b32 s54, s59, 2\n" \
  "v_mov_b32 v9, s57\n" \
  "s_mov_b32 s97, 0x1fffff\n" \
  "Lkp%=:\n" \
  "ds_read_b32 v17, v4 offset:8\n" \
  "s_waitcnt lgkmcnt(0)\n" \
  "v_readfirstlane_b32 s96, v17\n" \
  "s_and_b32 s96, s96, s54\n" \
  "s_cmp_eq_u32 s96, s54\n" \
  "s_cbranch_scc1 Lkp%=x\n" \
  "s_sub_u32 s97, s97, 1\n" \
  "s_cbranch_scc0 Lkp%=\n" \
  "Lkp%=x:\n" \
  GSTAMP4 \
  "ds_read2st64_b32 v[18:19], v9 offset0:34 offset1:35\n" \
  "ds_read_b128 v[24:27], v5 offset:0\n" \
  "ds_read_b128 v[28:31], v6 offset:0\n" \
  "ds_read_b128 v[56:59], v5 offset:256\n" \
  "ds_read_b128 v[60:63], v6 offset:256\n" \
  "ds_read_b128 v[32:35], v5 offset:64\n" \
  "ds_read_b128 v[36:39], v6 offset:64\n" \
  "ds_read_b128 v[64:67], v5 offset:320\n" \
  "ds_read_b128 v[68:71], v6 offset:320\n" \
  "ds_read_b128 v[40:43], v5 offset:128\n" \
  "ds_read_b128 v[44:47], v6 offset:128\n" \
  "ds_read_b128 v[72:75], v5 offset:384\n" \
  "ds_read_b128 v[76:79], v6 offset:384\n" \
  "ds_read_b128 v[48:51], v5 offset:192\n" \
  "ds_read_b128 v[52:55], v6 offset:192\n" \
  "ds_read_b128 v[80:83], v5 offset:448\n" \
  "ds_read_b128 v[84:87], v6 offset:448\n" \
  "s_waitcnt lgkmcnt(14)\n" \
  "v_max_u32 v17, v18, v19\n" \
  "v_sub_u32 v20, 121, v17\n" \
  "s_waitcnt lgkmcnt(14)\n" \
  "v_mfma_f32_16x16x32_f16 v[88:91], v[24:27], v[28:31], 0\n" \
  "v_add_u32 v18, v18, v20\n" \
  "v_add_u32 v19, v19, v20\n" \
  "s_waitcnt lgkmcnt(12)\n" \
  "v_mfma_f32_16x16x32_f16 v[92:95], v[56:59], v[60:63], 0\n" \
  "v_max_i32 v18, 0, v18\n" \
  "v_max_i32 v19, 0, v19\n" \
  "s_waitcnt lgkmcnt(10)\n" \
  "v_mfma_f32_16x16x32_f16 v[88:91], v[32:35], v[36:39], v[88:91]\n" \
  "v_lshlrev_b32 v18, 23, v18\n" \
  "v_lshlrev_b32 v19, 23, v19\n" \
  "s_waitcnt lgkmcnt(8)\n" \
  "v_mfma_f32_16x16x32_f16 v[92:95], v[64:67], v[68:71], v[92:95]\n" \
  "v_lshrrev_b32 v3, 1, v2\n" \
  "v_add3_u32 v3, v3, v1, 1\n" \
  "s_waitcnt lgkmcnt(6)\n" \
  "v_mfma_f32_16x16x32_f16 v[88:91], v[40:43], v[44:47], v[88:91]\n" \
  "v_and_b32 v8, 1, v3\n" \
  "v_mul_u32_u24 v8, 1470, v8\n" \
  "s_waitcnt lgkmcnt(4)\n" \
  "v_mfma_f32_16x16x32_f16 v[92:95], v[72:75], v[76:79], v[92:95]\n" \
  "v_lshl_add_u32 v3, v3, 1, v8\n" \
  "s_add_u32 s55, s58, 4544\n" \
  "s_waitcnt lgkmcnt(2)\n" \
  "v_mfma_f32_16x16x32_f16 v[88:91], v[48:51], v[52:55], v[88:91]\n" \
  "v_add_u32 v3, s55, v3\n" \
  "v_lshlrev_b32 v8, 5, v1\n" \
  "s_waitcnt lgkmcnt(0)\n" \
  "v_mfma_f32_16x16x32_f16 v[92:95], v[80:83], v[84:87], v[92:95]\n" \
  "v_sub_u32 v10, v2, v8\n" \
  "s_add_u32 s55, s58, 480\n" \
  "v_add_u32 v10, s55, v10\n" \
  "v_mul_u32_u24 v11, 20, v2\n" \
  "v_lshl_add_u32 v11, v1, 4, v11\n" \
  "v_add_u32 v11, s57, v11\n" \
  "v_lshlrev_b32 v8, 2, v1\n" \
  "v_sub_u32 v8, v8, v2\n" \
  "s_add_u32 s55, s58, 7540\n" \
  "v_add_u32 v8, s55, v8\n" \
  "v_pk_mul_f32 v[88:89], v[88:89], v[18:19] op_sel_hi:[1,0]\n" \
  "v_pk_mul_f32 v[90:91], v[90:91], v[18:19] op_sel_hi:[1,0]\n" \
  "v_pk_fma_f32 v[88:89], v[92:93], v[18:19], v[88:89] op_sel:[0,1,0]\n" \
  "v_pk_fma_f32 v[90:91], v[94:95], v[18:19], v[90:91] op_sel:[0,1,0]\n" \
  "v_cvt_pk_f16_f32 v88, v88, v89\n" \
  "v_cvt_pk_f16_f32 v89, v90, v91\n" \
  "s_cmp_lt_u32 s51, 2\n" \
  "s_cbranch_scc0 Lkf%=\n" \
  "ds_write_b64 v7, v[88:89] offset:7488\n" \
  "v_alignbit_b32 v90, v89, v88, 16\n" \
  "ds_write_b16 v7, v88 offset:8958\n" \
  "ds_write_b16_d16_hi v7, v89 offset:8964\n" \
  "ds_write_b32 v7, v90 offset:8960\n" \
  "s_cmp_eq_u32 s51, 0\n" \
  "s_cbranch_scc1 Lkd%=\n" \
  "s_mov_b32 exec_lo, 0\n" \
  "s_brev_b32 exec_hi, 1\n" \
  "ds_write_b16_d16_hi v7, v89 offset:7496\n" \
  "ds_write_b16_d16_hi v7, v89 offset:7498\n" \
  "ds_write_b16_d16_hi v7, v89 offset:7500\n" \
  "ds_write_b16_d16_hi v7, v89 offset:8966\n" \
  "ds_write_b16_d16_hi v7, v89 offset:8968\n" \
  "ds_write_b16_d16_hi v7, v89 offset:8970\n" \
  "s_branch Lke%=\n" \
  "Lkf%=:\n" \
  "s_mov_b32 exec_lo, 0x07b607b6\n" \
  "s_mov_b32 exec_hi, 0x07b607b6\n" \
  "ds_write_b64 v7, v[88:89] offset:7488\n" \
  "s_cmp_eq_u32 s51, 2\n" \
  "s_cbranch_scc1 Lk2%=\n" \
  "s_mov_b32 exec_lo, 0\n" \
  "s_mov_b32 exec_hi, 0x200000\n" \
  "ds_write_b16_d16_hi v7, v89 offset:5358\n" \
  "ds_write_b16_d16_hi v7, v89 offset:5360\n" \
  "ds_write_b16_d16_hi v7, v89 offset:6828\n" \
  "ds_write_b16_d16_hi v7, v89 offset:6830\n" \
  "s_branch Lke%=\n" \
  "Lk2%=:\n" \
  "s_mov_b32 exec_lo, 0x001f0007\n" \
  "s_mov_b32 exec_hi, 0x01ff007f\n" \
  "ds_read_b32 v18, v9 offset:10336\n" \
  "v_lshrrev_b32 v19, 3, v2\n" \
  "v_mul_u32_u24 v19, 29, v19\n" \
  "v_lshl_add_u32 v19, v1, 1, v19\n" \
  "v_add_u32 v19, s58, v19\n" \
  "v_mov_b32 v20, 0x3c00\n" \
  "ds_write_b16 v19, v20 offset:5798\n" \
  "ds_write_b16 v19, v20 offset:7268\n" \
  "v_add_u32 v17, -5, v17\n" \
  "v_lshlrev_b32 v17, 23, v17\n" \
  "s_lshl2_add_u32 s55, s50, 158224\n" \
  "v_mov_b32 v19, s55\n" \
  "s_waitcnt lgkmcnt(0)\n" \
  "v_mul_f32 v17, v17, v18\n" \
  "ds_write_b32 v19, v17\n" \
  "Lke%=:\n" \
  "s_mov_b64 exec, -1\n" \
  "Lkd%=:\n" \
  "v_mov_b32 v17, 1\n" \
  "ds_add_u32 v4, v16 offset:12\n" \
  "ds_add_u32 v4, v17 offset:4\n" \
  GSTAMP5 \
  "s_setprio 0\n" \
  "s_sub_u32 s55, s51, s50\n" \
  "s_cmp_eq_u32 s51, 3\n" \
  "s_cselect_b32 s55, 3, s55\n" \
  "s_cmp_eq_u32 s55, 2\n" \
  "s_cbranch_scc1 Lr4%=\n" \
  "s_cmp_eq_u32 s55, -1\n" \
  "s_cbranch_scc1 Lr4%=\n" \
  "s_mov_b32 s97, 0x1fffff\n" \
  "Lkq%=:\n" \
  "ds_read_b32 v17, v4 offset:12\n" \
  "s_waitcnt lgkmcnt(0)\n" \
  "v_readfirstlane_b32 s96, v17\n" \
  "s_and_b32 s96, s96, s54\n" \
  "s_cmp_eq_u32 s96, s54\n" \
  "s_cbranch_scc1 Lkq%=x\n" \
  "s_sub_u32 s97, s97, 1\n" \
  "s_cbranch_scc0 Lkq%=\n" \
  "Lkq%=x:\n" \
  "s_waitcnt vmcnt(0)\n" \
  GSTAMP6 \
  "s_cmp_eq_u32 s55, 3\n" \
  "s_cbranch_scc1 Lr1%=\n" \
  "s_cmp_lg_u32 s55, 0\n" \
  "s_cbranch_scc1 Lr2%=\n" \
  "ds_read2_b32 v[60:61], v3 offset0:0 offset1:1\n" \
  "ds_read2_b32 v[62:63], v3 offset0:2 offset1:3\n" \
  "ds_read2_b32 v[92:93], v3 offset0:128 offset1:129\n" \
  "ds_read2_b32 v[94:95], v3 offset0:130 offset1:131\n" \
  "ds_read2_b32 v[64:65], v3 offset0:16 offset1:17\n" \
  "ds_read2_b32 v[66:67], v3 offset0:18 offset1:19\n" \
  "ds_read2_b32 v[96:97], v3 offset0:144 offset1:145\n" \
  "ds_read2_b32 v[98:99], v3 offset0:146 offset1:147\n" \
  "ds_read2_b32 v[68:69], v3 offset0:32 offset1:33\n" \
  "ds_read2_b32 v[70:71], v3 offset0:34 offset1:35\n" \
  "ds_read2_b32 v[100:101], v3 offset0:160 offset1:161\n" \
  "ds_read2_b32 v[102:103], v3 offset0:162 offset1:163\n" \
  "ds_read2_b32 v[72:73], v3 offset0:48 offset1:49\n" \
  "ds_read2_b32 v[74:75], v3 offset0:50 offset1:51\n" \
  "ds_read2_b32 v[104:105], v3 offset0:176 offset1:177\n" \
  "ds_read2_b32 v[106:107], v3 offset0:178 offset1:179\n" \
  "ds_read2_b32 v[76:77], v3 offset0:64 offset1:65\n" \
  "ds_read2_b32 v[78:79], v3 offset0:66 offset1:67\n" \
  "ds_read2_b32 v[108:109], v3 offset0:192 offset1:193\n" \
  "ds_read2_b32 v[110:111], v3 offset0:194 offset1:195\n" \
  "ds_read2_b32 v[80:81], v3 offset0:80 offset1:81\n" \
  "ds_read2_b32 v[82:83], v3 offset0:82 offset1:83\n" \
  "ds_read2_b32 v[112:113], v3 offset0:208 offset1:209\n" \
  "ds_read2_b32 v[114:115], v3 offset0:210 offset1:211\n" \
  "ds_read2_b32 v[84:85], v3 offset0:96 offset1:97\n" \
  "ds_read2_b32 v[86:87], v3 offset0:98 offset1:99\n" \
  "ds_read2_b32 v[116:117], v3 offset0:224 offset1:225\n" \
  "ds_read2_b32 v[118:119], v3 offset0:226 offset1:227\n" \
  "ds_read2_b32 v[88:89], v3 offset0:112 offset1:113\n" \
  "ds_read2_b32 v[90:91], v3 offset0:114 offset1:115\n" \
  "ds_read2_b32 v[120:121], v3 offset0:240 offset1:241\n" \
  "ds_read2_b32 v[122:123], v3 offset0:242 offset1:243\n" \
  "v_add_u32 v9, 1024, v3\n" \
  "ds_read2_b32 v[124:125], v9 offset0:0 offset1:1\n" \
  "ds_read2_b32 v[126:127], v9 offset0:2 offset1:3\n" \
  "s_mov_b32 s97, 0x1fffff\n" \
  "Lnp%=:\n" \
  "ds_read_b32 v8, v4 offset:4\n" \
  "s_waitcnt lgkmcnt(0)\n" \
  "v_readfirstlane_b32 s96, v8\n" \
  "s_and_b32 s96, s96, 0xff\n" \
  "s_cmp_eq_u32 s96, 16\n" \
  "s_cbranch_scc1 Lnp%=x\n" \
  "s_sub_u32 s97, s97, 1\n" \
  "s_cbranch_scc0 Lnp%=\n" \
  "Lnp%=x:\n" \
  "s_setprio " STR(PRN) "\n" \
  "ds_read_b128 v[28:31], v10 offset:3584\n" \
  "ds_read_b128 v[32:35], v10 offset:3648\n" \
  "ds_read_b128 v[36:39], v10 offset:3712\n" \
  "ds_read_b128 v[40:43], v10 offset:3776\n" \
  "ds_read_b128 v[44:47], v10 offset:3840\n" \
  "ds_read_b128 v[48:51], v10 offset:3904\n" \
  "ds_read_b128 v[52:55], v10 offset:3968\n" \
  "ds_read_b128 v[56:59], v10 offset:4032\n" \
  "s_waitcnt lgkmcnt(7)\n" \
  "v_mfma_f32_16x16x32_f16 v[12:15], v[60:63], v[28:31], 0\n" \
  "v_mfma_f32_16x16x32_f16 v[16:19], v[92:95], v[28:31], 0\n" \
  "v_mfma_f32_16x16x32_f16 v[20:23], v[124:127], v[28:31], 0\n" \
  "ds_read_b128 v[28:31], v10 offset:3072\n" \
  "s_waitcnt lgkmcnt(7)\n" \
  "v_mfma_f32_16x16x32_f16 v[12:15], v[64:67], v[32:35], v[12:15]\n" \
  "v_mfma_f32_16x16x32_f16 v[16:19], v[96:99], v[32:35], v[16:19]\n" \
  "ds_read_b128 v[32:35], v10 offset:3136\n" \
  "s_waitcnt lgkmcnt(7)\n" \
  "v_mfma_f32_16x16x32_f16 v[12:15], v[68:71], v[36:39], v[12:15]\n" \
  "v_mfma_f32_16x16x32_f16 v[16:19], v[100:103], v[36:39], v[16:19]\n" \
  "ds_read_b128 v[36:39], v10 offset:3200\n" \
  "s_waitcnt lgkmcnt(7)\n" \
  "v_mfma_f32_16x16x32_f16 v[12:15], v[72:75], v[40:43], v[12:15]\n" \
  "v_mfma_f32_16x16x32_f16 v[16:19], v[104:107], v[40:43], v[16:19]\n" \
  "ds_read_b128 v[40:43], v10 offset:3264\n" \
  "s_waitcnt lgkmcnt(7)\n" \
  "v_mfma_f32_16x16x32_f16 v[12:15], v[76:79], v[44:47], v[12:15]\n" \
  "v_mfma_f32_16x16x32_f16 v[16:19], v[108:111], v[44:47], v[16:19]\n" \
  "ds_read_b128 v[44:47], v10 offset:3328\n" \
  "s_waitcnt lgkmcnt(7)\n" \
  "v_mfma_f32_16x16x32_f16 v[12:15], v[80:83], v[48:51], v[12:15]\n" \
  "v_mfma_f32_16x16x32_f16 v[16:19], v[112:115], v[48:51], v[16:19]\n" \
  "ds_read_b128 v[48:51], v10 offset:3392\n" \
  "s_waitcnt lgkmcnt(7)\n" \
  "v_mfma_f32_16x16x32_f16 v[12:15], v[84:87], v[52:55], v[12:15]\n" \
  "v_mfma_f32_16x16x32_f16 v[16:19], v[116:119], v[52:55], v[16:19]\n" \
  "ds_read_b128 v[52:55], v10 offset:3456\n" \
  "s_waitcnt lgkmcnt(7)\n" \
  "v_mfma_f32_16x16x32_f16 v[12:15], v[88:91], v[56:59], v[12:15]\n" \
  "v_mfma_f32_16x16x32_f16 v[16:19], v[120:123], v[56:59], v[16:19]\n" \
  "ds_read_b128 v[56:59], v10 offset:3520\n" \
  "s_waitcnt lgkmcnt(7)\n" \
  "v_mfma_f32_16x16x32_f16 v[16:19], v[60:63], v[28:31], v[16:19]\n" \
  "v_mfma_f32_16x16x32_f16 v[20:23], v[92:95], v[28:31], v[20:23]\n" \
  "v_mfma_f32_16x16x32_f16 v[24:27], v[124:127], v[28:31], 0\n" \
  "ds_read_b128 v[28:31], v10 offset:2560\n" \
  "s_waitcnt lgkmcnt(7)\n" \
  "v_mfma_f32_16x16x32_f16 v[16:19], v[64:67], v[32:35], v[16:19]\n" \
  "v_mfma_f32_16x16x32_f16 v[20:23], v[96:99], v[32:35], v[20:23]\n" \
  "ds_read_b128 v[32:35], v10 offset:2624\n" \
  "s_waitcnt lgkmcnt(7)\n" \
  "v_mfma_f32_16x16x32_f16 v[16:19], v[68:71], v[36:39], v[16:19]\n" \
  "v_mov_b32 v9, 0x1\n" \
  "ds_write_b128 v11, v[12:15] offset:0\n" \
  "ds_add_u32 v4, v9 offset:0\n" \
  "v_mfma_f32_16x16x32_f16 v[20:23], v[100:103], v[36:39], v[20:23]\n" \
  "ds_read_b128 v[36:39], v10 offset:2688\n" \
  "s_waitcnt lgkmcnt(9)\n" \
  "v_mfma_f32_16x16x32_f16 v[16:19], v[72:75], v[40:43], v[16:19]\n" \
  "v_mfma_f32_16x16x32_f16 v[20:23], v[104:107], v[40:43], v[20:23]\n" \
  "ds_read_b128 v[40:43], v10 offset:2752\n" \
  "s_waitcnt lgkmcnt(9)\n" \
  "v_mfma_f32_16x16x32_f16 v[16:19], v[76:79], v[44:47], v[16:19]\n" \
  "v_mfma_f32_16x16x32_f16 v[20:23], v[108:111], v[44:47], v[20:23]\n" \
  "ds_read_b128 v[44:47], v10 offset:2816\n" \
  "s_waitcnt lgkmcnt(9)\n" \
  "v_mfma_f32_16x16x32_f16 v[16:19], v[80:83], v[48:51], v[16:19]\n" \
  "v_mfma_f32_16x16x32_f16 v[20:23], v[112:115], v[48:51], v[20:23]\n" \
  "ds_read_b128 v[48:51], v10 offset:2880\n" \
  "s_waitcnt lgkmcnt(9)\n" \
  "v_mfma_f32_16x16x32_f16 v[16:19], v[84:87], v[52:55], v[16:19]\n" \
  "v_mfma_f32_16x16x32_f16 v[20:23], v[116:119], v[52:55], v[20:23]\n" \
  "ds_read_b128 v[52:55], v10 offset:2944\n" \
  "s_waitcnt lgkmcnt(9)\n" \
  "v_mfma_f32_16x16x32_f16 v[16:19], v[88:91], v[56:59], v[16:19]\n" \
  "v_mfma_f32_16x16x32_f16 v[20:23], v[120:123], v[56:59], v[20:23]\n" \
  "ds_read_b128 v[56:59], v10 offset:3008\n" \
  "s_waitcnt lgkmcnt(9)\n" \
  "v_mfma_f32_16x16x32_f16 v[20:23], v[60:63], v[28:31], v[20:23]\n" \
  "v_mfma_f32_16x16x32_f16 v[24:27], v[92:95], v[28:31], v[24:27]\n" \
  "ds_read_b128 v[28:31], v10 offset:2048\n" \
  "s_waitcnt lgkmcnt(9)\n" \
  "v_mfma_f32_16x16x32_f16 v[20:23], v[64:67], v[32:35], v[20:23]\n" \
  "v_mfma_f32_16x16x32_f16 v[24:27], v[96:99], v[32:35], v[24:27]\n" \
  "ds_read_b128 v[32:35], v10 offset:2112\n" \
  "s_waitcnt lgkmcnt(7)\n" \
  "v_mfma_f32_16x16x32_f16 v[20:23], v[68:71], v[36:39], v[20:23]\n" \
  "v_mov_b32 v9, 0x10\n" \
  "ds_write_b128 v11, v[16:19] offset:1280\n" \
  "ds_add_u32 v4, v9 offset:0\n" \
  "v_mfma_f32_16x16x32_f16 v[24:27], v[100:103], v[36:39], v[24:27]\n" \
  "ds_read_b128 v[36:39], v10 offset:2176\n" \
  "s_waitcnt lgkmcnt(9)\n" \
  "v_mfma_f32_16x16x32_f16 v[20:23], v[72:75], v[40:43], v[20:23]\n" \
  "v_mfma_f32_16x16x32_f16 v[24:27], v[104:107], v[40:43], v[24:27]\n" \
  "ds_read_b128 v[40:43], v10 offset:2240\n" \
  "s_waitcnt lgkmcnt(9)\n" \
  "v_mfma_f32_16x16x32_f16 v[20:23], v[76:79], v[44:47], v[20:23]\n" \
  "v_mfma_f32_16x16x32_f16 v[24:27], v[108:111], v[44:47], v[24:27]\n" \
  "ds_read_b128 v[44:47], v10 offset:2304\n" \
  "s_waitcnt lgkmcnt(9)\n" \
  "v_mfma_f32_16x16x32_f16 v[20:23], v[80:83], v[48:51], v[20:23]\n" \
  "v_mfma_f32_16x16x32_f16 v[24:27], v[112:115], v[48:51], v[24:27]\n" \
  "ds_read_b128 v[48:51], v10 offset:2368\n" \
  "s_waitcnt lgkmcnt(9)\n" \
  "v_mfma_f32_16x16x32_f16 v[20:23], v[84:87], v[52:55], v[20:23]\n" \
  "v_mfma_f32_16x16x32_f16 v[24:27], v[116:119], v[52:55], v[24:27]\n" \
  "ds_read_b128 v[52:55], v10 offset:2432\n" \
  "s_waitcnt lgkmcnt(9)\n" \
  "v_mfma_f32_16x16x32_f16 v[20:23], v[88:91], v[56:59], v[20:23]\n" \
  "v_mfma_f32_16x16x32_f16 v[24:27], v[120:123], v[56:59], v[24:27]\n" \
  "ds_read_b128 v[56:59], v10 offset:2496\n" \
  "s_waitcnt lgkmcnt(9)\n" \
  "v_mfma_f32_16x16x32_f16 v[24:27], v[60:63], v[28:31], v[24:27]\n" \
  "s_waitcnt lgkmcnt(8)\n" \
  "v_mfma_f32_16x16x32_f16 v[24:27], v[64:67], v[32:35], v[24:27]\n" \
  "s_waitcnt lgkmcnt(5)\n" \
  "v_mfma_f32_16x16x32_f16 v[24:27], v[68:71], v[36:39], v[24:27]\n" \
  "v_mov_b32 v9, 0x100\n" \
  "ds_write_b128 v11, v[20:23] offset:2560\n" \
  "ds_add_u32 v4, v9 offset:0\n" \
  "s_waitcnt lgkmcnt(6)\n" \
  "v_mfma_f32_16x16x32_f16 v[24:27], v[72:75], v[40:43], v[24:27]\n" \
  "s_waitcnt lgkmcnt(5)\n" \
  "v_mfma_f32_16x16x32_f16 v[24:27], v[76:79], v[44:47], v[24:27]\n" \
  "s_waitcnt lgkmcnt(4)\n" \
  "v_mfma_f32_16x16x32_f16 v[24:27], v[80:83], v[48:51], v[24:27]\n" \
  "s_waitcnt lgkmcnt(3)\n" \
  "v_mfma_f32_16x16x32_f16 v[24:27], v[84:87], v[52:55], v[24:27]\n" \
  "s_waitcnt lgkmcnt(2)\n" \
  "v_mfma_f32_16x16x32_f16 v[24:27], v[88:91], v[56:59], v[24:27]\n" \
  "v_mov_b32 v9, 0x1000\n" \
  "s_nop 6\n" \
  "ds_write_b128 v11, v[24:27] offset:3840\n" \
  "ds_add_u32 v4, v9 offset:0\n" \
  "s_branch Lr3%=\n" \
  "Lr1%=:\n" \
  "v_add_u32 v9, 1024, v3\n" \
  "ds_read2_b32 v[12:13], v9 offset0:48 offset1:49\n" \
  "ds_read2_b32 v[14:15], v9 offset0:50 offset1:51\n" \
  "ds_read2_b32 v[16:17], v9 offset0:44 offset1:45\n" \
  "ds_read2_b32 v[18:19], v9 offset0:46 offset1:47\n" \
  "ds_read2_b32 v[20:21], v9 offset0:61 offset1:62\n" \
  "ds_read2_b32 v[22:23], v9 offset0:63 offset1:64\n" \
  "ds_read2_b32 v[24:25], v8 offset0:0 offset1:1\n" \
  "ds_read2_b32 v[26:27], v8 offset0:2 offset1:3\n" \
  "ds_read2_b32 v[36:37], v8 offset0:48 offset1:49\n" \
  "ds_read2_b32 v[38:39], v8 offset0:50 offset1:51\n" \
  "ds_read2_b32 v[28:29], v8 offset0:16 offset1:17\n" \
  "ds_read2_b32 v[30:31], v8 offset0:18 offset1:19\n" \
  "ds_read2_b32 v[40:41], v8 offset0:64 offset1:65\n" \
  "ds_read2_b32 v[42:43], v8 offset0:66 offset1:67\n" \
  "ds_read2_b32 v[32:33], v8 offset0:32 offset1:33\n" \
  "ds_read2_b32 v[34:35], v8 offset0:34 offset1:35\n" \
  "ds_read2_b32 v[44:45], v8 offset0:80 offset1:81\n" \
  "ds_read2_b32 v[46:47], v8 offset0:82 offset1:83\n" \
  "s_mov_b32 s97, 0x1fffff\n" \
  "Lfp%=:\n" \
  "ds_read_b32 v8, v4 offset:4\n" \
  "s_waitcnt lgkmcnt(0)\n" \
  "v_readfirstlane_b32 s96, v8\n" \
  "s_and_b32 s96, s96, 0xff\n" \
  "s_cmp_eq_u32 s96, 16\n" \
  "s_cbranch_scc1 Lfp%=x\n" \
  "s_sleep 1\n" \
  "s_sub_u32 s97, s97, 1\n" \
  "s_cbranch_scc0 Lfp%=\n" \
  "Lfp%=x:\n" \
  "s_setprio " STR(PRF) "\n" \
  "ds_read_b128 v[48:51], v10 offset:3584\n" \
  "ds_read_b128 v[52:55], v10 offset:3600\n" \
  "ds_read_b128 v[56:59], v10 offset:3072\n" \
  "ds_read_b128 v[60:63], v10 offset:3088\n" \
  "s_waitcnt lgkmcnt(3)\n" \
  "v_mfma_f32_16x16x32_f16 v[64:67], v[48:51], v[12:15], 0\n" \
  "v_mfma_f32_16x16x32_f16 v[68:71], v[48:51], v[16:19], 0\n" \
  "v_mfma_f32_16x16x32_f16 v[72:75], v[48:51], v[20:23], 0\n" \
  "s_waitcnt lgkmcnt(2)\n" \
  "v_mfma_f32_16x16x32_f16 v[76:79], v[52:55], v[20:23], 0\n" \
  "ds_read_b128 v[48:51], v10 offset:2560\n" \
  "ds_read_b128 v[52:55], v10 offset:2576\n" \
  "s_nop 2\n" \
  "v_cvt_pk_f16_f32 v80, v67, v71\n" \
  "v_cvt_pk_f16_f32 v81, v66, v70\n" \
  "v_cvt_pk_f16_f32 v82, v65, v69\n" \
  "v_cvt_pk_f16_f32 v83, v64, v68\n" \
  "v_cvt_pk_f16_f32 v84, v75, v79\n" \
  "v_cvt_pk_f16_f32 v85, v74, v78\n" \
  "v_cvt_pk_f16_f32 v86, v73, v77\n" \
  "v_cvt_pk_f16_f32 v87, v72, v76\n" \
  "s_waitcnt lgkmcnt(3)\n" \
  "v_mfma_f32_16x16x32_f16 v[64:67], v[56:59], v[12:15], 0\n" \
  "v_mfma_f32_16x16x32_f16 v[68:71], v[56:59], v[16:19], 0\n" \
  "v_mfma_f32_16x16x32_f16 v[72:75], v[56:59], v[20:23], 0\n" \
  "s_waitcnt lgkmcnt(2)\n" \
  "v_mfma_f32_16x16x32_f16 v[76:79], v[60:63], v[20:23], 0\n" \
  "ds_read_b128 v[56:59], v10 offset:2048\n" \
  "ds_read_b128 v[60:63], v10 offset:2064\n" \
  "v_mfma_f32_16x16x32_f16 v[104:107], v[80:83], v[24:27], 0\n" \
  "v_mfma_f32_16x16x32_f16 v[108:111], v[80:83], v[28:31], 0\n" \
  "s_nop 0\n" \
  "v_cvt_pk_f16_f32 v88, v67, v71\n" \
  "v_cvt_pk_f16_f32 v89, v66, v70\n" \
  "v_mfma_f32_16x16x32_f16 v[104:107], v[84:87], v[36:39], v[104:107]\n" \
  "v_cvt_pk_f16_f32 v90, v65, v69\n" \
  "v_cvt_pk_f16_f32 v91, v64, v68\n" \
  "v_mfma_f32_16x16x32_f16 v[96:99], v[80:83], v[32:35], 0\n" \
  "v_cvt_pk_f16_f32 v92, v75, v79\n" \
  "v_cvt_pk_f16_f32 v93, v74, v78\n" \
  "v_mfma_f32_16x16x32_f16 v[108:111], v[84:87], v[40:43], v[108:111]\n" \
  "v_cvt_pk_f16_f32 v94, v73, v77\n" \
  "v_cvt_pk_f16_f32 v95, v72, v76\n" \
  "v_mfma_f32_16x16x32_f16 v[96:99], v[84:87], v[44:47], v[96:99]\n" \
  "s_waitcnt lgkmcnt(3)\n" \
  "v_mfma_f32_16x16x32_f16 v[64:67], v[48:51], v[12:15], 0\n" \
  "v_mfma_f32_16x16x32_f16 v[68:71], v[48:51], v[16:19], 0\n" \
  "v_mfma_f32_16x16x32_f16 v[72:75], v[48:51], v[20:23], 0\n" \
  "s_waitcnt lgkmcnt(2)\n" \
  "v_mfma_f32_16x16x32_f16 v[76:79], v[52:55], v[20:23], 0\n" \
  "s_mov_b32 s97, 0x1fffff\n" \
  "Lfq%=:\n" \
  "ds_read_b32 v8, v4 offset:4\n" \
  "s_waitcnt lgkmcnt(0)\n" \
  "v_readfirstlane_b32 s96, v8\n" \
  "s_and_b32 s96, s96, 0x400\n" \
  "s_cmp_eq_u32 s96, 0x400\n" \
  "s_cbranch_scc1 Lfq%=x\n" \
  "s_sub_u32 s97, s97, 1\n" \
  "s_cbranch_scc0 Lfq%=\n" \
  "Lfq%=x:\n" \
  "ds_read_b128 v[48:51], v10 offset:1536\n" \
  "ds_read_b128 v[52:55], v10 offset:1552\n" \
  "v_mov_b32 v9, 0x100\n" \
  "ds_write_b128 v11, v[104:107] offset:10240\n" \
  "ds_add_u32 v4, v9 offset:0\n" \
  "v_mfma_f32_16x16x32_f16 v[108:111], v[88:91], v[24:27], v[108:111]\n" \
  "v_mfma_f32_16x16x32_f16 v[96:99], v[88:91], v[28:31], v[96:99]\n" \
  "v_cvt_pk_f16_f32 v80, v67, v71\n" \
  "v_cvt_pk_f16_f32 v81, v66, v70\n" \
  "v_mfma_f32_16x16x32_f16 v[108:111], v[92:95], v[36:39], v[108:111]\n" \
  "v_cvt_pk_f16_f32 v82, v65, v69\n" \
  "v_cvt_pk_f16_f32 v83, v64, v68\n" \
  "v_mfma_f32_16x16x32_f16 v[100:103], v[88:91], v[32:35], 0\n" \
  "v_cvt_pk_f16_f32 v84, v75, v79\n" \
  "v_cvt_pk_f16_f32 v85, v74, v78\n" \
  "v_mfma_f32_16x16x32_f16 v[96:99], v[92:95], v[40:43], v[96:99]\n" \
  "v_cvt_pk_f16_f32 v86, v73, v77\n" \
  "v_cvt_pk_f16_f32 v87, v72, v76\n" \
  "v_mfma_f32_16x16x32_f16 v[100:103], v[92:95], v[44:47], v[100:103]\n" \
  "v_mfma_f32_16x16x32_f16 v[64:67], v[56:59], v[12:15], 0\n" \
  "v_mfma_f32_16x16x32_f16 v[68:71], v[56:59], v[16:19], 0\n" \
  "v_mfma_f32_16x16x32_f16 v[72:75], v[56:59], v[20:23], 0\n" \
  "v_mfma_f32_16x16x32_f16 v[76:79], v[60:63], v[20:23], 0\n" \
  "ds_read_b128 v[56:59], v10 offset:1024\n" \
  "ds_read_b128 v[60:63], v10 offset:1040\n" \
  "v_mov_b32 v9, 0x1000\n" \
  "ds_write_b128 v11, v[108:111] offset:11520\n" \
  "ds_add_u32 v4, v9 offset:0\n" \
  "v_mfma_f32_16x16x32_f16 v[96:99], v[80:83], v[24:27], v[96:99]\n" \
  "v_mfma_f32_16x16x32_f16 v[100:103], v[80:83], v[28:31], v[100:103]\n" \
  "v_cvt_pk_f16_f32 v88, v67, v71\n" \
  "v_cvt_pk_f16_f32 v89, v66, v70\n" \
  "v_mfma_f32_16x16x32_f16 v[96:99], v[84:87], v[36:39], v[96:99]\n" \
  "v_cvt_pk_f16_f32 v90, v65, v69\n" \
  "v_cvt_pk_f16_f32 v91, v64, v68\n" \
  "v_mfma_f32_16x16x32_f16 v[104:107], v[80:83], v[32:35], 0\n" \
  "v_cvt_pk_f16_f32 v92, v75, v79\n" \
  "v_cvt_pk_f16_f32 v93, v74, v78\n" \
  "v_mfma_f32_16x16x32_f16 v[100:103], v[84:87], v[40:43], v[100:103]\n" \
  "v_cvt_pk_f16_f32 v94, v73, v77\n" \
  "v_cvt_pk_f16_f32 v95, v72, v76\n" \
  "v_mfma_f32_16x16x32_f16 v[104:107], v[84:87], v[44:47], v[104:107]\n" \
  "s_waitcnt lgkmcnt(7)\n" \
  "v_mfma_f32_16x16x32_f16 v[64:67], v[48:51], v[12:15], 0\n" \
  "v_mfma_f32_16x16x32_f16 v[68:71], v[48:51], v[16:19], 0\n" \
  "v_mfma_f32_16x16x32_f16 v[72:75], v[48:51], v[20:23], 0\n" \
  "s_waitcnt lgkmcnt(6)\n" \
  "v_mfma_f32_16x16x32_f16 v[76:79], v[52:55], v[20:23], 0\n" \
  "v_mov_b32 v9, 0x10000\n" \
  "ds_write_b128 v11, v[96:99] offset:12800\n" \
  "ds_add_u32 v4, v9 offset:0\n" \
  "v_mfma_f32_16x16x32_f16 v[100:103], v[88:91], v[24:27], v[100:103]\n" \
  "v_mfma_f32_16x16x32_f16 v[104:107], v[88:91], v[28:31], v[104:107]\n" \
  "v_cvt_pk_f16_f32 v80, v67, v71\n" \
  "v_cvt_pk_f16_f32 v81, v66, v70\n" \
  "v_mfma_f32_16x16x32_f16 v[100:103], v[92:95], v[36:39], v[100:103]\n" \
  "v_cvt_pk_f16_f32 v82, v65, v69\n" \
  "v_cvt_pk_f16_f32 v83, v64, v68\n" \
  "v_mfma_f32_16x16x32_f16 v[108:111], v[88:91], v[32:35], 0\n" \
  "v_cvt_pk_f16_f32 v84, v75, v79\n" \
  "v_cvt_pk_f16_f32 v85, v74, v78\n" \
  "v_mfma_f32_16x16x32_f16 v[104:107], v[92:95], v[40:43], v[104:107]\n" \
  "v_cvt_pk_f16_f32 v86, v73, v77\n" \
  "v_cvt_pk_f16_f32 v87, v72, v76\n" \
  "v_mfma_f32_16x16x32_f16 v[108:111], v[92:95], v[44:47], v[108:111]\n" \
  "s_waitcnt lgkmcnt(5)\n" \
  "v_mfma_f32_16x16x32_f16 v[64:67], v[56:59], v[12:15], 0\n" \
  "v_mfma_f32_16x16x32_f16 v[68:71], v[56:59], v[16:19], 0\n" \
  "v_mfma_f32_16x16x32_f16 v[72:75], v[56:59], v[20:23], 0\n" \
  "s_waitcnt lgkmcnt(4)\n" \
  "v_mfma_f32_16x16x32_f16 v[76:79], v[60:63], v[20:23], 0\n" \
  "v_mov_b32 v9, 0x100000\n" \
  "ds_write_b128 v11, v[100:103] offset:14080\n" \
  "ds_add_u32 v4, v9 offset:0\n" \
  "v_mfma_f32_16x16x32_f16 v[104:107], v[80:83], v[24:27], v[104:107]\n" \
  "s_nop 0\n" \
  "v_cvt_pk_f16_f32 v88, v67, v71\n" \
  "v_cvt_pk_f16_f32 v89, v66, v70\n" \
  "v_mfma_f32_16x16x32_f16 v[108:111], v[80:83], v[28:31], v[108:111]\n" \
  "v_cvt_pk_f16_f32 v90, v65, v69\n" \
  "v_cvt_pk_f16_f32 v91, v64, v68\n" \
  "v_mfma_f32_16x16x32_f16 v[104:107], v[84:87], v[36:39], v[104:107]\n" \
  "v_cvt_pk_f16_f32 v92, v75, v79\n" \
  "v_cvt_pk_f16_f32 v93, v74, v78\n" \
  "v_mfma_f32_16x16x32_f16 v[108:111], v[84:87], v[40:43], v[108:111]\n" \
  "v_cvt_pk_f16_f32 v94, v73, v77\n" \
  "v_cvt_pk_f16_f32 v95, v72, v76\n" \
  "v_mov_b32 v9, 0x1000000\n" \
  "s_nop 1\n" \
  "ds_write_b128 v11, v[104:107] offset:15360\n" \
  "ds_add_u32 v4, v9 offset:0\n" \
  "v_mfma_f32_16x16x32_f16 v[108:111], v[88:91], v[24:27], v[108:111]\n" \
  "v_mfma_f32_16x16x32_f16 v[108:111], v[92:95], v[36:39], v[108:111]\n" \
  "v_mov_b32 v9, 0x10000000\n" \
  "s_nop 6\n" \
  "ds_write_b128 v11, v[108:111] offset:16640\n" \
  "ds_add_u32 v4, v9 offset:0\n" \
  "s_branch Lr3%=\n" \
  "Lr2%=:\n" \
  "ds_read2_b32 v[24:25], v8 offset0:96 offset1:97\n" \
  "ds_read2_b32 v[26:27], v8 offset0:98 offset1:99\n" \
  "ds_read2_b32 v[28:29], v8 offset0:112 offset1:113\n" \
  "ds_read2_b32 v[30:31], v8 offset0:114 offset1:115\n" \
  "ds_read2_b32 v[32:33], v8 offset0:128 offset1:129\n" \
  "ds_read2_b32 v[34:35], v8 offset0:130 offset1:131\n" \
  "ds_read2_b32 v[36:37], v8 offset0:144 offset1:145\n" \
  "ds_read2_b32 v[38:39], v8 offset0:146 offset1:147\n" \
  "v_add_u32 v9, 1024, v3\n" \
  "ds_read2_b32 v[124:125], v9 offset0:0 offset1:1\n" \
  "ds_read2_b32 v[126:127], v9 offset0:2 offset1:3\n" \
  "ds_read2_b32 v[20:21], v9 offset0:32 offset1:33\n" \
  "ds_read2_b32 v[22:23], v9 offset0:34 offset1:35\n" \
  "ds_read2_b32 v[12:13], v9 offset0:76 offset1:77\n" \
  "ds_read2_b32 v[14:15], v9 offset0:78 offset1:79\n" \
  "ds_read2_b32 v[16:17], v9 offset0:91 offset1:92\n" \
  "ds_read2_b32 v[18:19], v9 offset0:93 offset1:94\n" \
  "ds_read2_b32 v[40:41], v3 offset0:112 offset1:113\n" \
  "ds_read2_b32 v[42:43], v3 offset0:114 offset1:115\n" \
  "ds_read2_b32 v[44:45], v3 offset0:128 offset1:129\n" \
  "ds_read2_b32 v[46:47], v3 offset0:130 offset1:131\n" \
  "ds_read2_b32 v[48:49], v3 offset0:144 offset1:145\n" \
  "ds_read2_b32 v[50:51], v3 offset0:146 offset1:147\n" \
  "ds_read2_b32 v[52:53], v3 offset0:160 offset1:161\n" \
  "ds_read2_b32 v[54:55], v3 offset0:162 offset1:163\n" \
  "ds_read2_b32 v[56:57], v3 offset0:176 offset1:177\n" \
  "ds_read2_b32 v[58:59], v3 offset0:178 offset1:179\n" \
  "ds_read2_b32 v[60:61], v3 offset0:192 offset1:193\n" \
  "ds_read2_b32 v[62:63], v3 offset0:194 offset1:195\n" \
  "ds_read2_b32 v[64:65], v3 offset0:208 offset1:209\n" \
  "ds_read2_b32 v[66:67], v3 offset0:210 offset1:211\n" \
  "ds_read2_b32 v[68:69], v3 offset0:224 offset1:225\n" \
  "ds_read2_b32 v[70:71], v3 offset0:226 offset1:227\n" \
  "ds_read2_b32 v[72:73], v3 offset0:240 offset1:241\n" \
  "ds_read2_b32 v[74:75], v3 offset0:242 offset1:243\n" \
  "s_mov_b32 s97, 0x1fffff\n" \
  "Lgp%=:\n" \
  "ds_read_b32 v8, v4 offset:4\n" \
  "s_waitcnt lgkmcnt(0)\n" \
  "v_readfirstlane_b32 s96, v8\n" \
  "s_and_b32 s96, s96, 0xff\n" \
  "s_cmp_eq_u32 s96, 16\n" \
  "s_cbranch_scc1 Lgp%=x\n" \
  "s_sub_u32 s97, s97, 1\n" \
  "s_cbranch_scc0 Lgp%=\n" \
  "Lgp%=x:\n" \
  "s_setprio " STR(PRG) "\n" \
  "ds_read_b128 v[76:79], v10 offset:3584\n" \
  "ds_read_b128 v[80:83], v10 offset:3600\n" \
  "ds_read_b128 v[84:87], v10 offset:3072\n" \
  "ds_read_b128 v[88:91], v10 offset:3088\n" \
  "s_waitcnt lgkmcnt(3)\n" \
  "v_mfma_f32_16x16x32_f16 v[92:95], v[76:79], v[12:15], 0\n" \
  "s_waitcnt lgkmcnt(2)\n" \
  "v_mfma_f32_16x16x32_f16 v[96:99], v[80:83], v[16:19], 0\n" \
  "v_mfma_f32_16x16x32_f16 v[108:111], v[20:23], v[76:79], 0\n" \
  "ds_read_b128 v[76:79], v10 offset:2560\n" \
  "ds_read_b128 v[80:83], v10 offset:2576\n" \
  "s_nop 4\n" \
  "v_cvt_pk_f16_f32 v100, v95, v99\n" \
  "v_cvt_pk_f16_f32 v101, v94, v98\n" \
  "v_cvt_pk_f16_f32 v102, v93, v97\n" \
  "v_cvt_pk_f16_f32 v103, v92, v96\n" \
  "s_waitcnt lgkmcnt(3)\n" \
  "v_mfma_f32_16x16x32_f16 v[92:95], v[84:87], v[12:15], 0\n" \
  "s_waitcnt lgkmcnt(2)\n" \
  "v_mfma_f32_16x16x32_f16 v[96:99], v[88:91], v[16:19], 0\n" \
  "v_mfma_f32_16x16x32_f16 v[112:115], v[20:23], v[84:87], 0\n" \
  "ds_read_b128 v[84:87], v10 offset:2048\n" \
  "ds_read_b128 v[88:91], v10 offset:2064\n" \
  "v_mfma_f32_16x16x32_f16 v[108:111], v[100:103], v[24:27], v[108:111]\n" \
  "v_mfma_f32_16x16x32_f16 v[112:115], v[100:103], v[28:31], v[112:115]\n" \
  "v_mfma_f32_16x16x32_f16 v[116:119], v[100:103], v[32:35], 0\n" \
  "v_mfma_f32_16x16x32_f16 v[120:123], v[100:103], v[36:39], 0\n" \
  "ds_read2_b32 v[36:37], v3 offset0:96 offset1:97\n" \
  "ds_read2_b32 v[38:39], v3 offset0:98 offset1:99\n" \
  "v_cvt_pk_f16_f32 v104, v95, v99\n" \
  "v_cvt_pk_f16_f32 v105, v94, v98\n" \
  "v_cvt_pk_f16_f32 v106, v93, v97\n" \
  "v_cvt_pk_f16_f32 v107, v92, v96\n" \
  "s_waitcnt lgkmcnt(5)\n" \
  "v_mfma_f32_16x16x32_f16 v[92:95], v[76:79], v[12:15], 0\n" \
  "s_waitcnt lgkmcnt(4)\n" \
  "v_mfma_f32_16x16x32_f16 v[96:99], v[80:83], v[16:19], 0\n" \
  "v_mfma_f32_16x16x32_f16 v[116:119], v[20:23], v[76:79], v[116:119]\n" \
  "v_mfma_f32_16x16x32_f16 v[108:111], v[124:127], v[76:79], v[108:111]\n" \
  "ds_read_b128 v[76:79], v10 offset:2048\n" \
  "ds_read_b128 v[80:83], v10 offset:2112\n" \
  "v_mfma_f32_16x16x32_f16 v[112:115], v[104:107], v[24:27], v[112:115]\n" \
  "v_mfma_f32_16x16x32_f16 v[116:119], v[104:107], v[28:31], v[116:119]\n" \
  "v_mfma_f32_16x16x32_f16 v[120:123], v[104:107], v[32:35], v[120:123]\n" \
  "ds_read2_b32 v[32:33], v3 offset0:80 offset1:81\n" \
  "ds_read2_b32 v[34:35], v3 offset0:82 offset1:83\n" \
  "v_cvt_pk_f16_f32 v100, v95, v99\n" \
  "v_cvt_pk_f16_f32 v101, v94, v98\n" \
  "v_cvt_pk_f16_f32 v102, v93, v97\n" \
  "v_cvt_pk_f16_f32 v103, v92, v96\n" \
  "s_waitcnt lgkmcnt(7)\n" \
  "v_mfma_f32_16x16x32_f16 v[92:95], v[84:87], v[12:15], 0\n" \
  "s_waitcnt lgkmcnt(6)\n" \
  "v_mfma_f32_16x16x32_f16 v[96:99], v[88:91], v[16:19], 0\n" \
  "v_mfma_f32_16x16x32_f16 v[120:123], v[20:23], v[84:87], v[120:123]\n" \
  "ds_read_b128 v[84:87], v10 offset:2176\n" \
  "ds_read_b128 v[88:91], v10 offset:2240\n" \
  "ds_read2_b32 v[12:13], v3 offset0:0 offset1:1\n" \
  "ds_read2_b32 v[14:15], v3 offset0:2 offset1:3\n" \
  "ds_read2_b32 v[16:17], v3 offset0:16 offset1:17\n" \
  "ds_read2_b32 v[18:19], v3 offset0:18 offset1:19\n" \
  "ds_read2_b32 v[20:21], v3 offset0:32 offset1:33\n" \
  "ds_read2_b32 v[22:23], v3 offset0:34 offset1:35\n" \
  "v_mfma_f32_16x16x32_f16 v[116:119], v[100:103], v[24:27], v[116:119]\n" \
  "v_mfma_f32_16x16x32_f16 v[120:123], v[100:103], v[28:31], v[120:123]\n" \
  "ds_read2_b32 v[28:29], v3 offset0:64 offset1:65\n" \
  "ds_read2_b32 v[30:31], v3 offset0:66 offset1:67\n" \
  "v_cvt_pk_f16_f32 v104, v95, v99\n" \
  "v_cvt_pk_f16_f32 v105, v94, v98\n" \
  "v_cvt_pk_f16_f32 v106, v93, v97\n" \
  "v_cvt_pk_f16_f32 v107, v92, v96\n" \
  "s_mov_b32 s97, 0x1fffff\n" \
  "Lgq%=:\n" \
  "ds_read_b32 v8, v4 offset:4\n" \
  "s_waitcnt lgkmcnt(0)\n" \
  "v_readfirstlane_b32 s96, v8\n" \
  "s_and_b32 s96, s96, 0x400\n" \
  "s_cmp_eq_u32 s96, 0x400\n" \
  "s_cbranch_scc1 Lgq%=x\n" \
  "s_sub_u32 s97, s97, 1\n" \
  "s_cbranch_scc0 Lgq%=\n" \
  "Lgq%=x:\n" \
  "ds_read_b128 v[92:95], v10 offset:2304\n" \
  "ds_read_b128 v[96:99], v10 offset:2368\n" \
  "ds_read_b128 v[100:103], v10 offset:2432\n" \
  "v_mfma_f32_16x16x32_f16 v[120:123], v[104:107], v[24:27], v[120:123]\n" \
  "ds_read2_b32 v[24:25], v3 offset0:48 offset1:49\n" \
  "ds_read2_b32 v[26:27], v3 offset0:50 offset1:51\n" \
  "ds_read_b128 v[104:107], v10 offset:2496\n" \
  "v_mfma_f32_16x16x32_f16 v[108:111], v[44:47], v[76:79], v[108:111]\n" \
  "v_mfma_f32_16x16x32_f16 v[112:115], v[124:127], v[76:79], v[112:115]\n" \
  "ds_read_b128 v[76:79], v10 offset:1536\n" \
  "v_mfma_f32_16x16x32_f16 v[108:111], v[48:51], v[80:83], v[108:111]\n" \
  "ds_read_b128 v[80:83], v10 offset:1600\n" \
  "v_mfma_f32_16x16x32_f16 v[108:111], v[52:55], v[84:87], v[108:111]\n" \
  "ds_read_b128 v[84:87], v10 offset:1664\n" \
  "v_mfma_f32_16x16x32_f16 v[108:111], v[56:59], v[88:91], v[108:111]\n" \
  "ds_read_b128 v[88:91], v10 offset:1728\n" \
  "s_waitcnt lgkmcnt(9)\n" \
  "v_mfma_f32_16x16x32_f16 v[108:111], v[60:63], v[92:95], v[108:111]\n" \
  "ds_read_b128 v[92:95], v10 offset:1792\n" \
  "s_waitcnt lgkmcnt(9)\n" \
  "v_mfma_f32_16x16x32_f16 v[108:111], v[64:67], v[96:99], v[108:111]\n" \
  "ds_read_b128 v[96:99], v10 offset:1856\n" \
  "s_waitcnt lgkmcnt(9)\n" \
  "v_mfma_f32_16x16x32_f16 v[108:111], v[68:71], v[100:103], v[108:111]\n" \
  "ds_read_b128 v[100:103], v10 offset:1920\n" \
  "s_waitcnt lgkmcnt(7)\n" \
  "v_mfma_f32_16x16x32_f16 v[108:111], v[72:75], v[104:107], v[108:111]\n" \
  "ds_read_b128 v[104:107], v10 offset:1984\n" \
  "s_waitcnt lgkmcnt(7)\n" \
  "v_mfma_f32_16x16x32_f16 v[108:111], v[12:15], v[76:79], v[108:111]\n" \
  "v_mfma_f32_16x16x32_f16 v[112:115], v[44:47], v[76:79], v[112:115]\n" \
  "v_mfma_f32_16x16x32_f16 v[116:119], v[124:127], v[76:79], v[116:119]\n" \
  "ds_read_b128 v[76:79], v10 offset:1024\n" \
  "s_waitcnt lgkmcnt(7)\n" \
  "v_mfma_f32_16x16x32_f16 v[108:111], v[16:19], v[80:83], v[108:111]\n" \
  "v_mfma_f32_16x16x32_f16 v[112:115], v[48:51], v[80:83], v[112:115]\n" \
  "ds_read_b128 v[80:83], v10 offset:1088\n" \
  "s_waitcnt lgkmcnt(7)\n" \
  "v_mfma_f32_16x16x32_f16 v[108:111], v[20:23], v[84:87], v[108:111]\n" \
  "v_mfma_f32_16x16x32_f16 v[112:115], v[52:55], v[84:87], v[112:115]\n" \
  "ds_read_b128 v[84:87], v10 offset:1152\n" \
  "s_waitcnt lgkmcnt(7)\n" \
  "v_mfma_f32_16x16x32_f16 v[108:111], v[24:27], v[88:91], v[108:111]\n" \
  "v_mfma_f32_16x16x32_f16 v[112:115], v[56:59], v[88:91], v[112:115]\n" \
  "ds_read_b128 v[88:91], v10 offset:1216\n" \
  "s_waitcnt lgkmcnt(7)\n" \
  "v_mfma_f32_16x16x32_f16 v[108:111], v[28:31], v[92:95], v[108:111]\n" \
  "v_mfma_f32_16x16x32_f16 v[112:115], v[60:63], v[92:95], v[112:115]\n" \
  "ds_read_b128 v[92:95], v10 offset:1280\n" \
  "s_waitcnt lgkmcnt(7)\n" \
  "v_mfma_f32_16x16x32_f16 v[108:111], v[32:35], v[96:99], v[108:111]\n" \
  "v_mfma_f32_16x16x32_f16 v[112:115], v[64:67], v[96:99], v[112:115]\n" \
  "ds_read_b128 v[96:99], v10 offset:1344\n" \
  "s_waitcnt lgkmcnt(7)\n" \
  "v_mfma_f32_16x16x32_f16 v[108:111], v[36:39], v[100:103], v[108:111]\n" \
  "v_mfma_f32_16x16x32_f16 v[112:115], v[68:71], v[100:103], v[112:115]\n" \
  "ds_read_b128 v[100:103], v10 offset:1408\n" \
  "s_waitcnt lgkmcnt(7)\n" \
  "v_mfma_f32_16x16x32_f16 v[108:111], v[40:43], v[104:107], v[108:111]\n" \
  "v_mfma_f32_16x16x32_f16 v[112:115], v[72:75], v[104:107], v[112:115]\n" \
  "ds_read_b128 v[104:107], v10 offset:1472\n" \
  "s_waitcnt lgkmcnt(7)\n" \
  "v_mfma_f32_16x16x32_f16 v[112:115], v[12:15], v[76:79], v[112:115]\n" \
  "v_mfma_f32_16x16x32_f16 v[116:119], v[44:47], v[76:79], v[116:119]\n" \
  "v_mfma_f32_16x16x32_f16 v[120:123], v[124:127], v[76:79], v[120:123]\n" \
  "ds_read_b128 v[76:79], v10 offset:512\n" \
  "s_waitcnt lgkmcnt(7)\n" \
  "v_mfma_f32_16x16x32_f16 v[112:115], v[16:19], v[80:83], v[112:115]\n" \
  "v_mfma_f32_16x16x32_f16 v[116:119], v[48:51], v[80:83], v[116:119]\n" \
  "ds_read_b128 v[80:83], v10 offset:576\n" \
  "s_waitcnt lgkmcnt(7)\n" \
  "v_mfma_f32_16x16x32_f16 v[112:115], v[20:23], v[84:87], v[112:115]\n" \
  "v_mov_b32 v9, 0x10000\n" \
  "ds_write_b128 v11, v[108:111] offset:5120\n" \
  "ds_add_u32 v4, v9 offset:0\n" \
  "v_mfma_f32_16x16x32_f16 v[116:119], v[52:55], v[84:87], v[116:119]\n" \
  "ds_read_b128 v[84:87], v10 offset:640\n" \
  "s_waitcnt lgkmcnt(9)\n" \
  "v_mfma_f32_16x16x32_f16 v[112:115], v[24:27], v[88:91], v[112:115]\n" \
  "v_mfma_f32_16x16x32_f16 v[116:119], v[56:59], v[88:91], v[116:119]\n" \
  "ds_read_b128 v[88:91], v10 offset:704\n" \
  "s_waitcnt lgkmcnt(9)\n" \
  "v_mfma_f32_16x16x32_f16 v[112:115], v[28:31], v[92:95], v[112:115]\n" \
  "v_mfma_f32_16x16x32_f16 v[116:119], v[60:63], v[92:95], v[116:119]\n" \
  "ds_read_b128 v[92:95], v10 offset:768\n" \
  "s_waitcnt lgkmcnt(9)\n" \
  "v_mfma_f32_16x16x32_f16 v[112:115], v[32:35], v[96:99], v[112:115]\n" \
  "v_mfma_f32_16x16x32_f16 v[116:119], v[64:67], v[96:99], v[116:119]\n" \
  "ds_read_b128 v[96:99], v10 offset:832\n" \
  "s_waitcnt lgkmcnt(9)\n" \
  "v_mfma_f32_16x16x32_f16 v[112:115], v[36:39], v[100:103], v[112:115]\n" \
  "v_mfma_f32_16x16x32_f16 v[116:119], v[68:71], v[100:103], v[116:119]\n" \
  "ds_read_b128 v[100:103], v10 offset:896\n" \
  "s_waitcnt lgkmcnt(9)\n" \
  "v_mfma_f32_16x16x32_f16 v[112:115], v[40:43], v[104:107], v[112:115]\n" \
  "v_mfma_f32_16x16x32_f16 v[116:119], v[72:75], v[104:107], v[116:119]\n" \
  "ds_read_b128 v[104:107], v10 offset:960\n" \
  "s_waitcnt lgkmcnt(9)\n" \
  "v_mfma_f32_16x16x32_f16 v[116:119], v[12:15], v[76:79], v[116:119]\n" \
  "v_mfma_f32_16x16x32_f16 v[120:123], v[44:47], v[76:79], v[120:123]\n" \
  "ds_read_b128 v[76:79], v10 offset:0\n" \
  "s_waitcnt lgkmcnt(9)\n" \
  "v_mfma_f32_16x16x32_f16 v[116:119], v[16:19], v[80:83], v[116:119]\n" \
  "v_mfma_f32_16x16x32_f16 v[120:123], v[48:51], v[80:83], v[120:123]\n" \
  "ds_read_b128 v[80:83], v10 offset:64\n" \
  "s_waitcnt lgkmcnt(7)\n" \
  "v_mfma_f32_16x16x32_f16 v[116:119], v[20:23], v[84:87], v[116:119]\n" \
  "v_mov_b32 v9, 0x100000\n" \
  "ds_write_b128 v11, v[112:115] offset:6400\n" \
  "ds_add_u32 v4, v9 offset:0\n" \
  "v_mfma_f32_16x16x32_f16 v[120:123], v[52:55], v[84:87], v[120:123]\n" \
  "ds_read_b128 v[84:87], v10 offset:128\n" \
  "s_waitcnt lgkmcnt(9)\n" \
  "v_mfma_f32_16x16x32_f16 v[116:119], v[24:27], v[88:91], v[116:119]\n" \
  "v_mfma_f32_16x16x32_f16 v[120:123], v[56:59], v[88:91], v[120:123]\n" \
  "ds_read_b128 v[88:91], v10 offset:192\n" \
  "s_waitcnt lgkmcnt(9)\n" \
  "v_mfma_f32_16x16x32_f16 v[116:119], v[28:31], v[92:95], v[116:119]\n" \
  "v_mfma_f32_16x16x32_f16 v[120:123], v[60:63], v[92:95], v[120:123]\n" \
  "ds_read_b128 v[92:95], v10 offset:256\n" \
  "s_waitcnt lgkmcnt(9)\n" \
  "v_mfma_f32_16x16x32_f16 v[116:119], v[32:35], v[96:99], v[116:119]\n" \
  "v_mfma_f32_16x16x32_f16 v[120:123], v[64:67], v[96:99], v[120:123]\n" \
  "ds_read_b128 v[96:99], v10 offset:320\n" \
  "s_waitcnt lgkmcnt(9)\n" \
  "v_mfma_f32_16x16x32_f16 v[116:119], v[36:39], v[100:103], v[116:119]\n" \
  "v_mfma_f32_16x16x32_f16 v[120:123], v[68:71], v[100:103], v[120:123]\n" \
  "ds_read_b128 v[100:103], v10 offset:384\n" \
  "s_waitcnt lgkmcnt(9)\n" \
  "v_mfma_f32_16x16x32_f16 v[116:119], v[40:43], v[104:107], v[116:119]\n" \
  "v_mfma_f32_16x16x32_f16 v[120:123], v[72:75], v[104:107], v[120:123]\n" \
  "ds_read_b128 v[104:107], v10 offset:448\n" \
  "s_waitcnt lgkmcnt(9)\n" \
  "v_mfma_f32_16x16x32_f16 v[120:123], v[12:15], v[76:79], v[120:123]\n" \
  "s_waitcnt lgkmcnt(8)\n" \
  "v_mfma_f32_16x16x32_f16 v[120:123], v[16:19], v[80:83], v[120:123]\n" \
  "s_waitcnt lgkmcnt(5)\n" \
  "v_mfma_f32_16x16x32_f16 v[120:123], v[20:23], v[84:87], v[120:123]\n" \
  "v_mov_b32 v9, 0x1000000\n" \
  "ds_write_b128 v11, v[116:119] offset:7680\n" \
  "ds_add_u32 v4, v9 offset:0\n" \
  "s_waitcnt lgkmcnt(6)\n" \
  "v_mfma_f32_16x16x32_f16 v[120:123], v[24:27], v[88:91], v[120:123]\n" \
  "s_waitcnt lgkmcnt(5)\n" \
  "v_mfma_f32_16x16x32_f16 v[120:123], v[28:31], v[92:95], v[120:123]\n" \
  "s_waitcnt lgkmcnt(4)\n" \
  "v_mfma_f32_16x16x32_f16 v[120:123], v[32:35], v[96:99], v[120:123]\n" \
  "s_waitcnt lgkmcnt(3)\n" \
  "v_mfma_f32_16x16x32_f16 v[120:123], v[36:39], v[100:103], v[120:123]\n" \
  "s_waitcnt lgkmcnt(2)\n" \
  "v_mfma_f32_16x16x32_f16 v[120:123], v[40:43], v[104:107], v[120:123]\n" \
  "v_mov_b32 v9, 0x10000000\n" \
  "s_nop 6\n" \
  "ds_write_b128 v11, v[120:123] offset:8960\n" \
  "ds_add_u32 v4, v9 offset:0\n" \
  "s_branch Lr3%=\n" \
  "Lr4%=:\n" \
  "s_load_dwordx4 s[40:43], %[k], 0x40\n" \
  "s_load_dwordx2 s[44:45], %[k], 0x50\n" \
  "s_and_b32 s46, %[b], 7\n" \
  "s_lshr_b32 s47, %[b], 3\n" \
  "s_mul_i32 s47, s47, 3\n" \
  "s_add_u32 s47, s47, s50\n" \
  "v_and_b32 v9, 63, %[t]\n" \
  "s_cmpk_ge_u32 %[b], 128\n" \
  "s_cbranch_scc1 Lq0%=\n" \
  "s_mul_i32 s47, s47, 60\n" \
  "v_min_u32 v9, 59, v9\n" \
  "v_add_u32 v9, s47, v9\n" \
  "v_mul_u32_u24 v5, 7282, v9\n" \
  "v_lshrrev_b32 v5, 16, v5\n" \
  "v_mul_u32_u24 v5, 8064, v5\n" \
  "v_lshl_add_u32 v5, v9, 7, v5\n" \
  "s_mul_i32 s46, s46, 1152\n" \
  "s_add_u32 s46, s46, 2359296\n" \
  "v_add_u32 v5, s46, v5\n" \
  "s_mov_b32 s97, 0x1fffff\n" \
  "Lkr%=:\n" \
  "ds_read_b32 v17, v4 offset:4\n" \
  "s_waitcnt lgkmcnt(0)\n" \
  "v_readfirstlane_b32 s96, v17\n" \
  "s_and_b32 s96, s96, " STR(PFG) "\n" \
  "s_cmp_eq_u32 s96, " STR(PFG) "\n" \
  "s_cbranch_scc1 Lkr%=x\n" \
  "s_sleep 2\n" \
  "s_sub_u32 s97, s97, 1\n" \
  "s_cbranch_scc0 Lkr%=\n" \
  "Lkr%=x:\n" \
  ".if " STR(PFW) " & 1\n" \
  "global_load_dword v126, v5, s[40:41]" PFT "\n" \
  "global_load_dword v126, v5, s[42:43]" PFT "\n" \
  ".endif\n" \
  "s_branch Lr3%=\n" \
  "Lq0%=:\n" \
  "s_sub_u32 s47, s47, 48\n" \
  "s_mul_i32 s48, s46, 5184\n" \
  "s_mul_i32 s49, s47, 216\n" \
  "s_add_u32 s48, s48, s49\n" \
  "v_min_u32 v5, 53, v9\n" \
  "v_add_lshl_u32 v5, v5, s48, 7\n" \
  "s_mul_i32 s47, s47, 11\n" \
  "v_min_u32 v9, 21, v9\n" \
  "v_lshrrev_b32 v6, 1, v9\n" \
  "v_add_u32 v6, s47, v6\n" \
  "v_mul_u32_u24 v6, 9216, v6\n" \
  "v_and_b32 v9, 1, v9\n" \
  "s_waitcnt lgkmcnt(0)\n" \
  "s_lshr_b32 s47, s40, 7\n" \
  "s_add_u32 s47, s47, s46\n" \
  "s_sub_u32 s47, 3, s47\n" \
  "s_and_b32 s47, s47, 7\n" \
  "s_sub_u32 s48, 8, s47\n" \
  "v_mul_u32_u24 v9, s48, v9\n" \
  "s_mul_i32 s46, s46, 9\n" \
  "s_add_u32 s46, s46, s47\n" \
  "v_add_lshl_u32 v9, v9, s46, 7\n" \
  "v_add_u32 v6, v6, v9\n" \
  "s_mov_b32 s97, 0x1fffff\n" \
  "Lks%=:\n" \
  "ds_read_b32 v17, v4 offset:4\n" \
  "s_waitcnt lgkmcnt(0)\n" \
  "v_readfirstlane_b32 s96, v17\n" \
  "s_and_b32 s96, s96, " STR(PFG) "\n" \
  "s_cmp_eq_u32 s96, " STR(PFG) "\n" \
  "s_cbranch_scc1 Lks%=x\n" \
  "s_sleep 2\n" \
  "s_sub_u32 s97, s97, 1\n" \
  "s_cbranch_scc0 Lks%=\n" \
  "Lks%=x:\n" \
  ".if " STR(PFW) " & 2\n" \
  "global_load_dword v126, v6, s[40:41]\n" \
  "global_load_dword v126, v6, s[42:43]\n" \
  "s_waitcnt vmcnt(2)\n" \
  "global_load_dword v126, v5, s[44:45]" PFT "\n" \
  "v_add_u32 v5, 6912, v5\n" \
  "s_waitcnt vmcnt(" STR(PFD) ")\n" \
  "global_load_dword v126, v5, s[44:45]" PFT "\n" \
  "v_add_u32 v5, 6912, v5\n" \
  "s_waitcnt vmcnt(" STR(PFD) ")\n" \
  "global_load_dword v126, v5, s[44:45]" PFT "\n" \
  "v_add_u32 v5, 6912, v5\n" \
  "s_waitcnt vmcnt(" STR(PFD) ")\n" \
  "global_load_dword v126, v5, s[44:45]" PFT "\n" \
  "v_add_u32 v5, 6912, v5\n" \
  ".endif\n" \
  "Lr3%=:\n" \
  GSTAMP7 \
  "s_setprio 0\n"

#define SW_HEAD \
  "s_load_dwordx2 s[46:47], %[k], 0x0\n" \
  "s_load_dwordx2 s[48:49], %[k], 0x30\n" \
  "s_load_dwordx2 s[40:41], %[k], 0x70\n" \
  "s_setprio 3\n" \
  "s_and_b32 s50, %[w], 3\n" \
  "s_mul_i32 s51, %[b], 9216\n" \
  "s_mul_i32 s52, s50, 288\n" \
  "s_add_u32 s51, s51, s52\n" \
  "v_bfe_u32 v1, %[t], 3, 3\n" \
  "v_and_b32 v2, 7, %[t]\n" \
  "v_mul_u32_u24 v3, 1152, v1\n" \
  "v_mul_u32_u24 v4, 12, v2\n" \
  "v_add3_u32 v3, v3, v4, s51\n" \
  "v_and_b32 v5, 255, %[t]\n" \
  "v_mov_b32 v6, 158464\n" \
  "v_lshl_add_u32 v6, v5, 3, v6\n" \
  "v_mov_b64 v[8:9], 0\n" \
  "ds_write_b64 v6, v[8:9]\n" \
  "s_mov_b32 s42, 10403840\n" \
  "s_mov_b32 s43, 0x20000\n" \
  "s_mul_i32 s52, %[b], 6\n" \
  "s_and_b32 s52, s52, -4\n" \
  "s_cmp_lt_u32 %[b], 128\n" \
  SSTAMP1 \
  "s_waitcnt lgkmcnt(0)\n" \
  "global_load_dwordx3 v[16:18], v3, s[46:47] offset:0\n" \
  "global_load_dwordx3 v[20:22], v3, s[46:47] offset:96\n" \
  "global_load_dwordx3 v[24:26], v3, s[46:47] offset:192\n" \
  "buffer_load_dword v7, off, s[40:43], 0 sc1\n" \
  "s_load_dwordx2 s[54:55], s[48:49], s52\n" \
  "s_memtime s[58:59]\n" \
  "s_cbranch_scc1 Lone%=\n" \
  "v_add_u32 v3, 589824, v3\n" \
  "global_load_dwordx3 v[28:30], v3, s[46:47] offset:0\n" \
  "global_load_dwordx3 v[32:34], v3, s[46:47] offset:96\n" \
  "global_load_dwordx3 v[36:38], v3, s[46:47] offset:192\n" \
  "Lone%=:\n" \
  "s_barrier\n"

#define SW_BODY \
  "s_mul_i32 s51, s50, 3200\n" \
  "s_add_u32 s51, s51, 125088\n" \
  "v_mul_u32_u24 v3, 400, v1\n" \
  "v_lshlrev_b32 v4, 4, v2\n" \
  "v_add3_u32 v3, v3, v4, s51\n" \
  "v_and_b32 v10, 3, %[t]\n" \
  "v_mul_u32_u24 v4, 800, v10\n" \
  "v_lshl_add_u32 v4, v1, 4, v4\n" \
  "v_add_u32 v4, s51, v4\n" \
  "v_and_b32 v11, 4, %[t]\n" \
  "v_lshl_add_u32 v6, v11, 1, v4\n" \
  "v_mul_u32_u24 v12, 8421504, v11\n" \
  "v_lshrrev_b32 v13, 1, v11\n" \
  "v_add_u32 v12, v12, v13\n" \
  "v_add_u32 v12, 0x05040100, v12\n" \
  "s_lshr_b32 s51, %[b], 1\n" \
  "s_mul_i32 s51, s51, 24704\n" \
  "s_and_b32 s52, %[b], 1\n" \
  "s_lshl_b32 s52, s52, 6\n" \
  "s_add_u32 s51, s51, s52\n" \
  "s_mul_i32 s52, s50, 6144\n" \
  "s_add_u32 s51, s51, s52\n" \
  "s_add_u32 s51, s51, 7241728\n" \
  "v_bfe_u32 v13, %[t], 2, 4\n" \
  "v_lshlrev_b32 v13, 7, v13\n" \
  "v_lshl_add_u32 v13, v10, 4, v13\n" \
  "v_add_u32 v13, s51, v13\n" \
  "s_lshl4_add_u32 s51, s50, 158464\n" \
  "v_lshl_add_u32 v14, v1, 6, s51\n" \
  "v_mov_b32 v42, 1\n" \
  "s_mov_b32 s53, 3\n" \
  "s_movk_i32 s56, 0x1000\n" \
  "s_mov_b32 s57, 0\n" \
  "s_waitcnt vmcnt(0) lgkmcnt(0)\n" \
  SSTAMP2 \
  "v_readfirstlane_b32 s44, v7\n" \
  "Lun%=:\n" \
  "ds_write_b96 v3, v[16:18] offset:0\n" \
  "ds_write_b96 v3, v[20:22] offset:128\n" \
  "ds_write_b96 v3, v[24:26] offset:256\n" \
  "ds_read2_b32 v[44:45], v6 offset0:0 offset1:100\n" \
  "ds_read2_b32 v[56:57], v4 offset0:1 offset1:101\n" \
  "ds_read2_b32 v[48:49], v6 offset0:32 offset1:132\n" \
  "ds_read2_b32 v[58:59], v4 offset0:33 offset1:133\n" \
  "ds_read2_b32 v[52:53], v6 offset0:64 offset1:164\n" \
  "ds_read2_b32 v[60:61], v4 offset0:65 offset1:165\n" \
  "v_mov_b64 v[40:41], 0\n" \
  "s_add_u32 s45, s44, 1\n" \
  "v_dot2c_f32_bf16 v40, 0x3f803f80, v16\n" \
  "v_dot2c_f32_bf16 v41, v16, v16\n" \
  "v_dot2c_f32_bf16 v40, 0x3f803f80, v17\n" \
  "v_dot2c_f32_bf16 v41, v17, v17\n" \
  "v_dot2c_f32_bf16 v40, 0x3f803f80, v18\n" \
  "v_dot2c_f32_bf16 v41, v18, v18\n" \
  "v_dot2c_f32_bf16 v40, 0x3f803f80, v20\n" \
  "v_dot2c_f32_bf16 v41, v20, v20\n" \
  "v_dot2c_f32_bf16 v40, 0x3f803f80, v21\n" \
  "v_dot2c_f32_bf16 v41, v21, v21\n" \
  "v_dot2c_f32_bf16 v40, 0x3f803f80, v22\n" \
  "v_dot2c_f32_bf16 v41, v22, v22\n" \
  "v_dot2c_f32_bf16 v40, 0x3f803f80, v24\n" \
  "v_dot2c_f32_bf16 v41, v24, v24\n" \
  "v_dot2c_f32_bf16 v40, 0x3f803f80, v25\n" \
  "v_dot2c_f32_bf16 v41, v25, v25\n" \
  "v_dot2c_f32_bf16 v40, 0x3f803f80, v26\n" \
  "v_dot2c_f32_bf16 v41, v26, v26\n" \
  "v_mov_b32 v47, s45\n" \
  "v_mov_b32 v51, s45\n" \
  "v_mov_b32 v55, s45\n" \
  "s_waitcnt lgkmcnt(0)\n" \
  "v_perm_b32 v46, v57, v56, v12\n" \
  "v_add_f32_dpp v40, v40, v40 quad_perm:[1,0,3,2] row_mask:0xf bank_mask:0xf bound_ctrl:1\n" \
  "v_add_f32_dpp v41, v41, v41 quad_perm:[1,0,3,2] row_mask:0xf bank_mask:0xf bound_ctrl:1\n" \
  "buffer_store_dwordx4 v[44:47], v13, s[40:43], 0 offen sc1\n" \
  "v_perm_b32 v50, v59, v58, v12\n" \
  "v_add_f32_dpp v40, v40, v40 quad_perm:[2,3,0,1] row_mask:0xf bank_mask:0xf bound_ctrl:1\n" \
  "v_add_f32_dpp v41, v41, v41 quad_perm:[2,3,0,1] row_mask:0xf bank_mask:0xf bound_ctrl:1\n" \
  "buffer_store_dwordx4 v[48:51], v13, s[40:43], 0 offen offset:2048 sc1\n" \
  "v_perm_b32 v54, v61, v60, v12\n" \
  "v_add_f32_dpp v40, v40, v40 row_half_mirror row_mask:0xf bank_mask:0xf bound_ctrl:1\n" \
  "v_add_f32_dpp v41, v41, v41 row_half_mirror row_mask:0xf bank_mask:0xf bound_ctrl:1\n" \
  "buffer_store_dwordx4 v[52:55], v13, s[40:43], s56 offen sc1\n" \
  "ds_write_b96 v14, v[40:42]\n" \
  SSTAMP3 \
  "s_cmp_lg_u32 s50, s53\n" \
  "s_cbranch_scc1 Lsn%=\n" \
  "v_and_b32 v15, 31, %[t]\n" \
  "s_add_u32 s51, s57, 158464\n" \
  "v_lshl_add_u32 v15, v15, 4, s51\n" \
  "v_mov_b32 v43, 158208\n" \
  "ds_write_b32 v43, v55\n" \
  "s_movk_i32 s97, 0x7fff\n" \
  "Lsp%=:\n" \
  "ds_read_b96 v[56:58], v15\n" \
  "s_waitcnt lgkmcnt(0)\n" \
  "v_cmp_ne_u32 vcc, 1, v58\n" \
  "s_cbranch_vccz Lsq%=\n" \
  "s_sub_u32 s97, s97, 1\n" \
  "s_cbranch_scc0 Lsp%=\n" \
  "Lsq%=:\n" \
  "v_add_f32_dpp v56, v56, v56 quad_perm:[1,0,3,2] row_mask:0xf bank_mask:0xf bound_ctrl:1\n" \
  "v_add_f32_dpp v57, v57, v57 quad_perm:[1,0,3,2] row_mask:0xf bank_mask:0xf bound_ctrl:1\n" \
  "s_lshl_b32 s51, %[b], 6\n" \
  "v_add_f32_dpp v56, v56, v56 quad_perm:[2,3,0,1] row_mask:0xf bank_mask:0xf bound_ctrl:1\n" \
  "v_add_f32_dpp v57, v57, v57 quad_perm:[2,3,0,1] row_mask:0xf bank_mask:0xf bound_ctrl:1\n" \
  "v_mul_f32 v56, 0x3ae38e39, v56\n" \
  "v_mul_f32 v57, 0x3ae38e39, v57\n" \
  "v_fma_f32 v57, -v56, v56, v57\n" \
  "v_max_f32 v57, 0, v57\n" \
  "v_cvt_pk_bf16_f32 v57, 0, v57\n" \
  "v_add_f32 v57, 0x3727c5ac, v57\n" \
  "v_cvt_pk_bf16_f32 v57, 0, v57\n" \
  "v_sqrt_f32 v57, v57\n" \
  "v_mov_b32_dpp v58, v56 row_shl:4 row_mask:0xf bank_mask:0xf bound_ctrl:1\n" \
  "v_cvt_pk_bf16_f32 v57, 0, v57\n" \
  "v_rcp_f32 v60, v57\n" \
  "v_cvt_pk_bf16_f32 v62, v56, v58\n" \
  "v_mov_b32 v63, s45\n" \
  "s_lshl3_add_u32 s51, s57, s51\n" \
  "v_mov_b32_dpp v61, v60 row_shl:4 row_mask:0xf bank_mask:0xf bound_ctrl:1\n" \
  "s_add_u32 s51, s51, 119808\n" \
  "v_lshlrev_b32 v15, 4, v1\n" \
  "s_mov_b32 exec_lo, 0x01010101\n" \
  "s_mov_b32 exec_hi, 0\n" \
  "buffer_store_dwordx4 v[60:63], v15, s[40:43], s51 offen sc1\n" \
  "s_mov_b64 exec, -1\n" \
  "Lsn%=:\n" \
  "s_cmp_lt_u32 %[b], 128\n" \
  "s_cbranch_scc1 Lsd%=\n" \
  "s_cmp_eq_u32 s53, 2\n" \
  "s_cbranch_scc1 Lsd%=\n" \
  "s_mov_b32 s53, 2\n" \
  "s_movk_i32 s57, 512\n" \
  "v_add_u32 v13, 790528, v13\n" \
  "v_add_u32 v14, 512, v14\n" \
  "v_mov_b64 v[16:17], v[28:29]\n" \
  "v_mov_b32 v18, v30\n" \
  "v_mov_b64 v[20:21], v[32:33]\n" \
  "v_mov_b32 v22, v34\n" \
  "v_mov_b64 v[24:25], v[36:37]\n" \
  "v_mov_b32 v26, v38\n" \
  "s_branch Lun%=\n" \
  "Lsd%=:\n" \
  SSTAMP4 \
  "s_setprio 0\n" \
  "s_mov_b32 s44, s45\n" \
  "v_and_b32 v7, 63, %[t]\n" \
  "v_min_u32 v111, 1, v7\n" \
  "v_mul_u32_u24 v111, 40, v111\n" \
  "v_add_u32 v111, v111, v7\n" \
  "v_mov_b32 v8, 159488\n" \
  "v_lshl_add_u32 v111, v111, 2, v8\n" \
  "v_lshlrev_b32 v8, 2, v5\n" \
  "v_sub_u32 v105, 97308, v8\n" \
  "v_lshrrev_b32 v8, 3, v5\n" \
  "v_mul_u32_u24 v106, 24704, v8\n" \
  "v_lshl_add_u32 v106, v2, 4, v106\n" \
  "s_lshl_b32 s51, %[b], 7\n" \
  "s_add_u32 s51, s51, 7241728\n" \
  "v_add_u32 v106, s51, v106\n" \
  "v_lshlrev_b32 v107, 4, v5\n" \
  "s_mul_i32 s51, %[b], 16512\n" \
  "s_add_u32 s51, s51, 139264\n" \
  "v_add_u32 v117, s51, v107\n" \
  "v_add_u32 v107, 119808, v107\n" \
  "v_and_b32 v8, 15, v8\n" \
  "v_bfe_u32 v9, v5, 1, 2\n" \
  "v_mul_u32_u24 v9, 320, v9\n" \
  "v_lshl_add_u32 v115, v8, 4, v9\n" \
  "v_and_b32 v9, 1, v5\n" \
  "v_lshl_add_u32 v115, v9, 3, v115\n" \
  "s_lshr_b32 s45, s50, 1\n" \
  "s_mul_i32 s51, s45, 1280\n" \
  "v_add_u32 v115, s51, v115\n" \
  "v_add_u32 v116, 64192, v115\n" \
  "s_lshl_b32 s45, s45, 2\n" \
  "s_and_b32 s51, %[b], 1\n" \
  "v_min_u32 v8, 2, v1\n" \
  "v_add_u32 v8, s51, v8\n" \
  "v_cmp_ne_u32 vcc, 3, v8\n" \
  "v_min_u32 v9, 2, v10\n" \
  "v_mul_u32_u24 v9, 27648, v9\n" \
  "v_cndmask_b32 v8, 0, v8, vcc\n" \
  "v_lshl_add_u32 v9, v8, 3, v9\n" \
  "s_mul_i32 s52, %[b], 24\n" \
  "s_add_u32 s52, s52, 4096\n" \
  "v_add_u32 v9, s52, v9\n" \
  "v_mul_u32_u24 v108, 1152, v11\n" \
  "v_add_u32 v108, v108, v9\n" \
  "v_add_u32 v114, 9216, v9\n" \
  "v_add_lshl_u32 v8, v8, s51, 4\n" \
  "v_lshrrev_b64 v[8:9], v8, s[54:55]\n" \
  "v_cmp_ne_u32 vcc, 3, v10\n" \
  "v_lshlrev_b32 v109, 16, v8\n" \
  "s_nop 0\n" \
  "v_cndmask_b32 v110, 0, 1.0, vcc\n" \
  "s_movk_i32 s97, 64\n" \
  "Lxw%=:\n" \
  "s_memtime s[46:47]\n" \
  "s_waitcnt lgkmcnt(0)\n" \
  "s_sub_u32 s46, s46, s58\n" \
  "s_cmp_gt_u32 s46, " STR(XT) "\n" \
  "s_cbranch_scc1 Lxv%=\n" \
  "s_sleep 1\n" \
  "s_sub_u32 s97, s97, 1\n" \
  "s_cbranch_scc0 Lxw%=\n" \
  "Lxv%=:\n" \
  "s_setprio " STR(PRH) "\n" \
  SSTAMP5 \
  "buffer_load_dwordx2 v[102:103], v108, s[40:43], 0 offen sc1\n" \
  "s_mov_b32 s94, 0\n" \
  "s_mov_b32 s95, 0\n" \
  "buffer_load_dwordx4 v[64:67], v106, s[40:43], s94 offen sc1\n" \
  "buffer_load_dwordx4 v[68:71], v107, s[40:43], s95 offen sc1\n" \
  "s_mov_b32 s94, 790528\n" \
  "s_mov_b32 s95, 4096\n" \
  "buffer_load_dwordx4 v[72:75], v106, s[40:43], s94 offen sc1\n" \
  "buffer_load_dwordx4 v[76:79], v107, s[40:43], s95 offen sc1\n" \
  "s_waitcnt vmcnt(4)\n" \
  SSTAMP6 \
  "v_cmp_ne_u32 vcc, s44, v103\n" \
  "s_cmp_lg_u64 vcc, 0\n" \
  "s_cbranch_scc1 Lms%=\n" \
  "Lmb%=:\n" \
  "v_mul_f32 v102, v102, v110\n" \
  "v_mov_b32 v104, 226\n" \
  "s_mov_b32 s94, 0x42000000\n" \
  "v_add_f32_dpp v102, v102, v102 quad_perm:[1,0,3,2] row_mask:0xf bank_mask:0xf bound_ctrl:1\n" \
  "v_mov_b32 v112, 1\n" \
  "s_nop 0\n" \
  "v_add_f32_dpp v102, v102, v102 quad_perm:[2,3,0,1] row_mask:0xf bank_mask:0xf bound_ctrl:1\n" \
  "v_cvt_pk_bf16_f32 v102, 0, v102\n" \
  "s_nop 1\n" \
  "v_mov_b32_dpp v103, v102 row_shl:4 row_mask:0xf bank_mask:0xf bound_ctrl:1\n" \
  "v_add_f32 v103, 1.0, v103\n" \
  "v_cvt_pk_bf16_f32 v103, 0, v103\n" \
  "v_fma_f32 v96, |v103|, s94, |v102|\n" \
  "v_lshrrev_b32 v96, 23, v96\n" \
  "v_med3_u32 v96, v96, 26, v104\n" \
  "v_lshlrev_b32 v96, 23, v96\n" \
  "v_sub_u32 v97, 0x84000000, v96\n" \
  "v_add_u32 v104, 0xfb000000, v96\n" \
  "v_mul_f32 v103, v103, v97\n" \
  "v_mul_f32 v102, v102, v97\n" \
  "v_mul_f32 v113, v109, v104\n" \
  "v_readlane_b32 s84, v103, 0\n" \
  "v_readlane_b32 s87, v102, 0\n" \
  "v_readlane_b32 s85, v103, 8\n" \
  "v_readlane_b32 s88, v102, 8\n" \
  "v_readlane_b32 s86, v103, 16\n" \
  "v_readlane_b32 s89, v102, 16\n" \
  "v_mov_b32 v96, s84\n" \
  "v_mov_b32 v97, s87\n" \
  "v_mov_b32 v98, s85\n" \
  "v_mov_b32 v99, s88\n" \
  "v_mov_b32 v100, s86\n" \
  "v_mov_b32 v101, s89\n" \
  "v_readlane_b32 s84, v113, 0\n" \
  "v_readlane_b32 s87, v104, 0\n" \
  "v_readlane_b32 s85, v113, 8\n" \
  "v_readlane_b32 s88, v104, 8\n" \
  "v_readlane_b32 s86, v113, 16\n" \
  "v_readlane_b32 s89, v104, 16\n" \
  "s_waitcnt vmcnt(2)\n" \
  "v_cmp_ne_u32 vcc, s44, v67\n" \
  "v_cmp_ne_u32 s[94:95], s44, v71\n" \
  "s_or_b64 vcc, vcc, s[94:95]\n" \
  "s_cbranch_scc1 Lu0s%=\n" \
  "Lu0b%=:\n" \
  "v_lshlrev_b32 v102, 16, v70\n" \
  "v_and_b32 v103, 0xffff0000, v70\n" \
  "v_lshlrev_b32 v16, 16, v64\n" \
  "v_lshlrev_b32 v17, 16, v65\n" \
  "v_and_b32 v18, 0xffff0000, v64\n" \
  "v_and_b32 v19, 0xffff0000, v65\n" \
  "v_lshlrev_b32 v20, 16, v66\n" \
  "v_and_b32 v21, 0xffff0000, v66\n" \
  "v_pk_add_f32 v[40:41], v[16:17], v[102:103] neg_lo:[0,1] neg_hi:[0,1]\n" \
  "v_pk_add_f32 v[42:43], v[18:19], v[102:103] neg_lo:[0,1] neg_hi:[0,1]\n" \
  "v_pk_add_f32 v[44:45], v[20:21], v[102:103] neg_lo:[0,1] neg_hi:[0,1]\n" \
  "v_cvt_pk_bf16_f32 v40, 0, v40\n" \
  "v_cvt_pk_bf16_f32 v41, 0, v41\n" \
  "v_cvt_pk_bf16_f32 v42, 0, v42\n" \
  "v_cvt_pk_bf16_f32 v43, 0, v43\n" \
  "v_cvt_pk_bf16_f32 v44, 0, v44\n" \
  "v_cvt_pk_bf16_f32 v45, 0, v45\n" \
  "v_pk_mul_f32 v[40:41], v[40:41], v[68:69]\n" \
  "v_pk_mul_f32 v[42:43], v[42:43], v[68:69]\n" \
  "v_pk_mul_f32 v[44:45], v[44:45], v[68:69]\n" \
  "v_cvt_pk_bf16_f32 v40, 0, v40\n" \
  "v_cvt_pk_bf16_f32 v41, 0, v41\n" \
  "v_cvt_pk_bf16_f32 v42, 0, v42\n" \
  "v_cvt_pk_bf16_f32 v43, 0, v43\n" \
  "v_cvt_pk_bf16_f32 v44, 0, v44\n" \
  "v_cvt_pk_bf16_f32 v45, 0, v45\n" \
  "v_pk_mul_f32 v[40:41], v[40:41], v[96:97] op_sel_hi:[1,0]\n" \
  "v_pk_mul_f32 v[42:43], v[42:43], v[98:99] op_sel_hi:[1,0]\n" \
  "v_pk_mul_f32 v[44:45], v[44:45], v[100:101] op_sel_hi:[1,0]\n" \
  "v_cvt_pk_bf16_f32 v40, 0, v40\n" \
  "v_cvt_pk_bf16_f32 v41, 0, v41\n" \
  "v_cvt_pk_bf16_f32 v42, 0, v42\n" \
  "v_cvt_pk_bf16_f32 v43, 0, v43\n" \
  "v_cvt_pk_bf16_f32 v44, 0, v44\n" \
  "v_cvt_pk_bf16_f32 v45, 0, v45\n" \
  "v_pk_add_f32 v[40:41], v[40:41], v[96:97] op_sel:[0,1]\n" \
  "v_pk_add_f32 v[42:43], v[42:43], v[98:99] op_sel:[0,1]\n" \
  "v_pk_add_f32 v[44:45], v[44:45], v[100:101] op_sel:[0,1]\n" \
  "v_cvt_pk_bf16_f32 v40, 0, v40\n" \
  "v_cvt_pk_bf16_f32 v41, 0, v41\n" \
  "v_cvt_pk_bf16_f32 v42, 0, v42\n" \
  "v_cvt_pk_bf16_f32 v43, 0, v43\n" \
  "v_cvt_pk_bf16_f32 v44, 0, v44\n" \
  "v_cvt_pk_bf16_f32 v45, 0, v45\n" \
  "v_cvt_pkrtz_f16_f32 v102, v41, v40\n" \
  "v_cvt_pkrtz_f16_f32 v103, v43, v42\n" \
  "v_cvt_pkrtz_f16_f32 v104, v45, v44\n" \
  "ds_write_b32 v105, v102 offset:3072\n" \
  "ds_write_b32 v105, v103 offset:11264\n" \
  "ds_write_b32 v105, v104 offset:19456\n" \
  SSTAMP7 \
  "s_mov_b32 s94, 1581056\n" \
  "s_mov_b32 s95, 8192\n" \
  "buffer_load_dwordx4 v[80:83], v106, s[40:43], s94 offen sc1\n" \
  "buffer_load_dwordx4 v[84:87], v107, s[40:43], s95 offen sc1\n" \
  "s_mov_b32 s94, 2371584\n" \
  "s_mov_b32 s95, 12288\n" \
  "buffer_load_dwordx4 v[88:91], v106, s[40:43], s94 offen sc1\n" \
  "buffer_load_dwordx4 v[92:95], v107, s[40:43], s95 offen sc1\n" \
  "buffer_load_dwordx2 v[118:119], v114, s[40:43], 0 offen sc1\n" \
  "s_waitcnt vmcnt(5)\n" \
  "v_cmp_ne_u32 vcc, s44, v75\n" \
  "v_cmp_ne_u32 s[94:95], s44, v79\n" \
  "s_or_b64 vcc, vcc, s[94:95]\n" \
  "s_cbranch_scc1 Lu1s%=\n" \
  "Lu1b%=:\n" \
  "v_lshlrev_b32 v102, 16, v78\n" \
  "v_and_b32 v103, 0xffff0000, v78\n" \
  "v_lshlrev_b32 v22, 16, v72\n" \
  "v_lshlrev_b32 v23, 16, v73\n" \
  "v_and_b32 v24, 0xffff0000, v72\n" \
  "v_and_b32 v25, 0xffff0000, v73\n" \
  "v_lshlrev_b32 v26, 16, v74\n" \
  "v_and_b32 v27, 0xffff0000, v74\n" \
  "v_pk_add_f32 v[46:47], v[22:23], v[102:103] neg_lo:[0,1] neg_hi:[0,1]\n" \
  "v_pk_add_f32 v[48:49], v[24:25], v[102:103] neg_lo:[0,1] neg_hi:[0,1]\n" \
  "v_pk_add_f32 v[50:51], v[26:27], v[102:103] neg_lo:[0,1] neg_hi:[0,1]\n" \
  "v_cvt_pk_bf16_f32 v46, 0, v46\n" \
  "v_cvt_pk_bf16_f32 v47, 0, v47\n" \
  "v_cvt_pk_bf16_f32 v48, 0, v48\n" \
  "v_cvt_pk_bf16_f32 v49, 0, v49\n" \
  "v_cvt_pk_bf16_f32 v50, 0, v50\n" \
  "v_cvt_pk_bf16_f32 v51, 0, v51\n" \
  "v_pk_mul_f32 v[46:47], v[46:47], v[76:77]\n" \
  "v_pk_mul_f32 v[48:49], v[48:49], v[76:77]\n" \
  "v_pk_mul_f32 v[50:51], v[50:51], v[76:77]\n" \
  "v_cvt_pk_bf16_f32 v46, 0, v46\n" \
  "v_cvt_pk_bf16_f32 v47, 0, v47\n" \
  "v_cvt_pk_bf16_f32 v48, 0, v48\n" \
  "v_cvt_pk_bf16_f32 v49, 0, v49\n" \
  "v_cvt_pk_bf16_f32 v50, 0, v50\n" \
  "v_cvt_pk_bf16_f32 v51, 0, v51\n" \
  "v_pk_mul_f32 v[46:47], v[46:47], v[96:97] op_sel_hi:[1,0]\n" \
  "v_pk_mul_f32 v[48:49], v[48:49], v[98:99] op_sel_hi:[1,0]\n" \
  "v_pk_mul_f32 v[50:51], v[50:51], v[100:101] op_sel_hi:[1,0]\n" \
  "v_cvt_pk_bf16_f32 v46, 0, v46\n" \
  "v_cvt_pk_bf16_f32 v47, 0, v47\n" \
  "v_cvt_pk_bf16_f32 v48, 0, v48\n" \
  "v_cvt_pk_bf16_f32 v49, 0, v49\n" \
  "v_cvt_pk_bf16_f32 v50, 0, v50\n" \
  "v_cvt_pk_bf16_f32 v51, 0, v51\n" \
  "v_pk_add_f32 v[46:47], v[46:47], v[96:97] op_sel:[0,1]\n" \
  "v_pk_add_f32 v[48:49], v[48:49], v[98:99] op_sel:[0,1]\n" \
  "v_pk_add_f32 v[50:51], v[50:51], v[100:101] op_sel:[0,1]\n" \
  "v_cvt_pk_bf16_f32 v46, 0, v46\n" \
  "v_cvt_pk_bf16_f32 v47, 0, v47\n" \
  "v_cvt_pk_bf16_f32 v48, 0, v48\n" \
  "v_cvt_pk_bf16_f32 v49, 0, v49\n" \
  "v_cvt_pk_bf16_f32 v50, 0, v50\n" \
  "v_cvt_pk_bf16_f32 v51, 0, v51\n" \
  "v_cvt_pkrtz_f16_f32 v102, v47, v46\n" \
  "v_cvt_pkrtz_f16_f32 v103, v49, v48\n" \
  "v_cvt_pkrtz_f16_f32 v104, v51, v50\n" \
  "ds_write_b32 v105, v102 offset:2048\n" \
  "ds_write_b32 v105, v103 offset:10240\n" \
  "ds_write_b32 v105, v104 offset:18432\n" \
  "ds_add_u32 v111, v112 offset:4\n" \
  "v_mov_b32 v112, 0x100\n" \
  SSTAMP8 \
  "s_waitcnt vmcnt(3)\n" \
  "v_cmp_ne_u32 vcc, s44, v83\n" \
  "v_cmp_ne_u32 s[94:95], s44, v87\n" \
  "s_or_b64 vcc, vcc, s[94:95]\n" \
  "s_cbranch_scc1 Lu2s%=\n" \
  "Lu2b%=:\n" \
  "v_lshlrev_b32 v102, 16, v86\n" \
  "v_and_b32 v103, 0xffff0000, v86\n" \
  "v_lshlrev_b32 v28, 16, v80\n" \
  "v_lshlrev_b32 v29, 16, v81\n" \
  "v_and_b32 v30, 0xffff0000, v80\n" \
  "v_and_b32 v31, 0xffff0000, v81\n" \
  "v_lshlrev_b32 v32, 16, v82\n" \
  "v_and_b32 v33, 0xffff0000, v82\n" \
  "v_sub_f32 v52, v28, v102\n" \
  "v_sub_f32 v53, v29, v103\n" \
  "v_sub_f32 v54, v30, v102\n" \
  "v_sub_f32 v55, v31, v103\n" \
  "v_sub_f32 v56, v32, v102\n" \
  "v_sub_f32 v57, v33, v103\n" \
  "v_cvt_pk_bf16_f32 v52, 0, v52\n" \
  "v_cvt_pk_bf16_f32 v53, 0, v53\n" \
  "v_cvt_pk_bf16_f32 v54, 0, v54\n" \
  "v_cvt_pk_bf16_f32 v55, 0, v55\n" \
  "v_cvt_pk_bf16_f32 v56, 0, v56\n" \
  "v_cvt_pk_bf16_f32 v57, 0, v57\n" \
  "v_mul_f32 v52, v52, v84\n" \
  "v_mul_f32 v53, v53, v85\n" \
  "v_mul_f32 v54, v54, v84\n" \
  "v_mul_f32 v55, v55, v85\n" \
  "v_mul_f32 v56, v56, v84\n" \
  "v_mul_f32 v57, v57, v85\n" \
  "v_cvt_pk_bf16_f32 v52, 0, v52\n" \
  "v_cvt_pk_bf16_f32 v53, 0, v53\n" \
  "v_cvt_pk_bf16_f32 v54, 0, v54\n" \
  "v_cvt_pk_bf16_f32 v55, 0, v55\n" \
  "v_cvt_pk_bf16_f32 v56, 0, v56\n" \
  "v_cvt_pk_bf16_f32 v57, 0, v57\n" \
  "v_mul_f32 v52, v52, v96\n" \
  "v_mul_f32 v53, v53, v96\n" \
  "v_mul_f32 v54, v54, v98\n" \
  "v_mul_f32 v55, v55, v98\n" \
  "v_mul_f32 v56, v56, v100\n" \
  "v_mul_f32 v57, v57, v100\n" \
  "v_cvt_pk_bf16_f32 v52, 0, v52\n" \
  "v_cvt_pk_bf16_f32 v53, 0, v53\n" \
  "v_cvt_pk_bf16_f32 v54, 0, v54\n" \
  "v_cvt_pk_bf16_f32 v55, 0, v55\n" \
  "v_cvt_pk_bf16_f32 v56, 0, v56\n" \
  "v_cvt_pk_bf16_f32 v57, 0, v57\n" \
  "v_add_f32 v52, v52, v97\n" \
  "v_add_f32 v53, v53, v97\n" \
  "v_add_f32 v54, v54, v99\n" \
  "v_add_f32 v55, v55, v99\n" \
  "v_add_f32 v56, v56, v101\n" \
  "v_add_f32 v57, v57, v101\n" \
  "v_cvt_pk_bf16_f32 v52, 0, v52\n" \
  "v_cvt_pk_bf16_f32 v53, 0, v53\n" \
  "v_cvt_pk_bf16_f32 v54, 0, v54\n" \
  "v_cvt_pk_bf16_f32 v55, 0, v55\n" \
  "v_cvt_pk_bf16_f32 v56, 0, v56\n" \
  "v_cvt_pk_bf16_f32 v57, 0, v57\n" \
  "v_cvt_pkrtz_f16_f32 v102, v53, v52\n" \
  "v_cvt_pkrtz_f16_f32 v103, v55, v54\n" \
  "v_cvt_pkrtz_f16_f32 v104, v57, v56\n" \
  "ds_write_b32 v105, v102 offset:1024\n" \
  "ds_write_b32 v105, v103 offset:9216\n" \
  "ds_write_b32 v105, v104 offset:17408\n" \
  "s_waitcnt vmcnt(1)\n" \
  "v_cmp_ne_u32 vcc, s44, v91\n" \
  "v_cmp_ne_u32 s[94:95], s44, v95\n" \
  "s_or_b64 vcc, vcc, s[94:95]\n" \
  "s_cbranch_scc1 Lu3s%=\n" \
  "Lu3b%=:\n" \
  "v_lshlrev_b32 v102, 16, v94\n" \
  "v_and_b32 v103, 0xffff0000, v94\n" \
  "v_lshlrev_b32 v34, 16, v88\n" \
  "v_lshlrev_b32 v35, 16, v89\n" \
  "v_and_b32 v36, 0xffff0000, v88\n" \
  "v_and_b32 v37, 0xffff0000, v89\n" \
  "v_lshlrev_b32 v38, 16, v90\n" \
  "v_and_b32 v39, 0xffff0000, v90\n" \
  "v_sub_f32 v58, v34, v102\n" \
  "v_sub_f32 v59, v35, v103\n" \
  "v_sub_f32 v60, v36, v102\n" \
  "v_sub_f32 v61, v37, v103\n" \
  "v_sub_f32 v62, v38, v102\n" \
  "v_sub_f32 v63, v39, v103\n" \
  "v_cvt_pk_bf16_f32 v58, 0, v58\n" \
  "v_cvt_pk_bf16_f32 v59, 0, v59\n" \
  "v_cvt_pk_bf16_f32 v60, 0, v60\n" \
  "v_cvt_pk_bf16_f32 v61, 0, v61\n" \
  "v_cvt_pk_bf16_f32 v62, 0, v62\n" \
  "v_cvt_pk_bf16_f32 v63, 0, v63\n" \
  "v_mul_f32 v58, v58, v92\n" \
  "v_mul_f32 v59, v59, v93\n" \
  "v_mul_f32 v60, v60, v92\n" \
  "v_mul_f32 v61, v61, v93\n" \
  "v_mul_f32 v62, v62, v92\n" \
  "v_mul_f32 v63, v63, v93\n" \
  "v_cvt_pk_bf16_f32 v58, 0, v58\n" \
  "v_cvt_pk_bf16_f32 v59, 0, v59\n" \
  "v_cvt_pk_bf16_f32 v60, 0, v60\n" \
  "v_cvt_pk_bf16_f32 v61, 0, v61\n" \
  "v_cvt_pk_bf16_f32 v62, 0, v62\n" \
  "v_cvt_pk_bf16_f32 v63, 0, v63\n" \
  "v_mul_f32 v58, v58, v96\n" \
  "v_mul_f32 v59, v59, v96\n" \
  "v_mul_f32 v60, v60, v98\n" \
  "v_mul_f32 v61, v61, v98\n" \
  "v_mul_f32 v62, v62, v100\n" \
  "v_mul_f32 v63, v63, v100\n" \
  "v_cvt_pk_bf16_f32 v58, 0, v58\n" \
  "v_cvt_pk_bf16_f32 v59, 0, v59\n" \
  "v_cvt_pk_bf16_f32 v60, 0, v60\n" \
  "v_cvt_pk_bf16_f32 v61, 0, v61\n" \
  "v_cvt_pk_bf16_f32 v62, 0, v62\n" \
  "v_cvt_pk_bf16_f32 v63, 0, v63\n" \
  "v_add_f32 v58, v58, v97\n" \
  "v_add_f32 v59, v59, v97\n" \
  "v_add_f32 v60, v60, v99\n" \
  "v_add_f32 v61, v61, v99\n" \
  "v_add_f32 v62, v62, v101\n" \
  "v_add_f32 v63, v63, v101\n" \
  "v_cvt_pk_bf16_f32 v58, 0, v58\n" \
  "v_cvt_pk_bf16_f32 v59, 0, v59\n" \
  "v_cvt_pk_bf16_f32 v60, 0, v60\n" \
  "v_cvt_pk_bf16_f32 v61, 0, v61\n" \
  "v_cvt_pk_bf16_f32 v62, 0, v62\n" \
  "v_cvt_pk_bf16_f32 v63, 0, v63\n" \
  "v_cvt_pkrtz_f16_f32 v102, v59, v58\n" \
  "v_cvt_pkrtz_f16_f32 v103, v61, v60\n" \
  "v_cvt_pkrtz_f16_f32 v104, v63, v62\n" \
  "ds_write_b32 v105, v102 offset:0\n" \
  "ds_write_b32 v105, v103 offset:8192\n" \
  "ds_write_b32 v105, v104 offset:16384\n" \
  "ds_add_u32 v111, v112 offset:4\n" \
  SSTAMP9 \
  "s_setprio " STR(PRE_) "\n" \
  "s_waitcnt vmcnt(0)\n" \
  "v_cmp_ne_u32 vcc, s44, v119\n" \
  "s_cmp_lg_u64 vcc, 0\n" \
  "s_cbranch_scc1 Las%=\n" \
  "Lab%=:\n" \
  "v_mul_f32 v118, v118, v110\n" \
  "v_mov_b32 v90, 158224\n" \
  "v_mov_b32 v85, s44\n" \
  "v_add_f32_dpp v118, v118, v118 quad_perm:[1,0,3,2] row_mask:0xf bank_mask:0xf bound_ctrl:1\n" \
  "s_nop 1\n" \
  "v_add_f32_dpp v118, v118, v118 quad_perm:[2,3,0,1] row_mask:0xf bank_mask:0xf bound_ctrl:1\n" \
  "v_cvt_pk_bf16_f32 v118, 0, v118\n" \
  "s_nop 0\n" \
  "v_readlane_b32 s94, v118, 0\n" \
  "v_readlane_b32 s95, v118, 8\n" \
  "v_readlane_b32 s96, v118, 16\n" \
  "v_mov_b32 v77, s94\n" \
  "v_mov_b32 v79, s95\n" \
  "v_mov_b32 v81, s96\n" \
  "ds_read_b32 v86, v111\n" \
  "ds_read_b64 v[64:65], v115 offset:0\n" \
  "ds_read_b64 v[66:67], v115 offset:32096\n" \
  "ds_read_b64 v[68:69], v116 offset:0\n" \
  "ds_read_b128 v[92:95], v90\n" \
  "v_mul_f32 v40, s84, v40\n" \
  "v_mul_f32 v41, s84, v41\n" \
  "v_mul_f32 v42, s85, v42\n" \
  "v_mul_f32 v43, s85, v43\n" \
  "v_mul_f32 v44, s86, v44\n" \
  "v_mul_f32 v45, s86, v45\n" \
  "v_cvt_pk_bf16_f32 v40, 0, v40\n" \
  "v_cvt_pk_bf16_f32 v41, 0, v41\n" \
  "v_cvt_pk_bf16_f32 v42, 0, v42\n" \
  "v_cvt_pk_bf16_f32 v43, 0, v43\n" \
  "v_cvt_pk_bf16_f32 v44, 0, v44\n" \
  "v_cvt_pk_bf16_f32 v45, 0, v45\n" \
  "s_waitcnt lgkmcnt(0)\n" \
  "v_readfirstlane_b32 s94, v86\n" \
  "s_lshr_b32 s94, s94, s45\n" \
  "s_bfe_u32 s94, s94, 0x40000\n" \
  "s_cmp_lg_u32 s94, 3\n" \
  "s_cbranch_scc1 Le0s%=\n" \
  "Le0b%=:\n" \
  "v_mul_f32 v76, s87, v92\n" \
  "v_mul_f32 v78, s88, v93\n" \
  "v_mul_f32 v80, s89, v94\n" \
  "v_mul_f32 v64, v64, v76\n" \
  "v_mul_f32 v65, v65, v76\n" \
  "v_mul_f32 v66, v66, v78\n" \
  "v_mul_f32 v67, v67, v78\n" \
  "v_mul_f32 v68, v68, v80\n" \
  "v_mul_f32 v69, v69, v80\n" \
  "v_cvt_pk_bf16_f32 v64, 0, v64\n" \
  "v_cvt_pk_bf16_f32 v65, 0, v65\n" \
  "v_cvt_pk_bf16_f32 v66, 0, v66\n" \
  "v_cvt_pk_bf16_f32 v67, 0, v67\n" \
  "v_cvt_pk_bf16_f32 v68, 0, v68\n" \
  "v_cvt_pk_bf16_f32 v69, 0, v69\n" \
  "v_add_f32 v64, v64, v40\n" \
  "v_add_f32 v65, v65, v41\n" \
  "v_add_f32 v66, v66, v42\n" \
  "v_add_f32 v67, v67, v43\n" \
  "v_add_f32 v68, v68, v44\n" \
  "v_add_f32 v69, v69, v45\n" \
  "v_cvt_pk_bf16_f32 v64, 0, v64\n" \
  "v_cvt_pk_bf16_f32 v65, 0, v65\n" \
  "v_cvt_pk_bf16_f32 v66, 0, v66\n" \
  "v_cvt_pk_bf16_f32 v67, 0, v67\n" \
  "v_cvt_pk_bf16_f32 v68, 0, v68\n" \
  "v_cvt_pk_bf16_f32 v69, 0, v69\n" \
  "v_mul_f32 v64, v64, v77\n" \
  "v_mul_f32 v65, v65, v77\n" \
  "v_mul_f32 v66, v66, v79\n" \
  "v_mul_f32 v67, v67, v79\n" \
  "v_mul_f32 v68, v68, v81\n" \
  "v_mul_f32 v69, v69, v81\n" \
  "v_cvt_pk_bf16_f32 v64, 0, v64\n" \
  "v_cvt_pk_bf16_f32 v65, 0, v65\n" \
  "v_cvt_pk_bf16_f32 v66, 0, v66\n" \
  "v_cvt_pk_bf16_f32 v67, 0, v67\n" \
  "v_cvt_pk_bf16_f32 v68, 0, v68\n" \
  "v_cvt_pk_bf16_f32 v69, 0, v69\n" \
  "v_add_f32 v64, v64, v16\n" \
  "v_add_f32 v65, v65, v17\n" \
  "v_add_f32 v66, v66, v18\n" \
  "v_add_f32 v67, v67, v19\n" \
  "v_add_f32 v68, v68, v20\n" \
  "v_add_f32 v69, v69, v21\n" \
  "v_cvt_pk_bf16_f32 v82, v64, v66\n" \
  "v_cvt_pk_bf16_f32 v83, v65, v67\n" \
  "v_cvt_pk_bf16_f32 v84, v68, v69\n" \
  "s_movk_i32 s94, 0\n" \
  "buffer_store_dwordx4 v[82:85], v117, s[40:43], s94 offen sc1\n" \
  "ds_read_b32 v86, v111\n" \
  "ds_read_b64 v[64:65], v115 offset:2560\n" \
  "ds_read_b64 v[70:71], v115 offset:10240\n" \
  "ds_read_b64 v[66:67], v115 offset:34656\n" \
  "ds_read_b64 v[72:73], v115 offset:42336\n" \
  "ds_read_b64 v[68:69], v116 offset:2560\n" \
  "ds_read_b64 v[74:75], v116 offset:10240\n" \
  "v_mul_f32 v46, s84, v46\n" \
  "v_mul_f32 v47, s84, v47\n" \
  "v_mul_f32 v48, s85, v48\n" \
  "v_mul_f32 v49, s85, v49\n" \
  "v_mul_f32 v50, s86, v50\n" \
  "v_mul_f32 v51, s86, v51\n" \
  "v_cvt_pk_bf16_f32 v46, 0, v46\n" \
  "v_cvt_pk_bf16_f32 v47, 0, v47\n" \
  "v_cvt_pk_bf16_f32 v48, 0, v48\n" \
  "v_cvt_pk_bf16_f32 v49, 0, v49\n" \
  "v_cvt_pk_bf16_f32 v50, 0, v50\n" \
  "v_cvt_pk_bf16_f32 v51, 0, v51\n" \
  "s_waitcnt lgkmcnt(0)\n" \
  "v_readfirstlane_b32 s94, v86\n" \
  "s_lshr_b32 s94, s94, s45\n" \
  "s_bfe_u32 s94, s94, 0x40008\n" \
  "s_cmp_lg_u32 s94, 6\n" \
  "s_cbranch_scc1 Le1s%=\n" \
  "Le1b%=:\n" \
  "v_add_f32 v64, v64, v70\n" \
  "v_add_f32 v65, v65, v71\n" \
  "v_add_f32 v66, v66, v72\n" \
  "v_add_f32 v67, v67, v73\n" \
  "v_add_f32 v68, v68, v74\n" \
  "v_add_f32 v69, v69, v75\n" \
  "v_mul_f32 v64, v64, v76\n" \
  "v_mul_f32 v65, v65, v76\n" \
  "v_mul_f32 v66, v66, v78\n" \
  "v_mul_f32 v67, v67, v78\n" \
  "v_mul_f32 v68, v68, v80\n" \
  "v_mul_f32 v69, v69, v80\n" \
  "v_cvt_pk_bf16_f32 v64, 0, v64\n" \
  "v_cvt_pk_bf16_f32 v65, 0, v65\n" \
  "v_cvt_pk_bf16_f32 v66, 0, v66\n" \
  "v_cvt_pk_bf16_f32 v67, 0, v67\n" \
  "v_cvt_pk_bf16_f32 v68, 0, v68\n" \
  "v_cvt_pk_bf16_f32 v69, 0, v69\n" \
  "v_add_f32 v64, v64, v46\n" \
  "v_add_f32 v65, v65, v47\n" \
  "v_add_f32 v66, v66, v48\n" \
  "v_add_f32 v67, v67, v49\n" \
  "v_add_f32 v68, v68, v50\n" \
  "v_add_f32 v69, v69, v51\n" \
  "v_cvt_pk_bf16_f32 v64, 0, v64\n" \
  "v_cvt_pk_bf16_f32 v65, 0, v65\n" \
  "v_cvt_pk_bf16_f32 v66, 0, v66\n" \
  "v_cvt_pk_bf16_f32 v67, 0, v67\n" \
  "v_cvt_pk_bf16_f32 v68, 0, v68\n" \
  "v_cvt_pk_bf16_f32 v69, 0, v69\n" \
  "v_mul_f32 v64, v64, v77\n" \
  "v_mul_f32 v65, v65, v77\n" \
  "v_mul_f32 v66, v66, v79\n" \
  "v_mul_f32 v67, v67, v79\n" \
  "v_mul_f32 v68, v68, v81\n" \
  "v_mul_f32 v69, v69, v81\n" \
  "v_cvt_pk_bf16_f32 v64, 0, v64\n" \
  "v_cvt_pk_bf16_f32 v65, 0, v65\n" \
  "v_cvt_pk_bf16_f32 v66, 0, v66\n" \
  "v_cvt_pk_bf16_f32 v67, 0, v67\n" \
  "v_cvt_pk_bf16_f32 v68, 0, v68\n" \
  "v_cvt_pk_bf16_f32 v69, 0, v69\n" \
  "v_add_f32 v64, v64, v22\n" \
  "v_add_f32 v65, v65, v23\n" \
  "v_add_f32 v66, v66, v24\n" \
  "v_add_f32 v67, v67, v25\n" \
  "v_add_f32 v68, v68, v26\n" \
  "v_add_f32 v69, v69, v27\n" \
  "v_cvt_pk_bf16_f32 v82, v64, v66\n" \
  "v_cvt_pk_bf16_f32 v83, v65, v67\n" \
  "v_cvt_pk_bf16_f32 v84, v68, v69\n" \
  "s_movk_i32 s94, 4096\n" \
  "buffer_store_dwordx4 v[82:85], v117, s[40:43], s94 offen sc1\n" \
  SSTAMPE1 \
  "ds_read_b32 v86, v111\n" \
  "ds_read_b64 v[64:65], v115 offset:5120\n" \
  "ds_read_b64 v[70:71], v115 offset:12800\n" \
  "ds_read_b64 v[66:67], v115 offset:37216\n" \
  "ds_read_b64 v[72:73], v115 offset:44896\n" \
  "ds_read_b64 v[68:69], v116 offset:5120\n" \
  "ds_read_b64 v[74:75], v116 offset:12800\n" \
  "v_mul_f32 v52, s84, v52\n" \
  "v_mul_f32 v53, s84, v53\n" \
  "v_mul_f32 v54, s85, v54\n" \
  "v_mul_f32 v55, s85, v55\n" \
  "v_mul_f32 v56, s86, v56\n" \
  "v_mul_f32 v57, s86, v57\n" \
  "v_cvt_pk_bf16_f32 v52, 0, v52\n" \
  "v_cvt_pk_bf16_f32 v53, 0, v53\n" \
  "v_cvt_pk_bf16_f32 v54, 0, v54\n" \
  "v_cvt_pk_bf16_f32 v55, 0, v55\n" \
  "v_cvt_pk_bf16_f32 v56, 0, v56\n" \
  "v_cvt_pk_bf16_f32 v57, 0, v57\n" \
  "s_waitcnt lgkmcnt(0)\n" \
  "v_readfirstlane_b32 s94, v86\n" \
  "s_lshr_b32 s94, s94, s45\n" \
  "s_bfe_u32 s94, s94, 0x40010\n" \
  "s_cmp_lg_u32 s94, 6\n" \
  "s_cbranch_scc1 Le2s%=\n" \
  "Le2b%=:\n" \
  "v_add_f32 v64, v64, v70\n" \
  "v_add_f32 v65, v65, v71\n" \
  "v_add_f32 v66, v66, v72\n" \
  "v_add_f32 v67, v67, v73\n" \
  "v_add_f32 v68, v68, v74\n" \
  "v_add_f32 v69, v69, v75\n" \
  "v_mul_f32 v64, v64, v76\n" \
  "v_mul_f32 v65, v65, v76\n" \
  "v_mul_f32 v66, v66, v78\n" \
  "v_mul_f32 v67, v67, v78\n" \
  "v_mul_f32 v68, v68, v80\n" \
  "v_mul_f32 v69, v69, v80\n" \
  "v_cvt_pk_bf16_f32 v64, 0, v64\n" \
  "v_cvt_pk_bf16_f32 v65, 0, v65\n" \
  "v_cvt_pk_bf16_f32 v66, 0, v66\n" \
  "v_cvt_pk_bf16_f32 v67, 0, v67\n" \
  "v_cvt_pk_bf16_f32 v68, 0, v68\n" \
  "v_cvt_pk_bf16_f32 v69, 0, v69\n" \
  "v_add_f32 v64, v64, v52\n" \
  "v_add_f32 v65, v65, v53\n" \
  "v_add_f32 v66, v66, v54\n" \
  "v_add_f32 v67, v67, v55\n" \
  "v_add_f32 v68, v68, v56\n" \
  "v_add_f32 v69, v69, v57\n" \
  "v_cvt_pk_bf16_f32 v64, 0, v64\n" \
  "v_cvt_pk_bf16_f32 v65, 0, v65\n" \
  "v_cvt_pk_bf16_f32 v66, 0, v66\n" \
  "v_cvt_pk_bf16_f32 v67, 0, v67\n" \
  "v_cvt_pk_bf16_f32 v68, 0, v68\n" \
  "v_cvt_pk_bf16_f32 v69, 0, v69\n" \
  "v_mul_f32 v64, v64, v77\n" \
  "v_mul_f32 v65, v65, v77\n" \
  "v_mul_f32 v66, v66, v79\n" \
  "v_mul_f32 v67, v67, v79\n" \
  "v_mul_f32 v68, v68, v81\n" \
  "v_mul_f32 v69, v69, v81\n" \
  "v_cvt_pk_bf16_f32 v64, 0, v64\n" \
  "v_cvt_pk_bf16_f32 v65, 0, v65\n" \
  "v_cvt_pk_bf16_f32 v66, 0, v66\n" \
  "v_cvt_pk_bf16_f32 v67, 0, v67\n" \
  "v_cvt_pk_bf16_f32 v68, 0, v68\n" \
  "v_cvt_pk_bf16_f32 v69, 0, v69\n" \
  "v_add_f32 v64, v64, v28\n" \
  "v_add_f32 v65, v65, v29\n" \
  "v_add_f32 v66, v66, v30\n" \
  "v_add_f32 v67, v67, v31\n" \
  "v_add_f32 v68, v68, v32\n" \
  "v_add_f32 v69, v69, v33\n" \
  "v_cvt_pk_bf16_f32 v82, v64, v66\n" \
  "v_cvt_pk_bf16_f32 v83, v65, v67\n" \
  "v_cvt_pk_bf16_f32 v84, v68, v69\n" \
  "s_movk_i32 s94, 8192\n" \
  "buffer_store_dwordx4 v[82:85], v117, s[40:43], s94 offen sc1\n" \
  "ds_read_b32 v86, v111\n" \
  "ds_read_b64 v[64:65], v115 offset:7680\n" \
  "ds_read_b64 v[70:71], v115 offset:15360\n" \
  "ds_read_b64 v[66:67], v115 offset:39776\n" \
  "ds_read_b64 v[72:73], v115 offset:47456\n" \
  "ds_read_b64 v[68:69], v116 offset:7680\n" \
  "ds_read_b64 v[74:75], v116 offset:15360\n" \
  "v_mul_f32 v58, s84, v58\n" \
  "v_mul_f32 v59, s84, v59\n" \
  "v_mul_f32 v60, s85, v60\n" \
  "v_mul_f32 v61, s85, v61\n" \
  "v_mul_f32 v62, s86, v62\n" \
  "v_mul_f32 v63, s86, v63\n" \
  "v_cvt_pk_bf16_f32 v58, 0, v58\n" \
  "v_cvt_pk_bf16_f32 v59, 0, v59\n" \
  "v_cvt_pk_bf16_f32 v60, 0, v60\n" \
  "v_cvt_pk_bf16_f32 v61, 0, v61\n" \
  "v_cvt_pk_bf16_f32 v62, 0, v62\n" \
  "v_cvt_pk_bf16_f32 v63, 0, v63\n" \
  "s_waitcnt lgkmcnt(0)\n" \
  "v_readfirstlane_b32 s94, v86\n" \
  "s_lshr_b32 s94, s94, s45\n" \
  "s_bfe_u32 s94, s94, 0x40018\n" \
  "s_cmp_lg_u32 s94, 6\n" \
  "s_cbranch_scc1 Le3s%=\n" \
  "Le3b%=:\n" \
  SSTAMP10 \
  "v_pk_add_f32 v[64:65], v[64:65], v[70:71]\n" \
  "v_pk_add_f32 v[66:67], v[66:67], v[72:73]\n" \
  "v_pk_add_f32 v[68:69], v[68:69], v[74:75]\n" \
  "v_pk_mul_f32 v[64:65], v[64:65], v[76:77] op_sel_hi:[1,0]\n" \
  "v_pk_mul_f32 v[66:67], v[66:67], v[78:79] op_sel_hi:[1,0]\n" \
  "v_pk_mul_f32 v[68:69], v[68:69], v[80:81] op_sel_hi:[1,0]\n" \
  "v_cvt_pk_bf16_f32 v64, 0, v64\n" \
  "v_cvt_pk_bf16_f32 v65, 0, v65\n" \
  "v_cvt_pk_bf16_f32 v66, 0, v66\n" \
  "v_cvt_pk_bf16_f32 v67, 0, v67\n" \
  "v_cvt_pk_bf16_f32 v68, 0, v68\n" \
  "v_cvt_pk_bf16_f32 v69, 0, v69\n" \
  "v_pk_add_f32 v[64:65], v[64:65], v[58:59]\n" \
  "v_pk_add_f32 v[66:67], v[66:67], v[60:61]\n" \
  "v_pk_add_f32 v[68:69], v[68:69], v[62:63]\n" \
  "v_cvt_pk_bf16_f32 v64, 0, v64\n" \
  "v_cvt_pk_bf16_f32 v65, 0, v65\n" \
  "v_cvt_pk_bf16_f32 v66, 0, v66\n" \
  "v_cvt_pk_bf16_f32 v67, 0, v67\n" \
  "v_cvt_pk_bf16_f32 v68, 0, v68\n" \
  "v_cvt_pk_bf16_f32 v69, 0, v69\n" \
  "v_pk_mul_f32 v[64:65], v[64:65], v[76:77] op_sel:[0,1]\n" \
  "v_pk_mul_f32 v[66:67], v[66:67], v[78:79] op_sel:[0,1]\n" \
  "v_pk_mul_f32 v[68:69], v[68:69], v[80:81] op_sel:[0,1]\n" \
  "v_cvt_pk_bf16_f32 v64, 0, v64\n" \
  "v_cvt_pk_bf16_f32 v65, 0, v65\n" \
  "v_cvt_pk_bf16_f32 v66, 0, v66\n" \
  "v_cvt_pk_bf16_f32 v67, 0, v67\n" \
  "v_cvt_pk_bf16_f32 v68, 0, v68\n" \
  "v_cvt_pk_bf16_f32 v69, 0, v69\n" \
  "v_pk_add_f32 v[64:65], v[64:65], v[34:35]\n" \
  "v_pk_add_f32 v[66:67], v[66:67], v[36:37]\n" \
  "v_pk_add_f32 v[68:69], v[68:69], v[38:39]\n" \
  "v_cvt_pk_bf16_f32 v82, v64, v66\n" \
  "v_cvt_pk_bf16_f32 v83, v65, v67\n" \
  "v_cvt_pk_bf16_f32 v84, v68, v69\n" \
  "s_movk_i32 s94, 12288\n" \
  "buffer_store_dwordx4 v[82:85], v117, s[40:43], s94 offen sc1\n" \
  "s_branch Lhe%=\n" \
  "Lms%=:\n" \
  "s_mov_b32 s97, 0x7fff\n" \
  "Lmr%=:\n" \
  SSTAMPCm \
  "s_mov_b64 exec, vcc\n" \
  "buffer_load_dwordx2 v[102:103], v108, s[40:43], 0 offen sc1\n" \
  "s_mov_b64 exec, -1\n" \
  "s_waitcnt vmcnt(0)\n" \
  "v_cmp_ne_u32 vcc, s44, v103\n" \
  "s_cmp_lg_u64 vcc, 0\n" \
  "s_cbranch_scc0 Lmb%=\n" \
  "s_sub_u32 s97, s97, 1\n" \
  "s_cbranch_scc0 Lmr%=\n" \
  "s_branch Lmb%=\n" \
  "Lu0s%=:\n" \
  "s_mov_b32 s97, 0x7fff\n" \
  "Lu0r%=:\n" \
  SSTAMPCu0 \
  "s_mov_b64 exec, vcc\n" \
  "s_mov_b32 s94, 0\n" \
  "s_mov_b32 s95, 0\n" \
  "buffer_load_dwordx4 v[64:67], v106, s[40:43], s94 offen sc1\n" \
  "buffer_load_dwordx4 v[68:71], v107, s[40:43], s95 offen sc1\n" \
  "s_mov_b64 exec, -1\n" \
  "s_waitcnt vmcnt(0)\n" \
  "v_cmp_ne_u32 vcc, s44, v67\n" \
  "v_cmp_ne_u32 s[94:95], s44, v71\n" \
  "s_or_b64 vcc, vcc, s[94:95]\n" \
  "s_cbranch_scc0 Lu0b%=\n" \
  "s_sub_u32 s97, s97, 1\n" \
  "s_cbranch_scc0 Lu0r%=\n" \
  "s_branch Lu0b%=\n" \
  "Lu1s%=:\n" \
  "s_mov_b32 s97, 0x7fff\n" \
  "Lu1r%=:\n" \
  SSTAMPCu1 \
  "s_mov_b64 exec, vcc\n" \
  "s_mov_b32 s94, 790528\n" \
  "s_mov_b32 s95, 4096\n" \
  "buffer_load_dwordx4 v[72:75], v106, s[40:43], s94 offen sc1\n" \
  "buffer_load_dwordx4 v[76:79], v107, s[40:43], s95 offen sc1\n" \
  "s_mov_b64 exec, -1\n" \
  "s_waitcnt vmcnt(0)\n" \
  "v_cmp_ne_u32 vcc, s44, v75\n" \
  "v_cmp_ne_u32 s[94:95], s44, v79\n" \
  "s_or_b64 vcc, vcc, s[94:95]\n" \
  "s_cbranch_scc0 Lu1b%=\n" \
  "s_sub_u32 s97, s97, 1\n" \
  "s_cbranch_scc0 Lu1r%=\n" \
  "s_branch Lu1b%=\n" \
  "Lu2s%=:\n" \
  "s_mov_b32 s97, 0x7fff\n" \
  "Lu2r%=:\n" \
  SSTAMPCu2 \
  "s_mov_b64 exec, vcc\n" \
  "s_mov_b32 s94, 1581056\n" \
  "s_mov_b32 s95, 8192\n" \
  "buffer_load_dwordx4 v[80:83], v106, s[40:43], s94 offen sc1\n" \
  "buffer_load_dwordx4 v[84:87], v107, s[40:43], s95 offen sc1\n" \
  "s_mov_b64 exec, -1\n" \
  "s_waitcnt vmcnt(0)\n" \
  "v_cmp_ne_u32 vcc, s44, v83\n" \
  "v_cmp_ne_u32 s[94:95], s44, v87\n" \
  "s_or_b64 vcc, vcc, s[94:95]\n" \
  "s_cbranch_scc0 Lu2b%=\n" \
  "s_sub_u32 s97, s97, 1\n" \
  "s_cbranch_scc0 Lu2r%=\n" \
  "s_branch Lu2b%=\n" \
  "Lu3s%=:\n" \
  "s_mov_b32 s97, 0x7fff\n" \
  "Lu3r%=:\n" \
  SSTAMPCu3 \
  "s_mov_b64 exec, vcc\n" \
  "s_mov_b32 s94, 2371584\n" \
  "s_mov_b32 s95, 12288\n" \
  "buffer_load_dwordx4 v[88:91], v106, s[40:43], s94 offen sc1\n" \
  "buffer_load_dwordx4 v[92:95], v107, s[40:43], s95 offen sc1\n" \
  "s_mov_b64 exec, -1\n" \
  "s_waitcnt vmcnt(0)\n" \
  "v_cmp_ne_u32 vcc, s44, v91\n" \
  "v_cmp_ne_u32 s[94:95], s44, v95\n" \
  "s_or_b64 vcc, vcc, s[94:95]\n" \
  "s_cbranch_scc0 Lu3b%=\n" \
  "s_sub_u32 s97, s97, 1\n" \
  "s_cbranch_scc0 Lu3r%=\n" \
  "s_branch Lu3b%=\n" \
  "Las%=:\n" \
  "s_mov_b32 s97, 0x7fff\n" \
  "Lar%=:\n" \
  SSTAMPCa \
  "s_mov_b64 exec, vcc\n" \
  "buffer_load_dwordx2 v[118:119], v114, s[40:43], 0 offen sc1\n" \
  "s_mov_b64 exec, -1\n" \
  "s_waitcnt vmcnt(0)\n" \
  "v_cmp_ne_u32 vcc, s44, v119\n" \
  "s_cmp_lg_u64 vcc, 0\n" \
  "s_cbranch_scc0 Lab%=\n" \
  "s_sub_u32 s97, s97, 1\n" \
  "s_cbranch_scc0 Lar%=\n" \
  "s_branch Lab%=\n" \
  "Le0s%=:\n" \
  "s_mov_b32 s97, 0x1fffff\n" \
  "Le0r%=:\n" \
  "s_sleep 1\n" \
  "ds_read_b32 v86, v111\n" \
  "s_waitcnt lgkmcnt(0)\n" \
  "v_readfirstlane_b32 s94, v86\n" \
  "s_lshr_b32 s94, s94, s45\n" \
  "s_bfe_u32 s94, s94, 0x40000\n" \
  "s_cmp_lg_u32 s94, 3\n" \
  "s_cbranch_scc0 Le0x%=\n" \
  "s_sub_u32 s97, s97, 1\n" \
  "s_cbranch_scc0 Le0r%=\n" \
  "Le0x%=:\n" \
  "ds_read_b64 v[64:65], v115 offset:0\n" \
  "ds_read_b64 v[66:67], v115 offset:32096\n" \
  "ds_read_b64 v[68:69], v116 offset:0\n" \
  "ds_read_b128 v[92:95], v90\n" \
  "s_waitcnt lgkmcnt(0)\n" \
  "s_branch Le0b%=\n" \
  "Le1s%=:\n" \
  "s_mov_b32 s97, 0x1fffff\n" \
  "Le1r%=:\n" \
  "s_sleep 1\n" \
  "ds_read_b32 v86, v111\n" \
  "s_waitcnt lgkmcnt(0)\n" \
  "v_readfirstlane_b32 s94, v86\n" \
  "s_lshr_b32 s94, s94, s45\n" \
  "s_bfe_u32 s94, s94, 0x40008\n" \
  "s_cmp_lg_u32 s94, 6\n" \
  "s_cbranch_scc0 Le1x%=\n" \
  "s_sub_u32 s97, s97, 1\n" \
  "s_cbranch_scc0 Le1r%=\n" \
  "Le1x%=:\n" \
  "ds_read_b64 v[64:65], v115 offset:2560\n" \
  "ds_read_b64 v[70:71], v115 offset:10240\n" \
  "ds_read_b64 v[66:67], v115 offset:34656\n" \
  "ds_read_b64 v[72:73], v115 offset:42336\n" \
  "ds_read_b64 v[68:69], v116 offset:2560\n" \
  "ds_read_b64 v[74:75], v116 offset:10240\n" \
  "s_waitcnt lgkmcnt(0)\n" \
  "s_branch Le1b%=\n" \
  "Le2s%=:\n" \
  "s_mov_b32 s97, 0x1fffff\n" \
  "Le2r%=:\n" \
  "s_sleep 1\n" \
  "ds_read_b32 v86, v111\n" \
  "s_waitcnt lgkmcnt(0)\n" \
  "v_readfirstlane_b32 s94, v86\n" \
  "s_lshr_b32 s94, s94, s45\n" \
  "s_bfe_u32 s94, s94, 0x40010\n" \
  "s_cmp_lg_u32 s94, 6\n" \
  "s_cbranch_scc0 Le2x%=\n" \
  "s_sub_u32 s97, s97, 1\n" \
  "s_cbranch_scc0 Le2r%=\n" \
  "Le2x%=:\n" \
  "ds_read_b64 v[64:65], v115 offset:5120\n" \
  "ds_read_b64 v[70:71], v115 offset:12800\n" \
  "ds_read_b64 v[66:67], v115 offset:37216\n" \
  "ds_read_b64 v[72:73], v115 offset:44896\n" \
  "ds_read_b64 v[68:69], v116 offset:5120\n" \
  "ds_read_b64 v[74:75], v116 offset:12800\n" \
  "s_waitcnt lgkmcnt(0)\n" \
  "s_branch Le2b%=\n" \
  "Le3s%=:\n" \
  "s_mov_b32 s97, 0x1fffff\n" \
  "Le3r%=:\n" \
  "ds_read_b32 v86, v111\n" \
  "ds_read_b64 v[64:65], v115 offset:7680\n" \
  "ds_read_b64 v[70:71], v115 offset:15360\n" \
  "ds_read_b64 v[66:67], v115 offset:39776\n" \
  "ds_read_b64 v[72:73], v115 offset:47456\n" \
  "ds_read_b64 v[68:69], v116 offset:7680\n" \
  "ds_read_b64 v[74:75], v116 offset:15360\n" \
  "s_waitcnt lgkmcnt(0)\n" \
  "v_readfirstlane_b32 s94, v86\n" \
  "s_lshr_b32 s94, s94, s45\n" \
  "s_bfe_u32 s94, s94, 0x40018\n" \
  "s_cmp_lg_u32 s94, 6\n" \
  "s_cbranch_scc0 Le3b%=\n" \
  "s_sub_u32 s97, s97, 1\n" \
  "s_cbranch_scc0 Le3r%=\n" \
  "s_branch Le3b%=\n" \
  "Lhe%=:\n" \
  SSTAMP11 \
  "s_setprio 0\n"

#define LW_ASM \
  "s_load_dwordx2 s[40:41], %[k], 0x70\n" \
  "s_load_dwordx2 s[46:47], %[k], 0x38\n" \
  "s_load_dwordx2 s[48:49], %[k], 0x68\n" \
  "v_mov_b32 v1, 158208\n" \
  "ds_read_b32 v2, v1\n" \
  "s_mov_b32 s42, 10403840\n" \
  "s_mov_b32 s43, 0x20000\n" \
  "v_and_b32 v3, 7, %[t]\n" \
  "v_and_b32 v4, 63, %[t]\n" \
  "s_waitcnt lgkmcnt(0)\n" \
  "v_readfirstlane_b32 s44, v2\n" \
  "s_cmp_ge_u32 %[w], 12\n" \
  "s_cbranch_scc1 Mls%=\n" \
  "s_lshr_b32 s50, 0x235, %[w]\n" \
  "s_bitcmp0_b32 s50, 0\n" \
  "s_cbranch_scc1 Mla%=\n" \
  "s_sleep " STR(PSLP) "\n" \
  "s_add_u32 s50, %[w], 1\n" \
  "s_lshr_b32 s50, s50, 1\n" \
  "s_min_u32 s50, s50, 4\n" \
  "s_lshl_b32 s51, s50, 6\n" \
  "v_add_u32 v5, s51, v4\n" \
  "v_mov_b32 v60, 0\n" \
  "s_cmp_eq_u32 s50, 4\n" \
  "s_cselect_b32 exec_hi, 0, -1\n" \
  "v_lshlrev_b32 v6, 4, v5\n" \
  "v_lshlrev_b32 v7, 2, v5\n" \
  "global_load_dword v58, v7, s[46:47]\n" \
  "s_movk_i32 s97, 0x7fff\n" \
  "Mpr%=:\n" \
  "s_mov_b32 s51, 17920\n" \
  "buffer_load_dwordx4 v[12:15], v6, s[40:43], s51 offen sc1\n" \
  "s_mov_b32 s51, 45568\n" \
  "buffer_load_dwordx4 v[16:19], v6, s[40:43], s51 offen sc1\n" \
  "s_mov_b32 s51, 73216\n" \
  "buffer_load_dwordx4 v[20:23], v6, s[40:43], s51 offen sc1\n" \
  "s_mov_b32 s51, 22528\n" \
  "buffer_load_dwordx4 v[24:27], v6, s[40:43], s51 offen sc1\n" \
  "s_mov_b32 s51, 50176\n" \
  "buffer_load_dwordx4 v[28:31], v6, s[40:43], s51 offen sc1\n" \
  "s_mov_b32 s51, 77824\n" \
  "buffer_load_dwordx4 v[32:35], v6, s[40:43], s51 offen sc1\n" \
  "s_mov_b32 s51, 27136\n" \
  "buffer_load_dwordx4 v[36:39], v6, s[40:43], s51 offen sc1\n" \
  "s_mov_b32 s51, 54784\n" \
  "buffer_load_dwordx4 v[40:43], v6, s[40:43], s51 offen sc1\n" \
  "s_mov_b32 s51, 82432\n" \
  "buffer_load_dwordx4 v[44:47], v6, s[40:43], s51 offen sc1\n" \
  "s_waitcnt vmcnt(0)\n" \
  "v_bitop3_b32 v13, v13, v15, s44 bitop3:0x7e\n" \
  "v_bitop3_b32 v17, v17, v19, s44 bitop3:0x7e\n" \
  "v_bitop3_b32 v21, v21, v23, s44 bitop3:0x7e\n" \
  "v_bitop3_b32 v25, v25, v27, s44 bitop3:0x7e\n" \
  "v_bitop3_b32 v29, v29, v31, s44 bitop3:0x7e\n" \
  "v_bitop3_b32 v33, v33, v35, s44 bitop3:0x7e\n" \
  "v_bitop3_b32 v37, v37, v39, s44 bitop3:0x7e\n" \
  "v_bitop3_b32 v41, v41, v43, s44 bitop3:0x7e\n" \
  "v_bitop3_b32 v45, v45, v47, s44 bitop3:0x7e\n" \
  "v_or3_b32 v13, v13, v17, v21\n" \
  "v_or3_b32 v25, v25, v29, v33\n" \
  "v_or3_b32 v37, v37, v41, v45\n" \
  "v_or3_b32 v13, v13, v25, v37\n" \
  "v_cmp_ne_u32 vcc, 0, v13\n" \
  "s_cbranch_vccz Mpk%=\n" \
  "s_sleep 4\n" \
  "s_sub_u32 s97, s97, 1\n" \
  "s_cbranch_scc0 Mpr%=\n" \
  "Mpk%=:\n" \
  "v_add_f32 v54, v12, v16\n" \
  "v_add_f32 v55, v14, v18\n" \
  "v_add_f32 v52, v24, v28\n" \
  "v_add_f32 v53, v26, v30\n" \
  "v_add_f32 v56, v36, v40\n" \
  "v_add_f32 v57, v38, v42\n" \
  "v_add_f32 v54, v54, v20\n" \
  "v_add_f32 v55, v55, v22\n" \
  "v_add_f32 v52, v52, v32\n" \
  "v_add_f32 v53, v53, v34\n" \
  "v_add_f32 v56, v56, v44\n" \
  "v_add_f32 v57, v57, v46\n" \
  "v_cvt_pk_bf16_f32 v52, 0, v52\n" \
  "v_cvt_pk_bf16_f32 v53, 0, v53\n" \
  "v_cvt_pk_bf16_f32 v54, 0, v54\n" \
  "v_cvt_pk_bf16_f32 v55, 0, v55\n" \
  "v_cvt_pk_bf16_f32 v56, v56, 0\n" \
  "v_cvt_pk_bf16_f32 v57, 0, v57\n" \
  "v_add_f32 v52, 1.0, v52\n" \
  "v_add_f32 v53, 1.0, v53\n" \
  "v_cvt_pk_bf16_f32 v52, 0, v52\n" \
  "v_cvt_pk_bf16_f32 v53, 0, v53\n" \
  "v_lshlrev_b32 v8, 16, v58\n" \
  "v_and_b32 v9, 0xffff0000, v58\n" \
  "v_mul_f32 v8, v8, v52\n" \
  "v_mul_f32 v9, v9, v53\n" \
  "v_cvt_pk_bf16_f32 v8, 0, v8\n" \
  "v_cvt_pk_bf16_f32 v9, 0, v9\n" \
  "v_add_f32 v60, v8, v9\n" \
  "v_lshrrev_b32 v8, 16, v8\n" \
  "v_or_b32 v59, v8, v9\n" \
  "v_mov_b32 v8, 147456\n" \
  "v_lshl_add_u32 v6, v6, 1, v8\n" \
  "ds_write_b128 v6, v[52:55]\n" \
  "ds_write_b128 v6, v[56:59] offset:16\n" \
  "s_mov_b64 exec, -1\n" \
  "s_lshl_b32 s50, s50, 4\n" \
  "v_lshrrev_b32 v5, 2, v4\n" \
  "v_add_f32_dpp v60, v60, v60 quad_perm:[1,0,3,2] row_mask:0xf bank_mask:0xf bound_ctrl:1\n" \
  "v_and_b32 v5, 12, v5\n" \
  "v_add3_u32 v5, v5, v1, s50\n" \
  "v_add_f32_dpp v60, v60, v60 quad_perm:[2,3,0,1] row_mask:0xf bank_mask:0xf bound_ctrl:1\n" \
  "s_nop 1\n" \
  "v_add_f32_dpp v60, v60, v60 row_half_mirror row_mask:0xf bank_mask:0xf bound_ctrl:1\n" \
  "s_nop 1\n" \
  "v_add_f32_dpp v60, v60, v60 row_mirror row_mask:0xf bank_mask:0xf bound_ctrl:1\n" \
  "ds_write_b32 v5, v60 offset:32\n" \
  "Mla%=:\n" \
  "v_lshrrev_b32 v5, 3, %[t]\n" \
  "v_mul_u32_u24 v6, 33024, v5\n" \
  "v_lshl_add_u32 v6, v3, 4, v6\n" \
  "s_lshl_b32 s50, %[b], 7\n" \
  "s_add_u32 s50, s50, 139264\n" \
  "s_add_u32 s51, s50, 16512\n" \
  "v_mul_u32_u24 v7, 96, v5\n" \
  "v_add_u32 v7, 147456, v7\n" \
  "v_lshrrev_b32 v9, 5, v4\n" \
  "v_lshl_add_u32 v8, v3, 1, v9\n" \
  "v_mul_u32_u24 v10, 48, v8\n" \
  "s_lshl2_add_u32 s52, %[w], 124192\n" \
  "v_add_u32 v10, s52, v10\n" \
  "v_mul_u32_u24 v8, 208, v8\n" \
  "v_bfe_u32 v9, v4, 4, 1\n" \
  "v_lshl_add_u32 v8, v9, 2, v8\n" \
  "s_lshl4_add_u32 s52, %[w], 120864\n" \
  "v_add_u32 v8, s52, v8\n" \
  "v_mul_u32_u24 v9, 2336, v3\n" \
  "v_mul_u32_u24 v11, 12, v5\n" \
  "v_add_u32 v9, v9, v11\n" \
  "v_lshrrev_b32 v12, 3, v4\n" \
  "v_lshl_add_u32 v11, v3, 3, v12\n" \
  "v_lshlrev_b32 v11, 2, v11\n" \
  "v_mul_u32_u24 v12, 3072, v12\n" \
  "v_lshl_add_u32 v12, v3, 4, v12\n" \
  "s_mul_i32 s52, %[b], 24576\n" \
  "s_lshl_b32 s53, %[w], 7\n" \
  "s_add_u32 s52, s52, s53\n" \
  "s_add_u32 s52, s52, 3309568\n" \
  "v_lshl_add_u32 v13, v3, 4, v1\n" \
  "s_mov_b32 s54, 0x3f80\n" \
  "s_mov_b32 s55, 0x3f800000\n" \
  "v_mov_b64 v[48:49], 0\n" \
  "v_mov_b64 v[50:51], 0\n" \
  "v_mov_b64 v[52:53], 0\n" \
  "v_mov_b64 v[54:55], 0\n" \
  "s_mov_b32 s56, 0x05040100\n" \
  "s_mov_b32 s57, 0x07060302\n" \
  LSTAMP8 \
  "s_waitcnt lgkmcnt(0)\n" \
  "s_barrier\n" \
  LSTAMP9 \
  "buffer_load_dwordx4 v[16:19], v6, s[40:43], s50 offen sc1\n" \
  "buffer_load_dwordx4 v[20:23], v6, s[40:43], s51 offen sc1\n" \
  "ds_read_b128 v[24:27], v7 offset:0\n" \
  "ds_read_b128 v[28:31], v7 offset:16\n" \
  "ds_read_b128 v[32:35], v7 offset:32\n" \
  "ds_read_b128 v[36:39], v7 offset:48\n" \
  "ds_read_b128 v[40:43], v7 offset:64\n" \
  "ds_read_b128 v[44:47], v7 offset:80\n" \
  "s_waitcnt vmcnt(0)\n" \
  "v_bitop3_b32 v14, v19, v23, s44 bitop3:0x7e\n" \
  "v_cmp_ne_u32 vcc, 0, v14\n" \
  "s_cbranch_vccnz Mxs%=\n" \
  "Mxb%=:\n" \
  LSTAMP10 \
  "v_perm_b32 v19, v22, v18, s56\n" \
  "v_perm_b32 v23, v22, v18, s57\n" \
  "s_waitcnt lgkmcnt(0)\n" \
  "v_dot2c_f32_bf16 v48, 0x3f803f80, v16\n" \
  "v_dot2c_f32_bf16 v50, v16, v16\n" \
  "v_dot2c_f32_bf16 v52, v16, v31\n" \
  "v_dot2c_f32_bf16 v49, 0x3f803f80, v17\n" \
  "v_dot2c_f32_bf16 v51, v17, v17\n" \
  "v_dot2c_f32_bf16 v53, v17, v31\n" \
  "v_dot2c_f32_bf16 v48, 0x3f803f80, v20\n" \
  "v_dot2c_f32_bf16 v50, v20, v20\n" \
  "v_dot2c_f32_bf16 v52, v20, v47\n" \
  "v_dot2c_f32_bf16 v49, 0x3f803f80, v21\n" \
  "v_dot2c_f32_bf16 v51, v21, v21\n" \
  "v_dot2c_f32_bf16 v53, v21, v47\n" \
  "v_dot2c_f32_bf16 v48, 0x3f803f80, v19\n" \
  "v_dot2c_f32_bf16 v50, v19, v19\n" \
  "v_dot2c_f32_bf16 v52, v19, v39\n" \
  "v_dot2c_f32_bf16 v49, 0x3f803f80, v23\n" \
  "v_dot2c_f32_bf16 v51, v23, v23\n" \
  "v_dot2c_f32_bf16 v53, v23, v39\n" \
  "s_nop 0\n" \
  "v_permlane32_swap_b32 v48, v49\n" \
  "v_permlane32_swap_b32 v50, v51\n" \
  "v_permlane32_swap_b32 v52, v53\n" \
  "v_add_f32 v48, v48, v49\n" \
  "v_add_f32 v50, v50, v51\n" \
  "v_add_f32 v52, v52, v53\n" \
  "s_nop 0\n" \
  "v_permlane16_swap_b32 v48, v50\n" \
  "v_add_f32_dpp v52, v52, v52 row_ror:8 row_mask:0xf bank_mask:0xf bound_ctrl:1\n" \
  "v_add_f32 v48, v48, v50\n" \
  "s_nop 1\n" \
  "v_add_f32_dpp v48, v48, v48 row_ror:8 row_mask:0xf bank_mask:0xf bound_ctrl:1\n" \
  "ds_write2_b32 v8, v48, v52 offset1:2\n" \
  "s_waitcnt lgkmcnt(0)\n" \
  "s_barrier\n" \
  "s_barrier\n" \
  "ds_read_b128 v[56:59], v13 offset:128\n" \
  "s_waitcnt lgkmcnt(0)\n" \
  "v_dot2_f32_bf16 v64, v16, s54, v56\n" \
  "v_dot2_f32_bf16 v65, v16, s55, v56\n" \
  "v_dot2_f32_bf16 v66, v19, s54, v56\n" \
  "v_dot2_f32_bf16 v67, v19, s55, v56\n" \
  "v_dot2_f32_bf16 v68, v20, s54, v56\n" \
  "v_dot2_f32_bf16 v69, v20, s55, v56\n" \
  "v_dot2_f32_bf16 v70, v17, s54, v58\n" \
  "v_dot2_f32_bf16 v71, v17, s55, v58\n" \
  "v_dot2_f32_bf16 v72, v23, s54, v58\n" \
  "v_dot2_f32_bf16 v73, v23, s55, v58\n" \
  "v_dot2_f32_bf16 v74, v21, s54, v58\n" \
  "v_dot2_f32_bf16 v75, v21, s55, v58\n" \
  "v_cvt_pk_bf16_f32 v64, 0, v64\n" \
  "v_cvt_pk_bf16_f32 v65, 0, v65\n" \
  "v_cvt_pk_bf16_f32 v66, 0, v66\n" \
  "v_cvt_pk_bf16_f32 v67, 0, v67\n" \
  "v_cvt_pk_bf16_f32 v68, 0, v68\n" \
  "v_cvt_pk_bf16_f32 v69, 0, v69\n" \
  "v_cvt_pk_bf16_f32 v70, 0, v70\n" \
  "v_cvt_pk_bf16_f32 v71, 0, v71\n" \
  "v_cvt_pk_bf16_f32 v72, 0, v72\n" \
  "v_cvt_pk_bf16_f32 v73, 0, v73\n" \
  "v_cvt_pk_bf16_f32 v74, 0, v74\n" \
  "v_cvt_pk_bf16_f32 v75, 0, v75\n" \
  "v_pk_mul_f32 v[64:65], v[64:65], v[56:57] op_sel:[0,1]\n" \
  "v_pk_mul_f32 v[66:67], v[66:67], v[56:57] op_sel:[0,1]\n" \
  "v_pk_mul_f32 v[68:69], v[68:69], v[56:57] op_sel:[0,1]\n" \
  "v_pk_mul_f32 v[70:71], v[70:71], v[58:59] op_sel:[0,1]\n" \
  "v_pk_mul_f32 v[72:73], v[72:73], v[58:59] op_sel:[0,1]\n" \
  "v_pk_mul_f32 v[74:75], v[74:75], v[58:59] op_sel:[0,1]\n" \
  "v_cvt_pk_bf16_f32 v64, 0, v64\n" \
  "v_cvt_pk_bf16_f32 v65, 0, v65\n" \
  "v_cvt_pk_bf16_f32 v66, 0, v66\n" \
  "v_cvt_pk_bf16_f32 v67, 0, v67\n" \
  "v_cvt_pk_bf16_f32 v68, 0, v68\n" \
  "v_cvt_pk_bf16_f32 v69, 0, v69\n" \
  "v_cvt_pk_bf16_f32 v70, 0, v70\n" \
  "v_cvt_pk_bf16_f32 v71, 0, v71\n" \
  "v_cvt_pk_bf16_f32 v72, 0, v72\n" \
  "v_cvt_pk_bf16_f32 v73, 0, v73\n" \
  "v_cvt_pk_bf16_f32 v74, 0, v74\n" \
  "v_cvt_pk_bf16_f32 v75, 0, v75\n" \
  "v_pk_mul_f32 v[64:65], v[64:65], v[24:25]\n" \
  "v_pk_mul_f32 v[66:67], v[66:67], v[32:33]\n" \
  "v_pk_mul_f32 v[68:69], v[68:69], v[40:41]\n" \
  "v_pk_mul_f32 v[70:71], v[70:71], v[24:25]\n" \
  "v_pk_mul_f32 v[72:73], v[72:73], v[32:33]\n" \
  "v_pk_mul_f32 v[74:75], v[74:75], v[40:41]\n" \
  "v_cvt_pk_bf16_f32 v64, 0, v64\n" \
  "v_cvt_pk_bf16_f32 v65, 0, v65\n" \
  "v_cvt_pk_bf16_f32 v66, 0, v66\n" \
  "v_cvt_pk_bf16_f32 v67, 0, v67\n" \
  "v_cvt_pk_bf16_f32 v68, 0, v68\n" \
  "v_cvt_pk_bf16_f32 v69, 0, v69\n" \
  "v_cvt_pk_bf16_f32 v70, 0, v70\n" \
  "v_cvt_pk_bf16_f32 v71, 0, v71\n" \
  "v_cvt_pk_bf16_f32 v72, 0, v72\n" \
  "v_cvt_pk_bf16_f32 v73, 0, v73\n" \
  "v_cvt_pk_bf16_f32 v74, 0, v74\n" \
  "v_cvt_pk_bf16_f32 v75, 0, v75\n" \
  "v_pk_add_f32 v[64:65], v[64:65], v[26:27]\n" \
  "v_pk_add_f32 v[66:67], v[66:67], v[34:35]\n" \
  "v_pk_add_f32 v[68:69], v[68:69], v[42:43]\n" \
  "v_pk_add_f32 v[70:71], v[70:71], v[26:27]\n" \
  "v_pk_add_f32 v[72:73], v[72:73], v[34:35]\n" \
  "v_pk_add_f32 v[74:75], v[74:75], v[42:43]\n" \
  "v_cvt_pk_bf16_f32 v76, v64, v65\n" \
  "v_cvt_pk_bf16_f32 v77, v66, v67\n" \
  "v_cvt_pk_bf16_f32 v78, v68, v69\n" \
  "v_cvt_pk_bf16_f32 v79, v70, v71\n" \
  "v_cvt_pk_bf16_f32 v80, v72, v73\n" \
  "v_cvt_pk_bf16_f32 v81, v74, v75\n" \
  "ds_bpermute_b32 v84, v11, v76\n" \
  "ds_bpermute_b32 v85, v11, v77\n" \
  "ds_bpermute_b32 v86, v11, v78\n" \
  "ds_bpermute_b32 v88, v11, v79\n" \
  "ds_bpermute_b32 v89, v11, v80\n" \
  "ds_bpermute_b32 v90, v11, v81\n" \
  "v_dot2c_f32_bf16 v54, v76, v30\n" \
  "v_dot2c_f32_bf16 v55, v79, v30\n" \
  "v_dot2c_f32_bf16 v54, v77, v38\n" \
  "v_dot2c_f32_bf16 v55, v80, v38\n" \
  "v_dot2c_f32_bf16 v54, v78, v46\n" \
  "v_dot2c_f32_bf16 v55, v81, v46\n" \
  "v_mov_b32 v87, s44\n" \
  "v_mov_b32 v91, s44\n" \
  "s_nop 0\n" \
  "v_permlane32_swap_b32 v54, v55\n" \
  "v_add_f32 v54, v54, v55\n" \
  "v_mov_b32 v55, v54\n" \
  "s_waitcnt lgkmcnt(0)\n" \
  "buffer_store_dwordx4 v[84:87], v12, s[40:43], s52 offen sc1\n" \
  "v_permlane16_swap_b32 v54, v55\n" \
  "buffer_store_dwordx4 v[88:91], v12, s[40:43], s52 offen offset:1536 sc1\n" \
  "v_add_f32 v54, v54, v55\n" \
  "s_nop 1\n" \
  "v_add_f32_dpp v54, v54, v54 row_ror:8 row_mask:0xf bank_mask:0xf bound_ctrl:1\n" \
  "ds_write_b32 v10, v54\n" \
  LSTAMP11 \
  "s_waitcnt lgkmcnt(0)\n" \
  "s_barrier\n" \
  "v_dot2_f32_bf16 v64, v76, v28, 0\n" \
  "v_dot2_f32_bf16 v65, v76, v29, 0\n" \
  "v_dot2_f32_bf16 v66, v77, v36, 0\n" \
  "v_dot2_f32_bf16 v67, v77, v37, 0\n" \
  "v_dot2_f32_bf16 v68, v78, v44, 0\n" \
  "v_dot2_f32_bf16 v69, v78, v45, 0\n" \
  "v_dot2_f32_bf16 v70, v79, v28, 0\n" \
  "v_dot2_f32_bf16 v71, v79, v29, 0\n" \
  "v_dot2_f32_bf16 v72, v80, v36, 0\n" \
  "v_dot2_f32_bf16 v73, v80, v37, 0\n" \
  "v_dot2_f32_bf16 v74, v81, v44, 0\n" \
  "v_dot2_f32_bf16 v75, v81, v45, 0\n" \
  "v_cvt_pk_bf16_f32 v64, 0, v64\n" \
  "v_cvt_pk_bf16_f32 v65, 0, v65\n" \
  "v_cvt_pk_bf16_f32 v66, 0, v66\n" \
  "v_cvt_pk_bf16_f32 v67, 0, v67\n" \
  "v_cvt_pk_bf16_f32 v68, 0, v68\n" \
  "v_cvt_pk_bf16_f32 v69, 0, v69\n" \
  "v_cvt_pk_bf16_f32 v70, 0, v70\n" \
  "v_cvt_pk_bf16_f32 v71, 0, v71\n" \
  "v_cvt_pk_bf16_f32 v72, 0, v72\n" \
  "v_cvt_pk_bf16_f32 v73, 0, v73\n" \
  "v_cvt_pk_bf16_f32 v74, 0, v74\n" \
  "v_cvt_pk_bf16_f32 v75, 0, v75\n" \
  "v_dot2_f32_bf16 v64, v16, s54, v64\n" \
  "v_dot2_f32_bf16 v65, v16, s55, v65\n" \
  "v_dot2_f32_bf16 v66, v19, s54, v66\n" \
  "v_dot2_f32_bf16 v67, v19, s55, v67\n" \
  "v_dot2_f32_bf16 v68, v20, s54, v68\n" \
  "v_dot2_f32_bf16 v69, v20, s55, v69\n" \
  "v_dot2_f32_bf16 v70, v17, s54, v70\n" \
  "v_dot2_f32_bf16 v71, v17, s55, v71\n" \
  "v_dot2_f32_bf16 v72, v23, s54, v72\n" \
  "v_dot2_f32_bf16 v73, v23, s55, v73\n" \
  "v_dot2_f32_bf16 v74, v21, s54, v74\n" \
  "v_dot2_f32_bf16 v75, v21, s55, v75\n" \
  "v_cvt_pk_bf16_f32 v76, v64, v65\n" \
  "v_cvt_pk_bf16_f32 v77, v66, v67\n" \
  "v_cvt_pk_bf16_f32 v78, v68, v69\n" \
  "v_cvt_pk_bf16_f32 v79, v70, v71\n" \
  "v_cvt_pk_bf16_f32 v80, v72, v73\n" \
  "v_cvt_pk_bf16_f32 v81, v74, v75\n" \
  "ds_write2_b32 v9, v76, v77 offset1:1\n" \
  "ds_write_b32 v9, v78 offset:8\n" \
  "ds_write_b32 v9, v79 offset:1168\n" \
  "ds_write_b32 v9, v80 offset:1172\n" \
  "ds_write_b32 v9, v81 offset:1176\n" \
  "v_mul_u32_u24 v5, 911, %[t]\n" \
  "v_lshrrev_b32 v5, 16, v5\n" \
  "v_add_lshl_u32 v5, v5, %[t], 4\n" \
  "v_lshlrev_b32 v6, 4, %[t]\n" \
  "s_mul_i32 s50, %[b], 18432\n" \
  "s_add_u32 s48, s48, s50\n" \
  "s_addc_u32 s49, s49, 0\n" \
  "v_add_u32 v7, 9216, v6\n" \
  "s_waitcnt lgkmcnt(0)\n" \
  "s_barrier\n" \
  "s_cmp_ge_u32 %[w], 9\n" \
  "s_cbranch_scc1 Mlz%=\n" \
  "ds_read_b128 v[16:19], v5\n" \
  "ds_read_b128 v[20:23], v5 offset:9344\n" \
  "s_waitcnt lgkmcnt(1)\n" \
  "global_store_dwordx4 v6, v[16:19], s[48:49]" OFLAG "\n" \
  "s_waitcnt lgkmcnt(0)\n" \
  "global_store_dwordx4 v7, v[20:23], s[48:49]" OFLAG "\n" \
  "s_branch Mlz%=\n" \
  "Mxs%=:\n" \
  "s_movk_i32 s97, 0x7fff\n" \
  "Mxr%=:\n" \
  LSTAMPC \
  "s_mov_b64 exec, vcc\n" \
  "buffer_load_dwordx4 v[16:19], v6, s[40:43], s50 offen sc1\n" \
  "buffer_load_dwordx4 v[20:23], v6, s[40:43], s51 offen sc1\n" \
  "s_mov_b64 exec, -1\n" \
  "s_waitcnt vmcnt(0)\n" \
  "v_bitop3_b32 v14, v19, v23, s44 bitop3:0x7e\n" \
  "v_cmp_ne_u32 vcc, 0, v14\n" \
  "s_cbranch_vccz Mxb%=\n" \
  "s_sub_u32 s97, s97, 1\n" \
  "s_cbranch_scc0 Mxr%=\n" \
  "s_branch Mxb%=\n" \
  "Mls%=:\n" \
  "s_lshr_b32 s50, %[b], 4\n" \
  "s_cmp_lt_u32 s50, " STR(X1T) "\n" \
  "s_cbranch_scc1 Mld%=\n" \
  "s_waitcnt vmcnt(0)\n" \
  "s_sleep " STR(X1SLP) "\n" \
  "Mld%=:\n" \
  "s_cmp_eq_u32 %[w], 15\n" \
  "s_cbranch_scc1 Mlk%=\n" \
  "s_barrier\n" \
  "s_barrier\n" \
  "s_barrier\n" \
  "s_barrier\n" \
  "s_barrier\n" \
  "s_branch Mlz%=\n" \
  "Mlk%=:\n" \
  "s_barrier\n" \
  "s_setprio 3\n" \
  "ds_read_b128 v[16:19], v1 offset:32\n" \
  "ds_read_b128 v[20:23], v1 offset:48\n" \
  "ds_read_b128 v[24:27], v1 offset:64\n" \
  "ds_read_b128 v[28:31], v1 offset:80\n" \
  "ds_read_b128 v[32:35], v1 offset:96\n" \
  "v_lshrrev_b32 v5, 2, v4\n" \
  "v_mul_u32_u24 v6, 208, v5\n" \
  "v_and_b32 v7, 3, v4\n" \
  "v_mul_u32_u24 v8, 48, v7\n" \
  "s_mov_b32 s50, 120864\n" \
  "v_add3_u32 v6, v6, v8, s50\n" \
  "v_lshl_add_u32 v9, v5, 3, v1\n" \
  "s_lshl_b32 s50, %[b], 4\n" \
  "v_add_u32 v10, s50, v5\n" \
  "v_lshlrev_b32 v11, 3, v10\n" \
  "v_sub_u32 v10, 2047, v10\n" \
  "v_mov_b32 v13, s44\n" \
  "v_mul_u32_u24 v14, 48, v5\n" \
  "v_mul_u32_u24 v7, 12, v7\n" \
  "s_mov_b32 s50, 124192\n" \
  "v_add3_u32 v14, v14, v7, s50\n" \
  "s_mov_b32 s50, 87040\n" \
  "s_mov_b32 s51, 103424\n" \
  "s_waitcnt lgkmcnt(0)\n" \
  "v_pk_add_f32 v[16:17], v[16:17], v[18:19]\n" \
  "v_pk_add_f32 v[20:21], v[20:21], v[22:23]\n" \
  "v_pk_add_f32 v[24:25], v[24:25], v[26:27]\n" \
  "v_pk_add_f32 v[16:17], v[16:17], v[20:21]\n" \
  "v_pk_add_f32 v[16:17], v[16:17], v[24:25]\n" \
  "v_pk_add_f32 v[28:29], v[28:29], v[30:31]\n" \
  "v_pk_add_f32 v[32:33], v[32:33], v[34:35]\n" \
  "v_pk_add_f32 v[16:17], v[16:17], v[28:29]\n" \
  "v_pk_add_f32 v[16:17], v[16:17], v[32:33]\n" \
  "v_add_f32 v15, v16, v17\n" \
  "s_barrier\n" \
  "ds_read_b128 v[16:19], v6\n" \
  "ds_read_b128 v[20:23], v6 offset:16\n" \
  "ds_read_b128 v[24:27], v6 offset:32\n" \
  "s_waitcnt lgkmcnt(1)\n" \
  "v_pk_add_f32 v[16:17], v[16:17], v[20:21]\n" \
  "v_pk_add_f32 v[18:19], v[18:19], v[22:23]\n" \
  "s_waitcnt lgkmcnt(0)\n" \
  "v_pk_add_f32 v[16:17], v[16:17], v[24:25]\n" \
  "v_pk_add_f32 v[18:19], v[18:19], v[26:27]\n" \
  "v_add_f32 v18, v18, v19\n" \
  "v_add_f32_dpp v16, v16, v16 quad_perm:[1,0,3,2] row_mask:0xf bank_mask:0xf bound_ctrl:1\n" \
  "v_add_f32_dpp v17, v17, v17 quad_perm:[1,0,3,2] row_mask:0xf bank_mask:0xf bound_ctrl:1\n" \
  "v_add_f32_dpp v18, v18, v18 quad_perm:[1,0,3,2] row_mask:0xf bank_mask:0xf bound_ctrl:1\n" \
  "v_add_f32_dpp v16, v16, v16 quad_perm:[2,3,0,1] row_mask:0xf bank_mask:0xf bound_ctrl:1\n" \
  "v_add_f32_dpp v17, v17, v17 quad_perm:[2,3,0,1] row_mask:0xf bank_mask:0xf bound_ctrl:1\n" \
  "v_add_f32_dpp v18, v18, v18 quad_perm:[2,3,0,1] row_mask:0xf bank_mask:0xf bound_ctrl:1\n" \
  "v_mul_f32 v16, 0xbae38e39, v16\n" \
  "v_mul_f32 v17, 0x3ae38e39, v17\n" \
  "v_fma_f32 v17, -v16, v16, v17\n" \
  "v_max_f32 v17, 0, v17\n" \
  "v_cvt_pk_bf16_f32 v17, 0, v17\n" \
  "v_add_f32 v17, 0x3727c5ac, v17\n" \
  "v_cvt_pk_bf16_f32 v17, 0, v17\n" \
  "v_sqrt_f32 v17, v17\n" \
  "v_cvt_pk_bf16_f32 v16, 0, v16\n" \
  "v_cvt_pk_bf16_f32 v17, 0, v17\n" \
  "v_rcp_f32 v17, v17\n" \
  "v_fma_f32 v12, v16, v15, v18\n" \
  "v_mul_f32 v12, v12, v17\n" \
  "ds_write_b64 v9, v[16:17] offset:128\n" \
  "v_and_b32 v18, 0x7ff, v12\n" \
  "v_add_u32 v18, 0x7ff, v18\n" \
  "v_or_b32 v12, v12, v18\n" \
  "v_ashrrev_i32 v18, 31, v12\n" \
  "v_or_b32 v18, 0x80000000, v18\n" \
  "v_xor_b32 v12, v12, v18\n" \
  "v_and_b32 v12, 0xfffff800, v12\n" \
  "v_or_b32 v12, v12, v10\n" \
  "s_mov_b32 exec_lo, 0x11111111\n" \
  "s_mov_b32 exec_hi, 0x11111111\n" \
  "buffer_store_dwordx2 v[12:13], v11, s[40:43], s50 offen sc1\n" \
  "s_mov_b64 exec, -1\n" \
  LSTAMP13 \
  "s_waitcnt lgkmcnt(0)\n" \
  "s_barrier\n" \
  "s_barrier\n" \
  "ds_read2_b32 v[16:17], v14 offset1:1\n" \
  "ds_read_b32 v18, v14 offset:8\n" \
  "s_waitcnt lgkmcnt(0)\n" \
  "v_add_f32 v12, v16, v17\n" \
  "v_add_f32 v12, v12, v18\n" \
  "s_nop 1\n" \
  "v_add_f32_dpp v12, v12, v12 quad_perm:[1,0,3,2] row_mask:0xf bank_mask:0xf bound_ctrl:1\n" \
  "s_nop 1\n" \
  "v_add_f32_dpp v12, v12, v12 quad_perm:[2,3,0,1] row_mask:0xf bank_mask:0xf bound_ctrl:1\n" \
  "v_and_b32 v18, 0x7ff, v12\n" \
  "v_add_u32 v18, 0x7ff, v18\n" \
  "v_or_b32 v12, v12, v18\n" \
  "v_ashrrev_i32 v18, 31, v12\n" \
  "v_or_b32 v18, 0x80000000, v18\n" \
  "v_xor_b32 v12, v12, v18\n" \
  "v_and_b32 v12, 0xfffff800, v12\n" \
  "v_or_b32 v12, v12, v10\n" \
  "s_mov_b32 exec_lo, 0x11111111\n" \
  "s_mov_b32 exec_hi, 0x11111111\n" \
  "buffer_store_dwordx2 v[12:13], v11, s[40:43], s51 offen sc1\n" \
  "s_mov_b64 exec, -1\n" \
  LSTAMP14 \
  "s_barrier\n" \
  "Mlz%=:\n" \
  "s_waitcnt vmcnt(0)\n" \
  "s_barrier\n" \
  "s_cmp_lg_u32 %[w], 15\n" \
  "s_cbranch_scc1 Mly%=\n" \
  "s_lshl2_add_u32 s50, %[b], 136192\n" \
  "s_mov_b64 exec, 1\n" \
  "buffer_store_dword v13, off, s[40:43], s50 sc1\n" \
  "s_mov_b64 exec, -1\n" \
  "Mly%=:\n"

#define EW_HEAD \
  "s_load_dwordx4 s[52:55], %[k], 0x58\n" \
  "s_load_dwordx2 s[40:41], %[k], 0x70\n" \
  "s_lshr_b32 s50, %[w], 2\n" \
  "s_mov_b32 s42, 10403840\n" \
  "s_mov_b32 s43, 0x20000\n" \
  "s_cmp_eq_u32 s50, 3\n" \
  "s_cbranch_scc1 Eez%=\n" \
  "s_cmpk_ge_u32 %[b], 246\n" \
  "s_cbranch_scc1 Een%=\n" \
  "s_sub_u32 s51, %[b], 192\n" \
  "s_mul_i32 s56, s51, 43\n" \
  "s_lshr_b32 s56, s56, 7\n" \
  "s_mul_i32 s57, s56, 3\n" \
  "s_sub_u32 s57, s51, s57\n" \
  "s_mul_i32 s51, s57, 192\n" \
  "s_cmp_eq_u32 s50, 2\n" \
  "s_cbranch_scc1 Eec%=\n" \
  "v_bfe_u32 v1, %[t], 3, 5\n" \
  "v_add_u32 v2, s51, v1\n" \
  "v_mul_u32_u24 v2, 6912, v2\n" \
  "v_and_b32 v3, 2, v1\n" \
  "v_lshrrev_b32 v1, 1, v1\n" \
  "v_and_or_b32 v3, v1, 4, v3\n" \
  "v_xor_b32 v3, v3, %[t]\n" \
  "v_and_b32 v3, 7, v3\n" \
  "s_lshl_b32 s58, s56, 7\n" \
  "v_lshl_add_u32 v2, v3, 4, v2\n" \
  "v_add_u32 v2, s58, v2\n" \
  "s_cmp_eq_u32 s50, 1\n" \
  "s_cbranch_scc1 Eeb%=\n" \
  "s_lshl_b32 s51, %[w], 10\n" \
  "s_add_u32 m0, s51, 73728\n" \
  GSTAMP1 \
  "s_waitcnt lgkmcnt(0)\n" \
  "global_load_lds_dwordx4 v2, s[54:55]\n" \
  "s_add_u32 m0, m0, 4096\n" \
  "v_add_u32 v2, 221184, v2\n" \
  "global_load_lds_dwordx4 v2, s[54:55]\n" \
  "s_add_u32 m0, m0, 4096\n" \
  "v_add_u32 v2, 221184, v2\n" \
  "global_load_lds_dwordx4 v2, s[54:55]\n" \
  "s_add_u32 m0, m0, 4096\n" \
  "v_add_u32 v2, 221184, v2\n" \
  "global_load_lds_dwordx4 v2, s[54:55]\n" \
  "s_add_u32 m0, m0, 4096\n" \
  "v_add_u32 v2, 221184, v2\n" \
  "global_load_lds_dwordx4 v2, s[54:55]\n" \
  "s_add_u32 m0, m0, 4096\n" \
  "v_add_u32 v2, 221184, v2\n" \
  "global_load_lds_dwordx4 v2, s[54:55]\n" \
  "s_branch Eeb%=\n" \
  "Een%=:\n" \
  "s_mov_b32 s51, 0\n" \
  "s_cmp_eq_u32 s50, 2\n" \
  "s_cbranch_scc1 Eec%=\n" \
  "s_cmpk_lt_u32 %[b], 248\n" \
  "s_cbranch_scc1 Eeb%=\n" \
  "s_cmp_lg_u32 s50, 0\n" \
  "s_cbranch_scc1 Eeb%=\n" \
  "s_getpc_b64 s[48:49]\n" \
  "Epc%=:\n" \
  "v_lshlrev_b32 v1, 7, %[t]\n" \
  "v_min_u32 v1, Ez%=-Epc%=, v1\n" \
  "s_mov_b32 m0, 160032\n" \
  "s_nop 0\n" \
  "global_load_lds_dword v1, s[48:49]\n" \
  "s_branch Eeb%=\n" \
  "Eez%=:\n" \
  "v_and_b32 v5, 63, %[t]\n" \
  "v_mov_b32 v6, 159488\n" \
  "v_lshl_add_u32 v6, v5, 2, v6\n" \
  "v_mov_b32 v8, 0\n" \
  "ds_write_b32 v6, v8\n" \
  "s_branch Eeb%=\n" \
  "Eec%=:\n" \
  "v_and_b32 v5, 255, %[t]\n" \
  "v_min_u32 v5, 191, v5\n" \
  "v_add_lshl_u32 v6, v5, s51, 1\n" \
  GSTAMP1 \
  "s_waitcnt lgkmcnt(0)\n" \
  "global_load_short_d16_hi v7, v6, s[52:53]\n" \
  "buffer_load_dword v8, off, s[40:43], 0 sc1\n" \
  "Eeb%=:\n"

#define EW_BODY \
  "v_and_b32 v10, 15, %[t]\n" \
  "v_bfe_u32 v11, %[t], 4, 2\n" \
  "v_and_b32 v12, 63, %[t]\n" \
  "s_cmp_lg_u32 s50, 2\n" \
  "s_cbranch_scc1 Em0%=\n" \
  "s_setprio 2\n" \
  "v_bfe_u32 v1, %[t], 2, 2\n" \
  "v_lshl_add_u32 v1, v11, 3, v1\n" \
  "v_lshlrev_b32 v1, 7, v1\n" \
  "v_bfe_u32 v2, %[t], 3, 2\n" \
  "v_xor_b32 v2, %[w], v2\n" \
  "v_and_b32 v2, 3, v2\n" \
  "v_lshl_add_u32 v1, v2, 5, v1\n" \
  "v_and_b32 v2, 3, %[t]\n" \
  "v_lshl_add_u32 v1, v2, 3, v1\n" \
  "v_add_u32 v1, 73728, v1\n" \
  "v_mov_b32 v9, 160416\n" \
  "v_lshl_add_u32 v5, v5, 1, v9\n" \
  "v_lshl_add_u32 v9, v11, 4, v9\n" \
  "v_lshlrev_b32 v3, 3, v10\n" \
  "s_mul_i32 s58, s57, 27648\n" \
  "s_lshl_b32 s59, s56, 9\n" \
  "s_add_u32 s58, s58, s59\n" \
  "s_lshl_b32 s59, %[w], 7\n" \
  "s_add_u32 s58, s58, s59\n" \
  "s_add_u32 s58, s58, 3072\n" \
  "s_cmp_ge_u32 s56, 9\n" \
  "s_cselect_b32 s59, 4608, 0\n" \
  "v_mov_b32 v4, 158208\n" \
  "s_waitcnt vmcnt(0)\n" \
  GSTAMP2 \
  "v_and_b32 v7, 0xffff0000, v7\n" \
  "v_mul_f32 v6, 0xbfb8aa3b, v7\n" \
  "v_exp_f32 v6, v6\n" \
  "v_add_u32 v8, 1, v8\n" \
  "v_add_f32 v6, 1.0, v6\n" \
  "v_rcp_f32 v6, v6\n" \
  "v_readfirstlane_b32 s44, v8\n" \
  "v_mul_f32 v6, v6, v7\n" \
  "v_cvt_pk_bf16_f32 v6, 0, v6\n" \
  "ds_write_b32 v4, v8\n" \
  "s_cmpk_ge_u32 %[b], 246\n" \
  "s_cbranch_scc1 Ew0%=\n" \
  "ds_write_b16_d16_hi v5, v6\n" \
  "s_waitcnt lgkmcnt(0)\n" \
  "s_barrier\n" \
  GSTAMP3 \
  "ds_read_b64_tr_b16 v[40:41], v1 offset:0\n" \
  "ds_read_b64_tr_b16 v[42:43], v1 offset:512\n" \
  "ds_read_b64_tr_b16 v[44:45], v1 offset:4096\n" \
  "ds_read_b64_tr_b16 v[46:47], v1 offset:4608\n" \
  "ds_read_b64_tr_b16 v[48:49], v1 offset:8192\n" \
  "ds_read_b64_tr_b16 v[50:51], v1 offset:8704\n" \
  "ds_read_b64_tr_b16 v[52:53], v1 offset:12288\n" \
  "ds_read_b64_tr_b16 v[54:55], v1 offset:12800\n" \
  "ds_read_b64_tr_b16 v[56:57], v1 offset:16384\n" \
  "ds_read_b64_tr_b16 v[58:59], v1 offset:16896\n" \
  "ds_read_b64_tr_b16 v[60:61], v1 offset:20480\n" \
  "ds_read_b64_tr_b16 v[62:63], v1 offset:20992\n" \
  "ds_read_b128 v[64:67], v9 offset:0\n" \
  "ds_read_b128 v[68:71], v9 offset:64\n" \
  "ds_read_b128 v[72:75], v9 offset:128\n" \
  "ds_read_b128 v[76:79], v9 offset:192\n" \
  "ds_read_b128 v[80:83], v9 offset:256\n" \
  "ds_read_b128 v[84:87], v9 offset:320\n" \
  "s_mov_b64 s[46:47], 0xffff\n" \
  "s_waitcnt lgkmcnt(0)\n" \
  "v_mfma_f32_16x16x32_bf16 v[16:19], v[64:67], v[40:43], 0\n" \
  "v_mfma_f32_16x16x32_bf16 v[16:19], v[68:71], v[44:47], v[16:19]\n" \
  "v_mfma_f32_16x16x32_bf16 v[16:19], v[72:75], v[48:51], v[16:19]\n" \
  "v_mfma_f32_16x16x32_bf16 v[16:19], v[76:79], v[52:55], v[16:19]\n" \
  "v_mfma_f32_16x16x32_bf16 v[16:19], v[80:83], v[56:59], v[16:19]\n" \
  "v_mfma_f32_16x16x32_bf16 v[16:19], v[84:87], v[60:63], v[16:19]\n" \
  "s_nop 7\n" \
  "s_mov_b64 exec, s[46:47]\n" \
  "v_mov_b32 v17, s44\n" \
  "buffer_store_dwordx2 v[16:17], v3, s[40:43], s58 offen sc1\n" \
  "s_mov_b64 exec, -1\n" \
  GSTAMP4 \
  "s_add_u32 s58, s58, 9216\n" \
  "s_add_u32 s58, s58, s59\n" \
  "s_barrier\n" \
  "s_barrier\n" \
  "ds_read_b64_tr_b16 v[40:41], v1 offset:24576\n" \
  "ds_read_b64_tr_b16 v[42:43], v1 offset:25088\n" \
  "ds_read_b64_tr_b16 v[44:45], v1 offset:28672\n" \
  "ds_read_b64_tr_b16 v[46:47], v1 offset:29184\n" \
  "ds_read_b64_tr_b16 v[48:49], v1 offset:32768\n" \
  "ds_read_b64_tr_b16 v[50:51], v1 offset:33280\n" \
  "ds_read_b64_tr_b16 v[52:53], v1 offset:36864\n" \
  "ds_read_b64_tr_b16 v[54:55], v1 offset:37376\n" \
  "ds_read_b64_tr_b16 v[56:57], v1 offset:40960\n" \
  "ds_read_b64_tr_b16 v[58:59], v1 offset:41472\n" \
  "ds_read_b64_tr_b16 v[60:61], v1 offset:45056\n" \
  "ds_read_b64_tr_b16 v[62:63], v1 offset:45568\n" \
  "s_mov_b64 s[46:47], 0xffff\n" \
  "s_waitcnt lgkmcnt(0)\n" \
  "v_mfma_f32_16x16x32_bf16 v[16:19], v[64:67], v[40:43], 0\n" \
  "v_mfma_f32_16x16x32_bf16 v[16:19], v[68:71], v[44:47], v[16:19]\n" \
  "v_mfma_f32_16x16x32_bf16 v[16:19], v[72:75], v[48:51], v[16:19]\n" \
  "v_mfma_f32_16x16x32_bf16 v[16:19], v[76:79], v[52:55], v[16:19]\n" \
  "v_mfma_f32_16x16x32_bf16 v[16:19], v[80:83], v[56:59], v[16:19]\n" \
  "v_mfma_f32_16x16x32_bf16 v[16:19], v[84:87], v[60:63], v[16:19]\n" \
  "s_nop 7\n" \
  "s_mov_b64 exec, s[46:47]\n" \
  "v_mov_b32 v17, s44\n" \
  "buffer_store_dwordx2 v[16:17], v3, s[40:43], s58 offen sc1\n" \
  "s_mov_b64 exec, -1\n" \
  GSTAMP5 \
  "s_add_u32 s58, s58, 4608\n" \
  "v_add_u32 v1, 49152, v1\n" \
  "s_barrier\n" \
  "ds_read_b64_tr_b16 v[40:41], v1 offset:0\n" \
  "ds_read_b64_tr_b16 v[42:43], v1 offset:512\n" \
  "ds_read_b64_tr_b16 v[44:45], v1 offset:4096\n" \
  "ds_read_b64_tr_b16 v[46:47], v1 offset:4608\n" \
  "ds_read_b64_tr_b16 v[48:49], v1 offset:8192\n" \
  "ds_read_b64_tr_b16 v[50:51], v1 offset:8704\n" \
  "ds_read_b64_tr_b16 v[52:53], v1 offset:12288\n" \
  "ds_read_b64_tr_b16 v[54:55], v1 offset:12800\n" \
  "ds_read_b64_tr_b16 v[56:57], v1 offset:16384\n" \
  "ds_read_b64_tr_b16 v[58:59], v1 offset:16896\n" \
  "ds_read_b64_tr_b16 v[60:61], v1 offset:20480\n" \
  "ds_read_b64_tr_b16 v[62:63], v1 offset:20992\n" \
  "s_mov_b64 s[46:47], 0xffff\n" \
  "s_waitcnt lgkmcnt(0)\n" \
  "v_mfma_f32_16x16x32_bf16 v[16:19], v[64:67], v[40:43], 0\n" \
  "v_mfma_f32_16x16x32_bf16 v[16:19], v[68:71], v[44:47], v[16:19]\n" \
  "v_mfma_f32_16x16x32_bf16 v[16:19], v[72:75], v[48:51], v[16:19]\n" \
  "v_mfma_f32_16x16x32_bf16 v[16:19], v[76:79], v[52:55], v[16:19]\n" \
  "v_mfma_f32_16x16x32_bf16 v[16:19], v[80:83], v[56:59], v[16:19]\n" \
  "v_mfma_f32_16x16x32_bf16 v[16:19], v[84:87], v[60:63], v[16:19]\n" \
  "s_nop 7\n" \
  "s_mov_b64 exec, s[46:47]\n" \
  "v_mov_b32 v17, s44\n" \
  "buffer_store_dwordx2 v[16:17], v3, s[40:43], s58 offen sc1\n" \
  "s_mov_b64 exec, -1\n" \
  GSTAMP6 \
  "s_setprio 0\n" \
  "s_branch Ew0%=\n" \
  "Em0%=:\n" \
  "s_cmpk_ge_u32 %[b], 246\n" \
  "s_cbranch_scc1 Em2%=\n" \
  "s_cmp_eq_u32 s50, 3\n" \
  "s_cbranch_scc1 Em1%=\n" \
  "s_cmp_ge_u32 s56, 9\n" \
  "s_cselect_b32 s59, 1152, 0\n" \
  "s_cmp_eq_u32 s50, 0\n" \
  "s_cselect_b32 s58, -1102464, 0\n" \
  "s_cselect_b32 s51, 0, 2304\n" \
  "s_add_u32 s58, s58, s51\n" \
  "s_add_u32 s58, s58, s59\n" \
  "v_add_u32 v2, s58, v2\n" \
  "s_mul_i32 s51, s50, -28672\n" \
  "s_lshl_b32 s58, %[w], 10\n" \
  "s_add_u32 s51, s51, s58\n" \
  "s_add_u32 m0, s51, 122880\n" \
  "s_waitcnt vmcnt(0) lgkmcnt(0)\n" \
  GSTAMP3 \
  "s_cmp_eq_u32 s50, 0\n" \
  "s_cbranch_scc1 Em3%=\n" \
  "s_sleep " STR(P1D) "\n" \
  "global_load_lds_dwordx4 v2, s[54:55]\n" \
  "s_add_u32 m0, m0, 4096\n" \
  "v_add_u32 v2, 221184, v2\n" \
  "global_load_lds_dwordx4 v2, s[54:55]\n" \
  "s_add_u32 m0, m0, 4096\n" \
  "v_add_u32 v2, 221184, v2\n" \
  "global_load_lds_dwordx4 v2, s[54:55]\n" \
  "s_add_u32 m0, m0, 4096\n" \
  "v_add_u32 v2, 221184, v2\n" \
  "global_load_lds_dwordx4 v2, s[54:55]\n" \
  "s_add_u32 m0, m0, 4096\n" \
  "v_add_u32 v2, 221184, v2\n" \
  "global_load_lds_dwordx4 v2, s[54:55]\n" \
  "s_add_u32 m0, m0, 4096\n" \
  "v_add_u32 v2, 221184, v2\n" \
  "global_load_lds_dwordx4 v2, s[54:55]\n" \
  "s_load_dwordx4 s[52:55], %[k], 0x40\n" \
  "s_load_dwordx2 s[48:49], %[k], 0x50\n" \
  "s_load_dwordx2 s[84:85], %[k], 0x68\n" \
  "s_barrier\n" \
  "s_barrier\n" \
  "s_waitcnt lgkmcnt(0)\n" \
  "s_and_b32 s56, %[b], 7\n" \
  "s_bfe_u32 s57, %[b], 0x30003\n" \
  "s_lshr_b32 s58, s52, 7\n" \
  "s_add_u32 s58, s58, s56\n" \
  "s_sub_u32 s58, s57, s58\n" \
  "s_add_u32 s58, s58, 4\n" \
  "s_and_b32 s58, s58, 7\n" \
  "s_mul_i32 s59, s56, 9\n" \
  "s_add_u32 s58, s58, s59\n" \
  "s_lshl_b32 s58, s58, 6\n" \
  "s_add_u32 s59, s59, 8\n" \
  "s_lshl_b32 s59, s59, 6\n" \
  "s_lshl3_add_u32 s59, s57, s59\n" \
  "s_lshl3_add_u32 s57, s56, s57\n" \
  "v_mov_b32 v2, s52\n" \
  "v_mov_b32 v3, s53\n" \
  "v_mov_b32 v4, s54\n" \
  "v_mov_b32 v5, s55\n" \
  "s_mov_b32 s46, 294912\n" \
  "s_mov_b32 s47, 0\n" \
  "v_bfe_u32 v7, %[t], 2, 2\n" \
  "s_mov_b32 vcc_lo, 0xff00ff00\n" \
  "s_mov_b32 vcc_hi, 0xff00ff00\n" \
  "v_cndmask_b32 v2, v2, v4, vcc\n" \
  "v_cndmask_b32 v3, v3, v5, vcc\n" \
  "v_lshrrev_b32 v4, 4, %[t]\n" \
  "v_mul_u32_u24 v4, 9216, v4\n" \
  "s_lshl_b32 s51, %[w], 1\n" \
  "s_and_b32 s51, s51, 4\n" \
  "v_or_b32 v5, s51, v11\n" \
  "v_xor_b32 v5, v5, %[t]\n" \
  "v_and_b32 v5, 7, v5\n" \
  "v_lshl_add_u32 v4, v5, 4, v4\n" \
  "s_lshl_b32 s51, s58, 1\n" \
  "v_add_u32 v4, s51, v4\n" \
  "v_mov_b32 v5, 0\n" \
  "v_lshl_add_u64 v[2:3], v[2:3], 0, v[4:5]\n" \
  "s_lshl_b32 m0, %[w], 10\n" \
  "s_waitcnt vmcnt(0)\n" \
  GSTAMP5 \
  "s_barrier\n" \
  "s_sleep " STR(GED) "\n" \
  "global_load_lds_dwordx4 v[2:3], off" WNT "\n" \
  "s_add_u32 m0, m0, 8192\n" \
  "v_lshl_add_u64 v[2:3], v[2:3], 0, s[46:47]\n" \
  "global_load_lds_dwordx4 v[2:3], off" WNT "\n" \
  "s_add_u32 m0, m0, 8192\n" \
  "v_lshl_add_u64 v[2:3], v[2:3], 0, s[46:47]\n" \
  "global_load_lds_dwordx4 v[2:3], off" WNT "\n" \
  "s_add_u32 m0, m0, 8192\n" \
  "v_lshl_add_u64 v[2:3], v[2:3], 0, s[46:47]\n" \
  "global_load_lds_dwordx4 v[2:3], off" WNT "\n" \
  "s_add_u32 m0, m0, 8192\n" \
  "v_lshl_add_u64 v[2:3], v[2:3], 0, s[46:47]\n" \
  "global_load_lds_dwordx4 v[2:3], off" WNT "\n" \
  "s_add_u32 m0, m0, 8192\n" \
  "v_lshl_add_u64 v[2:3], v[2:3], 0, s[46:47]\n" \
  "global_load_lds_dwordx4 v[2:3], off" WNT "\n" \
  "s_add_u32 m0, m0, 8192\n" \
  "v_lshl_add_u64 v[2:3], v[2:3], 0, s[46:47]\n" \
  "s_barrier\n" \
  "s_barrier\n" \
  "v_mov_b32 v8, 158208\n" \
  "ds_read_b32 v8, v8\n" \
  "s_waitcnt lgkmcnt(0)\n" \
  "v_readfirstlane_b32 s44, v8\n" \
  "s_branch Ew1%=\n" \
  "Em3%=:\n" \
  "s_barrier\n" \
  "s_barrier\n" \
  "global_load_lds_dwordx4 v2, s[54:55]\n" \
  "s_add_u32 m0, m0, 4096\n" \
  "v_add_u32 v2, 221184, v2\n" \
  "global_load_lds_dwordx4 v2, s[54:55]\n" \
  "s_add_u32 m0, m0, 4096\n" \
  "v_add_u32 v2, 221184, v2\n" \
  "global_load_lds_dwordx4 v2, s[54:55]\n" \
  "s_add_u32 m0, m0, 4096\n" \
  "v_add_u32 v2, 221184, v2\n" \
  "global_load_lds_dwordx4 v2, s[54:55]\n" \
  "s_add_u32 m0, m0, 4096\n" \
  "v_add_u32 v2, 221184, v2\n" \
  "global_load_lds_dwordx4 v2, s[54:55]\n" \
  "s_add_u32 m0, m0, 4096\n" \
  "v_add_u32 v2, 221184, v2\n" \
  "global_load_lds_dwordx4 v2, s[54:55]\n" \
  "s_barrier\n" \
  "s_waitcnt vmcnt(0)\n" \
  GSTAMP5 \
  "s_barrier\n" \
  "s_branch Ew0%=\n" \
  "Em1%=:\n" \
  "s_waitcnt lgkmcnt(0)\n" \
  "s_barrier\n" \
  "s_barrier\n" \
  "s_barrier\n" \
  "s_barrier\n" \
  "s_branch Ew0%=\n" \
  "Em2%=:\n" \
  "s_sleep " STR(WDLY) "\n" \
  "Ew0%=:\n" \
  "s_load_dwordx4 s[52:55], %[k], 0x40\n" \
  "s_load_dwordx2 s[48:49], %[k], 0x50\n" \
  "s_load_dwordx2 s[84:85], %[k], 0x68\n" \
  "s_waitcnt lgkmcnt(0)\n" \
  "s_barrier\n" \
  "s_and_b32 s56, %[b], 7\n" \
  "s_bfe_u32 s57, %[b], 0x30003\n" \
  "s_lshr_b32 s58, s52, 7\n" \
  "s_add_u32 s58, s58, s56\n" \
  "s_sub_u32 s58, s57, s58\n" \
  "s_add_u32 s58, s58, 4\n" \
  "s_and_b32 s58, s58, 7\n" \
  "s_mul_i32 s59, s56, 9\n" \
  "s_add_u32 s58, s58, s59\n" \
  "s_lshl_b32 s58, s58, 6\n" \
  "s_add_u32 s59, s59, 8\n" \
  "s_lshl_b32 s59, s59, 6\n" \
  "s_lshl3_add_u32 s59, s57, s59\n" \
  "s_lshl3_add_u32 s57, s56, s57\n" \
  "v_mov_b32 v8, 158208\n" \
  "ds_read_b32 v8, v8\n" \
  "s_waitcnt lgkmcnt(0)\n" \
  "v_readfirstlane_b32 s44, v8\n" \
  "s_cmp_gt_u32 %[w], 8\n" \
  "s_cbranch_scc1 Ewd%=\n" \
  "v_mov_b32 v2, s52\n" \
  "v_mov_b32 v3, s53\n" \
  "v_mov_b32 v4, s54\n" \
  "v_mov_b32 v5, s55\n" \
  "s_mov_b32 s46, 294912\n" \
  "s_mov_b32 s47, 0\n" \
  "v_bfe_u32 v7, %[t], 2, 2\n" \
  "s_cmp_eq_u32 %[w], 8\n" \
  "s_cbranch_scc1 Ewx%=\n" \
  "s_mov_b32 vcc_lo, 0xff00ff00\n" \
  "s_mov_b32 vcc_hi, 0xff00ff00\n" \
  "v_cndmask_b32 v2, v2, v4, vcc\n" \
  "v_cndmask_b32 v3, v3, v5, vcc\n" \
  "v_lshrrev_b32 v4, 4, %[t]\n" \
  "v_mul_u32_u24 v4, 9216, v4\n" \
  "s_lshl_b32 s51, %[w], 1\n" \
  "s_and_b32 s51, s51, 4\n" \
  "v_or_b32 v5, s51, v11\n" \
  "v_xor_b32 v5, v5, %[t]\n" \
  "v_and_b32 v5, 7, v5\n" \
  "v_lshl_add_u32 v4, v5, 4, v4\n" \
  "s_lshl_b32 s51, s58, 1\n" \
  "v_add_u32 v4, s51, v4\n" \
  "v_mov_b32 v5, 0\n" \
  "v_lshl_add_u64 v[2:3], v[2:3], 0, v[4:5]\n" \
  "s_lshl_b32 m0, %[w], 10\n" \
  "s_nop 0\n" \
  "global_load_lds_dwordx4 v[2:3], off" WNT "\n" \
  "s_add_u32 m0, m0, 8192\n" \
  "v_lshl_add_u64 v[2:3], v[2:3], 0, s[46:47]\n" \
  "global_load_lds_dwordx4 v[2:3], off" WNT "\n" \
  "s_add_u32 m0, m0, 8192\n" \
  "v_lshl_add_u64 v[2:3], v[2:3], 0, s[46:47]\n" \
  "global_load_lds_dwordx4 v[2:3], off" WNT "\n" \
  "s_add_u32 m0, m0, 8192\n" \
  "v_lshl_add_u64 v[2:3], v[2:3], 0, s[46:47]\n" \
  "global_load_lds_dwordx4 v[2:3], off" WNT "\n" \
  "s_add_u32 m0, m0, 8192\n" \
  "v_lshl_add_u64 v[2:3], v[2:3], 0, s[46:47]\n" \
  "global_load_lds_dwordx4 v[2:3], off" WNT "\n" \
  "s_add_u32 m0, m0, 8192\n" \
  "v_lshl_add_u64 v[2:3], v[2:3], 0, s[46:47]\n" \
  "global_load_lds_dwordx4 v[2:3], off" WNT "\n" \
  "s_add_u32 m0, m0, 8192\n" \
  "v_lshl_add_u64 v[2:3], v[2:3], 0, s[46:47]\n" \
  "Ew1%=:\n" \
  "global_load_lds_dwordx4 v[2:3], off" WNT "\n" \
  "s_add_u32 m0, m0, 8192\n" \
  "v_lshl_add_u64 v[2:3], v[2:3], 0, s[46:47]\n" \
  "global_load_lds_dwordx4 v[2:3], off" WNT "\n" \
  "s_add_u32 m0, m0, 8192\n" \
  "v_lshl_add_u64 v[2:3], v[2:3], 0, s[46:47]\n" \
  "s_waitcnt vmcnt(" STR(GUW) ")\n" \
  "s_barrier\n" \
  "global_load_lds_dwordx4 v[2:3], off" WNT "\n" \
  "s_add_u32 m0, m0, 8192\n" \
  "v_lshl_add_u64 v[2:3], v[2:3], 0, s[46:47]\n" \
  "global_load_lds_dwordx4 v[2:3], off" WNT "\n" \
  "s_add_u32 m0, m0, 8192\n" \
  "v_lshl_add_u64 v[2:3], v[2:3], 0, s[46:47]\n" \
  "global_load_lds_dwordx4 v[2:3], off" WNT "\n" \
  "s_add_u32 m0, m0, 8192\n" \
  "v_lshl_add_u64 v[2:3], v[2:3], 0, s[46:47]\n" \
  "global_load_lds_dwordx4 v[2:3], off" WNT "\n" \
  "s_add_u32 m0, m0, 8192\n" \
  "v_lshl_add_u64 v[2:3], v[2:3], 0, s[46:47]\n" \
  "global_load_lds_dwordx4 v[2:3], off" WNT "\n" \
  "s_add_u32 m0, m0, 8192\n" \
  "v_lshl_add_u64 v[2:3], v[2:3], 0, s[46:47]\n" \
  "global_load_lds_dwordx4 v[2:3], off" WNT "\n" \
  "s_add_u32 m0, m0, 8192\n" \
  "v_lshl_add_u64 v[2:3], v[2:3], 0, s[46:47]\n" \
  "global_load_lds_dwordx4 v[2:3], off" WNT "\n" \
  "s_add_u32 m0, m0, 8192\n" \
  "v_lshl_add_u64 v[2:3], v[2:3], 0, s[46:47]\n" \
  "global_load_lds_dwordx4 v[2:3], off" WNT "\n" \
  "s_add_u32 m0, m0, 8192\n" \
  "v_lshl_add_u64 v[2:3], v[2:3], 0, s[46:47]\n" \
  "global_load_lds_dwordx4 v[2:3], off" WNT "\n" \
  "s_add_u32 m0, m0, 8192\n" \
  "v_lshl_add_u64 v[2:3], v[2:3], 0, s[46:47]\n" \
  "global_load_lds_dwordx4 v[2:3], off" WNT "\n" \
  "v_lshl_add_u32 v1, v11, 3, v7\n" \
  "v_lshlrev_b32 v1, 8, v1\n" \
  "v_and_b32 v4, 2, %[t]\n" \
  "v_lshl_add_u32 v1, v4, 6, v1\n" \
  "v_and_b32 v4, 1, v11\n" \
  "v_lshl_or_b32 v4, v4, 2, v7\n" \
  "v_xor_b32 v4, %[w], v4\n" \
  "v_lshl_add_u32 v1, v4, 4, v1\n" \
  "v_and_b32 v4, 1, %[t]\n" \
  "v_lshl_add_u32 v1, v4, 3, v1\n" \
  "s_waitcnt vmcnt(0)\n" \
  GSTAMP6 \
  "s_barrier\n" \
  "ds_read_b64_tr_b16 v[40:41], v1 offset:0\n" \
  "ds_read_b64_tr_b16 v[42:43], v1 offset:1024\n" \
  "ds_read_b64_tr_b16 v[44:45], v1 offset:8192\n" \
  "ds_read_b64_tr_b16 v[46:47], v1 offset:9216\n" \
  "ds_read_b64_tr_b16 v[48:49], v1 offset:16384\n" \
  "ds_read_b64_tr_b16 v[50:51], v1 offset:17408\n" \
  "ds_read_b64_tr_b16 v[52:53], v1 offset:24576\n" \
  "ds_read_b64_tr_b16 v[54:55], v1 offset:25600\n" \
  "ds_read_b64_tr_b16 v[56:57], v1 offset:32768\n" \
  "ds_read_b64_tr_b16 v[58:59], v1 offset:33792\n" \
  "ds_read_b64_tr_b16 v[60:61], v1 offset:40960\n" \
  "ds_read_b64_tr_b16 v[62:63], v1 offset:41984\n" \
  "v_add_u32 v1, 49152, v1\n" \
  "ds_read_b64_tr_b16 v[64:65], v1 offset:0\n" \
  "ds_read_b64_tr_b16 v[66:67], v1 offset:1024\n" \
  "ds_read_b64_tr_b16 v[68:69], v1 offset:8192\n" \
  "ds_read_b64_tr_b16 v[70:71], v1 offset:9216\n" \
  "ds_read_b64_tr_b16 v[72:73], v1 offset:16384\n" \
  "ds_read_b64_tr_b16 v[74:75], v1 offset:17408\n" \
  "ds_read_b64_tr_b16 v[76:77], v1 offset:24576\n" \
  "ds_read_b64_tr_b16 v[78:79], v1 offset:25600\n" \
  "ds_read_b64_tr_b16 v[80:81], v1 offset:32768\n" \
  "ds_read_b64_tr_b16 v[82:83], v1 offset:33792\n" \
  "ds_read_b64_tr_b16 v[84:85], v1 offset:40960\n" \
  "ds_read_b64_tr_b16 v[86:87], v1 offset:41984\n" \
  "v_add_u32 v1, 49152, v1\n" \
  "ds_read_b64_tr_b16 v[88:89], v1 offset:0\n" \
  "ds_read_b64_tr_b16 v[90:91], v1 offset:1024\n" \
  "ds_read_b64_tr_b16 v[92:93], v1 offset:8192\n" \
  "ds_read_b64_tr_b16 v[94:95], v1 offset:9216\n" \
  "ds_read_b64_tr_b16 v[96:97], v1 offset:16384\n" \
  "ds_read_b64_tr_b16 v[98:99], v1 offset:17408\n" \
  "ds_read_b64_tr_b16 v[100:101], v1 offset:24576\n" \
  "ds_read_b64_tr_b16 v[102:103], v1 offset:25600\n" \
  "ds_read_b64_tr_b16 v[104:105], v1 offset:32768\n" \
  "ds_read_b64_tr_b16 v[106:107], v1 offset:33792\n" \
  "ds_read_b64_tr_b16 v[108:109], v1 offset:40960\n" \
  "ds_read_b64_tr_b16 v[110:111], v1 offset:41984\n" \
  "v_add_u32 v1, 49152, v1\n" \
  GSTAMP7 \
  "s_branch Ek0%=\n" \
  "Ewx%=:\n" \
  "s_mov_b32 vcc_lo, 0xaaaaaaaa\n" \
  "s_mov_b32 vcc_hi, 0xaaaaaaaa\n" \
  "v_cndmask_b32 v2, v2, v4, vcc\n" \
  "v_cndmask_b32 v3, v3, v5, vcc\n" \
  "v_lshrrev_b32 v4, 1, v12\n" \
  "v_mul_u32_u24 v4, 9216, v4\n" \
  "s_lshl_b32 s51, s59, 1\n" \
  "v_add_u32 v4, s51, v4\n" \
  "v_mov_b32 v5, 0\n" \
  "v_lshl_add_u64 v[2:3], v[2:3], 0, v[4:5]\n" \
  "s_mov_b32 s88, 294912\n" \
  "s_mov_b32 s89, 0\n" \
  "s_barrier\n" \
  "s_barrier\n" \
  "global_load_dwordx4 v[40:43], v[2:3], off\n" \
  ".if !" STR(NOC) "\n" \
  "v_lshl_add_u64 v[2:3], v[2:3], 0, s[88:89]\n" \
  ".endif\n" \
  "global_load_dwordx4 v[44:47], v[2:3], off\n" \
  ".if !" STR(NOC) "\n" \
  "v_lshl_add_u64 v[2:3], v[2:3], 0, s[88:89]\n" \
  ".endif\n" \
  "global_load_dwordx4 v[48:51], v[2:3], off\n" \
  ".if !" STR(NOC) "\n" \
  "v_lshl_add_u64 v[2:3], v[2:3], 0, s[88:89]\n" \
  ".endif\n" \
  "global_load_dwordx4 v[52:55], v[2:3], off\n" \
  ".if !" STR(NOC) "\n" \
  "v_lshl_add_u64 v[2:3], v[2:3], 0, s[88:89]\n" \
  ".endif\n" \
  "global_load_dwordx4 v[56:59], v[2:3], off\n" \
  ".if !" STR(NOC) "\n" \
  "v_lshl_add_u64 v[2:3], v[2:3], 0, s[88:89]\n" \
  ".endif\n" \
  "global_load_dwordx4 v[60:63], v[2:3], off\n" \
  ".if !" STR(NOC) "\n" \
  "v_lshl_add_u64 v[2:3], v[2:3], 0, s[88:89]\n" \
  ".endif\n" \
  "global_load_dwordx4 v[64:67], v[2:3], off\n" \
  ".if !" STR(NOC) "\n" \
  "v_lshl_add_u64 v[2:3], v[2:3], 0, s[88:89]\n" \
  ".endif\n" \
  "global_load_dwordx4 v[68:71], v[2:3], off\n" \
  ".if !" STR(NOC) "\n" \
  "v_lshl_add_u64 v[2:3], v[2:3], 0, s[88:89]\n" \
  ".endif\n" \
  "global_load_dwordx4 v[72:75], v[2:3], off\n" \
  ".if !" STR(NOC) "\n" \
  "v_lshl_add_u64 v[2:3], v[2:3], 0, s[88:89]\n" \
  ".endif\n" \
  "global_load_dwordx4 v[76:79], v[2:3], off\n" \
  ".if !" STR(NOC) "\n" \
  "v_lshl_add_u64 v[2:3], v[2:3], 0, s[88:89]\n" \
  ".endif\n" \
  "global_load_dwordx4 v[80:83], v[2:3], off\n" \
  ".if !" STR(NOC) "\n" \
  "v_lshl_add_u64 v[2:3], v[2:3], 0, s[88:89]\n" \
  ".endif\n" \
  "global_load_dwordx4 v[84:87], v[2:3], off\n" \
  ".if !" STR(NOC) "\n" \
  "v_lshl_add_u64 v[2:3], v[2:3], 0, s[88:89]\n" \
  ".endif\n" \
  "global_load_dwordx4 v[88:91], v[2:3], off\n" \
  ".if !" STR(NOC) "\n" \
  "v_lshl_add_u64 v[2:3], v[2:3], 0, s[88:89]\n" \
  ".endif\n" \
  "global_load_dwordx4 v[92:95], v[2:3], off\n" \
  ".if !" STR(NOC) "\n" \
  "v_lshl_add_u64 v[2:3], v[2:3], 0, s[88:89]\n" \
  ".endif\n" \
  "global_load_dwordx4 v[96:99], v[2:3], off\n" \
  ".if !" STR(NOC) "\n" \
  "v_lshl_add_u64 v[2:3], v[2:3], 0, s[88:89]\n" \
  ".endif\n" \
  "global_load_dwordx4 v[100:103], v[2:3], off\n" \
  ".if !" STR(NOC) "\n" \
  "v_lshl_add_u64 v[2:3], v[2:3], 0, s[88:89]\n" \
  ".endif\n" \
  "global_load_dwordx4 v[104:107], v[2:3], off\n" \
  ".if !" STR(NOC) "\n" \
  "v_lshl_add_u64 v[2:3], v[2:3], 0, s[88:89]\n" \
  ".endif\n" \
  "global_load_dwordx4 v[108:111], v[2:3], off\n" \
  ".if !" STR(NOC) "\n" \
  "v_lshl_add_u64 v[2:3], v[2:3], 0, s[88:89]\n" \
  ".endif\n" \
  "v_mov_b32 v6, 147456\n" \
  "v_lshl_add_u32 v1, v10, 3, v6\n" \
  "v_lshl_add_u32 v1, v11, 8, v1\n" \
  "v_lshl_add_u32 v6, v12, 4, v6\n" \
  GSTAMP6 \
  "s_waitcnt vmcnt(17)\n" \
  "ds_write_b128 v6, v[40:43]\n" \
  "ds_read_b64_tr_b16 v[40:41], v1\n" \
  "ds_read_b64_tr_b16 v[42:43], v1 offset:128\n" \
  "s_waitcnt vmcnt(16)\n" \
  "ds_write_b128 v6, v[44:47]\n" \
  "ds_read_b64_tr_b16 v[44:45], v1\n" \
  "ds_read_b64_tr_b16 v[46:47], v1 offset:128\n" \
  "s_waitcnt vmcnt(15)\n" \
  "ds_write_b128 v6, v[48:51]\n" \
  "ds_read_b64_tr_b16 v[48:49], v1\n" \
  "ds_read_b64_tr_b16 v[50:51], v1 offset:128\n" \
  "s_waitcnt vmcnt(14)\n" \
  "ds_write_b128 v6, v[52:55]\n" \
  "ds_read_b64_tr_b16 v[52:53], v1\n" \
  "ds_read_b64_tr_b16 v[54:55], v1 offset:128\n" \
  "s_waitcnt vmcnt(13)\n" \
  "ds_write_b128 v6, v[56:59]\n" \
  "ds_read_b64_tr_b16 v[56:57], v1\n" \
  "ds_read_b64_tr_b16 v[58:59], v1 offset:128\n" \
  "s_waitcnt vmcnt(12)\n" \
  "ds_write_b128 v6, v[60:63]\n" \
  "ds_read_b64_tr_b16 v[60:61], v1\n" \
  "ds_read_b64_tr_b16 v[62:63], v1 offset:128\n" \
  "s_waitcnt vmcnt(11)\n" \
  "ds_write_b128 v6, v[64:67]\n" \
  "ds_read_b64_tr_b16 v[64:65], v1\n" \
  "ds_read_b64_tr_b16 v[66:67], v1 offset:128\n" \
  "s_waitcnt vmcnt(10)\n" \
  "ds_write_b128 v6, v[68:71]\n" \
  "ds_read_b64_tr_b16 v[68:69], v1\n" \
  "ds_read_b64_tr_b16 v[70:71], v1 offset:128\n" \
  "s_waitcnt vmcnt(9)\n" \
  "ds_write_b128 v6, v[72:75]\n" \
  "ds_read_b64_tr_b16 v[72:73], v1\n" \
  "ds_read_b64_tr_b16 v[74:75], v1 offset:128\n" \
  "s_waitcnt vmcnt(8)\n" \
  "ds_write_b128 v6, v[76:79]\n" \
  "ds_read_b64_tr_b16 v[76:77], v1\n" \
  "ds_read_b64_tr_b16 v[78:79], v1 offset:128\n" \
  "s_waitcnt vmcnt(7)\n" \
  "ds_write_b128 v6, v[80:83]\n" \
  "ds_read_b64_tr_b16 v[80:81], v1\n" \
  "ds_read_b64_tr_b16 v[82:83], v1 offset:128\n" \
  "s_waitcnt vmcnt(6)\n" \
  "ds_write_b128 v6, v[84:87]\n" \
  "ds_read_b64_tr_b16 v[84:85], v1\n" \
  "ds_read_b64_tr_b16 v[86:87], v1 offset:128\n" \
  "s_waitcnt vmcnt(5)\n" \
  "ds_write_b128 v6, v[88:91]\n" \
  "ds_read_b64_tr_b16 v[88:89], v1\n" \
  "ds_read_b64_tr_b16 v[90:91], v1 offset:128\n" \
  "s_waitcnt vmcnt(4)\n" \
  "ds_write_b128 v6, v[92:95]\n" \
  "ds_read_b64_tr_b16 v[92:93], v1\n" \
  "ds_read_b64_tr_b16 v[94:95], v1 offset:128\n" \
  "s_waitcnt vmcnt(3)\n" \
  "ds_write_b128 v6, v[96:99]\n" \
  "ds_read_b64_tr_b16 v[96:97], v1\n" \
  "ds_read_b64_tr_b16 v[98:99], v1 offset:128\n" \
  "s_waitcnt vmcnt(2)\n" \
  "ds_write_b128 v6, v[100:103]\n" \
  "ds_read_b64_tr_b16 v[100:101], v1\n" \
  "ds_read_b64_tr_b16 v[102:103], v1 offset:128\n" \
  "s_waitcnt vmcnt(1)\n" \
  "ds_write_b128 v6, v[104:107]\n" \
  "ds_read_b64_tr_b16 v[104:105], v1\n" \
  "ds_read_b64_tr_b16 v[106:107], v1 offset:128\n" \
  "s_waitcnt vmcnt(0)\n" \
  "ds_write_b128 v6, v[108:111]\n" \
  "ds_read_b64_tr_b16 v[108:109], v1\n" \
  "ds_read_b64_tr_b16 v[110:111], v1 offset:128\n" \
  "v_mov_b64 v[16:17], 0\n" \
  "v_mov_b64 v[18:19], 0\n" \
  "ds_write_b128 v6, v[16:19]\n" \
  GSTAMP7 \
  "s_branch Ek0%=\n" \
  "Ewd%=:\n" \
  "s_cmp_eq_u32 %[w], 15\n" \
  "s_cbranch_scc1 Ewz%=\n" \
  "s_sub_u32 s56, %[w], 9\n" \
  "v_lshrrev_b32 v4, 2, v12\n" \
  "v_lshrrev_b32 v5, 4, v12\n" \
  "v_and_b32 v5, 2, v5\n" \
  "v_xor_b32 v5, v5, %[t]\n" \
  "v_and_b32 v5, 3, v5\n" \
  "s_mul_i32 s51, s56, 192\n" \
  "v_lshl_add_u32 v5, v5, 4, s51\n" \
  "v_add_u32 v2, s58, v4\n" \
  "v_mul_u32_u24 v2, 1152, v2\n" \
  "v_add_u32 v2, v2, v5\n" \
  "v_and_b32 v4, 7, v4\n" \
  "v_add_u32 v3, s59, v4\n" \
  "v_mul_u32_u24 v3, 1152, v3\n" \
  "v_add_u32 v3, v3, v5\n" \
  "s_lshl_b32 s51, s56, 10\n" \
  "s_add_u32 s51, s51, 148480\n" \
  "v_lshl_add_u32 v6, v12, 4, s51\n" \
  "v_and_b32 v4, 1, v11\n" \
  "v_bfe_u32 v7, %[t], 2, 2\n" \
  "v_lshl_add_u32 v1, v4, 3, v7\n" \
  "v_lshlrev_b32 v1, 6, v1\n" \
  "v_lshrrev_b32 v5, 1, v11\n" \
  "v_xor_b32 v5, v5, v4\n" \
  "v_lshl_add_u32 v1, v5, 5, v1\n" \
  "v_and_b32 v5, 3, %[t]\n" \
  "v_lshl_add_u32 v1, v5, 3, v1\n" \
  "v_add_u32 v1, s51, v1\n" \
  "s_barrier\n" \
  "s_barrier\n" \
  "global_load_dwordx4 v[40:43], v2, s[48:49] offset:0" DNTF "\n" \
  "global_load_dwordx4 v[44:47], v2, s[48:49] offset:64" DNTF "\n" \
  "global_load_dwordx4 v[48:51], v2, s[48:49] offset:128" DNTF "\n" \
  "v_add_u32 v2, 18432, v2\n" \
  "global_load_dwordx4 v[52:55], v2, s[48:49] offset:0" DNTF "\n" \
  "global_load_dwordx4 v[56:59], v2, s[48:49] offset:64" DNTF "\n" \
  "global_load_dwordx4 v[60:63], v2, s[48:49] offset:128" DNTF "\n" \
  "v_add_u32 v2, 18432, v2\n" \
  "global_load_dwordx4 v[64:67], v2, s[48:49] offset:0" DNTF "\n" \
  "global_load_dwordx4 v[68:71], v2, s[48:49] offset:64" DNTF "\n" \
  "global_load_dwordx4 v[72:75], v2, s[48:49] offset:128" DNTF "\n" \
  "v_add_u32 v2, 18432, v2\n" \
  "global_load_dwordx4 v[76:79], v2, s[48:49] offset:0" DNTF "\n" \
  "global_load_dwordx4 v[80:83], v2, s[48:49] offset:64" DNTF "\n" \
  "global_load_dwordx4 v[84:87], v2, s[48:49] offset:128" DNTF "\n" \
  "global_load_dwordx4 v[88:91], v3, s[48:49] offset:0" DNTF "\n" \
  "global_load_dwordx4 v[92:95], v3, s[48:49] offset:64" DNTF "\n" \
  "global_load_dwordx4 v[96:99], v3, s[48:49] offset:128" DNTF "\n" \
  GSTAMP6 \
  "s_waitcnt vmcnt(14)\n" \
  "ds_write_b128 v6, v[40:43]\n" \
  "ds_read_b64_tr_b16 v[40:41], v1\n" \
  "ds_read_b64_tr_b16 v[42:43], v1 offset:256\n" \
  "s_waitcnt vmcnt(13)\n" \
  "ds_write_b128 v6, v[44:47]\n" \
  "ds_read_b64_tr_b16 v[44:45], v1\n" \
  "ds_read_b64_tr_b16 v[46:47], v1 offset:256\n" \
  "s_waitcnt vmcnt(12)\n" \
  "ds_write_b128 v6, v[48:51]\n" \
  "ds_read_b64_tr_b16 v[48:49], v1\n" \
  "ds_read_b64_tr_b16 v[50:51], v1 offset:256\n" \
  "s_waitcnt vmcnt(11)\n" \
  "ds_write_b128 v6, v[52:55]\n" \
  "ds_read_b64_tr_b16 v[52:53], v1\n" \
  "ds_read_b64_tr_b16 v[54:55], v1 offset:256\n" \
  "s_waitcnt vmcnt(10)\n" \
  "ds_write_b128 v6, v[56:59]\n" \
  "ds_read_b64_tr_b16 v[56:57], v1\n" \
  "ds_read_b64_tr_b16 v[58:59], v1 offset:256\n" \
  "s_waitcnt vmcnt(9)\n" \
  "ds_write_b128 v6, v[60:63]\n" \
  "ds_read_b64_tr_b16 v[60:61], v1\n" \
  "ds_read_b64_tr_b16 v[62:63], v1 offset:256\n" \
  "s_waitcnt vmcnt(8)\n" \
  "ds_write_b128 v6, v[64:67]\n" \
  "ds_read_b64_tr_b16 v[64:65], v1\n" \
  "ds_read_b64_tr_b16 v[66:67], v1 offset:256\n" \
  "s_waitcnt vmcnt(7)\n" \
  "ds_write_b128 v6, v[68:71]\n" \
  "ds_read_b64_tr_b16 v[68:69], v1\n" \
  "ds_read_b64_tr_b16 v[70:71], v1 offset:256\n" \
  "s_waitcnt vmcnt(6)\n" \
  "ds_write_b128 v6, v[72:75]\n" \
  "ds_read_b64_tr_b16 v[72:73], v1\n" \
  "ds_read_b64_tr_b16 v[74:75], v1 offset:256\n" \
  "s_waitcnt vmcnt(5)\n" \
  "ds_write_b128 v6, v[76:79]\n" \
  "ds_read_b64_tr_b16 v[76:77], v1\n" \
  "ds_read_b64_tr_b16 v[78:79], v1 offset:256\n" \
  "s_waitcnt vmcnt(4)\n" \
  "ds_write_b128 v6, v[80:83]\n" \
  "ds_read_b64_tr_b16 v[80:81], v1\n" \
  "ds_read_b64_tr_b16 v[82:83], v1 offset:256\n" \
  "s_waitcnt vmcnt(3)\n" \
  "ds_write_b128 v6, v[84:87]\n" \
  "ds_read_b64_tr_b16 v[84:85], v1\n" \
  "ds_read_b64_tr_b16 v[86:87], v1 offset:256\n" \
  "s_waitcnt vmcnt(2)\n" \
  "ds_write_b128 v6, v[88:91]\n" \
  "ds_read_b64_tr_b16 v[88:89], v1\n" \
  "ds_read_b64_tr_b16 v[90:91], v1 offset:256\n" \
  "s_waitcnt vmcnt(1)\n" \
  "ds_write_b128 v6, v[92:95]\n" \
  "ds_read_b64_tr_b16 v[92:93], v1\n" \
  "ds_read_b64_tr_b16 v[94:95], v1 offset:256\n" \
  "s_waitcnt vmcnt(0)\n" \
  "ds_write_b128 v6, v[96:99]\n" \
  "ds_read_b64_tr_b16 v[96:97], v1\n" \
  "ds_read_b64_tr_b16 v[98:99], v1 offset:256\n" \
  "v_mov_b64 v[16:17], 0\n" \
  "v_mov_b64 v[18:19], 0\n" \
  "ds_write_b128 v6, v[16:19]\n" \
  "s_lshl_b32 s51, s56, 5\n" \
  "v_lshrrev_b32 v5, 1, v11\n" \
  "v_lshl_add_u32 v5, v5, 4, v10\n" \
  "v_add_u32 v5, s51, v5\n" \
  "v_mul_u32_u24 v6, 171, v5\n" \
  "v_lshrrev_b32 v6, 11, v6\n" \
  "v_mul_u32_u24 v6, 12096, v6\n" \
  "v_lshl_add_u32 v6, v5, 4, v6\n" \
  "v_mul_u32_u24 v4, 393216, v4\n" \
  "s_mul_i32 s51, s57, 192\n" \
  "v_add3_u32 v38, v6, v4, s51\n" \
  "s_mov_b32 s86, 6455296\n" \
  "s_mov_b32 s87, 6651904\n" \
  "v_mov_b32 v31, s44\n" \
  "v_mov_b32 v35, s44\n" \
  GSTAMP7 \
  "s_branch Ek0%=\n" \
  "Ewz%=:\n" \
  "v_mov_b32 v4, 147456\n" \
  "v_lshl_add_u32 v4, v12, 4, v4\n" \
  "v_mov_b64 v[16:17], 0\n" \
  "v_mov_b64 v[18:19], 0\n" \
  "ds_write_b128 v4, v[16:19] offset:7168\n" \
  "ds_write_b128 v4, v[16:19] offset:8192\n" \
  "s_barrier\n" \
  "s_barrier\n" \
  "Ek0%=:\n" \
  "v_lshlrev_b32 v1, 4, %[t]\n" \
  "s_mov_b32 s51, 87040\n" \
  "v_and_b32 v2, 7, %[t]\n" \
  "v_mov_b32 v3, 159488\n" \
  "v_lshl_add_u32 v4, v2, 2, v3\n" \
  "v_and_b32 v5, 3, %[t]\n" \
  "v_lshl_add_u32 v5, v5, 4, v3\n" \
  "v_mov_b32 v6, 1\n" \
  "v_mov_b32 v7, 147456\n" \
  "v_lshrrev_b32 v8, 3, %[t]\n" \
  "v_lshl_add_u32 v8, v8, 2, v7\n" \
  "v_lshl_add_u32 v9, v2, 4, v7\n" \
  "s_lshl_b32 s56, %[w], 3\n" \
  "v_mov_b32 v13, 160800\n" \
  "v_mul_u32_u24 v14, 1366, %[t]\n" \
  "v_lshrrev_b32 v14, 16, v14\n" \
  "v_mul_u32_u24 v15, 48, v14\n" \
  "v_sub_u32 v15, %[t], v15\n" \
  "v_mul_u32_u24 v36, 1184, v14\n" \
  "v_mad_u32_u24 v36, v15, 12, v36\n" \
  "v_lshl_add_u32 v14, v14, 2, v13\n" \
  "v_lshlrev_b32 v15, 4, v15\n" \
  "v_add_u32 v15, 6453760, v15\n" \
  "s_mov_b32 s45, -1536\n" \
  ".if " STR(KBAR) "\n" \
  "s_barrier\n" \
  ".endif\n" \
  "s_movk_i32 s97, 0x7fff\n" \
  "Ekr%=:\n" \
  "buffer_load_dwordx4 v[20:23], v1, s[40:43], s51 offen sc1\n" \
  "s_waitcnt vmcnt(0)\n" \
  "v_bitop3_b32 v24, v21, v23, s44 bitop3:0x7e\n" \
  "v_cmp_ne_u32 vcc, 0, v24\n" \
  "s_cbranch_vccz Ekx%=\n" \
  "s_sleep " STR(KSLP) "\n" \
  "s_sub_u32 s97, s97, 1\n" \
  "s_cbranch_scc0 Ekr%=\n" \
  "Ekx%=:\n" \
  GSTAMP8 \
  "ds_max_u32 v4, v20 offset:96\n" \
  "ds_max_u32 v4, v22 offset:128\n" \
  "s_waitcnt lgkmcnt(0)\n" \
  "s_barrier\n" \
  "ds_read_b128 v[24:27], v5 offset:96\n" \
  "s_waitcnt lgkmcnt(0)\n" \
  "v_min3_u32 v24, v24, v25, v26\n" \
  "v_min_u32 v24, v24, v27\n" \
  "s_nop 1\n" \
  "v_min_u32_dpp v24, v24, v24 quad_perm:[1,0,3,2] row_mask:0xf bank_mask:0xf\n" \
  "s_nop 1\n" \
  "v_min_u32_dpp v24, v24, v24 quad_perm:[2,3,0,1] row_mask:0xf bank_mask:0xf\n" \
  "v_cmp_ge_u32 vcc, v20, v24\n" \
  "v_cmp_ge_u32 s[46:47], v22, v24\n" \
  "s_or_b64 s[48:49], vcc, s[46:47]\n" \
  "s_cbranch_scc0 Ec0%=\n" \
  "s_mov_b64 exec, vcc\n" \
  "ds_add_rtn_u32 v25, v3, v6 offset:24\n" \
  "s_mov_b64 exec, s[46:47]\n" \
  "ds_add_rtn_u32 v26, v3, v6 offset:24\n" \
  "s_waitcnt lgkmcnt(0)\n" \
  "v_lshl_add_u32 v26, v26, 2, v7\n" \
  "ds_write_b32 v26, v22\n" \
  "s_mov_b64 exec, vcc\n" \
  "v_lshl_add_u32 v25, v25, 2, v7\n" \
  "ds_write_b32 v25, v20\n" \
  "s_mov_b64 exec, -1\n" \
  "Ec0%=:\n" \
  "s_waitcnt lgkmcnt(0)\n" \
  "s_barrier\n" \
  "ds_read_b32 v18, v3 offset:24\n" \
  "Ec1%=:\n" \
  "ds_read_b32 v17, v8\n" \
  "ds_read_b128 v[20:23], v9\n" \
  "ds_read_b128 v[24:27], v9 offset:128\n" \
  "v_mov_b32 v16, 0\n" \
  "s_waitcnt lgkmcnt(3)\n" \
  "v_readfirstlane_b32 s46, v18\n" \
  "s_cmp_ge_u32 s56, s46\n" \
  "s_cbranch_scc1 Ec4%=\n" \
  "s_mov_b32 s47, 64\n" \
  "v_mov_b32 v19, v9\n" \
  "Ec2%=:\n" \
  "s_waitcnt lgkmcnt(0)\n" \
  "v_cmp_gt_u32 s[88:89], v20, v17\n" \
  "v_cmp_gt_u32 s[90:91], v21, v17\n" \
  "v_cmp_gt_u32 s[94:95], v22, v17\n" \
  "v_cmp_gt_u32 s[96:97], v23, v17\n" \
  "v_addc_co_u32 v16, vcc, 0, v16, s[88:89]\n" \
  "v_addc_co_u32 v16, vcc, 0, v16, s[90:91]\n" \
  "v_addc_co_u32 v16, vcc, 0, v16, s[94:95]\n" \
  "v_addc_co_u32 v16, vcc, 0, v16, s[96:97]\n" \
  "v_cmp_gt_u32 s[88:89], v24, v17\n" \
  "v_cmp_gt_u32 s[90:91], v25, v17\n" \
  "v_cmp_gt_u32 s[94:95], v26, v17\n" \
  "v_cmp_gt_u32 s[96:97], v27, v17\n" \
  "v_addc_co_u32 v16, vcc, 0, v16, s[88:89]\n" \
  "v_addc_co_u32 v16, vcc, 0, v16, s[90:91]\n" \
  "v_addc_co_u32 v16, vcc, 0, v16, s[94:95]\n" \
  "v_addc_co_u32 v16, vcc, 0, v16, s[96:97]\n" \
  "s_cmp_ge_u32 s47, s46\n" \
  "s_cbranch_scc1 Ec3%=\n" \
  "s_add_u32 s47, s47, 64\n" \
  "v_add_u32 v19, 256, v19\n" \
  "ds_read_b128 v[20:23], v19\n" \
  "ds_read_b128 v[24:27], v19 offset:128\n" \
  "s_branch Ec2%=\n" \
  "Ec3%=:\n" \
  "v_add_u32_dpp v16, v16, v16 quad_perm:[1,0,3,2] row_mask:0xf bank_mask:0xf\n" \
  "s_nop 1\n" \
  "v_add_u32_dpp v16, v16, v16 quad_perm:[2,3,0,1] row_mask:0xf bank_mask:0xf\n" \
  "s_addk_i32 s56, 128\n" \
  "s_cmp_lt_u32 s56, s46\n" \
  "v_add_u32_dpp v16, v16, v16 row_half_mirror row_mask:0xf bank_mask:0xf\n" \
  "v_min_u32 v16, 16, v16\n" \
  "v_lshl_add_u32 v16, v16, 2, v13\n" \
  "ds_write_b32 v16, v17\n" \
  "s_cbranch_scc0 Ec4%=\n" \
  "v_add_u32 v8, 512, v8\n" \
  "s_branch Ec1%=\n" \
  "Ec4%=:\n" \
  "s_waitcnt lgkmcnt(0)\n" \
  "s_barrier\n" \
  GSTAMP9 \
  "s_cmp_ge_u32 %[w], 12\n" \
  "s_cbranch_scc1 Eh0%=\n" \
  "ds_read_b32 v16, v14\n" \
  "s_waitcnt lgkmcnt(0)\n" \
  "v_and_b32 v16, 0x7ff, v16\n" \
  "v_mad_i32_i24 v15, v16, s45, v15\n" \
  "s_movk_i32 s97, 0x7fff\n" \
  "Ehr%=:\n" \
  "buffer_load_dwordx4 v[20:23], v15, s[40:43], 0 offen sc1\n" \
  "buffer_load_dwordx4 v[24:27], v15, s[40:43], 0 offen offset:768 sc1\n" \
  "v_mul_u32_u24 v1, 1184, v10\n" \
  "v_lshl_add_u32 v1, v11, 4, v1\n" \
  "v_lshrrev_b32 v2, 3, v12\n" \
  "v_mul_u32_u24 v2, 320, v2\n" \
  "v_and_b32 v4, 7, %[t]\n" \
  "v_lshl_add_u32 v2, v4, 1, v2\n" \
  "s_lshl4_add_u32 s46, %[w], 20480\n" \
  "v_add_u32 v2, s46, v2\n" \
  "s_waitcnt vmcnt(0)\n" \
  "v_bitop3_b32 v16, v23, v27, s44 bitop3:0x7e\n" \
  "v_cmp_ne_u32 vcc, 0, v16\n" \
  "s_cbranch_vccz Ehx%=\n" \
  "s_sub_u32 s97, s97, 1\n" \
  "s_cbranch_scc0 Ehr%=\n" \
  "Ehx%=:\n" \
  GSTAMP10 \
  "ds_write2_b32 v36, v20, v24 offset0:0 offset1:144\n" \
  "ds_write2_b32 v36, v21, v25 offset0:1 offset1:145\n" \
  "ds_write2_b32 v36, v22, v26 offset0:2 offset1:146\n" \
  "s_waitcnt lgkmcnt(0)\n" \
  "s_barrier\n" \
  "s_cmp_gt_u32 %[w], 8\n" \
  "s_cbranch_scc1 Ed0%=\n" \
  "ds_read_b128 v[112:115], v1 offset:0\n" \
  "ds_read_b128 v[116:119], v1 offset:64\n" \
  "ds_read_b128 v[120:123], v1 offset:128\n" \
  "ds_read_b128 v[124:127], v1 offset:192\n" \
  "ds_read_b128 v[20:23], v1 offset:256\n" \
  "ds_read_b128 v[24:27], v1 offset:320\n" \
  "ds_read_b128 v[28:31], v1 offset:384\n" \
  "ds_read_b128 v[32:35], v1 offset:448\n" \
  "s_waitcnt lgkmcnt(7)\n" \
  "v_mfma_f32_16x16x32_bf16 v[16:19], v[112:115], v[40:43], 0\n" \
  "ds_read_b128 v[112:115], v1 offset:512\n" \
  "s_waitcnt lgkmcnt(7)\n" \
  "v_mfma_f32_16x16x32_bf16 v[16:19], v[116:119], v[44:47], v[16:19]\n" \
  "ds_read_b128 v[116:119], v1 offset:576\n" \
  "s_waitcnt lgkmcnt(7)\n" \
  "v_mfma_f32_16x16x32_bf16 v[16:19], v[120:123], v[48:51], v[16:19]\n" \
  "ds_read_b128 v[120:123], v1 offset:640\n" \
  "s_waitcnt lgkmcnt(7)\n" \
  "v_mfma_f32_16x16x32_bf16 v[16:19], v[124:127], v[52:55], v[16:19]\n" \
  "ds_read_b128 v[124:127], v1 offset:704\n" \
  "s_waitcnt lgkmcnt(7)\n" \
  "v_mfma_f32_16x16x32_bf16 v[16:19], v[20:23], v[56:59], v[16:19]\n" \
  "ds_read_b128 v[20:23], v1 offset:768\n" \
  "s_waitcnt lgkmcnt(7)\n" \
  "v_mfma_f32_16x16x32_bf16 v[16:19], v[24:27], v[60:63], v[16:19]\n" \
  "ds_read_b128 v[24:27], v1 offset:832\n" \
  "s_waitcnt lgkmcnt(7)\n" \
  "v_mfma_f32_16x16x32_bf16 v[16:19], v[28:31], v[64:67], v[16:19]\n" \
  "ds_read_b128 v[28:31], v1 offset:896\n" \
  "s_waitcnt lgkmcnt(7)\n" \
  "v_mfma_f32_16x16x32_bf16 v[16:19], v[32:35], v[68:71], v[16:19]\n" \
  "ds_read_b128 v[32:35], v1 offset:960\n" \
  "s_waitcnt lgkmcnt(7)\n" \
  "v_mfma_f32_16x16x32_bf16 v[16:19], v[112:115], v[72:75], v[16:19]\n" \
  "ds_read_b128 v[112:115], v1 offset:1024\n" \
  "s_waitcnt lgkmcnt(7)\n" \
  "v_mfma_f32_16x16x32_bf16 v[16:19], v[116:119], v[76:79], v[16:19]\n" \
  "ds_read_b128 v[116:119], v1 offset:1088\n" \
  "s_waitcnt lgkmcnt(7)\n" \
  "v_mfma_f32_16x16x32_bf16 v[16:19], v[120:123], v[80:83], v[16:19]\n" \
  "s_waitcnt lgkmcnt(6)\n" \
  "v_mfma_f32_16x16x32_bf16 v[16:19], v[124:127], v[84:87], v[16:19]\n" \
  "s_waitcnt lgkmcnt(5)\n" \
  "v_mfma_f32_16x16x32_bf16 v[16:19], v[20:23], v[88:91], v[16:19]\n" \
  "s_waitcnt lgkmcnt(4)\n" \
  "v_mfma_f32_16x16x32_bf16 v[16:19], v[24:27], v[92:95], v[16:19]\n" \
  "s_waitcnt lgkmcnt(3)\n" \
  "v_mfma_f32_16x16x32_bf16 v[16:19], v[28:31], v[96:99], v[16:19]\n" \
  "s_waitcnt lgkmcnt(2)\n" \
  "v_mfma_f32_16x16x32_bf16 v[16:19], v[32:35], v[100:103], v[16:19]\n" \
  "s_waitcnt lgkmcnt(1)\n" \
  "v_mfma_f32_16x16x32_bf16 v[16:19], v[112:115], v[104:107], v[16:19]\n" \
  "s_waitcnt lgkmcnt(0)\n" \
  "v_mfma_f32_16x16x32_bf16 v[16:19], v[116:119], v[108:111], v[16:19]\n" \
  "s_nop 7\n" \
  "v_mov_b64 v[20:21], v[16:17]\n" \
  "v_mov_b32_dpp v16, v18 row_ror:8 row_mask:0xf bank_mask:0xc\n" \
  "v_mov_b32_dpp v17, v19 row_ror:8 row_mask:0xf bank_mask:0xc\n" \
  "v_mov_b32_dpp v18, v20 row_ror:8 row_mask:0xf bank_mask:0x3\n" \
  "v_mov_b32_dpp v19, v21 row_ror:8 row_mask:0xf bank_mask:0x3\n" \
  "v_cvt_pk_bf16_f32 v16, 0, v16\n" \
  "v_cvt_pk_bf16_f32 v17, 0, v17\n" \
  "v_mul_f32 v20, 0xbfb8aa3b, v16\n" \
  "v_mul_f32 v21, 0xbfb8aa3b, v17\n" \
  "v_exp_f32 v20, v20\n" \
  "v_exp_f32 v21, v21\n" \
  "v_cvt_pk_bf16_f32 v18, 0, v18\n" \
  "v_pk_add_f32 v[20:21], v[20:21], 1.0 op_sel_hi:[1,0]\n" \
  "v_rcp_f32 v20, v20\n" \
  "v_rcp_f32 v21, v21\n" \
  "v_cvt_pk_bf16_f32 v19, 0, v19\n" \
  "v_pk_mul_f32 v[16:17], v[16:17], v[20:21]\n" \
  "v_cvt_pk_bf16_f32 v16, 0, v16\n" \
  "v_cvt_pk_bf16_f32 v17, 0, v17\n" \
  "v_pk_mul_f32 v[16:17], v[16:17], v[18:19]\n" \
  "v_cvt_pk_bf16_f32 v16, v16, v17\n" \
  "ds_write_b16 v2, v16\n" \
  "ds_write_b16_d16_hi v2, v16 offset:160\n" \
  GSTAMP11 \
  "s_waitcnt lgkmcnt(0)\n" \
  "s_barrier\n" \
  "s_cmp_gt_u32 %[w], 2\n" \
  "s_cbranch_scc1 Ez%=\n" \
  "s_lshr_b32 s46, s57, 4\n" \
  "v_bfe_u32 v1, %[t], 3, 1\n" \
  "v_lshl_add_u32 v1, v1, 3, v3\n" \
  "s_lshl_b32 s46, s46, 4\n" \
  "v_add_u32 v1, s46, v1\n" \
  "ds_read_b64 v[4:5], v1 offset:32\n" \
  "s_and_b32 s46, s57, 15\n" \
  "s_mul_i32 s46, s46, 12\n" \
  "s_lshl2_add_u32 s46, %[w], s46\n" \
  "v_and_b32 v2, 3, %[t]\n" \
  "v_add_u32 v6, s46, v2\n" \
  "v_lshrrev_b32 v7, 5, v6\n" \
  "v_lshl_add_u32 v6, v7, 6, v6\n" \
  "v_min_u32 v7, 2, v11\n" \
  "v_lshl_add_u32 v6, v7, 5, v6\n" \
  "v_mul_u32_u24 v7, 683, v6\n" \
  "v_lshrrev_b32 v7, 11, v7\n" \
  "v_mul_u32_u24 v8, 3, v7\n" \
  "v_sub_u32 v8, v6, v8\n" \
  "v_and_b32 v9, 1, v7\n" \
  "v_sub_u32 v8, v8, v9\n" \
  "v_min_u32 v8, 2, v8\n" \
  "v_mul_u32_u24 v7, 16512, v7\n" \
  "v_lshlrev_b32 v9, 3, v6\n" \
  "s_waitcnt lgkmcnt(0)\n" \
  "v_not_b32 v13, v5\n" \
  "v_and_b32 v13, 0x7ff, v13\n" \
  "v_lshrrev_b32 v14, 1, v13\n" \
  "v_lshl_add_u32 v7, v14, 4, v7\n" \
  "v_lshrrev_b32 v14, 4, v13\n" \
  "v_lshlrev_b32 v14, 2, v14\n" \
  "v_mul_u32_u24 v4, 1184, v4\n" \
  "v_lshl_add_u32 v4, v6, 1, v4\n" \
  "v_add_u32 v4, -18944, v4\n" \
  "v_mov_b32 v15, 0\n" \
  "ds_read_u16_d16_hi v15, v4\n" \
  "s_mov_b32 s46, 139264\n" \
  "s_mov_b32 s47, 136192\n" \
  "s_mov_b32 s86, 27136\n" \
  "s_mov_b32 s87, 54784\n" \
  "s_mov_b32 s88, 82432\n" \
  "s_movk_i32 s97, 0x7fff\n" \
  "Efr%=:\n" \
  "buffer_load_dwordx4 v[16:19], v7, s[40:43], s46 offen sc1\n" \
  "buffer_load_dword v20, v14, s[40:43], s47 offen sc1\n" \
  "buffer_load_dwordx2 v[22:23], v9, s[40:43], s86 offen sc1\n" \
  "buffer_load_dwordx2 v[24:25], v9, s[40:43], s87 offen sc1\n" \
  "buffer_load_dwordx2 v[26:27], v9, s[40:43], s88 offen sc1\n" \
  "s_waitcnt vmcnt(0)\n" \
  "v_bitop3_b32 v21, v19, v20, s44 bitop3:0x7e\n" \
  "v_bitop3_b32 v28, v23, v25, s44 bitop3:0x7e\n" \
  "v_xor_b32 v29, s44, v27\n" \
  "v_or3_b32 v21, v21, v28, v29\n" \
  "v_cmp_ne_u32 vcc, 0, v21\n" \
  "s_cbranch_vccz Efx%=\n" \
  "s_sub_u32 s97, s97, 1\n" \
  "s_cbranch_scc0 Efr%=\n" \
  "Efx%=:\n" \
  "v_lshrrev_b32 v28, 2, v12\n" \
  "v_mul_u32_u24 v28, 192, v28\n" \
  "v_lshl_add_u32 v28, v2, 4, v28\n" \
  "s_mul_i32 s46, s57, 12288\n" \
  "s_lshl_b32 s47, %[w], 6\n" \
  "s_add_u32 s46, s46, s47\n" \
  "s_add_u32 s46, s46, 6455296\n" \
  "s_add_u32 s47, s46, 6144\n" \
  ".if " STR(FSLP) "\n" \
  "s_sleep " STR(FSLP) "\n" \
  ".endif\n" \
  GSTAMP12 \
  "buffer_load_dwordx4 v[40:43], v28, s[40:43], s46 offen sc1\n" \
  "buffer_load_dwordx4 v[44:47], v28, s[40:43], s46 offen offset:3072 sc1\n" \
  "buffer_load_dwordx4 v[48:51], v28, s[40:43], s47 offen sc1\n" \
  "buffer_load_dwordx4 v[52:55], v28, s[40:43], s47 offen offset:3072 sc1\n" \
  "v_add_f32 v22, v22, v24\n" \
  "v_add_f32 v22, v22, v26\n" \
  "v_cvt_pk_bf16_f32 v22, 0, v22\n" \
  "v_and_b32 v20, 1, v13\n" \
  "v_cmp_eq_u32 vcc, 1, v20\n" \
  "v_cmp_eq_u32 s[88:89], 2, v8\n" \
  "v_mul_u32_u24 v13, 1152, v13\n" \
  "v_cndmask_b32 v16, v16, v17, vcc\n" \
  "v_cndmask_b32 v8, v8, v20, s[88:89]\n" \
  "v_cndmask_b32 v16, v16, v18, s[88:89]\n" \
  "v_lshlrev_b32 v8, 4, v8\n" \
  "v_lshrrev_b32 v16, v8, v16\n" \
  "v_lshlrev_b32 v16, 16, v16\n" \
  "v_lshl_add_u32 v13, v6, 1, v13\n" \
  "v_ashrrev_i32 v17, 31, v5\n" \
  "v_not_b32 v17, v17\n" \
  "v_or_b32 v17, 0x80000000, v17\n" \
  "v_xor_b32 v5, v5, v17\n" \
  "v_and_b32 v5, 0xfffff800, v5\n" \
  "v_cvt_pk_bf16_f32 v5, 0, v5\n" \
  "v_mul_f32 v5, 0xbfb8aa3b, v5\n" \
  "v_exp_f32 v5, v5\n" \
  "v_and_b32 v17, 2, v10\n" \
  "v_add_f32 v5, 1.0, v5\n" \
  "v_rcp_f32 v5, v5\n" \
  "v_cmp_eq_u32 vcc, 0, v17\n" \
  "v_cvt_pk_bf16_f32 v5, 0, v5\n" \
  "v_and_b32 v17, 1, v10\n" \
  "v_lshlrev_b32 v17, 4, v17\n" \
  "v_mov_b32 v18, 0x3f80\n" \
  "v_lshlrev_b32 v17, v17, v18\n" \
  "v_cndmask_b32 v17, 0, v17, vcc\n" \
  "v_lshrrev_b32 v18, 2, v10\n" \
  "v_cmp_eq_u32 vcc, 0, v18\n" \
  "s_nop 1\n" \
  "v_cndmask_b32 v32, 0, v17, vcc\n" \
  "v_cmp_eq_u32 vcc, 1, v18\n" \
  "s_nop 1\n" \
  "v_cndmask_b32 v33, 0, v17, vcc\n" \
  "v_cmp_eq_u32 vcc, 2, v18\n" \
  "s_nop 1\n" \
  "v_cndmask_b32 v34, 0, v17, vcc\n" \
  "v_mov_b32 v35, 0\n" \
  "s_waitcnt vmcnt(0) lgkmcnt(0)\n" \
  "v_xor_b32 v43, s44, v43\n" \
  "v_xor_b32 v47, s44, v47\n" \
  "v_xor_b32 v51, s44, v51\n" \
  "v_xor_b32 v55, s44, v55\n" \
  "v_or3_b32 v17, v43, v47, v51\n" \
  "v_or_b32 v17, v17, v55\n" \
  "v_cmp_ne_u32 vcc, 0, v17\n" \
  "s_cbranch_vccnz Eps%=\n" \
  "Epb%=:\n" \
  GSTAMP13 \
  "v_mfma_f32_16x16x32_bf16 v[24:27], v[32:35], v[40:43], 0\n" \
  "v_mfma_f32_16x16x32_bf16 v[24:27], v[32:35], v[44:47], v[24:27]\n" \
  "v_mfma_f32_16x16x32_bf16 v[24:27], v[32:35], v[48:51], v[24:27]\n" \
  "v_mfma_f32_16x16x32_bf16 v[24:27], v[32:35], v[52:55], v[24:27]\n" \
  "s_mov_b32 s46, 0x0f0f0f0f\n" \
  "s_mov_b32 s47, 0x0f0f\n" \
  "s_nop 5\n" \
  "v_add_f32_dpp v17, v24, v24 row_ror:8 row_mask:0xf bank_mask:0x3\n" \
  "v_add_f32_dpp v17, v25, v25 row_ror:8 row_mask:0xf bank_mask:0xc\n" \
  "s_nop 1\n" \
  "v_add_f32_dpp v17, v17, v17 row_shl:4 row_mask:0xf bank_mask:0xf bound_ctrl:1\n" \
  "v_cvt_pk_bf16_f32 v17, 0, v17\n" \
  "v_mul_f32 v17, v17, v5\n" \
  "v_cvt_pk_bf16_f32 v17, 0, v17\n" \
  "v_add_f32 v17, v17, v15\n" \
  "v_cvt_pk_bf16_f32 v17, 0, v17\n" \
  "v_mul_f32 v17, v17, v22\n" \
  "v_cvt_pk_bf16_f32 v17, 0, v17\n" \
  "v_add_f32 v17, v17, v16\n" \
  "v_cvt_pk_bf16_f32 v17, 0, v17\n" \
  "s_mov_b64 exec, s[46:47]\n" \
  "global_store_short_d16_hi v13, v17, s[84:85]\n" \
  "s_or_b32 s46, s57, %[w]\n" \
  "s_cbranch_scc1 Ez%=\n" \
  "s_mov_b64 exec, 1\n" \
  "v_mov_b32 v17, s44\n" \
  "buffer_store_dword v17, off, s[40:43], 0 sc1\n" \
  "s_branch Ez%=\n" \
  "Eps%=:\n" \
  "s_movk_i32 s97, 0x7fff\n" \
  "Epr%=:\n" \
  GSTAMPC \
  "s_mov_b64 exec, vcc\n" \
  "buffer_load_dwordx4 v[40:43], v28, s[40:43], s46 offen sc1\n" \
  "buffer_load_dwordx4 v[44:47], v28, s[40:43], s46 offen offset:3072 sc1\n" \
  "buffer_load_dwordx4 v[48:51], v28, s[40:43], s47 offen sc1\n" \
  "buffer_load_dwordx4 v[52:55], v28, s[40:43], s47 offen offset:3072 sc1\n" \
  "s_waitcnt vmcnt(0)\n" \
  "v_xor_b32 v43, s44, v43\n" \
  "v_xor_b32 v47, s44, v47\n" \
  "v_xor_b32 v51, s44, v51\n" \
  "v_xor_b32 v55, s44, v55\n" \
  "v_or3_b32 v17, v43, v47, v51\n" \
  "v_or_b32 v17, v17, v55\n" \
  "v_cmp_ne_u32 vcc, 0, v17\n" \
  "s_mov_b64 exec, -1\n" \
  "s_cbranch_vccz Epb%=\n" \
  "s_sub_u32 s97, s97, 1\n" \
  "s_cbranch_scc0 Epr%=\n" \
  "s_branch Epb%=\n" \
  "Eh0%=:\n" \
  "s_cmp_eq_u32 %[w], 15\n" \
  "s_cbranch_scc1 Eh1%=\n" \
  "v_mov_b64 v[16:17], 0\n" \
  "v_mov_b64 v[18:19], 0\n" \
  "ds_write_b128 v1, v[16:19] offset:8192\n" \
  "s_waitcnt lgkmcnt(0)\n" \
  "s_barrier\n" \
  "s_branch Ed0%=\n" \
  "Eh1%=:\n" \
  "s_setprio 3\n" \
  "v_lshl_add_u32 v1, v10, 2, v13\n" \
  "ds_read_b32 v16, v1\n" \
  "s_mov_b32 s46, 103424\n" \
  "v_lshl_add_u32 v4, v10, 2, v3\n" \
  "v_lshl_add_u32 v5, v11, 4, v3\n" \
  "v_add_u32 v18, 16, v10\n" \
  "s_waitcnt lgkmcnt(0)\n" \
  "v_not_b32 v16, v16\n" \
  "v_and_b32 v16, 0x7ff, v16\n" \
  "v_lshlrev_b32 v1, 3, v16\n" \
  "buffer_load_dwordx2 v[16:17], v1, s[40:43], s46 offen sc1\n" \
  "s_barrier\n" \
  "s_waitcnt vmcnt(0)\n" \
  "v_cmp_ne_u32 vcc, s44, v17\n" \
  "s_cbranch_vccz Eqx%=\n" \
  "s_movk_i32 s97, 0x7fff\n" \
  "Eqr%=:\n" \
  "buffer_load_dwordx2 v[16:17], v1, s[40:43], s46 offen sc1\n" \
  "s_waitcnt vmcnt(0)\n" \
  "v_cmp_ne_u32 vcc, s44, v17\n" \
  "s_cbranch_vccz Eqx%=\n" \
  "s_sub_u32 s97, s97, 1\n" \
  "s_cbranch_scc0 Eqr%=\n" \
  "Eqx%=:\n" \
  "ds_write_b32 v4, v16 offset:96\n" \
  "ds_read_b128 v[20:23], v5 offset:96\n" \
  "v_mov_b32 v17, v16\n" \
  "v_mov_b32 v19, v16\n" \
  "v_mov_b32 v16, 0\n" \
  "s_waitcnt lgkmcnt(0)\n" \
  "v_cmp_gt_u32 s[88:89], v20, v17\n" \
  "v_cmp_gt_u32 s[90:91], v21, v17\n" \
  "v_cmp_gt_u32 s[94:95], v22, v17\n" \
  "v_cmp_gt_u32 s[96:97], v23, v17\n" \
  "v_addc_co_u32 v16, vcc, 0, v16, s[88:89]\n" \
  "v_addc_co_u32 v16, vcc, 0, v16, s[90:91]\n" \
  "v_addc_co_u32 v16, vcc, 0, v16, s[94:95]\n" \
  "v_addc_co_u32 v16, vcc, 0, v16, s[96:97]\n" \
  "v_mov_b32 v17, v16\n" \
  "s_nop 1\n" \
  "v_permlane16_swap_b32 v16, v17\n" \
  "v_add_u32 v16, v16, v17\n" \
  "v_mov_b32 v17, v16\n" \
  "s_nop 1\n" \
  "v_permlane32_swap_b32 v16, v17\n" \
  "v_add_u32 v16, v16, v17\n" \
  "v_cmp_gt_u32 vcc, 8, v16\n" \
  "v_lshl_add_u32 v16, v16, 3, v3\n" \
  "s_mov_b64 exec, vcc\n" \
  "ds_write_b64 v16, v[18:19] offset:32\n" \
  "s_mov_b64 exec, -1\n" \
  GSTAMP11 \
  "s_waitcnt lgkmcnt(0)\n" \
  "s_barrier\n" \
  "s_branch Ez%=\n" \
  "Ed0%=:\n" \
  "v_and_b32 v2, 7, %[t]\n" \
  "v_lshl_add_u32 v2, v2, 3, v3\n" \
  "s_mov_b32 s97, 0x1fffff\n" \
  "Ed1%=:\n" \
  "ds_read_b32 v1, v2 offset:32\n" \
  "s_waitcnt lgkmcnt(0)\n" \
  "v_cmp_eq_u32 vcc, 0, v1\n" \
  "s_cbranch_vccz Ed2%=\n" \
  "s_sleep 1\n" \
  "s_sub_u32 s97, s97, 1\n" \
  "s_cbranch_scc0 Ed1%=\n" \
  "Ed2%=:\n" \
  "s_mov_b32 vcc_lo, 0x00ff00ff\n" \
  "s_mov_b32 vcc_hi, 0xff00ff00\n" \
  "v_cndmask_b32 v1, 32, v1, vcc\n" \
  "v_mul_u32_u24 v1, 160, v1\n" \
  "v_and_b32 v2, 16, %[t]\n" \
  "v_add_u32 v2, 17920, v2\n" \
  "v_add_u32 v1, v1, v2\n" \
  GSTAMP11 \
  "s_barrier\n" \
  "ds_read_b128 v[100:103], v1 offset:0\n" \
  "ds_read_b128 v[104:107], v1 offset:32\n" \
  "ds_read_b128 v[108:111], v1 offset:64\n" \
  "ds_read_b128 v[112:115], v1 offset:96\n" \
  "ds_read_b128 v[116:119], v1 offset:128\n" \
  "s_waitcnt lgkmcnt(4)\n" \
  "v_mfma_f32_16x16x32_bf16 v[16:19], v[100:103], v[40:43], 0\n" \
  "v_mfma_f32_16x16x32_bf16 v[20:23], v[100:103], v[44:47], 0\n" \
  "v_mfma_f32_16x16x32_bf16 v[24:27], v[100:103], v[48:51], 0\n" \
  "s_waitcnt lgkmcnt(3)\n" \
  "v_mfma_f32_16x16x32_bf16 v[16:19], v[104:107], v[52:55], v[16:19]\n" \
  "v_mfma_f32_16x16x32_bf16 v[20:23], v[104:107], v[56:59], v[20:23]\n" \
  "v_mfma_f32_16x16x32_bf16 v[24:27], v[104:107], v[60:63], v[24:27]\n" \
  "s_waitcnt lgkmcnt(2)\n" \
  "v_mfma_f32_16x16x32_bf16 v[16:19], v[108:111], v[64:67], v[16:19]\n" \
  "v_mfma_f32_16x16x32_bf16 v[20:23], v[108:111], v[68:71], v[20:23]\n" \
  "v_mfma_f32_16x16x32_bf16 v[24:27], v[108:111], v[72:75], v[24:27]\n" \
  "s_waitcnt lgkmcnt(1)\n" \
  "v_mfma_f32_16x16x32_bf16 v[16:19], v[112:115], v[76:79], v[16:19]\n" \
  "v_mfma_f32_16x16x32_bf16 v[20:23], v[112:115], v[80:83], v[20:23]\n" \
  "v_mfma_f32_16x16x32_bf16 v[24:27], v[112:115], v[84:87], v[24:27]\n" \
  "s_waitcnt lgkmcnt(0)\n" \
  "v_mfma_f32_16x16x32_bf16 v[16:19], v[116:119], v[88:91], v[16:19]\n" \
  "v_mfma_f32_16x16x32_bf16 v[20:23], v[116:119], v[92:95], v[20:23]\n" \
  "v_mfma_f32_16x16x32_bf16 v[24:27], v[116:119], v[96:99], v[24:27]\n" \
  "s_nop 5\n" \
  "v_cvt_pk_bf16_f32 v28, v16, v17\n" \
  "v_cvt_pk_bf16_f32 v32, v18, v19\n" \
  "v_cvt_pk_bf16_f32 v29, v20, v21\n" \
  "v_cvt_pk_bf16_f32 v33, v22, v23\n" \
  "v_cvt_pk_bf16_f32 v30, v24, v25\n" \
  "v_cvt_pk_bf16_f32 v34, v26, v27\n" \
  "buffer_store_dwordx4 v[28:31], v38, s[40:43], s86 offen sc1\n" \
  "buffer_store_dwordx4 v[32:35], v38, s[40:43], s87 offen sc1\n" \
  GSTAMP12 \
  "Ez%=:\n" \
  "s_mov_b64 exec, -1\n"

#define V4(a) "v" #a "0", "v" #a "1", "v" #a "2", "v" #a "3", "v" #a "4", "v" #a "5", "v" #a "6", "v" #a "7", "v" #a "8", "v" #a "9"
#define S4(a) "s" #a "0", "s" #a "1", "s" #a "2", "s" #a "3", "s" #a "4", "s" #a "5", "s" #a "6", "s" #a "7", "s" #a "8", "s" #a "9"
#define WAVE_CLOB "v1", "v2", "v3", "v4", "v5", "v6", "v7", "v8", "v9", V4(1), V4(2), V4(3), V4(4), V4(5), V4(6), V4(7), V4(8), V4(9), V4(10), V4(11), \
                  "v120", "v121", "v122", "v123", "v124", "v125", "v126", "v127", S4(4), S4(5), "s84", "s85", "s86", "s87", "s88", "s89", "s90", "s91", "s92", "s93", "s94", "s95", "s96", "s97", "s98", "vcc", "scc", "memory"

__global__ void __launch_bounds__(1024) mega(const u16*, const u16*, const u16*, const u16*, const u16*, const u16*, const u16*, const u16*, const u16*, const u16*, const u16*,
                                             const u16*, const u16*, u16*, char* ws) {
  TS(0);
  int tid = threadIdx.x, blk = blockIdx.x, wave;
  const auto ka = __builtin_amdgcn_kernarg_segment_ptr();
  asm volatile("v_readfirstlane_b32 %[w], %[t]\n\ts_lshr_b32 %[w], %[w], 6\n\ts_cmpk_lt_u32 %[b], 192\n\ts_cbranch_scc1 Lcc%=\n" EW_HEAD "s_branch Lcm%=\n"
               "Lcc%=:\n\ts_cmp_lt_u32 %[w], 12\n\ts_cbranch_scc1 Lcg%=\n" SW_HEAD "s_branch Lcs%=\nLcg%=:\n" GW_HEAD GW_BODY "s_branch Lcl%=\nLcs%=:\n" SW_BODY
               "Lcl%=:\n\ts_cmpk_ge_u32 %[b], 128\n\ts_cbranch_scc1 Lce%=\n" LW_ASM "s_branch Lce%=\nLcm%=:\n" EW_BODY
               "Lce%=:\n\ts_waitcnt lgkmcnt(0)"
               : [w] "=&s"(wave) STAMP_IO : [t] "{v0}"(tid), [b] "s"(blk), [k] "s"(ka) : WAVE_CLOB);
  if (blk >= 192) TS(15);
}

extern "C" void kernel_launch(void* const* d_in, const int* in_sizes, int n_in, void* d_out, int out_size, void* d_ws, size_t ws_size, hipStream_t stream) {
  (void)hipFuncSetAttribute((const void*)mega, hipFuncAttributeMaxDynamicSharedMemorySize, LDS_BYTES);
  mega<<<256, 1024, LDS_BYTES, stream>>>((const u16*)d_in[0], (const u16*)d_in[1], (const u16*)d_in[2], (const u16*)d_in[3], (const u16*)d_in[4],
                                         (const u16*)d_in[5], (const u16*)d_in[6], (const u16*)d_in[8], (const u16*)d_in[9], (const u16*)d_in[10],
                                         (const u16*)d_in[11], (const u16*)d_in[12], (const u16*)d_in[13], (u16*)d_out, (char*)d_ws);
}
```

```cpp
#include <hip/hip_runtime.h>
#include <stdint.h>

typedef unsigned short u16;
typedef unsigned int u32;
typedef unsigned long long u64;

#define WNT " nt"
#define DNTF ""
#define PRN 3
#define PRF 2
#define PRG 2
#define PRH 3
#define PRE_ 1
#define GPRIO 1
#define XDLY 0
#define X1T 4
#define X1SLP 14
#define PSLP 0
#define KSLP 1
#define FSLP 16
#define OFLAG " sc1"
#define WARM 0
#define PAD(k) do { if (WARM) asm volatile("Lpad" #k ":\n\ts_cbranch_execz Lwret"); } while (0)
#define PADS(k) ""
#define STR_(x) #x
#define STR(x) STR_(x)
#define PB(k) "s_branch Lpad" #k "\n\t"
#define PN "s_branch Lwret\n\t"
#define NPADC 24
#define PADTAB PB(0) PB(1) PB(2) PB(3) PB(4) PB(5) PB(6) PB(7) PB(8) PB(9) PB(10) PB(11) PB(12) PB(13) PB(14) PB(15) \
               PB(16) PB(17) PB(18) PB(19) PB(20) PB(21) PB(22) PB(23) PN PN PN PN PN PN PN PN \
               PB(32) PB(33) PB(34) PB(35) PB(36) PB(37) PB(38) PB(39) PB(40) PB(41) PB(42) PB(43) PB(44) PB(45) PB(46) PB(47)
#define CODE_LINES 218
#define OLATE 1
#define MSLP 1
#define GSLP 10
#define XT 4000
#define JDLY 30
#define DT 9600
#define PFG 0x40000
#define PFT ""
#define PFD 4
#define P1D 8
#define GUW 8
#define GAE 2
#define KBAR 1
#define NOC 0
#define GED 6
#define WSLOW 0
#define YT 0
#define TLBW 0
#define NOPRM 0
#define B0D 2
#define KT 15500
#define QSLP 20
#define P2D 22
#define KALIGN 4096
#define ROT 0
#define GEAR 2
#define SDLY 1
#define PRK 0
#define PRP 1
#define PWT 0
#define XD2 0
#define WD2 0
#define WDLY 16
#define PFM 0
#define PFW 1

constexpr int LDS_BYTES = 163840;

#define TS(i) do {} while (0)
#define CNT(i) do {} while (0)

#define STA(i, r) ""
#define STAMP_IO
#define LSTAMP_IO
#define GSTAMP1 STA(1, "s[60:61]")
#define SSTAMP1 GSTAMP1
#define GSTAMP2 STA(2, "s[62:63]")
#define SSTAMP2 GSTAMP2
#define GSTAMP3 STA(3, "s[64:65]")
#define SSTAMP3 GSTAMP3
#define GSTAMP4 STA(4, "s[66:67]")
#define SSTAMP4 GSTAMP4
#define GSTAMP5 STA(5, "s[68:69]")
#define SSTAMP5 GSTAMP5
#define GSTAMP6 STA(6, "s[70:71]")
#define SSTAMP6 GSTAMP6
#define GSTAMP7 STA(7, "s[72:73]")
#define SSTAMP7 GSTAMP7
#define GSTAMP8 STA(8, "s[74:75]")
#define SSTAMP8 GSTAMP8
#define GSTAMP9 STA(9, "s[76:77]")
#define SSTAMP9 GSTAMP9
#define GSTAMP10 STA(10, "s[78:79]")
#define SSTAMP10 GSTAMP10
#define GSTAMP11 STA(11, "s[80:81]")
#define SSTAMP11 GSTAMP11
#define STC(r) ""
#define SSTAMPCm STC("s60")
#define SSTAMPCu0 STC("s62")
#define SSTAMPCu1 STC("s62")
#define SSTAMPCu2 STC("s66")
#define SSTAMPCu3 STC("s66")
#define SSTAMPCa STC("s64")
#define LSTAMPC STC("s38")
#define LSTAMPD STC("s36")
#define LSTAMP8 GSTAMP8
#define LSTAMP9 GSTAMP9
#define LSTAMP10 GSTAMP10
#define LSTAMP11 GSTAMP11
#define LSTAMP13 STA(13, "s[36:37]")
#define LSTAMP14 STA(14, "s[38:39]")
#define GSTAMP12 SSTAMPE1
#define GSTAMP13 LSTAMP13
#define GSTAMPC LSTAMPC
#define SSTAMPE0 GSTAMP6
#define SSTAMPE1 STA(12, "s[82:83]")
#define SSTAMPE2 ""
#define GW_HEAD \
  "s_load_dwordx8 s[40:47], %[k], 0x8\n" \
  "s_load_dwordx2 s[48:49], %[k], 0x28\n" \
  "s_setprio 1\n" \
  "s_lshr_b32 s50, %[w], 2\n" \
  "s_and_b32 s54, %[b], 1\n" \
  "s_add_u32 s54, s54, s50\n" \
  "s_cmp_eq_u32 s54, 3\n" \
  "s_cselect_b32 s54, 0, s54\n" \
  "s_mul_i32 s53, %[b], 3\n" \
  "s_add_u32 s53, s53, s54\n" \
  "s_add_u32 s52, %[w], s50\n" \
  "s_lshl_b32 s55, s52, 7\n" \
  "s_and_b32 s55, s55, 128\n" \
  "s_lshl_b32 s54, s53, 8\n" \
  "s_add_u32 s54, s54, s55\n" \
  "v_and_b32 v9, 63, %[t]\n" \
  "v_lshl_add_u32 v17, v9, 1, s54\n" \
  "v_mov_b64 v[108:109], 0\n" \
  "v_mov_b64 v[110:111], 0\n" \
  "s_mul_i32 s57, s50, 32096\n" \
  "s_lshl1_add_u32 m0, s55, s57\n" \
  "s_lshl_b32 s84, s53, 1\n" \
  "s_and_b32 s84, s84, -4\n" \
  "s_cmp_ge_u32 s50, " STR(GEAR) "\n" \
  "s_cbranch_scc1 Lnc%=\n" \
  "s_setprio 3\n" \
  "s_bitcmp1_b32 s52, 1\n" \
  GSTAMP1 \
  "s_waitcnt lgkmcnt(0)\n" \
  "global_load_short_d16_hi v108, v17, s[40:41]\n" \
  "global_load_short_d16_hi v109, v17, s[42:43]\n" \
  "s_load_dword s98, s[48:49], s84\n" \
  "s_cbranch_scc1 Lnd%=\n" \
  "global_load_short_d16_hi v110, v17, s[44:45]\n" \
  "global_load_short_d16_hi v111, v17, s[46:47]\n" \
  "Lnd%=:\n" \
  "s_setprio 1\n" \
  "Lnc%=:\n" \
  "s_and_b32 s51, %[w], 3\n" \
  "s_lshl_b32 s58, s50, 13\n" \
  "s_add_u32 s58, s58, 96288\n" \
  "v_and_b32 v1, 15, %[t]\n" \
  "v_and_b32 v2, 48, %[t]\n" \
  "v_mov_b64 v[12:13], 0\n" \
  "v_mov_b64 v[14:15], 0\n" \
  "v_and_b32 v5, 255, %[t]\n" \
  "v_lshl_add_u32 v5, v5, 4, s58\n" \
  "ds_write_b128 v5, v[12:15] offset:4096\n" \
  "v_min_u32 v4, 1, v9\n" \
  "v_mul_u32_u24 v4, 40, v4\n" \
  "v_add_u32 v4, v4, v9\n" \
  "v_mov_b32 v5, 159488\n" \
  "v_lshl_add_u32 v4, v4, 2, v5\n" \
  "s_movk_i32 s54, 544\n" \
  "v_mad_u32_u24 v5, v1, s54, v2\n" \
  "s_and_b32 s54, s51, 1\n" \
  "s_mul_i32 s55, s54, 8704\n" \
  "s_add_u32 s55, s55, 14688\n" \
  "s_cmp_lt_u32 s51, 2\n" \
  "s_cselect_b32 s56, 0, s55\n" \
  "s_cselect_b32 s55, s55, 8704\n" \
  "s_cselect_b32 s85, 16, 64\n" \
  "s_cselect_b32 s86, 512, 16\n" \
  "s_cselect_b32 s87, 8, 5\n" \
  "s_cselect_b32 s88, -2912, 0\n" \
  "v_add_u32 v5, s57, v5\n" \
  "v_add_u32 v6, s55, v5\n" \
  "v_add_u32 v5, s56, v5\n" \
  "s_lshl_b32 s54, s54, s87\n" \
  "s_add_u32 s54, s54, s88\n" \
  "s_add_u32 s54, s54, s58\n" \
  "v_lshrrev_b32 v7, 1, v2\n" \
  "v_and_b32 v7, 8, v7\n" \
  "v_mad_u32_u24 v7, v1, s85, v7\n" \
  "v_lshrrev_b32 v9, 5, v9\n" \
  "v_mad_u32_u24 v7, v9, s86, v7\n" \
  "v_add_u32 v7, s54, v7\n" \
  "s_lshl_b32 s59, s50, 3\n" \
  "s_lshl_b32 s59, 1, s59\n" \
  "v_mov_b32 v16, s59\n" \
  "s_not_b32 s53, s53\n" \
  "s_lshl_b32 s53, s53, 4\n" \
  "s_cmp_eq_u32 s50, 0\n" \
  "s_cbranch_scc0 Lp1%=\n" \
  "s_setprio 2\n" \
  "Lp1%=:\n" \
  "s_bitcmp1_b32 s52, 1\n" \
  "s_cbranch_scc1 Lgb%=\n" \
  "s_barrier\n"

#define GW_BODY \
  GSTAMP2 \
  "s_cmp_lt_u32 s50, " STR(GEAR) "\n" \
  "s_cbranch_scc1 Lne1%=\n" \
  "s_waitcnt lgkmcnt(0)\n" \
  "global_load_short_d16_hi v108, v17, s[40:41]\n" \
  "global_load_short_d16_hi v109, v17, s[42:43]\n" \
  "s_load_dword s98, s[48:49], s84\n" \
  "global_load_short_d16_hi v110, v17, s[44:45]\n" \
  "global_load_short_d16_hi v111, v17, s[46:47]\n" \
  "Lne1%=:\n" \
  "v_lshrrev_b32 v3, 1, v2\n" \
  "v_add3_u32 v3, v3, v1, 1\n" \
  "v_and_b32 v8, 1, v3\n" \
  "v_mul_u32_u24 v8, 1470, v8\n" \
  "v_lshl_add_u32 v3, v3, 1, v8\n" \
  "s_add_u32 s55, s58, 4544\n" \
  "v_add_u32 v3, s55, v3\n" \
  "v_lshlrev_b32 v8, 5, v1\n" \
  "v_sub_u32 v10, v2, v8\n" \
  "s_add_u32 s55, s58, 480\n" \
  "v_add_u32 v10, s55, v10\n" \
  "v_mul_u32_u24 v11, 20, v2\n" \
  "v_lshl_add_u32 v11, v1, 4, v11\n" \
  "v_add_u32 v11, s57, v11\n" \
  "v_lshlrev_b32 v8, 2, v1\n" \
  "v_sub_u32 v8, v8, v2\n" \
  "s_add_u32 s55, s58, 7540\n" \
  "v_add_u32 v8, s55, v8\n" \
  "s_bitcmp0_b32 s51, 0\n" \
  "s_cbranch_scc1 Lo1%=\n" \
  "s_lshr_b32 s55, s51, 1\n" \
  "s_mul_i32 s55, s55, 1470\n" \
  "s_add_u32 s55, s55, s58\n" \
  "v_lshrrev_b32 v19, 3, v2\n" \
  "v_mul_u32_u24 v19, 29, v19\n" \
  "v_lshl_add_u32 v19, v1, 1, v19\n" \
  "v_add_u32 v19, s55, v19\n" \
  "v_mov_b32 v20, 0x3c00\n" \
  "s_mov_b32 exec_lo, 0x001f0007\n" \
  "s_mov_b32 exec_hi, 0x01ff007f\n" \
  "ds_write_b16 v19, v20 offset:5798\n" \
  "s_mov_b64 exec, -1\n" \
  "Lo1%=:\n" \
  "s_lshl_b32 s54, s59, 2\n" \
  "v_mov_b32 v9, s57\n" \
  "s_sub_u32 s55, s51, s50\n" \
  "s_cmp_eq_u32 s51, 3\n" \
  "s_cselect_b32 s55, 3, s55\n" \
  "s_waitcnt vmcnt(0) lgkmcnt(0)\n" \
  "s_lshl_b32 s98, s98, s53\n" \
  "s_and_b32 s98, s98, 0xffff0000\n" \
  "v_mul_f32 v108, 0x3fb8aa3b, v108\n" \
  "v_mov_b32 v112, s98\n" \
  "v_mul_f32 v112, 0x3fb8aa3b, v112\n" \
  "v_exp_f32 v108, v108\n" \
  "v_exp_f32 v112, v112\n" \
  "v_max_f32 v122, |v110|, |v111|\n" \
  "v_cvt_pk_bf16_f32 v108, 0, v108\n" \
  "v_cvt_pk_bf16_f32 v112, 0, v112\n" \
  "v_max_u32_dpp v122, v122, v122 quad_perm:[1,0,3,2] row_mask:0xf bank_mask:0xf\n" \
  "v_pk_mul_f32 v[108:109], v[108:109], v[112:113] op_sel_hi:[1,0]\n" \
  "v_mul_f32 v115, 0x3e22f983, v109\n" \
  "v_max_u32_dpp v122, v122, v122 quad_perm:[2,3,0,1] row_mask:0xf bank_mask:0xf\n" \
  "v_mul_f32 v117, 0x4222f983, v109\n" \
  "v_mul_f32 v114, 0xbfb8aa3b, v108\n" \
  "v_max_u32_dpp v122, v122, v122 row_half_mirror row_mask:0xf bank_mask:0xf\n" \
  "v_mul_f32 v116, 0xc3b8aa3b, v108\n" \
  "v_fract_f32 v115, v115\n" \
  "v_max_u32_dpp v122, v122, v122 row_mirror row_mask:0xf bank_mask:0xf\n" \
  "v_fract_f32 v117, v117\n" \
  "v_exp_f32 v114, v114\n" \
  "v_max_u32_dpp v122, v122, v122 row_bcast:15 row_mask:0xa bank_mask:0xf\n" \
  "v_cos_f32 v118, v115\n" \
  "v_sin_f32 v119, v115\n" \
  "v_max_u32_dpp v122, v122, v122 row_bcast:31 row_mask:0xc bank_mask:0xf\n" \
  "v_exp_f32 v116, v116\n" \
  "v_cos_f32 v120, v117\n" \
  "v_readlane_b32 s94, v122, 63\n" \
  "v_sin_f32 v121, v117\n" \
  "v_pk_mul_f32 v[118:119], v[118:119], v[114:115] op_sel_hi:[1,0]\n" \
  "s_lshr_b32 s94, s94, 23\n" \
  "v_pk_mul_f32 v[120:121], v[120:121], v[116:117] op_sel_hi:[1,0]\n" \
  "s_max_u32 s94, s94, 26\n" \
  "s_min_u32 s94, s94, 226\n" \
  "v_mov_b32 v113, s94\n" \
  "s_sub_u32 s94, 266, s94\n" \
  "s_lshl_b32 s94, s94, 23\n" \
  "ds_write_addtid_b32 v113 offset:8704\n" \
  "ds_write_addtid_b32 v112 offset:10336\n" \
  "v_pk_mul_f32 v[110:111], v[110:111], s[94:95] op_sel_hi:[1,0]\n" \
  "v_pk_mul_f32 v[104:105], v[110:111], v[118:119] op_sel:[1,1] op_sel_hi:[1,0] neg_lo:[0,1]\n" \
  "v_pk_mul_f32 v[106:107], v[110:111], v[120:121] op_sel:[1,1] op_sel_hi:[1,0] neg_lo:[0,1]\n" \
  "v_cvt_pk_f16_f32 v112, v110, v111\n" \
  "v_pk_fma_f32 v[114:115], v[110:111], v[118:119], v[104:105] op_sel_hi:[0,1,1]\n" \
  "v_pk_fma_f32 v[116:117], v[110:111], v[120:121], v[106:107] op_sel_hi:[0,1,1]\n" \
  "ds_write_addtid_b32 v112 offset:0\n" \
  "v_pk_mul_f32 v[104:105], v[114:115], v[118:119] op_sel:[1,1] op_sel_hi:[1,0] neg_lo:[0,1]\n" \
  "v_cvt_pk_f16_f32 v112, v114, v115\n" \
  "v_cvt_pk_f16_f32 v103, v116, v117\n" \
  "v_pk_fma_f32 v[114:115], v[114:115], v[118:119], v[104:105] op_sel_hi:[0,1,1]\n" \
  "ds_write_addtid_b32 v112 offset:544\n" \
  "ds_write_addtid_b32 v103 offset:4352\n" \
  "ds_write_addtid_b32 v103 offset:4896\n" \
  "v_pk_mul_f32 v[104:105], v[114:115], v[118:119] op_sel:[1,1] op_sel_hi:[1,0] neg_lo:[0,1]\n" \
  "v_pk_mul_f32 v[106:107], v[114:115], v[120:121] op_sel:[1,1] op_sel_hi:[1,0] neg_lo:[0,1]\n" \
  "v_cvt_pk_f16_f32 v112, v114, v115\n" \
  "v_pk_fma_f32 v[122:123], v[114:115], v[118:119], v[104:105] op_sel_hi:[0,1,1]\n" \
  "v_pk_fma_f32 v[116:117], v[114:115], v[120:121], v[106:107] op_sel_hi:[0,1,1]\n" \
  "ds_write_addtid_b32 v112 offset:1088\n" \
  "v_pk_mul_f32 v[104:105], v[122:123], v[118:119] op_sel:[1,1] op_sel_hi:[1,0] neg_lo:[0,1]\n" \
  "v_cvt_pk_f16_f32 v112, v122, v123\n" \
  "v_cvt_pk_f16_f32 v103, v116, v117\n" \
  "v_pk_fma_f32 v[114:115], v[122:123], v[118:119], v[104:105] op_sel_hi:[0,1,1]\n" \
  "ds_write_addtid_b32 v112 offset:1632\n" \
  "ds_write_addtid_b32 v103 offset:5440\n" \
  "v_pk_mul_f32 v[104:105], v[114:115], v[118:119] op_sel:[1,1] op_sel_hi:[1,0] neg_lo:[0,1]\n" \
  "v_pk_mul_f32 v[106:107], v[114:115], v[120:121] op_sel:[1,1] op_sel_hi:[1,0] neg_lo:[0,1]\n" \
  "v_cvt_pk_f16_f32 v112, v114, v115\n" \
  "v_pk_fma_f32 v[122:123], v[114:115], v[118:119], v[104:105] op_sel_hi:[0,1,1]\n" \
  "v_pk_fma_f32 v[116:117], v[114:115], v[120:121], v[106:107] op_sel_hi:[0,1,1]\n" \
  "ds_write_addtid_b32 v112 offset:2176\n" \
  "v_pk_mul_f32 v[104:105], v[122:123], v[118:119] op_sel:[1,1] op_sel_hi:[1,0] neg_lo:[0,1]\n" \
  "v_pk_mul_f32 v[106:107], v[116:117], v[120:121] op_sel:[1,1] op_sel_hi:[1,0] neg_lo:[0,1]\n" \
  "v_cvt_pk_f16_f32 v112, v122, v123\n" \
  "v_cvt_pk_f16_f32 v103, v116, v117\n" \
  "v_pk_fma_f32 v[114:115], v[122:123], v[118:119], v[104:105] op_sel_hi:[0,1,1]\n" \
  "v_pk_fma_f32 v[116:117], v[116:117], v[120:121], v[106:107] op_sel_hi:[0,1,1]\n" \
  "ds_write_addtid_b32 v112 offset:2720\n" \
  "ds_write_addtid_b32 v103 offset:5984\n" \
  "ds_write_addtid_b32 v103 offset:6528\n" \
  "ds_write_addtid_b32 v103 offset:7072\n" \
  "v_pk_mul_f32 v[104:105], v[114:115], v[118:119] op_sel:[1,1] op_sel_hi:[1,0] neg_lo:[0,1]\n" \
  "v_pk_mul_f32 v[106:107], v[116:117], v[120:121] op_sel:[1,1] op_sel_hi:[1,0] neg_lo:[0,1]\n" \
  "v_cvt_pk_f16_f32 v112, v114, v115\n" \
  "v_cvt_pk_f16_f32 v103, v116, v117\n" \
  "v_pk_fma_f32 v[122:123], v[114:115], v[118:119], v[104:105] op_sel_hi:[0,1,1]\n" \
  "v_pk_fma_f32 v[116:117], v[116:117], v[120:121], v[106:107] op_sel_hi:[0,1,1]\n" \
  "ds_write_addtid_b32 v112 offset:3264\n" \
  "ds_write_addtid_b32 v103 offset:9248\n" \
  "v_pk_mul_f32 v[104:105], v[122:123], v[118:119] op_sel:[1,1] op_sel_hi:[1,0] neg_lo:[0,1]\n" \
  "v_pk_mul_f32 v[106:107], v[114:115], v[120:121] op_sel:[1,1] op_sel_hi:[1,0] neg_lo:[0,1]\n" \
  "v_cvt_pk_f16_f32 v112, v122, v123\n" \
  "v_cvt_pk_f16_f32 v103, v116, v117\n" \
  "v_pk_fma_f32 v[122:123], v[122:123], v[118:119], v[104:105] op_sel_hi:[0,1,1]\n" \
  "v_pk_fma_f32 v[116:117], v[114:115], v[120:121], v[106:107] op_sel_hi:[0,1,1]\n" \
  "ds_write_addtid_b32 v112 offset:3808\n" \
  "ds_write_addtid_b32 v103 offset:9792\n" \
  "v_pk_mul_f32 v[104:105], v[122:123], v[120:121] op_sel:[1,1] op_sel_hi:[1,0] neg_lo:[0,1]\n" \
  "v_pk_mul_f32 v[106:107], v[120:121], v[120:121] op_sel:[1,1] op_sel_hi:[1,0] neg_lo:[0,1]\n" \
  "v_cvt_pk_f16_f32 v112, v116, v117\n" \
  "v_pk_fma_f32 v[114:115], v[122:123], v[120:121], v[104:105] op_sel_hi:[0,1,1]\n" \
  "v_pk_fma_f32 v[116:117], v[120:121], v[120:121], v[106:107] op_sel_hi:[0,1,1]\n" \
  "ds_write_addtid_b32 v112 offset:7616\n" \
  "v_pk_mul_f32 v[104:105], v[122:123], v[116:117] op_sel:[1,1] op_sel_hi:[1,0] neg_lo:[0,1]\n" \
  "v_pk_mul_f32 v[106:107], v[114:115], v[116:117] op_sel:[1,1] op_sel_hi:[1,0] neg_lo:[0,1]\n" \
  "v_cvt_pk_f16_f32 v112, v114, v115\n" \
  "v_pk_fma_f32 v[122:123], v[122:123], v[116:117], v[104:105] op_sel_hi:[0,1,1]\n" \
  "v_pk_fma_f32 v[114:115], v[114:115], v[116:117], v[106:107] op_sel_hi:[0,1,1]\n" \
  "ds_write_addtid_b32 v112 offset:8160\n" \
  "v_pk_mul_f32 v[104:105], v[122:123], v[116:117] op_sel:[1,1] op_sel_hi:[1,0] neg_lo:[0,1]\n" \
  "v_pk_mul_f32 v[106:107], v[114:115], v[116:117] op_sel:[1,1] op_sel_hi:[1,0] neg_lo:[0,1]\n" \
  "v_cvt_pk_f16_f32 v112, v122, v123\n" \
  "v_cvt_pk_f16_f32 v103, v114, v115\n" \
  "v_pk_fma_f32 v[122:123], v[122:123], v[116:117], v[104:105] op_sel_hi:[0,1,1]\n" \
  "v_pk_fma_f32 v[114:115], v[114:115], v[116:117], v[106:107] op_sel_hi:[0,1,1]\n" \
  "ds_write_addtid_b32 v112 offset:10880\n" \
  "ds_write_addtid_b32 v103 offset:11424\n" \
  "v_pk_mul_f32 v[104:105], v[122:123], v[116:117] op_sel:[1,1] op_sel_hi:[1,0] neg_lo:[0,1]\n" \
  "v_pk_mul_f32 v[106:107], v[114:115], v[116:117] op_sel:[1,1] op_sel_hi:[1,0] neg_lo:[0,1]\n" \
  "v_cvt_pk_f16_f32 v112, v122, v123\n" \
  "v_cvt_pk_f16_f32 v103, v114, v115\n" \
  "v_pk_fma_f32 v[122:123], v[122:123], v[116:117], v[104:105] op_sel_hi:[0,1,1]\n" \
  "v_pk_fma_f32 v[114:115], v[114:115], v[116:117], v[106:107] op_sel_hi:[0,1,1]\n" \
  "ds_write_addtid_b32 v112 offset:12512\n" \
  "ds_write_addtid_b32 v103 offset:13056\n" \
  "v_cvt_pk_f16_f32 v112, v122, v123\n" \
  "v_cvt_pk_f16_f32 v103, v114, v115\n" \
  "ds_write_addtid_b32 v112 offset:13600\n" \
  "ds_write_addtid_b32 v103 offset:14144\n" \
  "s_branch Lgk%=\n" \
  "Lgb%=:\n" \
  "s_barrier\n" \
  GSTAMP2 \
  "s_cmp_lt_u32 s50, " STR(GEAR) "\n" \
  "s_cbranch_scc1 Lne8%=\n" \
  "s_waitcnt lgkmcnt(0)\n" \
  "global_load_short_d16_hi v108, v17, s[40:41]\n" \
  "global_load_short_d16_hi v109, v17, s[42:43]\n" \
  "s_load_dword s98, s[48:49], s84\n" \
  "Lne8%=:\n" \
  "v_lshrrev_b32 v3, 1, v2\n" \
  "v_add3_u32 v3, v3, v1, 1\n" \
  "v_and_b32 v8, 1, v3\n" \
  "v_mul_u32_u24 v8, 1470, v8\n" \
  "v_lshl_add_u32 v3, v3, 1, v8\n" \
  "s_add_u32 s55, s58, 4544\n" \
  "v_add_u32 v3, s55, v3\n" \
  "v_lshlrev_b32 v8, 5, v1\n" \
  "v_sub_u32 v10, v2, v8\n" \
  "s_add_u32 s55, s58, 480\n" \
  "v_add_u32 v10, s55, v10\n" \
  "v_mul_u32_u24 v11, 20, v2\n" \
  "v_lshl_add_u32 v11, v1, 4, v11\n" \
  "v_add_u32 v11, s57, v11\n" \
  "v_lshlrev_b32 v8, 2, v1\n" \
  "v_sub_u32 v8, v8, v2\n" \
  "s_add_u32 s55, s58, 7540\n" \
  "v_add_u32 v8, s55, v8\n" \
  "s_bitcmp0_b32 s51, 0\n" \
  "s_cbranch_scc1 Lo8%=\n" \
  "s_lshr_b32 s55, s51, 1\n" \
  "s_mul_i32 s55, s55, 1470\n" \
  "s_add_u32 s55, s55, s58\n" \
  "v_lshrrev_b32 v19, 3, v2\n" \
  "v_mul_u32_u24 v19, 29, v19\n" \
  "v_lshl_add_u32 v19, v1, 1, v19\n" \
  "v_add_u32 v19, s55, v19\n" \
  "v_mov_b32 v20, 0x3c00\n" \
  "s_mov_b32 exec_lo, 0x001f0007\n" \
  "s_mov_b32 exec_hi, 0x01ff007f\n" \
  "ds_write_b16 v19, v20 offset:5798\n" \
  "s_mov_b64 exec, -1\n" \
  "Lo8%=:\n" \
  "s_lshl_b32 s54, s59, 2\n" \
  "v_mov_b32 v9, s57\n" \
  "s_sub_u32 s55, s51, s50\n" \
  "s_cmp_eq_u32 s51, 3\n" \
  "s_cselect_b32 s55, 3, s55\n" \
  "s_waitcnt vmcnt(0) lgkmcnt(0)\n" \
  "s_lshl_b32 s98, s98, s53\n" \
  "s_and_b32 s98, s98, 0xffff0000\n" \
  "v_mul_f32 v108, 0x3fb8aa3b, v108\n" \
  "v_mov_b32 v112, s98\n" \
  "v_mul_f32 v112, 0x3fb8aa3b, v112\n" \
  "v_exp_f32 v108, v108\n" \
  "v_exp_f32 v112, v112\n" \
  "v_mov_b32 v103, 0x3c00\n" \
  "v_cvt_pk_bf16_f32 v108, 0, v108\n" \
  "v_cvt_pk_bf16_f32 v112, 0, v112\n" \
  "v_pk_mul_f32 v[108:109], v[108:109], v[112:113] op_sel_hi:[1,0]\n" \
  "v_mul_f32 v115, 0xbfa2f983, v109\n" \
  "v_mul_f32 v117, 0xc1a2f983, v109\n" \
  "v_mul_f32 v114, 0xc138aa3b, v108\n" \
  "v_mul_f32 v116, 0xc338aa3b, v108\n" \
  "v_fract_f32 v115, v115\n" \
  "v_fract_f32 v117, v117\n" \
  "v_exp_f32 v114, v114\n" \
  "v_cos_f32 v118, v115\n" \
  "v_sin_f32 v119, v115\n" \
  "v_exp_f32 v116, v116\n" \
  "v_cos_f32 v120, v117\n" \
  "v_sin_f32 v121, v117\n" \
  "v_pk_mul_f32 v[118:119], v[118:119], v[114:115] op_sel_hi:[1,0]\n" \
  "v_pk_mul_f32 v[120:121], v[120:121], v[116:117] op_sel_hi:[1,0]\n" \
  "ds_write_addtid_b32 v103 offset:14688\n" \
  "v_pk_mul_f32 v[104:105], v[118:119], v[118:119] op_sel:[1,1] op_sel_hi:[1,0] neg_lo:[0,1]\n" \
  "v_pk_mul_f32 v[106:107], v[120:121], v[118:119] op_sel:[1,1] op_sel_hi:[1,0] neg_lo:[0,1]\n" \
  "v_cvt_pk_f16_f32 v112, v118, v119\n" \
  "v_cvt_pk_f16_f32 v103, v120, v121\n" \
  "v_pk_fma_f32 v[110:111], v[118:119], v[118:119], v[104:105] op_sel_hi:[0,1,1]\n" \
  "v_pk_fma_f32 v[122:123], v[120:121], v[118:119], v[106:107] op_sel_hi:[0,1,1]\n" \
  "ds_write_addtid_b32 v112 offset:15232\n" \
  "ds_write_addtid_b32 v103 offset:23392\n" \
  "v_pk_mul_f32 v[104:105], v[110:111], v[118:119] op_sel:[1,1] op_sel_hi:[1,0] neg_lo:[0,1]\n" \
  "v_pk_mul_f32 v[106:107], v[122:123], v[118:119] op_sel:[1,1] op_sel_hi:[1,0] neg_lo:[0,1]\n" \
  "v_cvt_pk_f16_f32 v112, v110, v111\n" \
  "v_cvt_pk_f16_f32 v103, v122, v123\n" \
  "v_pk_fma_f32 v[110:111], v[110:111], v[118:119], v[104:105] op_sel_hi:[0,1,1]\n" \
  "v_pk_fma_f32 v[122:123], v[122:123], v[118:119], v[106:107] op_sel_hi:[0,1,1]\n" \
  "ds_write_addtid_b32 v112 offset:15776\n" \
  "ds_write_addtid_b32 v103 offset:23936\n" \
  "v_pk_mul_f32 v[104:105], v[110:111], v[118:119] op_sel:[1,1] op_sel_hi:[1,0] neg_lo:[0,1]\n" \
  "v_pk_mul_f32 v[106:107], v[122:123], v[118:119] op_sel:[1,1] op_sel_hi:[1,0] neg_lo:[0,1]\n" \
  "v_cvt_pk_f16_f32 v112, v110, v111\n" \
  "v_cvt_pk_f16_f32 v103, v122, v123\n" \
  "v_pk_fma_f32 v[110:111], v[110:111], v[118:119], v[104:105] op_sel_hi:[0,1,1]\n" \
  "v_pk_fma_f32 v[122:123], v[122:123], v[118:119], v[106:107] op_sel_hi:[0,1,1]\n" \
  "ds_write_addtid_b32 v112 offset:16320\n" \
  "ds_write_addtid_b32 v103 offset:24480\n" \
  "v_pk_mul_f32 v[104:105], v[110:111], v[118:119] op_sel:[1,1] op_sel_hi:[1,0] neg_lo:[0,1]\n" \
  "v_pk_mul_f32 v[106:107], v[122:123], v[118:119] op_sel:[1,1] op_sel_hi:[1,0] neg_lo:[0,1]\n" \
  "v_cvt_pk_f16_f32 v112, v110, v111\n" \
  "v_cvt_pk_f16_f32 v103, v122, v123\n" \
  "v_pk_fma_f32 v[110:111], v[110:111], v[118:119], v[104:105] op_sel_hi:[0,1,1]\n" \
  "v_pk_fma_f32 v[122:123], v[122:123], v[118:119], v[106:107] op_sel_hi:[0,1,1]\n" \
  "ds_write_addtid_b32 v112 offset:16864\n" \
  "ds_write_addtid_b32 v103 offset:25024\n" \
  "v_pk_mul_f32 v[104:105], v[110:111], v[118:119] op_sel:[1,1] op_sel_hi:[1,0] neg_lo:[0,1]\n" \
  "v_pk_mul_f32 v[106:107], v[122:123], v[118:119] op_sel:[1,1] op_sel_hi:[1,0] neg_lo:[0,1]\n" \
  "v_cvt_pk_f16_f32 v112, v110, v111\n" \
  "v_cvt_pk_f16_f32 v103, v122, v123\n" \
  "v_pk_fma_f32 v[110:111], v[110:111], v[118:119], v[104:105] op_sel_hi:[0,1,1]\n" \
  "v_pk_fma_f32 v[122:123], v[122:123], v[118:119], v[106:107] op_sel_hi:[0,1,1]\n" \
  "ds_write_addtid_b32 v112 offset:17408\n" \
  "ds_write_addtid_b32 v103 offset:25568\n" \
  "v_pk_mul_f32 v[104:105], v[110:111], v[118:119] op_sel:[1,1] op_sel_hi:[1,0] neg_lo:[0,1]\n" \
  "v_pk_mul_f32 v[106:107], v[122:123], v[118:119] op_sel:[1,1] op_sel_hi:[1,0] neg_lo:[0,1]\n" \
  "v_cvt_pk_f16_f32 v112, v110, v111\n" \
  "v_cvt_pk_f16_f32 v103, v122, v123\n" \
  "v_pk_fma_f32 v[110:111], v[110:111], v[118:119], v[104:105] op_sel_hi:[0,1,1]\n" \
  "v_pk_fma_f32 v[122:123], v[122:123], v[118:119], v[106:107] op_sel_hi:[0,1,1]\n" \
  "ds_write_addtid_b32 v112 offset:17952\n" \
  "ds_write_addtid_b32 v103 offset:26112\n" \
  "v_pk_mul_f32 v[104:105], v[110:111], v[118:119] op_sel:[1,1] op_sel_hi:[1,0] neg_lo:[0,1]\n" \
  "v_pk_mul_f32 v[106:107], v[122:123], v[118:119] op_sel:[1,1] op_sel_hi:[1,0] neg_lo:[0,1]\n" \
  "v_cvt_pk_f16_f32 v112, v110, v111\n" \
  "v_cvt_pk_f16_f32 v103, v122, v123\n" \
  "v_pk_fma_f32 v[110:111], v[110:111], v[118:119], v[104:105] op_sel_hi:[0,1,1]\n" \
  "v_pk_fma_f32 v[122:123], v[122:123], v[118:119], v[106:107] op_sel_hi:[0,1,1]\n" \
  "ds_write_addtid_b32 v112 offset:18496\n" \
  "ds_write_addtid_b32 v103 offset:26656\n" \
  "v_pk_mul_f32 v[104:105], v[110:111], v[118:119] op_sel:[1,1] op_sel_hi:[1,0] neg_lo:[0,1]\n" \
  "v_pk_mul_f32 v[106:107], v[122:123], v[118:119] op_sel:[1,1] op_sel_hi:[1,0] neg_lo:[0,1]\n" \
  "v_cvt_pk_f16_f32 v112, v110, v111\n" \
  "v_cvt_pk_f16_f32 v103, v122, v123\n" \
  "v_pk_fma_f32 v[110:111], v[110:111], v[118:119], v[104:105] op_sel_hi:[0,1,1]\n" \
  "v_pk_fma_f32 v[122:123], v[122:123], v[118:119], v[106:107] op_sel_hi:[0,1,1]\n" \
  "ds_write_addtid_b32 v112 offset:19040\n" \
  "ds_write_addtid_b32 v103 offset:27200\n" \
  "v_pk_mul_f32 v[104:105], v[110:111], v[118:119] op_sel:[1,1] op_sel_hi:[1,0] neg_lo:[0,1]\n" \
  "v_pk_mul_f32 v[106:107], v[122:123], v[118:119] op_sel:[1,1] op_sel_hi:[1,0] neg_lo:[0,1]\n" \
  "v_cvt_pk_f16_f32 v112, v110, v111\n" \
  "v_cvt_pk_f16_f32 v103, v122, v123\n" \
  "v_pk_fma_f32 v[110:111], v[110:111], v[118:119], v[104:105] op_sel_hi:[0,1,1]\n" \
  "v_pk_fma_f32 v[122:123], v[122:123], v[118:119], v[106:107] op_sel_hi:[0,1,1]\n" \
  "ds_write_addtid_b32 v112 offset:19584\n" \
  "ds_write_addtid_b32 v103 offset:27744\n" \
  "v_pk_mul_f32 v[104:105], v[110:111], v[118:119] op_sel:[1,1] op_sel_hi:[1,0] neg_lo:[0,1]\n" \
  "v_pk_mul_f32 v[106:107], v[122:123], v[118:119] op_sel:[1,1] op_sel_hi:[1,0] neg_lo:[0,1]\n" \
  "v_cvt_pk_f16_f32 v112, v110, v111\n" \
  "v_cvt_pk_f16_f32 v103, v122, v123\n" \
  "v_pk_fma_f32 v[110:111], v[110:111], v[118:119], v[104:105] op_sel_hi:[0,1,1]\n" \
  "v_pk_fma_f32 v[122:123], v[122:123], v[118:119], v[106:107] op_sel_hi:[0,1,1]\n" \
  "ds_write_addtid_b32 v112 offset:20128\n" \
  "ds_write_addtid_b32 v103 offset:28288\n" \
  "v_pk_mul_f32 v[104:105], v[110:111], v[118:119] op_sel:[1,1] op_sel_hi:[1,0] neg_lo:[0,1]\n" \
  "v_pk_mul_f32 v[106:107], v[122:123], v[118:119] op_sel:[1,1] op_sel_hi:[1,0] neg_lo:[0,1]\n" \
  "v_cvt_pk_f16_f32 v112, v110, v111\n" \
  "v_cvt_pk_f16_f32 v103, v122, v123\n" \
  "v_pk_fma_f32 v[110:111], v[110:111], v[118:119], v[104:105] op_sel_hi:[0,1,1]\n" \
  "v_pk_fma_f32 v[122:123], v[122:123], v[118:119], v[106:107] op_sel_hi:[0,1,1]\n" \
  "ds_write_addtid_b32 v112 offset:20672\n" \
  "ds_write_addtid_b32 v103 offset:28832\n" \
  "v_pk_mul_f32 v[104:105], v[110:111], v[118:119] op_sel:[1,1] op_sel_hi:[1,0] neg_lo:[0,1]\n" \
  "v_pk_mul_f32 v[106:107], v[122:123], v[118:119] op_sel:[1,1] op_sel_hi:[1,0] neg_lo:[0,1]\n" \
  "v_cvt_pk_f16_f32 v112, v110, v111\n" \
  "v_cvt_pk_f16_f32 v103, v122, v123\n" \
  "v_pk_fma_f32 v[110:111], v[110:111], v[118:119], v[104:105] op_sel_hi:[0,1,1]\n" \
  "v_pk_fma_f32 v[122:123], v[122:123], v[118:119], v[106:107] op_sel_hi:[0,1,1]\n" \
  "ds_write_addtid_b32 v112 offset:21216\n" \
  "ds_write_addtid_b32 v103 offset:29376\n" \
  "v_pk_mul_f32 v[104:105], v[110:111], v[118:119] op_sel:[1,1] op_sel_hi:[1,0] neg_lo:[0,1]\n" \
  "v_pk_mul_f32 v[106:107], v[122:123], v[118:119] op_sel:[1,1] op_sel_hi:[1,0] neg_lo:[0,1]\n" \
  "v_cvt_pk_f16_f32 v112, v110, v111\n" \
  "v_cvt_pk_f16_f32 v103, v122, v123\n" \
  "v_pk_fma_f32 v[110:111], v[110:111], v[118:119], v[104:105] op_sel_hi:[0,1,1]\n" \
  "v_pk_fma_f32 v[122:123], v[122:123], v[118:119], v[106:107] op_sel_hi:[0,1,1]\n" \
  "ds_write_addtid_b32 v112 offset:21760\n" \
  "ds_write_addtid_b32 v103 offset:29920\n" \
  "v_pk_mul_f32 v[104:105], v[110:111], v[118:119] op_sel:[1,1] op_sel_hi:[1,0] neg_lo:[0,1]\n" \
  "v_pk_mul_f32 v[106:107], v[122:123], v[118:119] op_sel:[1,1] op_sel_hi:[1,0] neg_lo:[0,1]\n" \
  "v_cvt_pk_f16_f32 v112, v110, v111\n" \
  "v_cvt_pk_f16_f32 v103, v122, v123\n" \
  "v_pk_fma_f32 v[110:111], v[110:111], v[118:119], v[104:105] op_sel_hi:[0,1,1]\n" \
  "v_pk_fma_f32 v[122:123], v[122:123], v[118:119], v[106:107] op_sel_hi:[0,1,1]\n" \
  "ds_write_addtid_b32 v112 offset:22304\n" \
  "ds_write_addtid_b32 v103 offset:30464\n" \
  "v_pk_mul_f32 v[104:105], v[122:123], v[118:119] op_sel:[1,1] op_sel_hi:[1,0] neg_lo:[0,1]\n" \
  "v_cvt_pk_f16_f32 v112, v110, v111\n" \
  "v_cvt_pk_f16_f32 v103, v122, v123\n" \
  "v_pk_fma_f32 v[122:123], v[122:123], v[118:119], v[104:105] op_sel_hi:[0,1,1]\n" \
  "ds_write_addtid_b32 v112 offset:22848\n" \
  "ds_write_addtid_b32 v103 offset:31008\n" \
  "v_cvt_pk_f16_f32 v112, v122, v123\n" \
  "ds_write_addtid_b32 v112 offset:31552\n" \
  "Lgk%=:\n" \
  "ds_add_u32 v4, v16 offset:8\n" \
  GSTAMP3 \
  "s_mov_b32 s97, 0x1fffff\n" \
  "Lkp%=:\n" \
  "ds_read_b32 v17, v4 offset:8\n" \
  "s_waitcnt lgkmcnt(0)\n" \
  "v_readfirstlane_b32 s96, v17\n" \
  "s_and_b32 s96, s96, s54\n" \
  "s_cmp_eq_u32 s96, s54\n" \
  "s_cbranch_scc1 Lkp%=x\n" \
  "s_sleep 1\n" \
  "s_sub_u32 s97, s97, 1\n" \
  "s_cbranch_scc0 Lkp%=\n" \
  "Lkp%=x:\n" \
  GSTAMP4 \
  "ds_read2st64_b32 v[18:19], v9 offset0:34 offset1:35\n" \
  "ds_read_b128 v[24:27], v5 offset:0\n" \
  "ds_read_b128 v[28:31], v6 offset:0\n" \
  "ds_read_b128 v[56:59], v5 offset:256\n" \
  "ds_read_b128 v[60:63], v6 offset:256\n" \
  "ds_read_b128 v[32:35], v5 offset:64\n" \
  "ds_read_b128 v[36:39], v6 offset:64\n" \
  "ds_read_b128 v[64:67], v5 offset:320\n" \
  "ds_read_b128 v[68:71], v6 offset:320\n" \
  "ds_read_b128 v[40:43], v5 offset:128\n" \
  "ds_read_b128 v[44:47], v6 offset:128\n" \
  "ds_read_b128 v[72:75], v5 offset:384\n" \
  "ds_read_b128 v[76:79], v6 offset:384\n" \
  "ds_read_b128 v[48:51], v5 offset:192\n" \
  "ds_read_b128 v[52:55], v6 offset:192\n" \
  "ds_read_b128 v[80:83], v5 offset:448\n" \
  "ds_read_b128 v[84:87], v6 offset:448\n" \
  "s_waitcnt lgkmcnt(14)\n" \
  "v_max_u32 v17, v18, v19\n" \
  "v_sub_u32 v20, 121, v17\n" \
  "s_waitcnt lgkmcnt(14)\n" \
  "v_mfma_f32_16x16x32_f16 v[88:91], v[24:27], v[28:31], 0\n" \
  "v_add_u32 v18, v18, v20\n" \
  "v_add_u32 v19, v19, v20\n" \
  "s_waitcnt lgkmcnt(12)\n" \
  "v_mfma_f32_16x16x32_f16 v[92:95], v[56:59], v[60:63], 0\n" \
  "v_max_i32 v18, 0, v18\n" \
  "v_max_i32 v19, 0, v19\n" \
  "s_waitcnt lgkmcnt(10)\n" \
  "v_mfma_f32_16x16x32_f16 v[88:91], v[32:35], v[36:39], v[88:91]\n" \
  "v_lshlrev_b32 v18, 23, v18\n" \
  "v_lshlrev_b32 v19, 23, v19\n" \
  "s_waitcnt lgkmcnt(8)\n" \
  "v_mfma_f32_16x16x32_f16 v[92:95], v[64:67], v[68:71], v[92:95]\n" \
  "s_waitcnt lgkmcnt(6)\n" \
  "v_mfma_f32_16x16x32_f16 v[88:91], v[40:43], v[44:47], v[88:91]\n" \
  "s_waitcnt lgkmcnt(4)\n" \
  "v_mfma_f32_16x16x32_f16 v[92:95], v[72:75], v[76:79], v[92:95]\n" \
  "s_waitcnt lgkmcnt(2)\n" \
  "v_mfma_f32_16x16x32_f16 v[88:91], v[48:51], v[52:55], v[88:91]\n" \
  "s_waitcnt lgkmcnt(0)\n" \
  "v_mfma_f32_16x16x32_f16 v[92:95], v[80:83], v[84:87], v[92:95]\n" \
  "s_cmp_lt_u32 s51, 2\n" \
  "v_mov_b32 v21, 1\n" \
  "s_nop 3\n" \
  "v_pk_mul_f32 v[88:89], v[88:89], v[18:19] op_sel_hi:[1,0]\n" \
  "v_pk_mul_f32 v[90:91], v[90:91], v[18:19] op_sel_hi:[1,0]\n" \
  "v_pk_fma_f32 v[88:89], v[92:93], v[18:19], v[88:89] op_sel:[0,1,0]\n" \
  "v_pk_fma_f32 v[90:91], v[94:95], v[18:19], v[90:91] op_sel:[0,1,0]\n" \
  "v_cvt_pk_f16_f32 v88, v88, v89\n" \
  "v_cvt_pk_f16_f32 v89, v90, v91\n" \
  "s_cbranch_scc0 Lkf%=\n" \
  "ds_write_b64 v7, v[88:89] offset:7488\n" \
  "v_alignbit_b32 v90, v89, v88, 16\n" \
  "ds_write_b16 v7, v88 offset:8958\n" \
  "ds_write_b16_d16_hi v7, v89 offset:8964\n" \
  "ds_write_b32 v7, v90 offset:8960\n" \
  "s_cmp_eq_u32 s51, 0\n" \
  "s_cbranch_scc1 Lkd%=\n" \
  "s_mov_b32 exec_lo, 0\n" \
  "s_brev_b32 exec_hi, 1\n" \
  "ds_write_b16_d16_hi v7, v89 offset:7496\n" \
  "ds_write_b16_d16_hi v7, v89 offset:7498\n" \
  "ds_write_b16_d16_hi v7, v89 offset:7500\n" \
  "ds_write_b16_d16_hi v7, v89 offset:8966\n" \
  "ds_write_b16_d16_hi v7, v89 offset:8968\n" \
  "ds_write_b16_d16_hi v7, v89 offset:8970\n" \
  "s_branch Lke%=\n" \
  "Lkf%=:\n" \
  "s_mov_b32 exec_lo, 0x07b607b6\n" \
  "s_mov_b32 exec_hi, 0x07b607b6\n" \
  "ds_write_b64 v7, v[88:89] offset:7488\n" \
  "s_cmp_eq_u32 s51, 2\n" \
  "s_cbranch_scc1 Lk2%=\n" \
  "s_mov_b32 exec_lo, 0\n" \
  "s_mov_b32 exec_hi, 0x200000\n" \
  "ds_write_b16_d16_hi v7, v89 offset:5358\n" \
  "ds_write_b16_d16_hi v7, v89 offset:5360\n" \
  "ds_write_b16_d16_hi v7, v89 offset:6828\n" \
  "ds_write_b16_d16_hi v7, v89 offset:6830\n" \
  "s_branch Lke%=\n" \
  "Lk2%=:\n" \
  "s_mov_b64 exec, -1\n" \
  "ds_add_u32 v4, v16 offset:12\n" \
  "ds_read_b32 v18, v9 offset:10336\n" \
  "v_add_u32 v17, -5, v17\n" \
  "v_lshlrev_b32 v17, 23, v17\n" \
  "s_lshl2_add_u32 s56, s50, 158224\n" \
  "v_mov_b32 v19, s56\n" \
  "s_waitcnt lgkmcnt(0)\n" \
  "v_mul_f32 v17, v17, v18\n" \
  "ds_write_b32 v19, v17\n" \
  "s_branch Lkg%=\n" \
  "Lke%=:\n" \
  "s_mov_b64 exec, -1\n" \
  "Lkd%=:\n" \
  "ds_add_u32 v4, v16 offset:12\n" \
  "Lkg%=:\n" \
  "ds_add_u32 v4, v21 offset:4\n" \
  GSTAMP5 \
  "s_setprio " STR(PRK) "\n" \
  "s_cmp_eq_u32 s55, 2\n" \
  "s_cbranch_scc1 Lr4%=\n" \
  "s_cmp_eq_u32 s55, -1\n" \
  "s_cbranch_scc1 Lr4%=\n" \
  "s_mov_b32 s97, 0x1fffff\n" \
  "Lkq%=:\n" \
  "ds_read_b32 v17, v4 offset:12\n" \
  "s_waitcnt lgkmcnt(0)\n" \
  "v_readfirstlane_b32 s96, v17\n" \
  "s_and_b32 s96, s96, s54\n" \
  "s_cmp_eq_u32 s96, s54\n" \
  "s_cbranch_scc1 Lkq%=x\n" \
  "s_sleep 1\n" \
  "s_sub_u32 s97, s97, 1\n" \
  "s_cbranch_scc0 Lkq%=\n" \
  "Lkq%=x:\n" \
  GSTAMP6 \
  "s_cmp_eq_u32 s55, 3\n" \
  "s_cbranch_scc1 Lr1%=\n" \
  "s_cmp_lg_u32 s55, 0\n" \
  "s_cbranch_scc1 Lr2%=\n" \
  "ds_read2_b32 v[60:61], v3 offset0:0 offset1:1\n" \
  "ds_read2_b32 v[62:63], v3 offset0:2 offset1:3\n" \
  "ds_read2_b32 v[92:93], v3 offset0:128 offset1:129\n" \
  "ds_read2_b32 v[94:95], v3 offset0:130 offset1:131\n" \
  "ds_read2_b32 v[64:65], v3 offset0:16 offset1:17\n" \
  "ds_read2_b32 v[66:67], v3 offset0:18 offset1:19\n" \
  "ds_read2_b32 v[96:97], v3 offset0:144 offset1:145\n" \
  "ds_read2_b32 v[98:99], v3 offset0:146 offset1:147\n" \
  "ds_read2_b32 v[68:69], v3 offset0:32 offset1:33\n" \
  "ds_read2_b32 v[70:71], v3 offset0:34 offset1:35\n" \
  "ds_read2_b32 v[100:101], v3 offset0:160 offset1:161\n" \
  "ds_read2_b32 v[102:103], v3 offset0:162 offset1:163\n" \
  "ds_read2_b32 v[72:73], v3 offset0:48 offset1:49\n" \
  "ds_read2_b32 v[74:75], v3 offset0:50 offset1:51\n" \
  "ds_read2_b32 v[104:105], v3 offset0:176 offset1:177\n" \
  "ds_read2_b32 v[106:107], v3 offset0:178 offset1:179\n" \
  "ds_read2_b32 v[76:77], v3 offset0:64 offset1:65\n" \
  "ds_read2_b32 v[78:79], v3 offset0:66 offset1:67\n" \
  "ds_read2_b32 v[108:109], v3 offset0:192 offset1:193\n" \
  "ds_read2_b32 v[110:111], v3 offset0:194 offset1:195\n" \
  "ds_read2_b32 v[80:81], v3 offset0:80 offset1:81\n" \
  "ds_read2_b32 v[82:83], v3 offset0:82 offset1:83\n" \
  "ds_read2_b32 v[112:113], v3 offset0:208 offset1:209\n" \
  "ds_read2_b32 v[114:115], v3 offset0:210 offset1:211\n" \
  "ds_read2_b32 v[84:85], v3 offset0:96 offset1:97\n" \
  "ds_read2_b32 v[86:87], v3 offset0:98 offset1:99\n" \
  "ds_read2_b32 v[116:117], v3 offset0:224 offset1:225\n" \
  "ds_read2_b32 v[118:119], v3 offset0:226 offset1:227\n" \
  "ds_read2_b32 v[88:89], v3 offset0:112 offset1:113\n" \
  "ds_read2_b32 v[90:91], v3 offset0:114 offset1:115\n" \
  "ds_read2_b32 v[120:121], v3 offset0:240 offset1:241\n" \
  "ds_read2_b32 v[122:123], v3 offset0:242 offset1:243\n" \
  "v_add_u32 v9, 1024, v3\n" \
  "ds_read2_b32 v[124:125], v9 offset0:0 offset1:1\n" \
  "ds_read2_b32 v[126:127], v9 offset0:2 offset1:3\n" \
  "s_mov_b32 s97, 0x1fffff\n" \
  "Lnp%=:\n" \
  "ds_read_b32 v8, v4 offset:4\n" \
  "s_waitcnt lgkmcnt(0)\n" \
  "v_readfirstlane_b32 s96, v8\n" \
  "s_and_b32 s96, s96, 0xff\n" \
  "s_cmp_eq_u32 s96, 16\n" \
  "s_cbranch_scc1 Lnp%=x\n" \
  "s_sleep 1\n" \
  "s_sub_u32 s97, s97, 1\n" \
  "s_cbranch_scc0 Lnp%=\n" \
  "Lnp%=x:\n" \
  "s_setprio " STR(PRN) "\n" \
  "ds_read_b128 v[28:31], v10 offset:3584\n" \
  "ds_read_b128 v[32:35], v10 offset:3648\n" \
  "ds_read_b128 v[36:39], v10 offset:3712\n" \
  "ds_read_b128 v[40:43], v10 offset:3776\n" \
  "ds_read_b128 v[44:47], v10 offset:3840\n" \
  "ds_read_b128 v[48:51], v10 offset:3904\n" \
  "ds_read_b128 v[52:55], v10 offset:3968\n" \
  "ds_read_b128 v[56:59], v10 offset:4032\n" \
  "s_waitcnt lgkmcnt(7)\n" \
  "v_mfma_f32_16x16x32_f16 v[12:15], v[60:63], v[28:31], 0\n" \
  "v_mfma_f32_16x16x32_f16 v[16:19], v[92:95], v[28:31], 0\n" \
  "v_mfma_f32_16x16x32_f16 v[20:23], v[124:127], v[28:31], 0\n" \
  "ds_read_b128 v[28:31], v10 offset:3072\n" \
  "s_waitcnt lgkmcnt(7)\n" \
  "v_mfma_f32_16x16x32_f16 v[12:15], v[64:67], v[32:35], v[12:15]\n" \
  "v_mfma_f32_16x16x32_f16 v[16:19], v[96:99], v[32:35], v[16:19]\n" \
  "ds_read_b128 v[32:35], v10 offset:3136\n" \
  "s_waitcnt lgkmcnt(7)\n" \
  "v_mfma_f32_16x16x32_f16 v[12:15], v[68:71], v[36:39], v[12:15]\n" \
  "v_mfma_f32_16x16x32_f16 v[16:19], v[100:103], v[36:39], v[16:19]\n" \
  "ds_read_b128 v[36:39], v10 offset:3200\n" \
  "s_waitcnt lgkmcnt(7)\n" \
  "v_mfma_f32_16x16x32_f16 v[12:15], v[72:75], v[40:43], v[12:15]\n" \
  "v_mfma_f32_16x16x32_f16 v[16:19], v[104:107], v[40:43], v[16:19]\n" \
  "ds_read_b128 v[40:43], v10 offset:3264\n" \
  "s_waitcnt lgkmcnt(7)\n" \
  "v_mfma_f32_16x16x32_f16 v[12:15], v[76:79], v[44:47], v[12:15]\n" \
  "v_mfma_f32_16x16x32_f16 v[16:19], v[108:111], v[44:47], v[16:19]\n" \
  "ds_read_b128 v[44:47], v10 offset:3328\n" \
  "s_waitcnt lgkmcnt(7)\n" \
  "v_mfma_f32_16x16x32_f16 v[12:15], v[80:83], v[48:51], v[12:15]\n" \
  "v_mfma_f32_16x16x32_f16 v[16:19], v[112:115], v[48:51], v[16:19]\n" \
  "ds_read_b128 v[48:51], v10 offset:3392\n" \
  "s_waitcnt lgkmcnt(7)\n" \
  "v_mfma_f32_16x16x32_f16 v[12:15], v[84:87], v[52:55], v[12:15]\n" \
  "v_mfma_f32_16x16x32_f16 v[16:19], v[116:119], v[52:55], v[16:19]\n" \
  "ds_read_b128 v[52:55], v10 offset:3456\n" \
  "s_waitcnt lgkmcnt(7)\n" \
  "v_mfma_f32_16x16x32_f16 v[12:15], v[88:91], v[56:59], v[12:15]\n" \
  "v_mfma_f32_16x16x32_f16 v[16:19], v[120:123], v[56:59], v[16:19]\n" \
  "ds_read_b128 v[56:59], v10 offset:3520\n" \
  "s_waitcnt lgkmcnt(7)\n" \
  "v_mfma_f32_16x16x32_f16 v[16:19], v[60:63], v[28:31], v[16:19]\n" \
  "v_mfma_f32_16x16x32_f16 v[20:23], v[92:95], v[28:31], v[20:23]\n" \
  "v_mfma_f32_16x16x32_f16 v[24:27], v[124:127], v[28:31], 0\n" \
  "ds_read_b128 v[28:31], v10 offset:2560\n" \
  "s_waitcnt lgkmcnt(7)\n" \
  "v_mfma_f32_16x16x32_f16 v[16:19], v[64:67], v[32:35], v[16:19]\n" \
  "v_mfma_f32_16x16x32_f16 v[20:23], v[96:99], v[32:35], v[20:23]\n" \
  "ds_read_b128 v[32:35], v10 offset:2624\n" \
  "s_waitcnt lgkmcnt(7)\n" \
  "v_mfma_f32_16x16x32_f16 v[16:19], v[68:71], v[36:39], v[16:19]\n" \
  "v_mov_b32 v9, 0x1\n" \
  "ds_write_b128 v11, v[12:15] offset:0\n" \
  "ds_add_u32 v4, v9 offset:0\n" \
  "v_mfma_f32_16x16x32_f16 v[20:23], v[100:103], v[36:39], v[20:23]\n" \
  "ds_read_b128 v[36:39], v10 offset:2688\n" \
  "s_waitcnt lgkmcnt(9)\n" \
  "v_mfma_f32_16x16x32_f16 v[16:19], v[72:75], v[40:43], v[16:19]\n" \
  "v_mfma_f32_16x16x32_f16 v[20:23], v[104:107], v[40:43], v[20:23]\n" \
  "ds_read_b128 v[40:43], v10 offset:2752\n" \
  "s_waitcnt lgkmcnt(9)\n" \
  "v_mfma_f32_16x16x32_f16 v[16:19], v[76:79], v[44:47], v[16:19]\n" \
  "v_mfma_f32_16x16x32_f16 v[20:23], v[108:111], v[44:47], v[20:23]\n" \
  "ds_read_b128 v[44:47], v10 offset:2816\n" \
  "s_waitcnt lgkmcnt(9)\n" \
  "v_mfma_f32_16x16x32_f16 v[16:19], v[80:83], v[48:51], v[16:19]\n" \
  "v_mfma_f32_16x16x32_f16 v[20:23], v[112:115], v[48:51], v[20:23]\n" \
  "ds_read_b128 v[48:51], v10 offset:2880\n" \
  "s_waitcnt lgkmcnt(9)\n" \
  "v_mfma_f32_16x16x32_f16 v[16:19], v[84:87], v[52:55], v[16:19]\n" \
  "v_mfma_f32_16x16x32_f16 v[20:23], v[116:119], v[52:55], v[20:23]\n" \
  "ds_read_b128 v[52:55], v10 offset:2944\n" \
  "s_waitcnt lgkmcnt(9)\n" \
  "v_mfma_f32_16x16x32_f16 v[16:19], v[88:91], v[56:59], v[16:19]\n" \
  "v_mfma_f32_16x16x32_f16 v[20:23], v[120:123], v[56:59], v[20:23]\n" \
  "ds_read_b128 v[56:59], v10 offset:3008\n" \
  "s_waitcnt lgkmcnt(9)\n" \
  "v_mfma_f32_16x16x32_f16 v[20:23], v[60:63], v[28:31], v[20:23]\n" \
  "v_mfma_f32_16x16x32_f16 v[24:27], v[92:95], v[28:31], v[24:27]\n" \
  "ds_read_b128 v[28:31], v10 offset:2048\n" \
  "s_waitcnt lgkmcnt(9)\n" \
  "v_mfma_f32_16x16x32_f16 v[20:23], v[64:67], v[32:35], v[20:23]\n" \
  "v_mfma_f32_16x16x32_f16 v[24:27], v[96:99], v[32:35], v[24:27]\n" \
  "ds_read_b128 v[32:35], v10 offset:2112\n" \
  "s_waitcnt lgkmcnt(7)\n" \
  "v_mfma_f32_16x16x32_f16 v[20:23], v[68:71], v[36:39], v[20:23]\n" \
  "v_mov_b32 v9, 0x10\n" \
  "ds_write_b128 v11, v[16:19] offset:1280\n" \
  "ds_add_u32 v4, v9 offset:0\n" \
  "v_mfma_f32_16x16x32_f16 v[24:27], v[100:103], v[36:39], v[24:27]\n" \
  "ds_read_b128 v[36:39], v10 offset:2176\n" \
  "s_waitcnt lgkmcnt(9)\n" \
  "v_mfma_f32_16x16x32_f16 v[20:23], v[72:75], v[40:43], v[20:23]\n" \
  "v_mfma_f32_16x16x32_f16 v[24:27], v[104:107], v[40:43], v[24:27]\n" \
  "ds_read_b128 v[40:43], v10 offset:2240\n" \
  "s_waitcnt lgkmcnt(9)\n" \
  "v_mfma_f32_16x16x32_f16 v[20:23], v[76:79], v[44:47], v[20:23]\n" \
  "v_mfma_f32_16x16x32_f16 v[24:27], v[108:111], v[44:47], v[24:27]\n" \
  "ds_read_b128 v[44:47], v10 offset:2304\n" \
  "s_waitcnt lgkmcnt(9)\n" \
  "v_mfma_f32_16x16x32_f16 v[20:23], v[80:83], v[48:51], v[20:23]\n" \
  "v_mfma_f32_16x16x32_f16 v[24:27], v[112:115], v[48:51], v[24:27]\n" \
  "ds_read_b128 v[48:51], v10 offset:2368\n" \
  "s_waitcnt lgkmcnt(9)\n" \
  "v_mfma_f32_16x16x32_f16 v[20:23], v[84:87], v[52:55], v[20:23]\n" \
  "v_mfma_f32_16x16x32_f16 v[24:27], v[116:119], v[52:55], v[24:27]\n" \
  "ds_read_b128 v[52:55], v10 offset:2432\n" \
  "s_waitcnt lgkmcnt(9)\n" \
  "v_mfma_f32_16x16x32_f16 v[20:23], v[88:91], v[56:59], v[20:23]\n" \
  "v_mfma_f32_16x16x32_f16 v[24:27], v[120:123], v[56:59], v[24:27]\n" \
  "ds_read_b128 v[56:59], v10 offset:2496\n" \
  "s_waitcnt lgkmcnt(9)\n" \
  "v_mfma_f32_16x16x32_f16 v[24:27], v[60:63], v[28:31], v[24:27]\n" \
  "s_waitcnt lgkmcnt(8)\n" \
  "v_mfma_f32_16x16x32_f16 v[24:27], v[64:67], v[32:35], v[24:27]\n" \
  "s_waitcnt lgkmcnt(5)\n" \
  "v_mfma_f32_16x16x32_f16 v[24:27], v[68:71], v[36:39], v[24:27]\n" \
  "v_mov_b32 v9, 0x100\n" \
  "ds_write_b128 v11, v[20:23] offset:2560\n" \
  "ds_add_u32 v4, v9 offset:0\n" \
  "s_waitcnt lgkmcnt(6)\n" \
  "v_mfma_f32_16x16x32_f16 v[24:27], v[72:75], v[40:43], v[24:27]\n" \
  "s_waitcnt lgkmcnt(5)\n" \
  "v_mfma_f32_16x16x32_f16 v[24:27], v[76:79], v[44:47], v[24:27]\n" \
  "s_waitcnt lgkmcnt(4)\n" \
  "v_mfma_f32_16x16x32_f16 v[24:27], v[80:83], v[48:51], v[24:27]\n" \
  "s_waitcnt lgkmcnt(3)\n" \
  "v_mfma_f32_16x16x32_f16 v[24:27], v[84:87], v[52:55], v[24:27]\n" \
  "s_waitcnt lgkmcnt(2)\n" \
  "v_mfma_f32_16x16x32_f16 v[24:27], v[88:91], v[56:59], v[24:27]\n" \
  "v_mov_b32 v9, 0x1000\n" \
  "s_nop 6\n" \
  "ds_write_b128 v11, v[24:27] offset:3840\n" \
  "ds_add_u32 v4, v9 offset:0\n" \
  "s_branch Lr3%=\n" \
  "Lr1%=:\n" \
  "v_add_u32 v9, 1024, v3\n" \
  "ds_read2_b32 v[12:13], v9 offset0:48 offset1:49\n" \
  "ds_read2_b32 v[14:15], v9 offset0:50 offset1:51\n" \
  "ds_read2_b32 v[16:17], v9 offset0:44 offset1:45\n" \
  "ds_read2_b32 v[18:19], v9 offset0:46 offset1:47\n" \
  "ds_read2_b32 v[20:21], v9 offset0:61 offset1:62\n" \
  "ds_read2_b32 v[22:23], v9 offset0:63 offset1:64\n" \
  "ds_read2_b32 v[24:25], v8 offset0:0 offset1:1\n" \
  "ds_read2_b32 v[26:27], v8 offset0:2 offset1:3\n" \
  "ds_read2_b32 v[36:37], v8 offset0:48 offset1:49\n" \
  "ds_read2_b32 v[38:39], v8 offset0:50 offset1:51\n" \
  "ds_read2_b32 v[28:29], v8 offset0:16 offset1:17\n" \
  "ds_read2_b32 v[30:31], v8 offset0:18 offset1:19\n" \
  "ds_read2_b32 v[40:41], v8 offset0:64 offset1:65\n" \
  "ds_read2_b32 v[42:43], v8 offset0:66 offset1:67\n" \
  "ds_read2_b32 v[32:33], v8 offset0:32 offset1:33\n" \
  "ds_read2_b32 v[34:35], v8 offset0:34 offset1:35\n" \
  "ds_read2_b32 v[44:45], v8 offset0:80 offset1:81\n" \
  "ds_read2_b32 v[46:47], v8 offset0:82 offset1:83\n" \
  "s_mov_b32 s97, 0x1fffff\n" \
  "Lfp%=:\n" \
  "ds_read_b32 v8, v4 offset:4\n" \
  "s_waitcnt lgkmcnt(0)\n" \
  "v_readfirstlane_b32 s96, v8\n" \
  "s_and_b32 s96, s96, 0xff\n" \
  "s_cmp_eq_u32 s96, 16\n" \
  "s_cbranch_scc1 Lfp%=x\n" \
  "s_sleep 1\n" \
  "s_sub_u32 s97, s97, 1\n" \
  "s_cbranch_scc0 Lfp%=\n" \
  "Lfp%=x:\n" \
  "s_setprio " STR(PRF) "\n" \
  "ds_read_b128 v[48:51], v10 offset:3584\n" \
  "ds_read_b128 v[52:55], v10 offset:3600\n" \
  "ds_read_b128 v[56:59], v10 offset:3072\n" \
  "ds_read_b128 v[60:63], v10 offset:3088\n" \
  "s_waitcnt lgkmcnt(3)\n" \
  "v_mfma_f32_16x16x32_f16 v[64:67], v[48:51], v[12:15], 0\n" \
  "v_mfma_f32_16x16x32_f16 v[68:71], v[48:51], v[16:19], 0\n" \
  "v_mfma_f32_16x16x32_f16 v[72:75], v[48:51], v[20:23], 0\n" \
  "s_waitcnt lgkmcnt(2)\n" \
  "v_mfma_f32_16x16x32_f16 v[76:79], v[52:55], v[20:23], 0\n" \
  "ds_read_b128 v[48:51], v10 offset:2560\n" \
  "ds_read_b128 v[52:55], v10 offset:2576\n" \
  "s_nop 2\n" \
  "v_cvt_pk_f16_f32 v80, v67, v71\n" \
  "v_cvt_pk_f16_f32 v81, v66, v70\n" \
  "v_cvt_pk_f16_f32 v82, v65, v69\n" \
  "v_cvt_pk_f16_f32 v83, v64, v68\n" \
  "v_cvt_pk_f16_f32 v84, v75, v79\n" \
  "v_cvt_pk_f16_f32 v85, v74, v78\n" \
  "v_cvt_pk_f16_f32 v86, v73, v77\n" \
  "v_cvt_pk_f16_f32 v87, v72, v76\n" \
  "s_waitcnt lgkmcnt(3)\n" \
  "v_mfma_f32_16x16x32_f16 v[64:67], v[56:59], v[12:15], 0\n" \
  "v_mfma_f32_16x16x32_f16 v[68:71], v[56:59], v[16:19], 0\n" \
  "v_mfma_f32_16x16x32_f16 v[72:75], v[56:59], v[20:23], 0\n" \
  "s_waitcnt lgkmcnt(2)\n" \
  "v_mfma_f32_16x16x32_f16 v[76:79], v[60:63], v[20:23], 0\n" \
  "ds_read_b128 v[56:59], v10 offset:2048\n" \
  "ds_read_b128 v[60:63], v10 offset:2064\n" \
  "v_mfma_f32_16x16x32_f16 v[104:107], v[80:83], v[24:27], 0\n" \
  "v_mfma_f32_16x16x32_f16 v[108:111], v[80:83], v[28:31], 0\n" \
  "s_nop 0\n" \
  "v_cvt_pk_f16_f32 v88, v67, v71\n" \
  "v_cvt_pk_f16_f32 v89, v66, v70\n" \
  "v_mfma_f32_16x16x32_f16 v[104:107], v[84:87], v[36:39], v[104:107]\n" \
  "v_cvt_pk_f16_f32 v90, v65, v69\n" \
  "v_cvt_pk_f16_f32 v91, v64, v68\n" \
  "v_mfma_f32_16x16x32_f16 v[96:99], v[80:83], v[32:35], 0\n" \
  "v_cvt_pk_f16_f32 v92, v75, v79\n" \
  "v_cvt_pk_f16_f32 v93, v74, v78\n" \
  "v_mfma_f32_16x16x32_f16 v[108:111], v[84:87], v[40:43], v[108:111]\n" \
  "v_cvt_pk_f16_f32 v94, v73, v77\n" \
  "v_cvt_pk_f16_f32 v95, v72, v76\n" \
  "v_mfma_f32_16x16x32_f16 v[96:99], v[84:87], v[44:47], v[96:99]\n" \
  "s_waitcnt lgkmcnt(3)\n" \
  "v_mfma_f32_16x16x32_f16 v[64:67], v[48:51], v[12:15], 0\n" \
  "v_mfma_f32_16x16x32_f16 v[68:71], v[48:51], v[16:19], 0\n" \
  "v_mfma_f32_16x16x32_f16 v[72:75], v[48:51], v[20:23], 0\n" \
  "s_waitcnt lgkmcnt(2)\n" \
  "v_mfma_f32_16x16x32_f16 v[76:79], v[52:55], v[20:23], 0\n" \
  "s_mov_b32 s97, 0x1fffff\n" \
  "Lfq%=:\n" \
  "ds_read_b32 v8, v4 offset:4\n" \
  "s_waitcnt lgkmcnt(0)\n" \
  "v_readfirstlane_b32 s96, v8\n" \
  "s_and_b32 s96, s96, 0x400\n" \
  "s_cmp_eq_u32 s96, 0x400\n" \
  "s_cbranch_scc1 Lfq%=x\n" \
  "s_sub_u32 s97, s97, 1\n" \
  "s_cbranch_scc0 Lfq%=\n" \
  "Lfq%=x:\n" \
  "ds_read_b128 v[48:51], v10 offset:1536\n" \
  "ds_read_b128 v[52:55], v10 offset:1552\n" \
  "v_mov_b32 v9, 0x100\n" \
  "ds_write_b128 v11, v[104:107] offset:10240\n" \
  "ds_add_u32 v4, v9 offset:0\n" \
  "v_mfma_f32_16x16x32_f16 v[108:111], v[88:91], v[24:27], v[108:111]\n" \
  "v_mfma_f32_16x16x32_f16 v[96:99], v[88:91], v[28:31], v[96:99]\n" \
  "v_cvt_pk_f16_f32 v80, v67, v71\n" \
  "v_cvt_pk_f16_f32 v81, v66, v70\n" \
  "v_mfma_f32_16x16x32_f16 v[108:111], v[92:95], v[36:39], v[108:111]\n" \
  "v_cvt_pk_f16_f32 v82, v65, v69\n" \
  "v_cvt_pk_f16_f32 v83, v64, v68\n" \
  "v_mfma_f32_16x16x32_f16 v[100:103], v[88:91], v[32:35], 0\n" \
  "v_cvt_pk_f16_f32 v84, v75, v79\n" \
  "v_cvt_pk_f16_f32 v85, v74, v78\n" \
  "v_mfma_f32_16x16x32_f16 v[96:99], v[92:95], v[40:43], v[96:99]\n" \
  "v_cvt_pk_f16_f32 v86, v73, v77\n" \
  "v_cvt_pk_f16_f32 v87, v72, v76\n" \
  "v_mfma_f32_16x16x32_f16 v[100:103], v[92:95], v[44:47], v[100:103]\n" \
  "v_mfma_f32_16x16x32_f16 v[64:67], v[56:59], v[12:15], 0\n" \
  "v_mfma_f32_16x16x32_f16 v[68:71], v[56:59], v[16:19], 0\n" \
  "v_mfma_f32_16x16x32_f16 v[72:75], v[56:59], v[20:23], 0\n" \
  "v_mfma_f32_16x16x32_f16 v[76:79], v[60:63], v[20:23], 0\n" \
  "ds_read_b128 v[56:59], v10 offset:1024\n" \
  "ds_read_b128 v[60:63], v10 offset:1040\n" \
  "v_mov_b32 v9, 0x1000\n" \
  "ds_write_b128 v11, v[108:111] offset:11520\n" \
  "ds_add_u32 v4, v9 offset:0\n" \
  "v_mfma_f32_16x16x32_f16 v[96:99], v[80:83], v[24:27], v[96:99]\n" \
  "v_mfma_f32_16x16x32_f16 v[100:103], v[80:83], v[28:31], v[100:103]\n" \
  "v_cvt_pk_f16_f32 v88, v67, v71\n" \
  "v_cvt_pk_f16_f32 v89, v66, v70\n" \
  "v_mfma_f32_16x16x32_f16 v[96:99], v[84:87], v[36:39], v[96:99]\n" \
  "v_cvt_pk_f16_f32 v90, v65, v69\n" \
  "v_cvt_pk_f16_f32 v91, v64, v68\n" \
  "v_mfma_f32_16x16x32_f16 v[104:107], v[80:83], v[32:35], 0\n" \
  "v_cvt_pk_f16_f32 v92, v75, v79\n" \
  "v_cvt_pk_f16_f32 v93, v74, v78\n" \
  "v_mfma_f32_16x16x32_f16 v[100:103], v[84:87], v[40:43], v[100:103]\n" \
  "v_cvt_pk_f16_f32 v94, v73, v77\n" \
  "v_cvt_pk_f16_f32 v95, v72, v76\n" \
  "v_mfma_f32_16x16x32_f16 v[104:107], v[84:87], v[44:47], v[104:107]\n" \
  "s_waitcnt lgkmcnt(7)\n" \
  "v_mfma_f32_16x16x32_f16 v[64:67], v[48:51], v[12:15], 0\n" \
  "v_mfma_f32_16x16x32_f16 v[68:71], v[48:51], v[16:19], 0\n" \
  "v_mfma_f32_16x16x32_f16 v[72:75], v[48:51], v[20:23], 0\n" \
  "s_waitcnt lgkmcnt(6)\n" \
  "v_mfma_f32_16x16x32_f16 v[76:79], v[52:55], v[20:23], 0\n" \
  "v_mov_b32 v9, 0x10000\n" \
  "ds_write_b128 v11, v[96:99] offset:12800\n" \
  "ds_add_u32 v4, v9 offset:0\n" \
  "v_mfma_f32_16x16x32_f16 v[100:103], v[88:91], v[24:27], v[100:103]\n" \
  "v_mfma_f32_16x16x32_f16 v[104:107], v[88:91], v[28:31], v[104:107]\n" \
  "v_cvt_pk_f16_f32 v80, v67, v71\n" \
  "v_cvt_pk_f16_f32 v81, v66, v70\n" \
  "v_mfma_f32_16x16x32_f16 v[100:103], v[92:95], v[36:39], v[100:103]\n" \
  "v_cvt_pk_f16_f32 v82, v65, v69\n" \
  "v_cvt_pk_f16_f32 v83, v64, v68\n" \
  "v_mfma_f32_16x16x32_f16 v[108:111], v[88:91], v[32:35], 0\n" \
  "v_cvt_pk_f16_f32 v84, v75, v79\n" \
  "v_cvt_pk_f16_f32 v85, v74, v78\n" \
  "v_mfma_f32_16x16x32_f16 v[104:107], v[92:95], v[40:43], v[104:107]\n" \
  "v_cvt_pk_f16_f32 v86, v73, v77\n" \
  "v_cvt_pk_f16_f32 v87, v72, v76\n" \
  "v_mfma_f32_16x16x32_f16 v[108:111], v[92:95], v[44:47], v[108:111]\n" \
  "s_waitcnt lgkmcnt(5)\n" \
  "v_mfma_f32_16x16x32_f16 v[64:67], v[56:59], v[12:15], 0\n" \
  "v_mfma_f32_16x16x32_f16 v[68:71], v[56:59], v[16:19], 0\n" \
  "v_mfma_f32_16x16x32_f16 v[72:75], v[56:59], v[20:23], 0\n" \
  "s_waitcnt lgkmcnt(4)\n" \
  "v_mfma_f32_16x16x32_f16 v[76:79], v[60:63], v[20:23], 0\n" \
  "v_mov_b32 v9, 0x100000\n" \
  "ds_write_b128 v11, v[100:103] offset:14080\n" \
  "ds_add_u32 v4, v9 offset:0\n" \
  "v_mfma_f32_16x16x32_f16 v[104:107], v[80:83], v[24:27], v[104:107]\n" \
  "s_nop 0\n" \
  "v_cvt_pk_f16_f32 v88, v67, v71\n" \
  "v_cvt_pk_f16_f32 v89, v66, v70\n" \
  "v_mfma_f32_16x16x32_f16 v[108:111], v[80:83], v[28:31], v[108:111]\n" \
  "v_cvt_pk_f16_f32 v90, v65, v69\n" \
  "v_cvt_pk_f16_f32 v91, v64, v68\n" \
  "v_mfma_f32_16x16x32_f16 v[104:107], v[84:87], v[36:39], v[104:107]\n" \
  "v_cvt_pk_f16_f32 v92, v75, v79\n" \
  "v_cvt_pk_f16_f32 v93, v74, v78\n" \
  "v_mfma_f32_16x16x32_f16 v[108:111], v[84:87], v[40:43], v[108:111]\n" \
  "v_cvt_pk_f16_f32 v94, v73, v77\n" \
  "v_cvt_pk_f16_f32 v95, v72, v76\n" \
  "v_mov_b32 v9, 0x1000000\n" \
  "s_nop 1\n" \
  "ds_write_b128 v11, v[104:107] offset:15360\n" \
  "ds_add_u32 v4, v9 offset:0\n" \
  "v_mfma_f32_16x16x32_f16 v[108:111], v[88:91], v[24:27], v[108:111]\n" \
  "v_mfma_f32_16x16x32_f16 v[108:111], v[92:95], v[36:39], v[108:111]\n" \
  "v_mov_b32 v9, 0x10000000\n" \
  "s_nop 6\n" \
  "ds_write_b128 v11, v[108:111] offset:16640\n" \
  "ds_add_u32 v4, v9 offset:0\n" \
  "s_branch Lr3%=\n" \
  "Lr2%=:\n" \
  "ds_read2_b32 v[24:25], v8 offset0:96 offset1:97\n" \
  "ds_read2_b32 v[26:27], v8 offset0:98 offset1:99\n" \
  "ds_read2_b32 v[28:29], v8 offset0:112 offset1:113\n" \
  "ds_read2_b32 v[30:31], v8 offset0:114 offset1:115\n" \
  "ds_read2_b32 v[32:33], v8 offset0:128 offset1:129\n" \
  "ds_read2_b32 v[34:35], v8 offset0:130 offset1:131\n" \
  "ds_read2_b32 v[36:37], v8 offset0:144 offset1:145\n" \
  "ds_read2_b32 v[38:39], v8 offset0:146 offset1:147\n" \
  "v_add_u32 v9, 1024, v3\n" \
  "ds_read2_b32 v[124:125], v9 offset0:0 offset1:1\n" \
  "ds_read2_b32 v[126:127], v9 offset0:2 offset1:3\n" \
  "ds_read2_b32 v[20:21], v9 offset0:32 offset1:33\n" \
  "ds_read2_b32 v[22:23], v9 offset0:34 offset1:35\n" \
  "ds_read2_b32 v[12:13], v9 offset0:76 offset1:77\n" \
  "ds_read2_b32 v[14:15], v9 offset0:78 offset1:79\n" \
  "ds_read2_b32 v[16:17], v9 offset0:91 offset1:92\n" \
  "ds_read2_b32 v[18:19], v9 offset0:93 offset1:94\n" \
  "ds_read2_b32 v[40:41], v3 offset0:112 offset1:113\n" \
  "ds_read2_b32 v[42:43], v3 offset0:114 offset1:115\n" \
  "ds_read2_b32 v[44:45], v3 offset0:128 offset1:129\n" \
  "ds_read2_b32 v[46:47], v3 offset0:130 offset1:131\n" \
  "ds_read2_b32 v[48:49], v3 offset0:144 offset1:145\n" \
  "ds_read2_b32 v[50:51], v3 offset0:146 offset1:147\n" \
  "ds_read2_b32 v[52:53], v3 offset0:160 offset1:161\n" \
  "ds_read2_b32 v[54:55], v3 offset0:162 offset1:163\n" \
  "ds_read2_b32 v[56:57], v3 offset0:176 offset1:177\n" \
  "ds_read2_b32 v[58:59], v3 offset0:178 offset1:179\n" \
  "ds_read2_b32 v[60:61], v3 offset0:192 offset1:193\n" \
  "ds_read2_b32 v[62:63], v3 offset0:194 offset1:195\n" \
  "ds_read2_b32 v[64:65], v3 offset0:208 offset1:209\n" \
  "ds_read2_b32 v[66:67], v3 offset0:210 offset1:211\n" \
  "ds_read2_b32 v[68:69], v3 offset0:224 offset1:225\n" \
  "ds_read2_b32 v[70:71], v3 offset0:226 offset1:227\n" \
  "ds_read2_b32 v[72:73], v3 offset0:240 offset1:241\n" \
  "ds_read2_b32 v[74:75], v3 offset0:242 offset1:243\n" \
  "s_mov_b32 s97, 0x1fffff\n" \
  "Lgp%=:\n" \
  "ds_read_b32 v8, v4 offset:4\n" \
  "s_waitcnt lgkmcnt(0)\n" \
  "v_readfirstlane_b32 s96, v8\n" \
  "s_and_b32 s96, s96, 0xff\n" \
  "s_cmp_eq_u32 s96, 16\n" \
  "s_cbranch_scc1 Lgp%=x\n" \
  "s_sleep 1\n" \
  "s_sub_u32 s97, s97, 1\n" \
  "s_cbranch_scc0 Lgp%=\n" \
  "Lgp%=x:\n" \
  "s_setprio " STR(PRG) "\n" \
  "ds_read_b128 v[76:79], v10 offset:3584\n" \
  "ds_read_b128 v[80:83], v10 offset:3600\n" \
  "ds_read_b128 v[84:87], v10 offset:3072\n" \
  "ds_read_b128 v[88:91], v10 offset:3088\n" \
  "s_waitcnt lgkmcnt(3)\n" \
  "v_mfma_f32_16x16x32_f16 v[92:95], v[76:79], v[12:15], 0\n" \
  "s_waitcnt lgkmcnt(2)\n" \
  "v_mfma_f32_16x16x32_f16 v[96:99], v[80:83], v[16:19], 0\n" \
  "v_mfma_f32_16x16x32_f16 v[108:111], v[20:23], v[76:79], 0\n" \
  "ds_read_b128 v[76:79], v10 offset:2560\n" \
  "ds_read_b128 v[80:83], v10 offset:2576\n" \
  "s_nop 4\n" \
  "v_cvt_pk_f16_f32 v100, v95, v99\n" \
  "v_cvt_pk_f16_f32 v101, v94, v98\n" \
  "v_cvt_pk_f16_f32 v102, v93, v97\n" \
  "v_cvt_pk_f16_f32 v103, v92, v96\n" \
  "s_waitcnt lgkmcnt(3)\n" \
  "v_mfma_f32_16x16x32_f16 v[92:95], v[84:87], v[12:15], 0\n" \
  "s_waitcnt lgkmcnt(2)\n" \
  "v_mfma_f32_16x16x32_f16 v[96:99], v[88:91], v[16:19], 0\n" \
  "v_mfma_f32_16x16x32_f16 v[112:115], v[20:23], v[84:87], 0\n" \
  "ds_read_b128 v[84:87], v10 offset:2048\n" \
  "ds_read_b128 v[88:91], v10 offset:2064\n" \
  "v_mfma_f32_16x16x32_f16 v[108:111], v[100:103], v[24:27], v[108:111]\n" \
  "v_mfma_f32_16x16x32_f16 v[112:115], v[100:103], v[28:31], v[112:115]\n" \
  "v_mfma_f32_16x16x32_f16 v[116:119], v[100:103], v[32:35], 0\n" \
  "v_mfma_f32_16x16x32_f16 v[120:123], v[100:103], v[36:39], 0\n" \
  "ds_read2_b32 v[36:37], v3 offset0:96 offset1:97\n" \
  "ds_read2_b32 v[38:39], v3 offset0:98 offset1:99\n" \
  "v_cvt_pk_f16_f32 v104, v95, v99\n" \
  "v_cvt_pk_f16_f32 v105, v94, v98\n" \
  "v_cvt_pk_f16_f32 v106, v93, v97\n" \
  "v_cvt_pk_f16_f32 v107, v92, v96\n" \
  "s_waitcnt lgkmcnt(5)\n" \
  "v_mfma_f32_16x16x32_f16 v[92:95], v[76:79], v[12:15], 0\n" \
  "s_waitcnt lgkmcnt(4)\n" \
  "v_mfma_f32_16x16x32_f16 v[96:99], v[80:83], v[16:19], 0\n" \
  "v_mfma_f32_16x16x32_f16 v[116:119], v[20:23], v[76:79], v[116:119]\n" \
  "v_mfma_f32_16x16x32_f16 v[108:111], v[124:127], v[76:79], v[108:111]\n" \
  "ds_read_b128 v[76:79], v10 offset:2048\n" \
  "ds_read_b128 v[80:83], v10 offset:2112\n" \
  "v_mfma_f32_16x16x32_f16 v[112:115], v[104:107], v[24:27], v[112:115]\n" \
  "v_mfma_f32_16x16x32_f16 v[116:119], v[104:107], v[28:31], v[116:119]\n" \
  "v_mfma_f32_16x16x32_f16 v[120:123], v[104:107], v[32:35], v[120:123]\n" \
  "ds_read2_b32 v[32:33], v3 offset0:80 offset1:81\n" \
  "ds_read2_b32 v[34:35], v3 offset0:82 offset1:83\n" \
  "v_cvt_pk_f16_f32 v100, v95, v99\n" \
  "v_cvt_pk_f16_f32 v101, v94, v98\n" \
  "v_cvt_pk_f16_f32 v102, v93, v97\n" \
  "v_cvt_pk_f16_f32 v103, v92, v96\n" \
  "s_waitcnt lgkmcnt(7)\n" \
  "v_mfma_f32_16x16x32_f16 v[92:95], v[84:87], v[12:15], 0\n" \
  "s_waitcnt lgkmcnt(6)\n" \
  "v_mfma_f32_16x16x32_f16 v[96:99], v[88:91], v[16:19], 0\n" \
  "v_mfma_f32_16x16x32_f16 v[120:123], v[20:23], v[84:87], v[120:123]\n" \
  "ds_read_b128 v[84:87], v10 offset:2176\n" \
  "ds_read_b128 v[88:91], v10 offset:2240\n" \
  "ds_read2_b32 v[12:13], v3 offset0:0 offset1:1\n" \
  "ds_read2_b32 v[14:15], v3 offset0:2 offset1:3\n" \
  "ds_read2_b32 v[16:17], v3 offset0:16 offset1:17\n" \
  "ds_read2_b32 v[18:19], v3 offset0:18 offset1:19\n" \
  "ds_read2_b32 v[20:21], v3 offset0:32 offset1:33\n" \
  "ds_read2_b32 v[22:23], v3 offset0:34 offset1:35\n" \
  "v_mfma_f32_16x16x32_f16 v[116:119], v[100:103], v[24:27], v[116:119]\n" \
  "v_mfma_f32_16x16x32_f16 v[120:123], v[100:103], v[28:31], v[120:123]\n" \
  "ds_read2_b32 v[28:29], v3 offset0:64 offset1:65\n" \
  "ds_read2_b32 v[30:31], v3 offset0:66 offset1:67\n" \
  "v_cvt_pk_f16_f32 v104, v95, v99\n" \
  "v_cvt_pk_f16_f32 v105, v94, v98\n" \
  "v_cvt_pk_f16_f32 v106, v93, v97\n" \
  "v_cvt_pk_f16_f32 v107, v92, v96\n" \
  "s_mov_b32 s97, 0x1fffff\n" \
  "Lgq%=:\n" \
  "ds_read_b32 v8, v4 offset:4\n" \
  "s_waitcnt lgkmcnt(0)\n" \
  "v_readfirstlane_b32 s96, v8\n" \
  "s_and_b32 s96, s96, 0x400\n" \
  "s_cmp_eq_u32 s96, 0x400\n" \
  "s_cbranch_scc1 Lgq%=x\n" \
  "s_sub_u32 s97, s97, 1\n" \
  "s_cbranch_scc0 Lgq%=\n" \
  "Lgq%=x:\n" \
  "ds_read_b128 v[92:95], v10 offset:2304\n" \
  "ds_read_b128 v[96:99], v10 offset:2368\n" \
  "ds_read_b128 v[100:103], v10 offset:2432\n" \
  "v_mfma_f32_16x16x32_f16 v[120:123], v[104:107], v[24:27], v[120:123]\n" \
  "ds_read2_b32 v[24:25], v3 offset0:48 offset1:49\n" \
  "ds_read2_b32 v[26:27], v3 offset0:50 offset1:51\n" \
  "ds_read_b128 v[104:107], v10 offset:2496\n" \
  "v_mfma_f32_16x16x32_f16 v[108:111], v[44:47], v[76:79], v[108:111]\n" \
  "v_mfma_f32_16x16x32_f16 v[112:115], v[124:127], v[76:79], v[112:115]\n" \
  "ds_read_b128 v[76:79], v10 offset:1536\n" \
  "v_mfma_f32_16x16x32_f16 v[108:111], v[48:51], v[80:83], v[108:111]\n" \
  "ds_read_b128 v[80:83], v10 offset:1600\n" \
  "v_mfma_f32_16x16x32_f16 v[108:111], v[52:55], v[84:87], v[108:111]\n" \
  "ds_read_b128 v[84:87], v10 offset:1664\n" \
  "v_mfma_f32_16x16x32_f16 v[108:111], v[56:59], v[88:91], v[108:111]\n" \
  "ds_read_b128 v[88:91], v10 offset:1728\n" \
  "s_waitcnt lgkmcnt(9)\n" \
  "v_mfma_f32_16x16x32_f16 v[108:111], v[60:63], v[92:95], v[108:111]\n" \
  "ds_read_b128 v[92:95], v10 offset:1792\n" \
  "s_waitcnt lgkmcnt(9)\n" \
  "v_mfma_f32_16x16x32_f16 v[108:111], v[64:67], v[96:99], v[108:111]\n" \
  "ds_read_b128 v[96:99], v10 offset:1856\n" \
  "s_waitcnt lgkmcnt(9)\n" \
  "v_mfma_f32_16x16x32_f16 v[108:111], v[68:71], v[100:103], v[108:111]\n" \
  "ds_read_b128 v[100:103], v10 offset:1920\n" \
  "s_waitcnt lgkmcnt(7)\n" \
  "v_mfma_f32_16x16x32_f16 v[108:111], v[72:75], v[104:107], v[108:111]\n" \
  "ds_read_b128 v[104:107], v10 offset:1984\n" \
  "s_waitcnt lgkmcnt(7)\n" \
  "v_mfma_f32_16x16x32_f16 v[108:111], v[12:15], v[76:79], v[108:111]\n" \
  "v_mfma_f32_16x16x32_f16 v[112:115], v[44:47], v[76:79], v[112:115]\n" \
  "v_mfma_f32_16x16x32_f16 v[116:119], v[124:127], v[76:79], v[116:119]\n" \
  "ds_read_b128 v[76:79], v10 offset:1024\n" \
  "s_waitcnt lgkmcnt(7)\n" \
  "v_mfma_f32_16x16x32_f16 v[108:111], v[16:19], v[80:83], v[108:111]\n" \
  "v_mfma_f32_16x16x32_f16 v[112:115], v[48:51], v[80:83], v[112:115]\n" \
  "ds_read_b128 v[80:83], v10 offset:1088\n" \
  "s_waitcnt lgkmcnt(7)\n" \
  "v_mfma_f32_16x16x32_f16 v[108:111], v[20:23], v[84:87], v[108:111]\n" \
  "v_mfma_f32_16x16x32_f16 v[112:115], v[52:55], v[84:87], v[112:115]\n" \
  "ds_read_b128 v[84:87], v10 offset:1152\n" \
  "s_waitcnt lgkmcnt(7)\n" \
  "v_mfma_f32_16x16x32_f16 v[108:111], v[24:27], v[88:91], v[108:111]\n" \
  "v_mfma_f32_16x16x32_f16 v[112:115], v[56:59], v[88:91], v[112:115]\n" \
  "ds_read_b128 v[88:91], v10 offset:1216\n" \
  "s_waitcnt lgkmcnt(7)\n" \
  "v_mfma_f32_16x16x32_f16 v[108:111], v[28:31], v[92:95], v[108:111]\n" \
  "v_mfma_f32_16x16x32_f16 v[112:115], v[60:63], v[92:95], v[112:115]\n" \
  "ds_read_b128 v[92:95], v10 offset:1280\n" \
  "s_waitcnt lgkmcnt(7)\n" \
  "v_mfma_f32_16x16x32_f16 v[108:111], v[32:35], v[96:99], v[108:111]\n" \
  "v_mfma_f32_16x16x32_f16 v[112:115], v[64:67], v[96:99], v[112:115]\n" \
  "ds_read_b128 v[96:99], v10 offset:1344\n" \
  "s_waitcnt lgkmcnt(7)\n" \
  "v_mfma_f32_16x16x32_f16 v[108:111], v[36:39], v[100:103], v[108:111]\n" \
  "v_mfma_f32_16x16x32_f16 v[112:115], v[68:71], v[100:103], v[112:115]\n" \
  "ds_read_b128 v[100:103], v10 offset:1408\n" \
  "s_waitcnt lgkmcnt(7)\n" \
  "v_mfma_f32_16x16x32_f16 v[108:111], v[40:43], v[104:107], v[108:111]\n" \
  "v_mfma_f32_16x16x32_f16 v[112:115], v[72:75], v[104:107], v[112:115]\n" \
  "ds_read_b128 v[104:107], v10 offset:1472\n" \
  "s_waitcnt lgkmcnt(7)\n" \
  "v_mfma_f32_16x16x32_f16 v[112:115], v[12:15], v[76:79], v[112:115]\n" \
  "v_mfma_f32_16x16x32_f16 v[116:119], v[44:47], v[76:79], v[116:119]\n" \
  "v_mfma_f32_16x16x32_f16 v[120:123], v[124:127], v[76:79], v[120:123]\n" \
  "ds_read_b128 v[76:79], v10 offset:512\n" \
  "s_waitcnt lgkmcnt(7)\n" \
  "v_mfma_f32_16x16x32_f16 v[112:115], v[16:19], v[80:83], v[112:115]\n" \
  "v_mfma_f32_16x16x32_f16 v[116:119], v[48:51], v[80:83], v[116:119]\n" \
  "ds_read_b128 v[80:83], v10 offset:576\n" \
  "s_waitcnt lgkmcnt(7)\n" \
  "v_mfma_f32_16x16x32_f16 v[112:115], v[20:23], v[84:87], v[112:115]\n" \
  "v_mov_b32 v9, 0x10000\n" \
  "ds_write_b128 v11, v[108:111] offset:5120\n" \
  "ds_add_u32 v4, v9 offset:0\n" \
  "v_mfma_f32_16x16x32_f16 v[116:119], v[52:55], v[84:87], v[116:119]\n" \
  "ds_read_b128 v[84:87], v10 offset:640\n" \
  "s_waitcnt lgkmcnt(9)\n" \
  "v_mfma_f32_16x16x32_f16 v[112:115], v[24:27], v[88:91], v[112:115]\n" \
  "v_mfma_f32_16x16x32_f16 v[116:119], v[56:59], v[88:91], v[116:119]\n" \
  "ds_read_b128 v[88:91], v10 offset:704\n" \
  "s_waitcnt lgkmcnt(9)\n" \
  "v_mfma_f32_16x16x32_f16 v[112:115], v[28:31], v[92:95], v[112:115]\n" \
  "v_mfma_f32_16x16x32_f16 v[116:119], v[60:63], v[92:95], v[116:119]\n" \
  "ds_read_b128 v[92:95], v10 offset:768\n" \
  "s_waitcnt lgkmcnt(9)\n" \
  "v_mfma_f32_16x16x32_f16 v[112:115], v[32:35], v[96:99], v[112:115]\n" \
  "v_mfma_f32_16x16x32_f16 v[116:119], v[64:67], v[96:99], v[116:119]\n" \
  "ds_read_b128 v[96:99], v10 offset:832\n" \
  "s_waitcnt lgkmcnt(9)\n" \
  "v_mfma_f32_16x16x32_f16 v[112:115], v[36:39], v[100:103], v[112:115]\n" \
  "v_mfma_f32_16x16x32_f16 v[116:119], v[68:71], v[100:103], v[116:119]\n" \
  "ds_read_b128 v[100:103], v10 offset:896\n" \
  "s_waitcnt lgkmcnt(9)\n" \
  "v_mfma_f32_16x16x32_f16 v[112:115], v[40:43], v[104:107], v[112:115]\n" \
  "v_mfma_f32_16x16x32_f16 v[116:119], v[72:75], v[104:107], v[116:119]\n" \
  "ds_read_b128 v[104:107], v10 offset:960\n" \
  "s_waitcnt lgkmcnt(9)\n" \
  "v_mfma_f32_16x16x32_f16 v[116:119], v[12:15], v[76:79], v[116:119]\n" \
  "v_mfma_f32_16x16x32_f16 v[120:123], v[44:47], v[76:79], v[120:123]\n" \
  "ds_read_b128 v[76:79], v10 offset:0\n" \
  "s_waitcnt lgkmcnt(9)\n" \
  "v_mfma_f32_16x16x32_f16 v[116:119], v[16:19], v[80:83], v[116:119]\n" \
  "v_mfma_f32_16x16x32_f16 v[120:123], v[48:51], v[80:83], v[120:123]\n" \
  "ds_read_b128 v[80:83], v10 offset:64\n" \
  "s_waitcnt lgkmcnt(7)\n" \
  "v_mfma_f32_16x16x32_f16 v[116:119], v[20:23], v[84:87], v[116:119]\n" \
  "v_mov_b32 v9, 0x100000\n" \
  "ds_write_b128 v11, v[112:115] offset:6400\n" \
  "ds_add_u32 v4, v9 offset:0\n" \
  "v_mfma_f32_16x16x32_f16 v[120:123], v[52:55], v[84:87], v[120:123]\n" \
  "ds_read_b128 v[84:87], v10 offset:128\n" \
  "s_waitcnt lgkmcnt(9)\n" \
  "v_mfma_f32_16x16x32_f16 v[116:119], v[24:27], v[88:91], v[116:119]\n" \
  "v_mfma_f32_16x16x32_f16 v[120:123], v[56:59], v[88:91], v[120:123]\n" \
  "ds_read_b128 v[88:91], v10 offset:192\n" \
  "s_waitcnt lgkmcnt(9)\n" \
  "v_mfma_f32_16x16x32_f16 v[116:119], v[28:31], v[92:95], v[116:119]\n" \
  "v_mfma_f32_16x16x32_f16 v[120:123], v[60:63], v[92:95], v[120:123]\n" \
  "ds_read_b128 v[92:95], v10 offset:256\n" \
  "s_waitcnt lgkmcnt(9)\n" \
  "v_mfma_f32_16x16x32_f16 v[116:119], v[32:35], v[96:99], v[116:119]\n" \
  "v_mfma_f32_16x16x32_f16 v[120:123], v[64:67], v[96:99], v[120:123]\n" \
  "ds_read_b128 v[96:99], v10 offset:320\n" \
  "s_waitcnt lgkmcnt(9)\n" \
  "v_mfma_f32_16x16x32_f16 v[116:119], v[36:39], v[100:103], v[116:119]\n" \
  "v_mfma_f32_16x16x32_f16 v[120:123], v[68:71], v[100:103], v[120:123]\n" \
  "ds_read_b128 v[100:103], v10 offset:384\n" \
  "s_waitcnt lgkmcnt(9)\n" \
  "v_mfma_f32_16x16x32_f16 v[116:119], v[40:43], v[104:107], v[116:119]\n" \
  "v_mfma_f32_16x16x32_f16 v[120:123], v[72:75], v[104:107], v[120:123]\n" \
  "ds_read_b128 v[104:107], v10 offset:448\n" \
  "s_waitcnt lgkmcnt(9)\n" \
  "v_mfma_f32_16x16x32_f16 v[120:123], v[12:15], v[76:79], v[120:123]\n" \
  "s_waitcnt lgkmcnt(8)\n" \
  "v_mfma_f32_16x16x32_f16 v[120:123], v[16:19], v[80:83], v[120:123]\n" \
  "s_waitcnt lgkmcnt(5)\n" \
  "v_mfma_f32_16x16x32_f16 v[120:123], v[20:23], v[84:87], v[120:123]\n" \
  "v_mov_b32 v9, 0x1000000\n" \
  "ds_write_b128 v11, v[116:119] offset:7680\n" \
  "ds_add_u32 v4, v9 offset:0\n" \
  "s_waitcnt lgkmcnt(6)\n" \
  "v_mfma_f32_16x16x32_f16 v[120:123], v[24:27], v[88:91], v[120:123]\n" \
  "s_waitcnt lgkmcnt(5)\n" \
  "v_mfma_f32_16x16x32_f16 v[120:123], v[28:31], v[92:95], v[120:123]\n" \
  "s_waitcnt lgkmcnt(4)\n" \
  "v_mfma_f32_16x16x32_f16 v[120:123], v[32:35], v[96:99], v[120:123]\n" \
  "s_waitcnt lgkmcnt(3)\n" \
  "v_mfma_f32_16x16x32_f16 v[120:123], v[36:39], v[100:103], v[120:123]\n" \
  "s_waitcnt lgkmcnt(2)\n" \
  "v_mfma_f32_16x16x32_f16 v[120:123], v[40:43], v[104:107], v[120:123]\n" \
  "v_mov_b32 v9, 0x10000000\n" \
  "s_nop 6\n" \
  "ds_write_b128 v11, v[120:123] offset:8960\n" \
  "ds_add_u32 v4, v9 offset:0\n" \
  "s_branch Lr3%=\n" \
  "Lr4%=:\n" \
  "s_load_dwordx4 s[84:87], %[k], 0x40\n" \
  "s_load_dwordx2 s[88:89], %[k], 0x50\n" \
  "s_and_b32 s46, %[b], 7\n" \
  "s_lshr_b32 s47, %[b], 3\n" \
  "s_mul_i32 s47, s47, 3\n" \
  "s_add_u32 s47, s47, s50\n" \
  "v_and_b32 v9, 63, %[t]\n" \
  "s_mul_i32 s48, s46, 72\n" \
  "s_add_u32 s48, s48, s47\n" \
  "s_mul_i32 s48, s48, 72\n" \
  "v_min_u32 v5, 35, v9\n" \
  "v_add_lshl_u32 v121, v5, s48, 7\n" \
  "v_add_u32 v122, 4608, v121\n" \
  "s_mul_i32 s48, s47, 0\n" \
  "v_add_u32 v6, s48, v9\n" \
  "v_mul_u32_u24 v7, 7282, v6\n" \
  "v_lshrrev_b32 v7, 16, v7\n" \
  "v_mul_u32_u24 v7, 8064, v7\n" \
  "v_lshl_add_u32 v6, v6, 7, v7\n" \
  "v_add_u32 v6, 5308416, v6\n" \
  "v_cmp_gt_u32 vcc, 0, v9\n" \
  "v_subrev_u32 v9, 0, v9\n" \
  "v_min_u32 v9, 15, v9\n" \
  "s_mul_i32 s47, s47, 8\n" \
  "v_lshrrev_b32 v7, 1, v9\n" \
  "v_add_u32 v7, s47, v7\n" \
  "v_mul_u32_u24 v7, 9216, v7\n" \
  "v_and_b32 v9, 1, v9\n" \
  "s_waitcnt lgkmcnt(0)\n" \
  "s_lshr_b32 s47, s84, 7\n" \
  "s_add_u32 s47, s47, s46\n" \
  "s_sub_u32 s47, 3, s47\n" \
  "s_and_b32 s47, s47, 7\n" \
  "s_sub_u32 s48, 8, s47\n" \
  "v_mul_u32_u24 v9, s48, v9\n" \
  "v_add_lshl_u32 v9, v9, s47, 7\n" \
  "v_add_u32 v7, v7, v9\n" \
  "v_cndmask_b32 v6, v7, v6, vcc\n" \
  "s_mul_i32 s46, s46, 1152\n" \
  "v_add_u32 v120, s46, v6\n" \
  "s_setprio 3\n" \
  "s_mov_b32 s97, 0x1fffff\n" \
  "Lkr%=:\n" \
  "ds_read_b32 v17, v4 offset:4\n" \
  "s_waitcnt lgkmcnt(0)\n" \
  "v_readfirstlane_b32 s96, v17\n" \
  "s_and_b32 s96, s96, " STR(PFG) "\n" \
  "s_cmp_eq_u32 s96, " STR(PFG) "\n" \
  "s_cbranch_scc1 Lkr%=x\n" \
  "s_sleep 2\n" \
  "s_sub_u32 s97, s97, 1\n" \
  "s_cbranch_scc0 Lkr%=\n" \
  "Lkr%=x:\n" \
  "global_load_dword v126, v120, s[84:85]" PFT "\n" \
  "global_load_dword v126, v120, s[86:87]" PFT "\n" \
  "global_load_dword v126, v121, s[88:89]" PFT "\n" \
  "global_load_dword v126, v122, s[88:89]" PFT "\n" \
  "Lr3%=:\n" \
  GSTAMP7 \
  "s_setprio 0\n" \
  "Lr5%=:\n"

#define SW_HEAD \
  "s_load_dwordx2 s[46:47], %[k], 0x0\n" \
  "s_load_dwordx2 s[48:49], %[k], 0x30\n" \
  "s_load_dwordx2 s[40:41], %[k], 0x70\n" \
  "s_setprio 3\n" \
  "s_and_b32 s50, %[w], 3\n" \
  "s_mul_i32 s51, %[b], 9216\n" \
  "s_mul_i32 s52, s50, 288\n" \
  "s_add_u32 s51, s51, s52\n" \
  "v_bfe_u32 v1, %[t], 3, 3\n" \
  "v_and_b32 v2, 7, %[t]\n" \
  "v_mul_u32_u24 v3, 1152, v1\n" \
  "v_mul_u32_u24 v4, 12, v2\n" \
  "v_add3_u32 v3, v3, v4, s51\n" \
  "v_and_b32 v5, 255, %[t]\n" \
  "v_mov_b32 v6, 158464\n" \
  "v_lshl_add_u32 v6, v5, 3, v6\n" \
  "v_mov_b64 v[8:9], 0\n" \
  "ds_write_b64 v6, v[8:9]\n" \
  "s_mov_b32 s42, 10403840\n" \
  "s_mov_b32 s43, 0x20000\n" \
  "s_mul_i32 s52, %[b], 6\n" \
  "s_and_b32 s52, s52, -4\n" \
  "s_cmp_lt_u32 %[b], 128\n" \
  SSTAMP1 \
  "s_waitcnt lgkmcnt(0)\n" \
  "s_memtime s[58:59]\n" \
  ".if " STR(SDLY) "\n" \
  "s_sleep " STR(SDLY) "\n" \
  ".endif\n" \
  "global_load_dwordx3 v[16:18], v3, s[46:47] offset:0\n" \
  "global_load_dwordx3 v[20:22], v3, s[46:47] offset:96\n" \
  "global_load_dwordx3 v[24:26], v3, s[46:47] offset:192\n" \
  "buffer_load_dword v7, off, s[40:43], 0 sc1\n" \
  "s_load_dwordx2 s[54:55], s[48:49], s52\n" \
  "s_cbranch_scc1 Lone%=\n" \
  "v_add_u32 v3, 589824, v3\n" \
  "global_load_dwordx3 v[28:30], v3, s[46:47] offset:0\n" \
  "global_load_dwordx3 v[32:34], v3, s[46:47] offset:96\n" \
  "global_load_dwordx3 v[36:38], v3, s[46:47] offset:192\n" \
  "Lone%=:\n" \
  "s_barrier\n"

#define SW_BODY \
  "s_mul_i32 s51, s50, 3200\n" \
  "s_add_u32 s51, s51, 125088\n" \
  "v_mul_u32_u24 v3, 400, v1\n" \
  "v_lshlrev_b32 v4, 4, v2\n" \
  "v_add3_u32 v3, v3, v4, s51\n" \
  "v_and_b32 v10, 3, %[t]\n" \
  "v_mul_u32_u24 v4, 800, v10\n" \
  "v_lshl_add_u32 v4, v1, 4, v4\n" \
  "v_add_u32 v4, s51, v4\n" \
  "v_and_b32 v11, 4, %[t]\n" \
  "v_lshl_add_u32 v6, v11, 1, v4\n" \
  "v_mul_u32_u24 v12, 8421504, v11\n" \
  "v_lshrrev_b32 v13, 1, v11\n" \
  "v_add_u32 v12, v12, v13\n" \
  "v_add_u32 v12, 0x05040100, v12\n" \
  "s_lshr_b32 s51, %[b], 1\n" \
  "s_mul_i32 s51, s51, 24704\n" \
  "s_and_b32 s52, %[b], 1\n" \
  "s_lshl_b32 s52, s52, 6\n" \
  "s_add_u32 s51, s51, s52\n" \
  "s_mul_i32 s52, s50, 6144\n" \
  "s_add_u32 s51, s51, s52\n" \
  "s_add_u32 s51, s51, 7241728\n" \
  "v_bfe_u32 v13, %[t], 2, 4\n" \
  "v_lshlrev_b32 v13, 7, v13\n" \
  "v_lshl_add_u32 v13, v10, 4, v13\n" \
  "v_add_u32 v13, s51, v13\n" \
  "s_lshl4_add_u32 s51, s50, 158464\n" \
  "v_lshl_add_u32 v14, v1, 6, s51\n" \
  "v_mov_b32 v42, 1\n" \
  "s_mov_b32 s53, 3\n" \
  "s_movk_i32 s56, 0x1000\n" \
  "s_mov_b32 s57, 0\n" \
  "v_and_b32 v15, 63, %[t]\n" \
  "v_min_u32 v111, 1, v15\n" \
  "v_mul_u32_u24 v111, 40, v111\n" \
  "v_add_u32 v111, v111, v15\n" \
  "v_mov_b32 v8, 159488\n" \
  "v_lshl_add_u32 v111, v111, 2, v8\n" \
  "v_lshlrev_b32 v8, 2, v5\n" \
  "v_sub_u32 v105, 97308, v8\n" \
  "v_lshrrev_b32 v8, 3, v5\n" \
  "v_mul_u32_u24 v106, 24704, v8\n" \
  "v_lshl_add_u32 v106, v2, 4, v106\n" \
  "s_lshl_b32 s51, %[b], 7\n" \
  "s_add_u32 s51, s51, 7241728\n" \
  "v_add_u32 v106, s51, v106\n" \
  "v_lshlrev_b32 v107, 4, v5\n" \
  "s_mul_i32 s51, %[b], 16512\n" \
  "s_add_u32 s51, s51, 139264\n" \
  "v_add_u32 v117, s51, v107\n" \
  "v_add_u32 v107, 119808, v107\n" \
  "v_and_b32 v8, 15, v8\n" \
  "v_bfe_u32 v9, v5, 1, 2\n" \
  "v_mul_u32_u24 v9, 320, v9\n" \
  "v_lshl_add_u32 v115, v8, 4, v9\n" \
  "v_and_b32 v9, 1, v5\n" \
  "v_lshl_add_u32 v115, v9, 3, v115\n" \
  "s_lshr_b32 s84, s50, 1\n" \
  "s_mul_i32 s51, s84, 1280\n" \
  "v_add_u32 v115, s51, v115\n" \
  "v_add_u32 v116, 64192, v115\n" \
  "s_lshl_b32 s84, s84, 2\n" \
  "s_and_b32 s51, %[b], 1\n" \
  "v_min_u32 v8, 2, v1\n" \
  "v_add_u32 v8, s51, v8\n" \
  "v_cmp_ne_u32 vcc, 3, v8\n" \
  "v_min_u32 v9, 2, v10\n" \
  "v_mul_u32_u24 v9, 27648, v9\n" \
  "v_cndmask_b32 v8, 0, v8, vcc\n" \
  "v_lshl_add_u32 v9, v8, 3, v9\n" \
  "s_mul_i32 s52, %[b], 24\n" \
  "s_add_u32 s52, s52, 4096\n" \
  "v_add_u32 v9, s52, v9\n" \
  "v_mul_u32_u24 v108, 1152, v11\n" \
  "v_add_u32 v108, v108, v9\n" \
  "v_add_u32 v114, 9216, v9\n" \
  "v_add_lshl_u32 v109, v8, s51, 4\n" \
  "v_cmp_ne_u32 vcc, 3, v10\n" \
  "s_nop 1\n" \
  "v_cndmask_b32 v110, 0, 1.0, vcc\n" \
  "s_waitcnt vmcnt(0) lgkmcnt(0)\n" \
  SSTAMP2 \
  "v_readfirstlane_b32 s44, v7\n" \
  "Lun%=:\n" \
  "ds_write_b96 v3, v[16:18] offset:0\n" \
  "ds_write_b96 v3, v[20:22] offset:128\n" \
  "ds_write_b96 v3, v[24:26] offset:256\n" \
  "ds_read2_b32 v[44:45], v6 offset0:0 offset1:100\n" \
  "ds_read2_b32 v[56:57], v4 offset0:1 offset1:101\n" \
  "ds_read2_b32 v[48:49], v6 offset0:32 offset1:132\n" \
  "ds_read2_b32 v[58:59], v4 offset0:33 offset1:133\n" \
  "ds_read2_b32 v[52:53], v6 offset0:64 offset1:164\n" \
  "ds_read2_b32 v[60:61], v4 offset0:65 offset1:165\n" \
  "v_mov_b64 v[40:41], 0\n" \
  "s_add_u32 s45, s44, 1\n" \
  "v_dot2c_f32_bf16 v40, 0x3f803f80, v16\n" \
  "v_dot2c_f32_bf16 v41, v16, v16\n" \
  "v_dot2c_f32_bf16 v40, 0x3f803f80, v17\n" \
  "v_dot2c_f32_bf16 v41, v17, v17\n" \
  "v_dot2c_f32_bf16 v40, 0x3f803f80, v18\n" \
  "v_dot2c_f32_bf16 v41, v18, v18\n" \
  "v_dot2c_f32_bf16 v40, 0x3f803f80, v20\n" \
  "v_dot2c_f32_bf16 v41, v20, v20\n" \
  "v_dot2c_f32_bf16 v40, 0x3f803f80, v21\n" \
  "v_dot2c_f32_bf16 v41, v21, v21\n" \
  "v_dot2c_f32_bf16 v40, 0x3f803f80, v22\n" \
  "v_dot2c_f32_bf16 v41, v22, v22\n" \
  "v_dot2c_f32_bf16 v40, 0x3f803f80, v24\n" \
  "v_dot2c_f32_bf16 v41, v24, v24\n" \
  "v_dot2c_f32_bf16 v40, 0x3f803f80, v25\n" \
  "v_dot2c_f32_bf16 v41, v25, v25\n" \
  "v_dot2c_f32_bf16 v40, 0x3f803f80, v26\n" \
  "v_dot2c_f32_bf16 v41, v26, v26\n" \
  "v_mov_b32 v47, s45\n" \
  "v_mov_b32 v51, s45\n" \
  "v_mov_b32 v55, s45\n" \
  "s_waitcnt lgkmcnt(0)\n" \
  "v_perm_b32 v46, v57, v56, v12\n" \
  "v_add_f32_dpp v40, v40, v40 quad_perm:[1,0,3,2] row_mask:0xf bank_mask:0xf bound_ctrl:1\n" \
  "v_add_f32_dpp v41, v41, v41 quad_perm:[1,0,3,2] row_mask:0xf bank_mask:0xf bound_ctrl:1\n" \
  "buffer_store_dwordx4 v[44:47], v13, s[40:43], 0 offen sc1\n" \
  "v_perm_b32 v50, v59, v58, v12\n" \
  "v_add_f32_dpp v40, v40, v40 quad_perm:[2,3,0,1] row_mask:0xf bank_mask:0xf bound_ctrl:1\n" \
  "v_add_f32_dpp v41, v41, v41 quad_perm:[2,3,0,1] row_mask:0xf bank_mask:0xf bound_ctrl:1\n" \
  "buffer_store_dwordx4 v[48:51], v13, s[40:43], 0 offen offset:2048 sc1\n" \
  "v_perm_b32 v54, v61, v60, v12\n" \
  "v_add_f32_dpp v40, v40, v40 row_half_mirror row_mask:0xf bank_mask:0xf bound_ctrl:1\n" \
  "v_add_f32_dpp v41, v41, v41 row_half_mirror row_mask:0xf bank_mask:0xf bound_ctrl:1\n" \
  "buffer_store_dwordx4 v[52:55], v13, s[40:43], s56 offen sc1\n" \
  "ds_write_b96 v14, v[40:42]\n" \
  SSTAMP3 \
  "s_cmp_lg_u32 s50, s53\n" \
  "s_cbranch_scc1 Lsn%=\n" \
  "v_and_b32 v15, 31, %[t]\n" \
  "s_add_u32 s51, s57, 158464\n" \
  "v_lshl_add_u32 v15, v15, 4, s51\n" \
  "v_mov_b32 v43, 158208\n" \
  "ds_write_b32 v43, v55\n" \
  "s_movk_i32 s97, 0x7fff\n" \
  "Lsp%=:\n" \
  "ds_read_b96 v[56:58], v15\n" \
  "s_waitcnt lgkmcnt(0)\n" \
  "v_cmp_ne_u32 vcc, 1, v58\n" \
  "s_cbranch_vccz Lsq%=\n" \
  "s_sub_u32 s97, s97, 1\n" \
  "s_cbranch_scc0 Lsp%=\n" \
  "Lsq%=:\n" \
  "v_add_f32_dpp v56, v56, v56 quad_perm:[1,0,3,2] row_mask:0xf bank_mask:0xf bound_ctrl:1\n" \
  "v_add_f32_dpp v57, v57, v57 quad_perm:[1,0,3,2] row_mask:0xf bank_mask:0xf bound_ctrl:1\n" \
  "s_lshl_b32 s51, %[b], 6\n" \
  "v_add_f32_dpp v56, v56, v56 quad_perm:[2,3,0,1] row_mask:0xf bank_mask:0xf bound_ctrl:1\n" \
  "v_add_f32_dpp v57, v57, v57 quad_perm:[2,3,0,1] row_mask:0xf bank_mask:0xf bound_ctrl:1\n" \
  "v_mul_f32 v56, 0x3ae38e39, v56\n" \
  "v_mul_f32 v57, 0x3ae38e39, v57\n" \
  "v_fma_f32 v57, -v56, v56, v57\n" \
  "v_max_f32 v57, 0, v57\n" \
  "v_cvt_pk_bf16_f32 v57, 0, v57\n" \
  "v_add_f32 v57, 0x3727c5ac, v57\n" \
  "v_cvt_pk_bf16_f32 v57, 0, v57\n" \
  "v_sqrt_f32 v57, v57\n" \
  "v_mov_b32_dpp v58, v56 row_shl:4 row_mask:0xf bank_mask:0xf bound_ctrl:1\n" \
  "v_cvt_pk_bf16_f32 v57, 0, v57\n" \
  "v_rcp_f32 v60, v57\n" \
  "v_cvt_pk_bf16_f32 v62, v56, v58\n" \
  "v_mov_b32 v63, s45\n" \
  "s_lshl3_add_u32 s51, s57, s51\n" \
  "v_mov_b32_dpp v61, v60 row_shl:4 row_mask:0xf bank_mask:0xf bound_ctrl:1\n" \
  "s_add_u32 s51, s51, 119808\n" \
  "v_lshlrev_b32 v15, 4, v1\n" \
  "s_mov_b32 exec_lo, 0x01010101\n" \
  "s_mov_b32 exec_hi, 0\n" \
  "buffer_store_dwordx4 v[60:63], v15, s[40:43], s51 offen sc1\n" \
  "s_mov_b64 exec, -1\n" \
  "Lsn%=:\n" \
  "s_cmp_lt_u32 %[b], 128\n" \
  "s_cbranch_scc1 Lsd%=\n" \
  "s_cmp_eq_u32 s53, 2\n" \
  "s_cbranch_scc1 Lsd%=\n" \
  "s_mov_b32 s53, 2\n" \
  "s_movk_i32 s57, 512\n" \
  "v_add_u32 v13, 790528, v13\n" \
  "v_add_u32 v14, 512, v14\n" \
  "v_mov_b64 v[16:17], v[28:29]\n" \
  "v_mov_b32 v18, v30\n" \
  "v_mov_b64 v[20:21], v[32:33]\n" \
  "v_mov_b32 v22, v34\n" \
  "v_mov_b64 v[24:25], v[36:37]\n" \
  "v_mov_b32 v26, v38\n" \
  "s_branch Lun%=\n" \
  "Lsd%=:\n" \
  SSTAMP4 \
  "s_mov_b32 s44, s45\n" \
  "s_mov_b32 s45, s84\n" \
  "v_lshrrev_b64 v[8:9], v109, s[54:55]\n" \
  "v_lshlrev_b32 v109, 16, v8\n" \
  "s_movk_i32 s97, 64\n" \
  "Lxw%=:\n" \
  "s_memtime s[46:47]\n" \
  "s_waitcnt lgkmcnt(0)\n" \
  "s_sub_u32 s46, s46, s58\n" \
  "s_cmp_gt_u32 s46, " STR(XT) "\n" \
  "s_cbranch_scc1 Lxv%=\n" \
  "s_sleep 1\n" \
  "s_sub_u32 s97, s97, 1\n" \
  "s_cbranch_scc0 Lxw%=\n" \
  "Lxv%=:\n" \
  "s_setprio " STR(PRH) "\n" \
  SSTAMP5 \
  "buffer_load_dwordx2 v[102:103], v108, s[40:43], 0 offen sc1\n" \
  "s_mov_b32 s94, 0\n" \
  "s_mov_b32 s95, 0\n" \
  "buffer_load_dwordx4 v[64:67], v106, s[40:43], s94 offen sc1\n" \
  "buffer_load_dwordx4 v[68:71], v107, s[40:43], s95 offen sc1\n" \
  "s_mov_b32 s94, 790528\n" \
  "s_mov_b32 s95, 4096\n" \
  "buffer_load_dwordx4 v[72:75], v106, s[40:43], s94 offen sc1\n" \
  "buffer_load_dwordx4 v[76:79], v107, s[40:43], s95 offen sc1\n" \
  "s_waitcnt vmcnt(4)\n" \
  SSTAMP6 \
  "v_cmp_ne_u32 vcc, s44, v103\n" \
  "s_cmp_lg_u64 vcc, 0\n" \
  "s_cbranch_scc1 Lms%=\n" \
  "Lmb%=:\n" \
  "v_mul_f32 v102, v102, v110\n" \
  "v_mov_b32 v104, 226\n" \
  "s_mov_b32 s94, 0x42000000\n" \
  "v_add_f32_dpp v102, v102, v102 quad_perm:[1,0,3,2] row_mask:0xf bank_mask:0xf bound_ctrl:1\n" \
  "v_mov_b32 v112, 1\n" \
  "s_nop 0\n" \
  "v_add_f32_dpp v102, v102, v102 quad_perm:[2,3,0,1] row_mask:0xf bank_mask:0xf bound_ctrl:1\n" \
  "v_cvt_pk_bf16_f32 v102, 0, v102\n" \
  "s_nop 1\n" \
  "v_mov_b32_dpp v103, v102 row_shl:4 row_mask:0xf bank_mask:0xf bound_ctrl:1\n" \
  "v_add_f32 v103, 1.0, v103\n" \
  "v_cvt_pk_bf16_f32 v103, 0, v103\n" \
  "v_fma_f32 v96, |v103|, s94, |v102|\n" \
  "v_lshrrev_b32 v96, 23, v96\n" \
  "v_med3_u32 v96, v96, 26, v104\n" \
  "v_lshlrev_b32 v96, 23, v96\n" \
  "v_sub_u32 v97, 0x84000000, v96\n" \
  "v_add_u32 v104, 0xfb000000, v96\n" \
  "v_mul_f32 v103, v103, v97\n" \
  "v_mul_f32 v102, v102, v97\n" \
  "v_mul_f32 v113, v109, v104\n" \
  "v_readlane_b32 s84, v103, 0\n" \
  "v_readlane_b32 s87, v102, 0\n" \
  "v_readlane_b32 s85, v103, 8\n" \
  "v_readlane_b32 s88, v102, 8\n" \
  "v_readlane_b32 s86, v103, 16\n" \
  "v_readlane_b32 s89, v102, 16\n" \
  "v_mov_b32 v96, s84\n" \
  "v_mov_b32 v97, s87\n" \
  "v_mov_b32 v98, s85\n" \
  "v_mov_b32 v99, s88\n" \
  "v_mov_b32 v100, s86\n" \
  "v_mov_b32 v101, s89\n" \
  "v_readlane_b32 s84, v113, 0\n" \
  "v_readlane_b32 s87, v104, 0\n" \
  "v_readlane_b32 s85, v113, 8\n" \
  "v_readlane_b32 s88, v104, 8\n" \
  "v_readlane_b32 s86, v113, 16\n" \
  "v_readlane_b32 s89, v104, 16\n" \
  "s_waitcnt vmcnt(2)\n" \
  "v_cmp_ne_u32 vcc, s44, v67\n" \
  "v_cmp_ne_u32 s[94:95], s44, v71\n" \
  "s_or_b64 vcc, vcc, s[94:95]\n" \
  "s_cbranch_scc1 Lu0s%=\n" \
  "Lu0b%=:\n" \
  "v_lshlrev_b32 v102, 16, v70\n" \
  "v_and_b32 v103, 0xffff0000, v70\n" \
  "v_lshlrev_b32 v16, 16, v64\n" \
  "v_lshlrev_b32 v17, 16, v65\n" \
  "v_and_b32 v18, 0xffff0000, v64\n" \
  "v_and_b32 v19, 0xffff0000, v65\n" \
  "v_lshlrev_b32 v20, 16, v66\n" \
  "v_and_b32 v21, 0xffff0000, v66\n" \
  "v_pk_add_f32 v[40:41], v[16:17], v[102:103] neg_lo:[0,1] neg_hi:[0,1]\n" \
  "v_pk_add_f32 v[42:43], v[18:19], v[102:103] neg_lo:[0,1] neg_hi:[0,1]\n" \
  "v_pk_add_f32 v[44:45], v[20:21], v[102:103] neg_lo:[0,1] neg_hi:[0,1]\n" \
  "v_cvt_pk_bf16_f32 v40, 0, v40\n" \
  "v_cvt_pk_bf16_f32 v41, 0, v41\n" \
  "v_cvt_pk_bf16_f32 v42, 0, v42\n" \
  "v_cvt_pk_bf16_f32 v43, 0, v43\n" \
  "v_cvt_pk_bf16_f32 v44, 0, v44\n" \
  "v_cvt_pk_bf16_f32 v45, 0, v45\n" \
  "v_pk_mul_f32 v[40:41], v[40:41], v[68:69]\n" \
  "v_pk_mul_f32 v[42:43], v[42:43], v[68:69]\n" \
  "v_pk_mul_f32 v[44:45], v[44:45], v[68:69]\n" \
  "v_cvt_pk_bf16_f32 v40, 0, v40\n" \
  "v_cvt_pk_bf16_f32 v41, 0, v41\n" \
  "v_cvt_pk_bf16_f32 v42, 0, v42\n" \
  "v_cvt_pk_bf16_f32 v43, 0, v43\n" \
  "v_cvt_pk_bf16_f32 v44, 0, v44\n" \
  "v_cvt_pk_bf16_f32 v45, 0, v45\n" \
  "v_pk_mul_f32 v[40:41], v[40:41], v[96:97] op_sel_hi:[1,0]\n" \
  "v_pk_mul_f32 v[42:43], v[42:43], v[98:99] op_sel_hi:[1,0]\n" \
  "v_pk_mul_f32 v[44:45], v[44:45], v[100:101] op_sel_hi:[1,0]\n" \
  "v_cvt_pk_bf16_f32 v40, 0, v40\n" \
  "v_cvt_pk_bf16_f32 v41, 0, v41\n" \
  "v_cvt_pk_bf16_f32 v42, 0, v42\n" \
  "v_cvt_pk_bf16_f32 v43, 0, v43\n" \
  "v_cvt_pk_bf16_f32 v44, 0, v44\n" \
  "v_cvt_pk_bf16_f32 v45, 0, v45\n" \
  "v_pk_add_f32 v[40:41], v[40:41], v[96:97] op_sel:[0,1]\n" \
  "v_pk_add_f32 v[42:43], v[42:43], v[98:99] op_sel:[0,1]\n" \
  "v_pk_add_f32 v[44:45], v[44:45], v[100:101] op_sel:[0,1]\n" \
  "v_cvt_pk_bf16_f32 v40, 0, v40\n" \
  "v_cvt_pk_bf16_f32 v41, 0, v41\n" \
  "v_cvt_pk_bf16_f32 v42, 0, v42\n" \
  "v_cvt_pk_bf16_f32 v43, 0, v43\n" \
  "v_cvt_pk_bf16_f32 v44, 0, v44\n" \
  "v_cvt_pk_bf16_f32 v45, 0, v45\n" \
  "v_cvt_pkrtz_f16_f32 v102, v41, v40\n" \
  "v_cvt_pkrtz_f16_f32 v103, v43, v42\n" \
  "v_cvt_pkrtz_f16_f32 v104, v45, v44\n" \
  "ds_write_b32 v105, v102 offset:3072\n" \
  "ds_write_b32 v105, v103 offset:11264\n" \
  "ds_write_b32 v105, v104 offset:19456\n" \
  SSTAMP7 \
  "s_mov_b32 s94, 1581056\n" \
  "s_mov_b32 s95, 8192\n" \
  "buffer_load_dwordx4 v[80:83], v106, s[40:43], s94 offen sc1\n" \
  "buffer_load_dwordx4 v[84:87], v107, s[40:43], s95 offen sc1\n" \
  "s_mov_b32 s94, 2371584\n" \
  "s_mov_b32 s95, 12288\n" \
  "buffer_load_dwordx4 v[88:91], v106, s[40:43], s94 offen sc1\n" \
  "buffer_load_dwordx4 v[92:95], v107, s[40:43], s95 offen sc1\n" \
  "buffer_load_dwordx2 v[118:119], v114, s[40:43], 0 offen sc1\n" \
  "v_mov_b32 v102, 0x10000\n" \
  "ds_add_u32 v111, v102 offset:4\n" \
  "s_waitcnt vmcnt(5)\n" \
  "v_cmp_ne_u32 vcc, s44, v75\n" \
  "v_cmp_ne_u32 s[94:95], s44, v79\n" \
  "s_or_b64 vcc, vcc, s[94:95]\n" \
  "s_cbranch_scc1 Lu1s%=\n" \
  "Lu1b%=:\n" \
  "v_lshlrev_b32 v102, 16, v78\n" \
  "v_and_b32 v103, 0xffff0000, v78\n" \
  "v_lshlrev_b32 v22, 16, v72\n" \
  "v_lshlrev_b32 v23, 16, v73\n" \
  "v_and_b32 v24, 0xffff0000, v72\n" \
  "v_and_b32 v25, 0xffff0000, v73\n" \
  "v_lshlrev_b32 v26, 16, v74\n" \
  "v_and_b32 v27, 0xffff0000, v74\n" \
  "v_pk_add_f32 v[46:47], v[22:23], v[102:103] neg_lo:[0,1] neg_hi:[0,1]\n" \
  "v_pk_add_f32 v[48:49], v[24:25], v[102:103] neg_lo:[0,1] neg_hi:[0,1]\n" \
  "v_pk_add_f32 v[50:51], v[26:27], v[102:103] neg_lo:[0,1] neg_hi:[0,1]\n" \
  "v_cvt_pk_bf16_f32 v46, 0, v46\n" \
  "v_cvt_pk_bf16_f32 v47, 0, v47\n" \
  "v_cvt_pk_bf16_f32 v48, 0, v48\n" \
  "v_cvt_pk_bf16_f32 v49, 0, v49\n" \
  "v_cvt_pk_bf16_f32 v50, 0, v50\n" \
  "v_cvt_pk_bf16_f32 v51, 0, v51\n" \
  "v_pk_mul_f32 v[46:47], v[46:47], v[76:77]\n" \
  "v_pk_mul_f32 v[48:49], v[48:49], v[76:77]\n" \
  "v_pk_mul_f32 v[50:51], v[50:51], v[76:77]\n" \
  "v_cvt_pk_bf16_f32 v46, 0, v46\n" \
  "v_cvt_pk_bf16_f32 v47, 0, v47\n" \
  "v_cvt_pk_bf16_f32 v48, 0, v48\n" \
  "v_cvt_pk_bf16_f32 v49, 0, v49\n" \
  "v_cvt_pk_bf16_f32 v50, 0, v50\n" \
  "v_cvt_pk_bf16_f32 v51, 0, v51\n" \
  "v_pk_mul_f32 v[46:47], v[46:47], v[96:97] op_sel_hi:[1,0]\n" \
  "v_pk_mul_f32 v[48:49], v[48:49], v[98:99] op_sel_hi:[1,0]\n" \
  "v_pk_mul_f32 v[50:51], v[50:51], v[100:101] op_sel_hi:[1,0]\n" \
  "v_cvt_pk_bf16_f32 v46, 0, v46\n" \
  "v_cvt_pk_bf16_f32 v47, 0, v47\n" \
  "v_cvt_pk_bf16_f32 v48, 0, v48\n" \
  "v_cvt_pk_bf16_f32 v49, 0, v49\n" \
  "v_cvt_pk_bf16_f32 v50, 0, v50\n" \
  "v_cvt_pk_bf16_f32 v51, 0, v51\n" \
  "v_pk_add_f32 v[46:47], v[46:47], v[96:97] op_sel:[0,1]\n" \
  "v_pk_add_f32 v[48:49], v[48:49], v[98:99] op_sel:[0,1]\n" \
  "v_pk_add_f32 v[50:51], v[50:51], v[100:101] op_sel:[0,1]\n" \
  "v_cvt_pk_bf16_f32 v46, 0, v46\n" \
  "v_cvt_pk_bf16_f32 v47, 0, v47\n" \
  "v_cvt_pk_bf16_f32 v48, 0, v48\n" \
  "v_cvt_pk_bf16_f32 v49, 0, v49\n" \
  "v_cvt_pk_bf16_f32 v50, 0, v50\n" \
  "v_cvt_pk_bf16_f32 v51, 0, v51\n" \
  "v_cvt_pkrtz_f16_f32 v102, v47, v46\n" \
  "v_cvt_pkrtz_f16_f32 v103, v49, v48\n" \
  "v_cvt_pkrtz_f16_f32 v104, v51, v50\n" \
  "ds_write_b32 v105, v102 offset:2048\n" \
  "ds_write_b32 v105, v103 offset:10240\n" \
  "ds_write_b32 v105, v104 offset:18432\n" \
  "ds_add_u32 v111, v112 offset:4\n" \
  "v_mov_b32 v112, 0x100\n" \
  SSTAMP8 \
  "s_waitcnt vmcnt(3)\n" \
  "v_cmp_ne_u32 vcc, s44, v83\n" \
  "v_cmp_ne_u32 s[94:95], s44, v87\n" \
  "s_or_b64 vcc, vcc, s[94:95]\n" \
  "s_cbranch_scc1 Lu2s%=\n" \
  "Lu2b%=:\n" \
  "v_lshlrev_b32 v102, 16, v86\n" \
  "v_and_b32 v103, 0xffff0000, v86\n" \
  "v_lshlrev_b32 v28, 16, v80\n" \
  "v_lshlrev_b32 v29, 16, v81\n" \
  "v_and_b32 v30, 0xffff0000, v80\n" \
  "v_and_b32 v31, 0xffff0000, v81\n" \
  "v_lshlrev_b32 v32, 16, v82\n" \
  "v_and_b32 v33, 0xffff0000, v82\n" \
  "v_sub_f32 v52, v28, v102\n" \
  "v_sub_f32 v53, v29, v103\n" \
  "v_sub_f32 v54, v30, v102\n" \
  "v_sub_f32 v55, v31, v103\n" \
  "v_sub_f32 v56, v32, v102\n" \
  "v_sub_f32 v57, v33, v103\n" \
  "v_cvt_pk_bf16_f32 v52, 0, v52\n" \
  "v_cvt_pk_bf16_f32 v53, 0, v53\n" \
  "v_cvt_pk_bf16_f32 v54, 0, v54\n" \
  "v_cvt_pk_bf16_f32 v55, 0, v55\n" \
  "v_cvt_pk_bf16_f32 v56, 0, v56\n" \
  "v_cvt_pk_bf16_f32 v57, 0, v57\n" \
  "v_mul_f32 v52, v52, v84\n" \
  "v_mul_f32 v53, v53, v85\n" \
  "v_mul_f32 v54, v54, v84\n" \
  "v_mul_f32 v55, v55, v85\n" \
  "v_mul_f32 v56, v56, v84\n" \
  "v_mul_f32 v57, v57, v85\n" \
  "v_cvt_pk_bf16_f32 v52, 0, v52\n" \
  "v_cvt_pk_bf16_f32 v53, 0, v53\n" \
  "v_cvt_pk_bf16_f32 v54, 0, v54\n" \
  "v_cvt_pk_bf16_f32 v55, 0, v55\n" \
  "v_cvt_pk_bf16_f32 v56, 0, v56\n" \
  "v_cvt_pk_bf16_f32 v57, 0, v57\n" \
  "v_mul_f32 v52, v52, v96\n" \
  "v_mul_f32 v53, v53, v96\n" \
  "v_mul_f32 v54, v54, v98\n" \
  "v_mul_f32 v55, v55, v98\n" \
  "v_mul_f32 v56, v56, v100\n" \
  "v_mul_f32 v57, v57, v100\n" \
  "v_cvt_pk_bf16_f32 v52, 0, v52\n" \
  "v_cvt_pk_bf16_f32 v53, 0, v53\n" \
  "v_cvt_pk_bf16_f32 v54, 0, v54\n" \
  "v_cvt_pk_bf16_f32 v55, 0, v55\n" \
  "v_cvt_pk_bf16_f32 v56, 0, v56\n" \
  "v_cvt_pk_bf16_f32 v57, 0, v57\n" \
  "v_add_f32 v52, v52, v97\n" \
  "v_add_f32 v53, v53, v97\n" \
  "v_add_f32 v54, v54, v99\n" \
  "v_add_f32 v55, v55, v99\n" \
  "v_add_f32 v56, v56, v101\n" \
  "v_add_f32 v57, v57, v101\n" \
  "v_cvt_pk_bf16_f32 v52, 0, v52\n" \
  "v_cvt_pk_bf16_f32 v53, 0, v53\n" \
  "v_cvt_pk_bf16_f32 v54, 0, v54\n" \
  "v_cvt_pk_bf16_f32 v55, 0, v55\n" \
  "v_cvt_pk_bf16_f32 v56, 0, v56\n" \
  "v_cvt_pk_bf16_f32 v57, 0, v57\n" \
  "v_cvt_pkrtz_f16_f32 v102, v53, v52\n" \
  "v_cvt_pkrtz_f16_f32 v103, v55, v54\n" \
  "v_cvt_pkrtz_f16_f32 v104, v57, v56\n" \
  "ds_write_b32 v105, v102 offset:1024\n" \
  "ds_write_b32 v105, v103 offset:9216\n" \
  "ds_write_b32 v105, v104 offset:17408\n" \
  "s_waitcnt vmcnt(1)\n" \
  "v_cmp_ne_u32 vcc, s44, v91\n" \
  "v_cmp_ne_u32 s[94:95], s44, v95\n" \
  "s_or_b64 vcc, vcc, s[94:95]\n" \
  "s_cbranch_scc1 Lu3s%=\n" \
  "Lu3b%=:\n" \
  "v_lshlrev_b32 v102, 16, v94\n" \
  "v_and_b32 v103, 0xffff0000, v94\n" \
  "v_lshlrev_b32 v34, 16, v88\n" \
  "v_lshlrev_b32 v35, 16, v89\n" \
  "v_and_b32 v36, 0xffff0000, v88\n" \
  "v_and_b32 v37, 0xffff0000, v89\n" \
  "v_lshlrev_b32 v38, 16, v90\n" \
  "v_and_b32 v39, 0xffff0000, v90\n" \
  "v_sub_f32 v58, v34, v102\n" \
  "v_sub_f32 v59, v35, v103\n" \
  "v_sub_f32 v60, v36, v102\n" \
  "v_sub_f32 v61, v37, v103\n" \
  "v_sub_f32 v62, v38, v102\n" \
  "v_sub_f32 v63, v39, v103\n" \
  "v_cvt_pk_bf16_f32 v58, 0, v58\n" \
  "v_cvt_pk_bf16_f32 v59, 0, v59\n" \
  "v_cvt_pk_bf16_f32 v60, 0, v60\n" \
  "v_cvt_pk_bf16_f32 v61, 0, v61\n" \
  "v_cvt_pk_bf16_f32 v62, 0, v62\n" \
  "v_cvt_pk_bf16_f32 v63, 0, v63\n" \
  "v_mul_f32 v58, v58, v92\n" \
  "v_mul_f32 v59, v59, v93\n" \
  "v_mul_f32 v60, v60, v92\n" \
  "v_mul_f32 v61, v61, v93\n" \
  "v_mul_f32 v62, v62, v92\n" \
  "v_mul_f32 v63, v63, v93\n" \
  "v_cvt_pk_bf16_f32 v58, 0, v58\n" \
  "v_cvt_pk_bf16_f32 v59, 0, v59\n" \
  "v_cvt_pk_bf16_f32 v60, 0, v60\n" \
  "v_cvt_pk_bf16_f32 v61, 0, v61\n" \
  "v_cvt_pk_bf16_f32 v62, 0, v62\n" \
  "v_cvt_pk_bf16_f32 v63, 0, v63\n" \
  "v_mul_f32 v58, v58, v96\n" \
  "v_mul_f32 v59, v59, v96\n" \
  "v_mul_f32 v60, v60, v98\n" \
  "v_mul_f32 v61, v61, v98\n" \
  "v_mul_f32 v62, v62, v100\n" \
  "v_mul_f32 v63, v63, v100\n" \
  "v_cvt_pk_bf16_f32 v58, 0, v58\n" \
  "v_cvt_pk_bf16_f32 v59, 0, v59\n" \
  "v_cvt_pk_bf16_f32 v60, 0, v60\n" \
  "v_cvt_pk_bf16_f32 v61, 0, v61\n" \
  "v_cvt_pk_bf16_f32 v62, 0, v62\n" \
  "v_cvt_pk_bf16_f32 v63, 0, v63\n" \
  "v_add_f32 v58, v58, v97\n" \
  "v_add_f32 v59, v59, v97\n" \
  "v_add_f32 v60, v60, v99\n" \
  "v_add_f32 v61, v61, v99\n" \
  "v_add_f32 v62, v62, v101\n" \
  "v_add_f32 v63, v63, v101\n" \
  "v_cvt_pk_bf16_f32 v58, 0, v58\n" \
  "v_cvt_pk_bf16_f32 v59, 0, v59\n" \
  "v_cvt_pk_bf16_f32 v60, 0, v60\n" \
  "v_cvt_pk_bf16_f32 v61, 0, v61\n" \
  "v_cvt_pk_bf16_f32 v62, 0, v62\n" \
  "v_cvt_pk_bf16_f32 v63, 0, v63\n" \
  "v_cvt_pkrtz_f16_f32 v102, v59, v58\n" \
  "v_cvt_pkrtz_f16_f32 v103, v61, v60\n" \
  "v_cvt_pkrtz_f16_f32 v104, v63, v62\n" \
  "ds_write_b32 v105, v102 offset:0\n" \
  "ds_write_b32 v105, v103 offset:8192\n" \
  "ds_write_b32 v105, v104 offset:16384\n" \
  "ds_add_u32 v111, v112 offset:4\n" \
  SSTAMP9 \
  "s_setprio " STR(PRE_) "\n" \
  "s_waitcnt vmcnt(0)\n" \
  "v_cmp_ne_u32 vcc, s44, v119\n" \
  "s_cmp_lg_u64 vcc, 0\n" \
  "s_cbranch_scc1 Las%=\n" \
  "Lab%=:\n" \
  "v_mul_f32 v118, v118, v110\n" \
  "v_mov_b32 v90, 158224\n" \
  "v_mov_b32 v85, s44\n" \
  "v_add_f32_dpp v118, v118, v118 quad_perm:[1,0,3,2] row_mask:0xf bank_mask:0xf bound_ctrl:1\n" \
  "s_nop 1\n" \
  "v_add_f32_dpp v118, v118, v118 quad_perm:[2,3,0,1] row_mask:0xf bank_mask:0xf bound_ctrl:1\n" \
  "v_cvt_pk_bf16_f32 v118, 0, v118\n" \
  "s_nop 0\n" \
  "v_readlane_b32 s94, v118, 0\n" \
  "v_readlane_b32 s95, v118, 8\n" \
  "v_readlane_b32 s96, v118, 16\n" \
  "v_mov_b32 v77, s94\n" \
  "v_mov_b32 v79, s95\n" \
  "v_mov_b32 v81, s96\n" \
  "ds_read_b32 v86, v111\n" \
  "ds_read_b64 v[64:65], v115 offset:0\n" \
  "ds_read_b64 v[66:67], v115 offset:32096\n" \
  "ds_read_b64 v[68:69], v116 offset:0\n" \
  "ds_read_b128 v[92:95], v90\n" \
  "v_mul_f32 v40, s84, v40\n" \
  "v_mul_f32 v41, s84, v41\n" \
  "v_mul_f32 v42, s85, v42\n" \
  "v_mul_f32 v43, s85, v43\n" \
  "v_mul_f32 v44, s86, v44\n" \
  "v_mul_f32 v45, s86, v45\n" \
  "v_cvt_pk_bf16_f32 v40, 0, v40\n" \
  "v_cvt_pk_bf16_f32 v41, 0, v41\n" \
  "v_cvt_pk_bf16_f32 v42, 0, v42\n" \
  "v_cvt_pk_bf16_f32 v43, 0, v43\n" \
  "v_cvt_pk_bf16_f32 v44, 0, v44\n" \
  "v_cvt_pk_bf16_f32 v45, 0, v45\n" \
  "s_waitcnt lgkmcnt(0)\n" \
  "v_readfirstlane_b32 s94, v86\n" \
  "s_lshr_b32 s94, s94, s45\n" \
  "s_bfe_u32 s94, s94, 0x40000\n" \
  "s_cmp_lg_u32 s94, 3\n" \
  "s_cbranch_scc1 Le0s%=\n" \
  "Le0b%=:\n" \
  "v_mul_f32 v76, s87, v92\n" \
  "v_mul_f32 v78, s88, v93\n" \
  "v_mul_f32 v80, s89, v94\n" \
  "v_mul_f32 v64, v64, v76\n" \
  "v_mul_f32 v65, v65, v76\n" \
  "v_mul_f32 v66, v66, v78\n" \
  "v_mul_f32 v67, v67, v78\n" \
  "v_mul_f32 v68, v68, v80\n" \
  "v_mul_f32 v69, v69, v80\n" \
  "v_cvt_pk_bf16_f32 v64, 0, v64\n" \
  "v_cvt_pk_bf16_f32 v65, 0, v65\n" \
  "v_cvt_pk_bf16_f32 v66, 0, v66\n" \
  "v_cvt_pk_bf16_f32 v67, 0, v67\n" \
  "v_cvt_pk_bf16_f32 v68, 0, v68\n" \
  "v_cvt_pk_bf16_f32 v69, 0, v69\n" \
  "v_add_f32 v64, v64, v40\n" \
  "v_add_f32 v65, v65, v41\n" \
  "v_add_f32 v66, v66, v42\n" \
  "v_add_f32 v67, v67, v43\n" \
  "v_add_f32 v68, v68, v44\n" \
  "v_add_f32 v69, v69, v45\n" \
  "v_cvt_pk_bf16_f32 v64, 0, v64\n" \
  "v_cvt_pk_bf16_f32 v65, 0, v65\n" \
  "v_cvt_pk_bf16_f32 v66, 0, v66\n" \
  "v_cvt_pk_bf16_f32 v67, 0, v67\n" \
  "v_cvt_pk_bf16_f32 v68, 0, v68\n" \
  "v_cvt_pk_bf16_f32 v69, 0, v69\n" \
  "v_mul_f32 v64, v64, v77\n" \
  "v_mul_f32 v65, v65, v77\n" \
  "v_mul_f32 v66, v66, v79\n" \
  "v_mul_f32 v67, v67, v79\n" \
  "v_mul_f32 v68, v68, v81\n" \
  "v_mul_f32 v69, v69, v81\n" \
  "v_cvt_pk_bf16_f32 v64, 0, v64\n" \
  "v_cvt_pk_bf16_f32 v65, 0, v65\n" \
  "v_cvt_pk_bf16_f32 v66, 0, v66\n" \
  "v_cvt_pk_bf16_f32 v67, 0, v67\n" \
  "v_cvt_pk_bf16_f32 v68, 0, v68\n" \
  "v_cvt_pk_bf16_f32 v69, 0, v69\n" \
  "v_add_f32 v64, v64, v16\n" \
  "v_add_f32 v65, v65, v17\n" \
  "v_add_f32 v66, v66, v18\n" \
  "v_add_f32 v67, v67, v19\n" \
  "v_add_f32 v68, v68, v20\n" \
  "v_add_f32 v69, v69, v21\n" \
  "v_cvt_pk_bf16_f32 v82, v64, v66\n" \
  "v_cvt_pk_bf16_f32 v83, v65, v67\n" \
  "v_cvt_pk_bf16_f32 v84, v68, v69\n" \
  "s_movk_i32 s94, 0\n" \
  "buffer_store_dwordx4 v[82:85], v117, s[40:43], s94 offen sc1\n" \
  "ds_read_b32 v86, v111\n" \
  "ds_read_b64 v[64:65], v115 offset:2560\n" \
  "ds_read_b64 v[70:71], v115 offset:10240\n" \
  "ds_read_b64 v[66:67], v115 offset:34656\n" \
  "ds_read_b64 v[72:73], v115 offset:42336\n" \
  "ds_read_b64 v[68:69], v116 offset:2560\n" \
  "ds_read_b64 v[74:75], v116 offset:10240\n" \
  "v_mul_f32 v46, s84, v46\n" \
  "v_mul_f32 v47, s84, v47\n" \
  "v_mul_f32 v48, s85, v48\n" \
  "v_mul_f32 v49, s85, v49\n" \
  "v_mul_f32 v50, s86, v50\n" \
  "v_mul_f32 v51, s86, v51\n" \
  "v_cvt_pk_bf16_f32 v46, 0, v46\n" \
  "v_cvt_pk_bf16_f32 v47, 0, v47\n" \
  "v_cvt_pk_bf16_f32 v48, 0, v48\n" \
  "v_cvt_pk_bf16_f32 v49, 0, v49\n" \
  "v_cvt_pk_bf16_f32 v50, 0, v50\n" \
  "v_cvt_pk_bf16_f32 v51, 0, v51\n" \
  "s_waitcnt lgkmcnt(0)\n" \
  "v_readfirstlane_b32 s94, v86\n" \
  "s_lshr_b32 s94, s94, s45\n" \
  "s_bfe_u32 s94, s94, 0x40008\n" \
  "s_cmp_lg_u32 s94, 6\n" \
  "s_cbranch_scc1 Le1s%=\n" \
  "Le1b%=:\n" \
  "v_add_f32 v64, v64, v70\n" \
  "v_add_f32 v65, v65, v71\n" \
  "v_add_f32 v66, v66, v72\n" \
  "v_add_f32 v67, v67, v73\n" \
  "v_add_f32 v68, v68, v74\n" \
  "v_add_f32 v69, v69, v75\n" \
  "v_mul_f32 v64, v64, v76\n" \
  "v_mul_f32 v65, v65, v76\n" \
  "v_mul_f32 v66, v66, v78\n" \
  "v_mul_f32 v67, v67, v78\n" \
  "v_mul_f32 v68, v68, v80\n" \
  "v_mul_f32 v69, v69, v80\n" \
  "v_cvt_pk_bf16_f32 v64, 0, v64\n" \
  "v_cvt_pk_bf16_f32 v65, 0, v65\n" \
  "v_cvt_pk_bf16_f32 v66, 0, v66\n" \
  "v_cvt_pk_bf16_f32 v67, 0, v67\n" \
  "v_cvt_pk_bf16_f32 v68, 0, v68\n" \
  "v_cvt_pk_bf16_f32 v69, 0, v69\n" \
  "v_add_f32 v64, v64, v46\n" \
  "v_add_f32 v65, v65, v47\n" \
  "v_add_f32 v66, v66, v48\n" \
  "v_add_f32 v67, v67, v49\n" \
  "v_add_f32 v68, v68, v50\n" \
  "v_add_f32 v69, v69, v51\n" \
  "v_cvt_pk_bf16_f32 v64, 0, v64\n" \
  "v_cvt_pk_bf16_f32 v65, 0, v65\n" \
  "v_cvt_pk_bf16_f32 v66, 0, v66\n" \
  "v_cvt_pk_bf16_f32 v67, 0, v67\n" \
  "v_cvt_pk_bf16_f32 v68, 0, v68\n" \
  "v_cvt_pk_bf16_f32 v69, 0, v69\n" \
  "v_mul_f32 v64, v64, v77\n" \
  "v_mul_f32 v65, v65, v77\n" \
  "v_mul_f32 v66, v66, v79\n" \
  "v_mul_f32 v67, v67, v79\n" \
  "v_mul_f32 v68, v68, v81\n" \
  "v_mul_f32 v69, v69, v81\n" \
  "v_cvt_pk_bf16_f32 v64, 0, v64\n" \
  "v_cvt_pk_bf16_f32 v65, 0, v65\n" \
  "v_cvt_pk_bf16_f32 v66, 0, v66\n" \
  "v_cvt_pk_bf16_f32 v67, 0, v67\n" \
  "v_cvt_pk_bf16_f32 v68, 0, v68\n" \
  "v_cvt_pk_bf16_f32 v69, 0, v69\n" \
  "v_add_f32 v64, v64, v22\n" \
  "v_add_f32 v65, v65, v23\n" \
  "v_add_f32 v66, v66, v24\n" \
  "v_add_f32 v67, v67, v25\n" \
  "v_add_f32 v68, v68, v26\n" \
  "v_add_f32 v69, v69, v27\n" \
  "v_cvt_pk_bf16_f32 v82, v64, v66\n" \
  "v_cvt_pk_bf16_f32 v83, v65, v67\n" \
  "v_cvt_pk_bf16_f32 v84, v68, v69\n" \
  "s_movk_i32 s94, 4096\n" \
  "buffer_store_dwordx4 v[82:85], v117, s[40:43], s94 offen sc1\n" \
  SSTAMPE1 \
  "ds_read_b32 v86, v111\n" \
  "ds_read_b64 v[64:65], v115 offset:5120\n" \
  "ds_read_b64 v[70:71], v115 offset:12800\n" \
  "ds_read_b64 v[66:67], v115 offset:37216\n" \
  "ds_read_b64 v[72:73], v115 offset:44896\n" \
  "ds_read_b64 v[68:69], v116 offset:5120\n" \
  "ds_read_b64 v[74:75], v116 offset:12800\n" \
  "v_mul_f32 v52, s84, v52\n" \
  "v_mul_f32 v53, s84, v53\n" \
  "v_mul_f32 v54, s85, v54\n" \
  "v_mul_f32 v55, s85, v55\n" \
  "v_mul_f32 v56, s86, v56\n" \
  "v_mul_f32 v57, s86, v57\n" \
  "v_cvt_pk_bf16_f32 v52, 0, v52\n" \
  "v_cvt_pk_bf16_f32 v53, 0, v53\n" \
  "v_cvt_pk_bf16_f32 v54, 0, v54\n" \
  "v_cvt_pk_bf16_f32 v55, 0, v55\n" \
  "v_cvt_pk_bf16_f32 v56, 0, v56\n" \
  "v_cvt_pk_bf16_f32 v57, 0, v57\n" \
  "s_waitcnt lgkmcnt(0)\n" \
  "v_readfirstlane_b32 s94, v86\n" \
  "s_lshr_b32 s94, s94, s45\n" \
  "s_bfe_u32 s94, s94, 0x40010\n" \
  "s_cmp_lg_u32 s94, 6\n" \
  "s_cbranch_scc1 Le2s%=\n" \
  "Le2b%=:\n" \
  "v_add_f32 v64, v64, v70\n" \
  "v_add_f32 v65, v65, v71\n" \
  "v_add_f32 v66, v66, v72\n" \
  "v_add_f32 v67, v67, v73\n" \
  "v_add_f32 v68, v68, v74\n" \
  "v_add_f32 v69, v69, v75\n" \
  "v_mul_f32 v64, v64, v76\n" \
  "v_mul_f32 v65, v65, v76\n" \
  "v_mul_f32 v66, v66, v78\n" \
  "v_mul_f32 v67, v67, v78\n" \
  "v_mul_f32 v68, v68, v80\n" \
  "v_mul_f32 v69, v69, v80\n" \
  "v_cvt_pk_bf16_f32 v64, 0, v64\n" \
  "v_cvt_pk_bf16_f32 v65, 0, v65\n" \
  "v_cvt_pk_bf16_f32 v66, 0, v66\n" \
  "v_cvt_pk_bf16_f32 v67, 0, v67\n" \
  "v_cvt_pk_bf16_f32 v68, 0, v68\n" \
  "v_cvt_pk_bf16_f32 v69, 0, v69\n" \
  "v_add_f32 v64, v64, v52\n" \
  "v_add_f32 v65, v65, v53\n" \
  "v_add_f32 v66, v66, v54\n" \
  "v_add_f32 v67, v67, v55\n" \
  "v_add_f32 v68, v68, v56\n" \
  "v_add_f32 v69, v69, v57\n" \
  "v_cvt_pk_bf16_f32 v64, 0, v64\n" \
  "v_cvt_pk_bf16_f32 v65, 0, v65\n" \
  "v_cvt_pk_bf16_f32 v66, 0, v66\n" \
  "v_cvt_pk_bf16_f32 v67, 0, v67\n" \
  "v_cvt_pk_bf16_f32 v68, 0, v68\n" \
  "v_cvt_pk_bf16_f32 v69, 0, v69\n" \
  "v_mul_f32 v64, v64, v77\n" \
  "v_mul_f32 v65, v65, v77\n" \
  "v_mul_f32 v66, v66, v79\n" \
  "v_mul_f32 v67, v67, v79\n" \
  "v_mul_f32 v68, v68, v81\n" \
  "v_mul_f32 v69, v69, v81\n" \
  "v_cvt_pk_bf16_f32 v64, 0, v64\n" \
  "v_cvt_pk_bf16_f32 v65, 0, v65\n" \
  "v_cvt_pk_bf16_f32 v66, 0, v66\n" \
  "v_cvt_pk_bf16_f32 v67, 0, v67\n" \
  "v_cvt_pk_bf16_f32 v68, 0, v68\n" \
  "v_cvt_pk_bf16_f32 v69, 0, v69\n" \
  "v_add_f32 v64, v64, v28\n" \
  "v_add_f32 v65, v65, v29\n" \
  "v_add_f32 v66, v66, v30\n" \
  "v_add_f32 v67, v67, v31\n" \
  "v_add_f32 v68, v68, v32\n" \
  "v_add_f32 v69, v69, v33\n" \
  "v_cvt_pk_bf16_f32 v82, v64, v66\n" \
  "v_cvt_pk_bf16_f32 v83, v65, v67\n" \
  "v_cvt_pk_bf16_f32 v84, v68, v69\n" \
  "s_movk_i32 s94, 8192\n" \
  "buffer_store_dwordx4 v[82:85], v117, s[40:43], s94 offen sc1\n" \
  "ds_read_b32 v86, v111\n" \
  "ds_read_b64 v[64:65], v115 offset:7680\n" \
  "ds_read_b64 v[70:71], v115 offset:15360\n" \
  "ds_read_b64 v[66:67], v115 offset:39776\n" \
  "ds_read_b64 v[72:73], v115 offset:47456\n" \
  "ds_read_b64 v[68:69], v116 offset:7680\n" \
  "ds_read_b64 v[74:75], v116 offset:15360\n" \
  "v_mul_f32 v58, s84, v58\n" \
  "v_mul_f32 v59, s84, v59\n" \
  "v_mul_f32 v60, s85, v60\n" \
  "v_mul_f32 v61, s85, v61\n" \
  "v_mul_f32 v62, s86, v62\n" \
  "v_mul_f32 v63, s86, v63\n" \
  "v_cvt_pk_bf16_f32 v58, 0, v58\n" \
  "v_cvt_pk_bf16_f32 v59, 0, v59\n" \
  "v_cvt_pk_bf16_f32 v60, 0, v60\n" \
  "v_cvt_pk_bf16_f32 v61, 0, v61\n" \
  "v_cvt_pk_bf16_f32 v62, 0, v62\n" \
  "v_cvt_pk_bf16_f32 v63, 0, v63\n" \
  "s_waitcnt lgkmcnt(0)\n" \
  "v_readfirstlane_b32 s94, v86\n" \
  "s_lshr_b32 s94, s94, s45\n" \
  "s_bfe_u32 s94, s94, 0x40018\n" \
  "s_cmp_lg_u32 s94, 6\n" \
  "s_cbranch_scc1 Le3s%=\n" \
  "Le3b%=:\n" \
  SSTAMP10 \
  "v_pk_add_f32 v[64:65], v[64:65], v[70:71]\n" \
  "v_pk_add_f32 v[66:67], v[66:67], v[72:73]\n" \
  "v_pk_add_f32 v[68:69], v[68:69], v[74:75]\n" \
  "v_pk_mul_f32 v[64:65], v[64:65], v[76:77] op_sel_hi:[1,0]\n" \
  "v_pk_mul_f32 v[66:67], v[66:67], v[78:79] op_sel_hi:[1,0]\n" \
  "v_pk_mul_f32 v[68:69], v[68:69], v[80:81] op_sel_hi:[1,0]\n" \
  "v_cvt_pk_bf16_f32 v64, 0, v64\n" \
  "v_cvt_pk_bf16_f32 v65, 0, v65\n" \
  "v_cvt_pk_bf16_f32 v66, 0, v66\n" \
  "v_cvt_pk_bf16_f32 v67, 0, v67\n" \
  "v_cvt_pk_bf16_f32 v68, 0, v68\n" \
  "v_cvt_pk_bf16_f32 v69, 0, v69\n" \
  "v_pk_add_f32 v[64:65], v[64:65], v[58:59]\n" \
  "v_pk_add_f32 v[66:67], v[66:67], v[60:61]\n" \
  "v_pk_add_f32 v[68:69], v[68:69], v[62:63]\n" \
  "v_cvt_pk_bf16_f32 v64, 0, v64\n" \
  "v_cvt_pk_bf16_f32 v65, 0, v65\n" \
  "v_cvt_pk_bf16_f32 v66, 0, v66\n" \
  "v_cvt_pk_bf16_f32 v67, 0, v67\n" \
  "v_cvt_pk_bf16_f32 v68, 0, v68\n" \
  "v_cvt_pk_bf16_f32 v69, 0, v69\n" \
  "v_pk_mul_f32 v[64:65], v[64:65], v[76:77] op_sel:[0,1]\n" \
  "v_pk_mul_f32 v[66:67], v[66:67], v[78:79] op_sel:[0,1]\n" \
  "v_pk_mul_f32 v[68:69], v[68:69], v[80:81] op_sel:[0,1]\n" \
  "v_cvt_pk_bf16_f32 v64, 0, v64\n" \
  "v_cvt_pk_bf16_f32 v65, 0, v65\n" \
  "v_cvt_pk_bf16_f32 v66, 0, v66\n" \
  "v_cvt_pk_bf16_f32 v67, 0, v67\n" \
  "v_cvt_pk_bf16_f32 v68, 0, v68\n" \
  "v_cvt_pk_bf16_f32 v69, 0, v69\n" \
  "v_pk_add_f32 v[64:65], v[64:65], v[34:35]\n" \
  "v_pk_add_f32 v[66:67], v[66:67], v[36:37]\n" \
  "v_pk_add_f32 v[68:69], v[68:69], v[38:39]\n" \
  "v_cvt_pk_bf16_f32 v82, v64, v66\n" \
  "v_cvt_pk_bf16_f32 v83, v65, v67\n" \
  "v_cvt_pk_bf16_f32 v84, v68, v69\n" \
  "s_movk_i32 s94, 12288\n" \
  "buffer_store_dwordx4 v[82:85], v117, s[40:43], s94 offen sc1\n" \
  "s_branch Lhe%=\n" \
  "Lms%=:\n" \
  "s_mov_b32 s97, 0x7fff\n" \
  "Lmr%=:\n" \
  SSTAMPCm \
  "s_mov_b64 exec, vcc\n" \
  "buffer_load_dwordx2 v[102:103], v108, s[40:43], 0 offen sc1\n" \
  "s_mov_b64 exec, -1\n" \
  "s_waitcnt vmcnt(0)\n" \
  "v_cmp_ne_u32 vcc, s44, v103\n" \
  "s_cmp_lg_u64 vcc, 0\n" \
  "s_cbranch_scc0 Lmb%=\n" \
  "s_sub_u32 s97, s97, 1\n" \
  "s_cbranch_scc0 Lmr%=\n" \
  "s_branch Lmb%=\n" \
  "Lu0s%=:\n" \
  "s_mov_b32 s97, 0x7fff\n" \
  "Lu0r%=:\n" \
  SSTAMPCu0 \
  "s_mov_b64 exec, vcc\n" \
  "s_mov_b32 s94, 0\n" \
  "s_mov_b32 s95, 0\n" \
  "buffer_load_dwordx4 v[64:67], v106, s[40:43], s94 offen sc1\n" \
  "buffer_load_dwordx4 v[68:71], v107, s[40:43], s95 offen sc1\n" \
  "s_mov_b64 exec, -1\n" \
  "s_waitcnt vmcnt(0)\n" \
  "v_cmp_ne_u32 vcc, s44, v67\n" \
  "v_cmp_ne_u32 s[94:95], s44, v71\n" \
  "s_or_b64 vcc, vcc, s[94:95]\n" \
  "s_cbranch_scc0 Lu0b%=\n" \
  "s_sub_u32 s97, s97, 1\n" \
  "s_cbranch_scc0 Lu0r%=\n" \
  "s_branch Lu0b%=\n" \
  "Lu1s%=:\n" \
  "s_mov_b32 s97, 0x7fff\n" \
  "Lu1r%=:\n" \
  SSTAMPCu1 \
  "s_mov_b64 exec, vcc\n" \
  "s_mov_b32 s94, 790528\n" \
  "s_mov_b32 s95, 4096\n" \
  "buffer_load_dwordx4 v[72:75], v106, s[40:43], s94 offen sc1\n" \
  "buffer_load_dwordx4 v[76:79], v107, s[40:43], s95 offen sc1\n" \
  "s_mov_b64 exec, -1\n" \
  "s_waitcnt vmcnt(0)\n" \
  "v_cmp_ne_u32 vcc, s44, v75\n" \
  "v_cmp_ne_u32 s[94:95], s44, v79\n" \
  "s_or_b64 vcc, vcc, s[94:95]\n" \
  "s_cbranch_scc0 Lu1b%=\n" \
  "s_sub_u32 s97, s97, 1\n" \
  "s_cbranch_scc0 Lu1r%=\n" \
  "s_branch Lu1b%=\n" \
  "Lu2s%=:\n" \
  "s_mov_b32 s97, 0x7fff\n" \
  "Lu2r%=:\n" \
  SSTAMPCu2 \
  "s_mov_b64 exec, vcc\n" \
  "s_mov_b32 s94, 1581056\n" \
  "s_mov_b32 s95, 8192\n" \
  "buffer_load_dwordx4 v[80:83], v106, s[40:43], s94 offen sc1\n" \
  "buffer_load_dwordx4 v[84:87], v107, s[40:43], s95 offen sc1\n" \
  "s_mov_b64 exec, -1\n" \
  "s_waitcnt vmcnt(0)\n" \
  "v_cmp_ne_u32 vcc, s44, v83\n" \
  "v_cmp_ne_u32 s[94:95], s44, v87\n" \
  "s_or_b64 vcc, vcc, s[94:95]\n" \
  "s_cbranch_scc0 Lu2b%=\n" \
  "s_sub_u32 s97, s97, 1\n" \
  "s_cbranch_scc0 Lu2r%=\n" \
  "s_branch Lu2b%=\n" \
  "Lu3s%=:\n" \
  "s_mov_b32 s97, 0x7fff\n" \
  "Lu3r%=:\n" \
  SSTAMPCu3 \
  "s_mov_b64 exec, vcc\n" \
  "s_mov_b32 s94, 2371584\n" \
  "s_mov_b32 s95, 12288\n" \
  "buffer_load_dwordx4 v[88:91], v106, s[40:43], s94 offen sc1\n" \
  "buffer_load_dwordx4 v[92:95], v107, s[40:43], s95 offen sc1\n" \
  "s_mov_b64 exec, -1\n" \
  "s_waitcnt vmcnt(0)\n" \
  "v_cmp_ne_u32 vcc, s44, v91\n" \
  "v_cmp_ne_u32 s[94:95], s44, v95\n" \
  "s_or_b64 vcc, vcc, s[94:95]\n" \
  "s_cbranch_scc0 Lu3b%=\n" \
  "s_sub_u32 s97, s97, 1\n" \
  "s_cbranch_scc0 Lu3r%=\n" \
  "s_branch Lu3b%=\n" \
  "Las%=:\n" \
  "s_mov_b32 s97, 0x7fff\n" \
  "Lar%=:\n" \
  SSTAMPCa \
  "s_mov_b64 exec, vcc\n" \
  "buffer_load_dwordx2 v[118:119], v114, s[40:43], 0 offen sc1\n" \
  "s_mov_b64 exec, -1\n" \
  "s_waitcnt vmcnt(0)\n" \
  "v_cmp_ne_u32 vcc, s44, v119\n" \
  "s_cmp_lg_u64 vcc, 0\n" \
  "s_cbranch_scc0 Lab%=\n" \
  "s_sub_u32 s97, s97, 1\n" \
  "s_cbranch_scc0 Lar%=\n" \
  "s_branch Lab%=\n" \
  "Le0s%=:\n" \
  "s_mov_b32 s97, 0x1fffff\n" \
  "Le0r%=:\n" \
  "s_sleep 1\n" \
  "ds_read_b32 v86, v111\n" \
  "s_waitcnt lgkmcnt(0)\n" \
  "v_readfirstlane_b32 s94, v86\n" \
  "s_lshr_b32 s94, s94, s45\n" \
  "s_bfe_u32 s94, s94, 0x40000\n" \
  "s_cmp_lg_u32 s94, 3\n" \
  "s_cbranch_scc0 Le0x%=\n" \
  "s_sub_u32 s97, s97, 1\n" \
  "s_cbranch_scc0 Le0r%=\n" \
  "Le0x%=:\n" \
  "ds_read_b64 v[64:65], v115 offset:0\n" \
  "ds_read_b64 v[66:67], v115 offset:32096\n" \
  "ds_read_b64 v[68:69], v116 offset:0\n" \
  "ds_read_b128 v[92:95], v90\n" \
  "s_waitcnt lgkmcnt(0)\n" \
  "s_branch Le0b%=\n" \
  "Le1s%=:\n" \
  "s_mov_b32 s97, 0x1fffff\n" \
  "Le1r%=:\n" \
  "s_sleep 1\n" \
  "ds_read_b32 v86, v111\n" \
  "s_waitcnt lgkmcnt(0)\n" \
  "v_readfirstlane_b32 s94, v86\n" \
  "s_lshr_b32 s94, s94, s45\n" \
  "s_bfe_u32 s94, s94, 0x40008\n" \
  "s_cmp_lg_u32 s94, 6\n" \
  "s_cbranch_scc0 Le1x%=\n" \
  "s_sub_u32 s97, s97, 1\n" \
  "s_cbranch_scc0 Le1r%=\n" \
  "Le1x%=:\n" \
  "ds_read_b64 v[64:65], v115 offset:2560\n" \
  "ds_read_b64 v[70:71], v115 offset:10240\n" \
  "ds_read_b64 v[66:67], v115 offset:34656\n" \
  "ds_read_b64 v[72:73], v115 offset:42336\n" \
  "ds_read_b64 v[68:69], v116 offset:2560\n" \
  "ds_read_b64 v[74:75], v116 offset:10240\n" \
  "s_waitcnt lgkmcnt(0)\n" \
  "s_branch Le1b%=\n" \
  "Le2s%=:\n" \
  "s_mov_b32 s97, 0x1fffff\n" \
  "Le2r%=:\n" \
  "s_sleep 1\n" \
  "ds_read_b32 v86, v111\n" \
  "s_waitcnt lgkmcnt(0)\n" \
  "v_readfirstlane_b32 s94, v86\n" \
  "s_lshr_b32 s94, s94, s45\n" \
  "s_bfe_u32 s94, s94, 0x40010\n" \
  "s_cmp_lg_u32 s94, 6\n" \
  "s_cbranch_scc0 Le2x%=\n" \
  "s_sub_u32 s97, s97, 1\n" \
  "s_cbranch_scc0 Le2r%=\n" \
  "Le2x%=:\n" \
  "ds_read_b64 v[64:65], v115 offset:5120\n" \
  "ds_read_b64 v[70:71], v115 offset:12800\n" \
  "ds_read_b64 v[66:67], v115 offset:37216\n" \
  "ds_read_b64 v[72:73], v115 offset:44896\n" \
  "ds_read_b64 v[68:69], v116 offset:5120\n" \
  "ds_read_b64 v[74:75], v116 offset:12800\n" \
  "s_waitcnt lgkmcnt(0)\n" \
  "s_branch Le2b%=\n" \
  "Le3s%=:\n" \
  "s_mov_b32 s97, 0x1fffff\n" \
  "Le3r%=:\n" \
  "ds_read_b32 v86, v111\n" \
  "ds_read_b64 v[64:65], v115 offset:7680\n" \
  "ds_read_b64 v[70:71], v115 offset:15360\n" \
  "ds_read_b64 v[66:67], v115 offset:39776\n" \
  "ds_read_b64 v[72:73], v115 offset:47456\n" \
  "ds_read_b64 v[68:69], v116 offset:7680\n" \
  "ds_read_b64 v[74:75], v116 offset:15360\n" \
  "s_waitcnt lgkmcnt(0)\n" \
  "v_readfirstlane_b32 s94, v86\n" \
  "s_lshr_b32 s94, s94, s45\n" \
  "s_bfe_u32 s94, s94, 0x40018\n" \
  "s_cmp_lg_u32 s94, 6\n" \
  "s_cbranch_scc0 Le3b%=\n" \
  "s_sub_u32 s97, s97, 1\n" \
  "s_cbranch_scc0 Le3r%=\n" \
  "s_branch Le3b%=\n" \
  "Lhe%=:\n" \
  SSTAMP11 \
  "s_setprio 0\n"

#define LW_ASM \
  "s_load_dwordx2 s[40:41], %[k], 0x70\n" \
  "s_load_dwordx2 s[46:47], %[k], 0x38\n" \
  "s_load_dwordx2 s[48:49], %[k], 0x68\n" \
  "v_mov_b32 v1, 158208\n" \
  "ds_read_b32 v2, v1\n" \
  "s_mov_b32 s42, 10403840\n" \
  "s_mov_b32 s43, 0x20000\n" \
  "v_and_b32 v3, 7, %[t]\n" \
  "v_and_b32 v4, 63, %[t]\n" \
  "s_waitcnt lgkmcnt(0)\n" \
  "v_readfirstlane_b32 s44, v2\n" \
  "s_cmp_ge_u32 %[w], 12\n" \
  "s_cbranch_scc1 Mls%=\n" \
  "s_lshr_b32 s50, 0x214, %[w]\n" \
  "s_bitcmp0_b32 s50, 0\n" \
  "s_cbranch_scc1 Mla%=\n" \
  "s_sleep " STR(PSLP) "\n" \
  "s_lshr_b32 s50, %[w], 2\n" \
  "s_mul_i32 s51, s50, 96\n" \
  "v_add_u32 v5, s51, v4\n" \
  "v_mov_b32 v92, 0\n" \
  "s_mov_b32 exec_hi, 0xffff\n" \
  "v_lshlrev_b32 v6, 4, v5\n" \
  "v_lshlrev_b32 v7, 2, v5\n" \
  "s_waitcnt vmcnt(0)\n" \
  "s_setprio " STR(PRP) "\n" \
  "global_load_dword v90, v7, s[46:47]\n" \
  "global_load_dword v93, v7, s[46:47] offset:192\n" \
  "s_mov_b32 s51, 17920\n" \
  "buffer_load_dwordx4 v[12:15], v6, s[40:43], s51 offen sc1\n" \
  "s_mov_b32 s51, 45568\n" \
  "buffer_load_dwordx4 v[16:19], v6, s[40:43], s51 offen sc1\n" \
  "s_mov_b32 s51, 73216\n" \
  "buffer_load_dwordx4 v[20:23], v6, s[40:43], s51 offen sc1\n" \
  "s_mov_b32 s51, 22528\n" \
  "buffer_load_dwordx4 v[24:27], v6, s[40:43], s51 offen sc1\n" \
  "s_mov_b32 s51, 50176\n" \
  "buffer_load_dwordx4 v[28:31], v6, s[40:43], s51 offen sc1\n" \
  "s_mov_b32 s51, 77824\n" \
  "buffer_load_dwordx4 v[32:35], v6, s[40:43], s51 offen sc1\n" \
  "s_mov_b32 s51, 27136\n" \
  "buffer_load_dwordx4 v[36:39], v6, s[40:43], s51 offen sc1\n" \
  "s_mov_b32 s51, 54784\n" \
  "buffer_load_dwordx4 v[40:43], v6, s[40:43], s51 offen sc1\n" \
  "s_mov_b32 s51, 82432\n" \
  "buffer_load_dwordx4 v[44:47], v6, s[40:43], s51 offen sc1\n" \
  "s_mov_b32 s51, 17920\n" \
  "buffer_load_dwordx4 v[48:51], v6, s[40:43], s51 offen offset:768 sc1\n" \
  "s_mov_b32 s51, 45568\n" \
  "buffer_load_dwordx4 v[52:55], v6, s[40:43], s51 offen offset:768 sc1\n" \
  "s_mov_b32 s51, 73216\n" \
  "buffer_load_dwordx4 v[56:59], v6, s[40:43], s51 offen offset:768 sc1\n" \
  "s_mov_b32 s51, 22528\n" \
  "buffer_load_dwordx4 v[60:63], v6, s[40:43], s51 offen offset:768 sc1\n" \
  "s_mov_b32 s51, 50176\n" \
  "buffer_load_dwordx4 v[64:67], v6, s[40:43], s51 offen offset:768 sc1\n" \
  "s_mov_b32 s51, 77824\n" \
  "buffer_load_dwordx4 v[68:71], v6, s[40:43], s51 offen offset:768 sc1\n" \
  "s_mov_b32 s51, 27136\n" \
  "buffer_load_dwordx4 v[72:75], v6, s[40:43], s51 offen offset:768 sc1\n" \
  "s_mov_b32 s51, 54784\n" \
  "buffer_load_dwordx4 v[76:79], v6, s[40:43], s51 offen offset:768 sc1\n" \
  "s_mov_b32 s51, 82432\n" \
  "buffer_load_dwordx4 v[80:83], v6, s[40:43], s51 offen offset:768 sc1\n" \
  "s_mov_b64 exec, -1\n" \
  "s_setprio 0\n" \
  "Mla%=:\n" \
  "v_lshrrev_b32 v95, 3, %[t]\n" \
  "v_mul_u32_u24 v94, 33024, v95\n" \
  "v_lshl_add_u32 v94, v3, 4, v94\n" \
  "s_lshl_b32 s58, %[b], 7\n" \
  "s_add_u32 s58, s58, 139264\n" \
  "s_add_u32 s59, s58, 16512\n" \
  LSTAMP8 \
  "s_barrier\n" \
  LSTAMP9 \
  "buffer_load_dwordx4 v[96:99], v94, s[40:43], s58 offen sc1\n" \
  "buffer_load_dwordx4 v[100:103], v94, s[40:43], s59 offen sc1\n" \
  "s_lshr_b32 s51, 0x214, %[w]\n" \
  "s_bitcmp0_b32 s51, 0\n" \
  "s_cbranch_scc1 Mlb%=\n" \
  "s_setprio 2\n" \
  "s_mov_b32 exec_hi, 0xffff\n" \
  "v_mov_b32 v8, 147456\n" \
  "v_lshl_add_u32 v7, v6, 1, v8\n" \
  "s_movk_i32 s97, 0x7fff\n" \
  "s_waitcnt vmcnt(11)\n" \
  "Mpr0%=:\n" \
  "v_bitop3_b32 v13, v13, v15, s44 bitop3:0x7e\n" \
  "v_bitop3_b32 v17, v17, v19, s44 bitop3:0x7e\n" \
  "v_bitop3_b32 v21, v21, v23, s44 bitop3:0x7e\n" \
  "v_bitop3_b32 v25, v25, v27, s44 bitop3:0x7e\n" \
  "v_bitop3_b32 v29, v29, v31, s44 bitop3:0x7e\n" \
  "v_bitop3_b32 v33, v33, v35, s44 bitop3:0x7e\n" \
  "v_bitop3_b32 v37, v37, v39, s44 bitop3:0x7e\n" \
  "v_bitop3_b32 v41, v41, v43, s44 bitop3:0x7e\n" \
  "v_bitop3_b32 v45, v45, v47, s44 bitop3:0x7e\n" \
  "v_or3_b32 v13, v13, v17, v21\n" \
  "v_or3_b32 v25, v25, v29, v33\n" \
  "v_or3_b32 v37, v37, v41, v45\n" \
  "v_or3_b32 v13, v13, v25, v37\n" \
  "v_cmp_ne_u32 vcc, 0, v13\n" \
  "s_cbranch_vccnz Mps0%=\n" \
  "Mpk0%=:\n" \
  "v_add_f32 v86, v12, v16\n" \
  "v_add_f32 v87, v14, v18\n" \
  "v_add_f32 v84, v24, v28\n" \
  "v_add_f32 v85, v26, v30\n" \
  "v_add_f32 v88, v36, v40\n" \
  "v_add_f32 v89, v38, v42\n" \
  "v_add_f32 v86, v86, v20\n" \
  "v_add_f32 v87, v87, v22\n" \
  "v_add_f32 v84, v84, v32\n" \
  "v_add_f32 v85, v85, v34\n" \
  "v_add_f32 v88, v88, v44\n" \
  "v_add_f32 v89, v89, v46\n" \
  "v_cvt_pk_bf16_f32 v84, 0, v84\n" \
  "v_cvt_pk_bf16_f32 v85, 0, v85\n" \
  "v_cvt_pk_bf16_f32 v86, 0, v86\n" \
  "v_cvt_pk_bf16_f32 v87, 0, v87\n" \
  "v_cvt_pk_bf16_f32 v88, v88, 0\n" \
  "v_cvt_pk_bf16_f32 v89, 0, v89\n" \
  "v_add_f32 v84, 1.0, v84\n" \
  "v_add_f32 v85, 1.0, v85\n" \
  "v_cvt_pk_bf16_f32 v84, 0, v84\n" \
  "v_cvt_pk_bf16_f32 v85, 0, v85\n" \
  "v_lshlrev_b32 v8, 16, v90\n" \
  "v_and_b32 v9, 0xffff0000, v90\n" \
  "v_mul_f32 v8, v8, v84\n" \
  "v_mul_f32 v9, v9, v85\n" \
  "v_cvt_pk_bf16_f32 v8, 0, v8\n" \
  "v_cvt_pk_bf16_f32 v9, 0, v9\n" \
  "v_add_f32 v92, v92, v8\n" \
  "v_add_f32 v92, v92, v9\n" \
  "v_lshrrev_b32 v8, 16, v8\n" \
  "v_or_b32 v91, v8, v9\n" \
  "ds_write_b128 v7, v[84:87] offset:0\n" \
  "ds_write_b128 v7, v[88:91] offset:16\n" \
  "s_movk_i32 s97, 0x7fff\n" \
  "s_waitcnt vmcnt(2)\n" \
  "Mpr1%=:\n" \
  "v_bitop3_b32 v49, v49, v51, s44 bitop3:0x7e\n" \
  "v_bitop3_b32 v53, v53, v55, s44 bitop3:0x7e\n" \
  "v_bitop3_b32 v57, v57, v59, s44 bitop3:0x7e\n" \
  "v_bitop3_b32 v61, v61, v63, s44 bitop3:0x7e\n" \
  "v_bitop3_b32 v65, v65, v67, s44 bitop3:0x7e\n" \
  "v_bitop3_b32 v69, v69, v71, s44 bitop3:0x7e\n" \
  "v_bitop3_b32 v73, v73, v75, s44 bitop3:0x7e\n" \
  "v_bitop3_b32 v77, v77, v79, s44 bitop3:0x7e\n" \
  "v_bitop3_b32 v81, v81, v83, s44 bitop3:0x7e\n" \
  "v_or3_b32 v49, v49, v53, v57\n" \
  "v_or3_b32 v61, v61, v65, v69\n" \
  "v_or3_b32 v73, v73, v77, v81\n" \
  "v_or3_b32 v49, v49, v61, v73\n" \
  "v_cmp_ne_u32 vcc, 0, v49\n" \
  "s_cbranch_vccnz Mps1%=\n" \
  "Mpk1%=:\n" \
  "v_add_f32 v86, v48, v52\n" \
  "v_add_f32 v87, v50, v54\n" \
  "v_add_f32 v84, v60, v64\n" \
  "v_add_f32 v85, v62, v66\n" \
  "v_add_f32 v88, v72, v76\n" \
  "v_add_f32 v89, v74, v78\n" \
  "v_add_f32 v86, v86, v56\n" \
  "v_add_f32 v87, v87, v58\n" \
  "v_add_f32 v84, v84, v68\n" \
  "v_add_f32 v85, v85, v70\n" \
  "v_add_f32 v88, v88, v80\n" \
  "v_add_f32 v89, v89, v82\n" \
  "v_mov_b32 v90, v93\n" \
  "v_cvt_pk_bf16_f32 v84, 0, v84\n" \
  "v_cvt_pk_bf16_f32 v85, 0, v85\n" \
  "v_cvt_pk_bf16_f32 v86, 0, v86\n" \
  "v_cvt_pk_bf16_f32 v87, 0, v87\n" \
  "v_cvt_pk_bf16_f32 v88, v88, 0\n" \
  "v_cvt_pk_bf16_f32 v89, 0, v89\n" \
  "v_add_f32 v84, 1.0, v84\n" \
  "v_add_f32 v85, 1.0, v85\n" \
  "v_cvt_pk_bf16_f32 v84, 0, v84\n" \
  "v_cvt_pk_bf16_f32 v85, 0, v85\n" \
  "v_lshlrev_b32 v8, 16, v90\n" \
  "v_and_b32 v9, 0xffff0000, v90\n" \
  "v_mul_f32 v8, v8, v84\n" \
  "v_mul_f32 v9, v9, v85\n" \
  "v_cvt_pk_bf16_f32 v8, 0, v8\n" \
  "v_cvt_pk_bf16_f32 v9, 0, v9\n" \
  "v_add_f32 v92, v92, v8\n" \
  "v_add_f32 v92, v92, v9\n" \
  "v_lshrrev_b32 v8, 16, v8\n" \
  "v_or_b32 v91, v8, v9\n" \
  "ds_write_b128 v7, v[84:87] offset:1536\n" \
  "ds_write_b128 v7, v[88:91] offset:1552\n" \
  "s_mov_b64 exec, -1\n" \
  "s_lshl_b32 s50, s50, 4\n" \
  "v_lshrrev_b32 v5, 2, v4\n" \
  "v_add_f32_dpp v92, v92, v92 quad_perm:[1,0,3,2] row_mask:0xf bank_mask:0xf bound_ctrl:1\n" \
  "v_and_b32 v5, 12, v5\n" \
  "v_add3_u32 v5, v5, v1, s50\n" \
  "v_add_f32_dpp v92, v92, v92 quad_perm:[2,3,0,1] row_mask:0xf bank_mask:0xf bound_ctrl:1\n" \
  "s_nop 1\n" \
  "v_add_f32_dpp v92, v92, v92 row_half_mirror row_mask:0xf bank_mask:0xf bound_ctrl:1\n" \
  "s_nop 1\n" \
  "v_add_f32_dpp v92, v92, v92 row_mirror row_mask:0xf bank_mask:0xf bound_ctrl:1\n" \
  "ds_write_b32 v5, v92 offset:32\n" \
  "s_setprio 0\n" \
  "Mlb%=:\n" \
  "v_mov_b32 v5, v95\n" \
  "v_mul_u32_u24 v7, 96, v5\n" \
  "v_add_u32 v7, 147456, v7\n" \
  "v_lshrrev_b32 v9, 5, v4\n" \
  "v_lshl_add_u32 v8, v3, 1, v9\n" \
  "v_mul_u32_u24 v10, 48, v8\n" \
  "s_lshl2_add_u32 s52, %[w], 124192\n" \
  "v_add_u32 v10, s52, v10\n" \
  "v_mul_u32_u24 v8, 208, v8\n" \
  "v_bfe_u32 v9, v4, 4, 1\n" \
  "v_lshl_add_u32 v8, v9, 2, v8\n" \
  "s_lshl4_add_u32 s52, %[w], 120864\n" \
  "v_add_u32 v8, s52, v8\n" \
  "v_mul_u32_u24 v9, 2336, v3\n" \
  "v_mul_u32_u24 v11, 12, v5\n" \
  "v_add_u32 v9, v9, v11\n" \
  "v_lshrrev_b32 v12, 3, v4\n" \
  "v_lshl_add_u32 v11, v3, 3, v12\n" \
  "v_lshlrev_b32 v11, 2, v11\n" \
  "v_mul_u32_u24 v12, 3072, v12\n" \
  "v_lshl_add_u32 v12, v3, 4, v12\n" \
  "s_mul_i32 s52, %[b], 24576\n" \
  "s_lshl_b32 s53, %[w], 7\n" \
  "s_add_u32 s52, s52, s53\n" \
  "s_add_u32 s52, s52, 3309568\n" \
  "v_lshl_add_u32 v13, v3, 4, v1\n" \
  "s_mov_b32 s54, 0x3f80\n" \
  "s_mov_b32 s55, 0x3f800000\n" \
  "v_mov_b64 v[48:49], 0\n" \
  "v_mov_b64 v[50:51], 0\n" \
  "v_mov_b64 v[52:53], 0\n" \
  "v_mov_b64 v[54:55], 0\n" \
  "s_mov_b32 s56, 0x05040100\n" \
  "s_mov_b32 s57, 0x07060302\n" \
  "s_waitcnt lgkmcnt(0)\n" \
  "s_barrier\n" \
  "ds_read_b128 v[24:27], v7 offset:0\n" \
  "ds_read_b128 v[28:31], v7 offset:16\n" \
  "ds_read_b128 v[32:35], v7 offset:32\n" \
  "ds_read_b128 v[36:39], v7 offset:48\n" \
  "ds_read_b128 v[40:43], v7 offset:64\n" \
  "ds_read_b128 v[44:47], v7 offset:80\n" \
  "s_waitcnt vmcnt(0)\n" \
  "v_bitop3_b32 v14, v99, v103, s44 bitop3:0x7e\n" \
  "v_cmp_ne_u32 vcc, 0, v14\n" \
  "s_cbranch_vccnz Mxs%=\n" \
  "Mxb%=:\n" \
  LSTAMP10 \
  "v_perm_b32 v99, v102, v98, s56\n" \
  "v_perm_b32 v103, v102, v98, s57\n" \
  "s_waitcnt lgkmcnt(0)\n" \
  "v_dot2c_f32_bf16 v48, 0x3f803f80, v96\n" \
  "v_dot2c_f32_bf16 v50, v96, v96\n" \
  "v_dot2c_f32_bf16 v52, v96, v31\n" \
  "v_dot2c_f32_bf16 v49, 0x3f803f80, v97\n" \
  "v_dot2c_f32_bf16 v51, v97, v97\n" \
  "v_dot2c_f32_bf16 v53, v97, v31\n" \
  "v_dot2c_f32_bf16 v48, 0x3f803f80, v100\n" \
  "v_dot2c_f32_bf16 v50, v100, v100\n" \
  "v_dot2c_f32_bf16 v52, v100, v47\n" \
  "v_dot2c_f32_bf16 v49, 0x3f803f80, v101\n" \
  "v_dot2c_f32_bf16 v51, v101, v101\n" \
  "v_dot2c_f32_bf16 v53, v101, v47\n" \
  "v_dot2c_f32_bf16 v48, 0x3f803f80, v99\n" \
  "v_dot2c_f32_bf16 v50, v99, v99\n" \
  "v_dot2c_f32_bf16 v52, v99, v39\n" \
  "v_dot2c_f32_bf16 v49, 0x3f803f80, v103\n" \
  "v_dot2c_f32_bf16 v51, v103, v103\n" \
  "v_dot2c_f32_bf16 v53, v103, v39\n" \
  "s_nop 0\n" \
  "v_permlane32_swap_b32 v48, v49\n" \
  "v_permlane32_swap_b32 v50, v51\n" \
  "v_permlane32_swap_b32 v52, v53\n" \
  "v_add_f32 v48, v48, v49\n" \
  "v_add_f32 v50, v50, v51\n" \
  "v_add_f32 v52, v52, v53\n" \
  "s_nop 0\n" \
  "v_permlane16_swap_b32 v48, v50\n" \
  "v_add_f32_dpp v52, v52, v52 row_ror:8 row_mask:0xf bank_mask:0xf bound_ctrl:1\n" \
  "v_add_f32 v48, v48, v50\n" \
  "s_nop 1\n" \
  "v_add_f32_dpp v48, v48, v48 row_ror:8 row_mask:0xf bank_mask:0xf bound_ctrl:1\n" \
  "ds_write2_b32 v8, v48, v52 offset1:2\n" \
  "s_waitcnt lgkmcnt(0)\n" \
  "s_barrier\n" \
  "s_barrier\n" \
  "ds_read_b128 v[56:59], v13 offset:128\n" \
  "s_waitcnt lgkmcnt(0)\n" \
  "v_dot2_f32_bf16 v64, v96, s54, v56\n" \
  "v_dot2_f32_bf16 v65, v96, s55, v56\n" \
  "v_dot2_f32_bf16 v66, v99, s54, v56\n" \
  "v_dot2_f32_bf16 v67, v99, s55, v56\n" \
  "v_dot2_f32_bf16 v68, v100, s54, v56\n" \
  "v_dot2_f32_bf16 v69, v100, s55, v56\n" \
  "v_dot2_f32_bf16 v70, v97, s54, v58\n" \
  "v_dot2_f32_bf16 v71, v97, s55, v58\n" \
  "v_dot2_f32_bf16 v72, v103, s54, v58\n" \
  "v_dot2_f32_bf16 v73, v103, s55, v58\n" \
  "v_dot2_f32_bf16 v74, v101, s54, v58\n" \
  "v_dot2_f32_bf16 v75, v101, s55, v58\n" \
  "v_cvt_pk_bf16_f32 v64, 0, v64\n" \
  "v_cvt_pk_bf16_f32 v65, 0, v65\n" \
  "v_cvt_pk_bf16_f32 v66, 0, v66\n" \
  "v_cvt_pk_bf16_f32 v67, 0, v67\n" \
  "v_cvt_pk_bf16_f32 v68, 0, v68\n" \
  "v_cvt_pk_bf16_f32 v69, 0, v69\n" \
  "v_cvt_pk_bf16_f32 v70, 0, v70\n" \
  "v_cvt_pk_bf16_f32 v71, 0, v71\n" \
  "v_cvt_pk_bf16_f32 v72, 0, v72\n" \
  "v_cvt_pk_bf16_f32 v73, 0, v73\n" \
  "v_cvt_pk_bf16_f32 v74, 0, v74\n" \
  "v_cvt_pk_bf16_f32 v75, 0, v75\n" \
  "v_pk_mul_f32 v[64:65], v[64:65], v[56:57] op_sel:[0,1]\n" \
  "v_pk_mul_f32 v[66:67], v[66:67], v[56:57] op_sel:[0,1]\n" \
  "v_pk_mul_f32 v[68:69], v[68:69], v[56:57] op_sel:[0,1]\n" \
  "v_pk_mul_f32 v[70:71], v[70:71], v[58:59] op_sel:[0,1]\n" \
  "v_pk_mul_f32 v[72:73], v[72:73], v[58:59] op_sel:[0,1]\n" \
  "v_pk_mul_f32 v[74:75], v[74:75], v[58:59] op_sel:[0,1]\n" \
  "v_cvt_pk_bf16_f32 v64, 0, v64\n" \
  "v_cvt_pk_bf16_f32 v65, 0, v65\n" \
  "v_cvt_pk_bf16_f32 v66, 0, v66\n" \
  "v_cvt_pk_bf16_f32 v67, 0, v67\n" \
  "v_cvt_pk_bf16_f32 v68, 0, v68\n" \
  "v_cvt_pk_bf16_f32 v69, 0, v69\n" \
  "v_cvt_pk_bf16_f32 v70, 0, v70\n" \
  "v_cvt_pk_bf16_f32 v71, 0, v71\n" \
  "v_cvt_pk_bf16_f32 v72, 0, v72\n" \
  "v_cvt_pk_bf16_f32 v73, 0, v73\n" \
  "v_cvt_pk_bf16_f32 v74, 0, v74\n" \
  "v_cvt_pk_bf16_f32 v75, 0, v75\n" \
  "v_pk_mul_f32 v[64:65], v[64:65], v[24:25]\n" \
  "v_pk_mul_f32 v[66:67], v[66:67], v[32:33]\n" \
  "v_pk_mul_f32 v[68:69], v[68:69], v[40:41]\n" \
  "v_pk_mul_f32 v[70:71], v[70:71], v[24:25]\n" \
  "v_pk_mul_f32 v[72:73], v[72:73], v[32:33]\n" \
  "v_pk_mul_f32 v[74:75], v[74:75], v[40:41]\n" \
  "v_cvt_pk_bf16_f32 v64, 0, v64\n" \
  "v_cvt_pk_bf16_f32 v65, 0, v65\n" \
  "v_cvt_pk_bf16_f32 v66, 0, v66\n" \
  "v_cvt_pk_bf16_f32 v67, 0, v67\n" \
  "v_cvt_pk_bf16_f32 v68, 0, v68\n" \
  "v_cvt_pk_bf16_f32 v69, 0, v69\n" \
  "v_cvt_pk_bf16_f32 v70, 0, v70\n" \
  "v_cvt_pk_bf16_f32 v71, 0, v71\n" \
  "v_cvt_pk_bf16_f32 v72, 0, v72\n" \
  "v_cvt_pk_bf16_f32 v73, 0, v73\n" \
  "v_cvt_pk_bf16_f32 v74, 0, v74\n" \
  "v_cvt_pk_bf16_f32 v75, 0, v75\n" \
  "v_pk_add_f32 v[64:65], v[64:65], v[26:27]\n" \
  "v_pk_add_f32 v[66:67], v[66:67], v[34:35]\n" \
  "v_pk_add_f32 v[68:69], v[68:69], v[42:43]\n" \
  "v_pk_add_f32 v[70:71], v[70:71], v[26:27]\n" \
  "v_pk_add_f32 v[72:73], v[72:73], v[34:35]\n" \
  "v_pk_add_f32 v[74:75], v[74:75], v[42:43]\n" \
  "v_cvt_pk_bf16_f32 v76, v64, v65\n" \
  "v_cvt_pk_bf16_f32 v77, v66, v67\n" \
  "v_cvt_pk_bf16_f32 v78, v68, v69\n" \
  "v_cvt_pk_bf16_f32 v79, v70, v71\n" \
  "v_cvt_pk_bf16_f32 v80, v72, v73\n" \
  "v_cvt_pk_bf16_f32 v81, v74, v75\n" \
  "ds_bpermute_b32 v84, v11, v76\n" \
  "ds_bpermute_b32 v85, v11, v77\n" \
  "ds_bpermute_b32 v86, v11, v78\n" \
  "ds_bpermute_b32 v88, v11, v79\n" \
  "ds_bpermute_b32 v89, v11, v80\n" \
  "ds_bpermute_b32 v90, v11, v81\n" \
  "v_dot2c_f32_bf16 v54, v76, v30\n" \
  "v_dot2c_f32_bf16 v55, v79, v30\n" \
  "v_dot2c_f32_bf16 v54, v77, v38\n" \
  "v_dot2c_f32_bf16 v55, v80, v38\n" \
  "v_dot2c_f32_bf16 v54, v78, v46\n" \
  "v_dot2c_f32_bf16 v55, v81, v46\n" \
  "v_mov_b32 v87, s44\n" \
  "v_mov_b32 v91, s44\n" \
  "s_nop 0\n" \
  "v_permlane32_swap_b32 v54, v55\n" \
  "v_add_f32 v54, v54, v55\n" \
  "v_mov_b32 v55, v54\n" \
  "s_waitcnt lgkmcnt(0)\n" \
  "buffer_store_dwordx4 v[84:87], v12, s[40:43], s52 offen sc1\n" \
  "v_permlane16_swap_b32 v54, v55\n" \
  "buffer_store_dwordx4 v[88:91], v12, s[40:43], s52 offen offset:1536 sc1\n" \
  "v_add_f32 v54, v54, v55\n" \
  "s_nop 1\n" \
  "v_add_f32_dpp v54, v54, v54 row_ror:8 row_mask:0xf bank_mask:0xf bound_ctrl:1\n" \
  "ds_write_b32 v10, v54\n" \
  LSTAMP11 \
  "s_waitcnt lgkmcnt(0)\n" \
  "s_barrier\n" \
  "v_dot2_f32_bf16 v64, v76, v28, 0\n" \
  "v_dot2_f32_bf16 v65, v76, v29, 0\n" \
  "v_dot2_f32_bf16 v66, v77, v36, 0\n" \
  "v_dot2_f32_bf16 v67, v77, v37, 0\n" \
  "v_dot2_f32_bf16 v68, v78, v44, 0\n" \
  "v_dot2_f32_bf16 v69, v78, v45, 0\n" \
  "v_dot2_f32_bf16 v70, v79, v28, 0\n" \
  "v_dot2_f32_bf16 v71, v79, v29, 0\n" \
  "v_dot2_f32_bf16 v72, v80, v36, 0\n" \
  "v_dot2_f32_bf16 v73, v80, v37, 0\n" \
  "v_dot2_f32_bf16 v74, v81, v44, 0\n" \
  "v_dot2_f32_bf16 v75, v81, v45, 0\n" \
  "v_cvt_pk_bf16_f32 v64, 0, v64\n" \
  "v_cvt_pk_bf16_f32 v65, 0, v65\n" \
  "v_cvt_pk_bf16_f32 v66, 0, v66\n" \
  "v_cvt_pk_bf16_f32 v67, 0, v67\n" \
  "v_cvt_pk_bf16_f32 v68, 0, v68\n" \
  "v_cvt_pk_bf16_f32 v69, 0, v69\n" \
  "v_cvt_pk_bf16_f32 v70, 0, v70\n" \
  "v_cvt_pk_bf16_f32 v71, 0, v71\n" \
  "v_cvt_pk_bf16_f32 v72, 0, v72\n" \
  "v_cvt_pk_bf16_f32 v73, 0, v73\n" \
  "v_cvt_pk_bf16_f32 v74, 0, v74\n" \
  "v_cvt_pk_bf16_f32 v75, 0, v75\n" \
  "v_dot2_f32_bf16 v64, v96, s54, v64\n" \
  "v_dot2_f32_bf16 v65, v96, s55, v65\n" \
  "v_dot2_f32_bf16 v66, v99, s54, v66\n" \
  "v_dot2_f32_bf16 v67, v99, s55, v67\n" \
  "v_dot2_f32_bf16 v68, v100, s54, v68\n" \
  "v_dot2_f32_bf16 v69, v100, s55, v69\n" \
  "v_dot2_f32_bf16 v70, v97, s54, v70\n" \
  "v_dot2_f32_bf16 v71, v97, s55, v71\n" \
  "v_dot2_f32_bf16 v72, v103, s54, v72\n" \
  "v_dot2_f32_bf16 v73, v103, s55, v73\n" \
  "v_dot2_f32_bf16 v74, v101, s54, v74\n" \
  "v_dot2_f32_bf16 v75, v101, s55, v75\n" \
  "v_cvt_pk_bf16_f32 v76, v64, v65\n" \
  "v_cvt_pk_bf16_f32 v77, v66, v67\n" \
  "v_cvt_pk_bf16_f32 v78, v68, v69\n" \
  "v_cvt_pk_bf16_f32 v79, v70, v71\n" \
  "v_cvt_pk_bf16_f32 v80, v72, v73\n" \
  "v_cvt_pk_bf16_f32 v81, v74, v75\n" \
  "ds_write2_b32 v9, v76, v77 offset1:1\n" \
  "ds_write_b32 v9, v78 offset:8\n" \
  "ds_write_b32 v9, v79 offset:1168\n" \
  "ds_write_b32 v9, v80 offset:1172\n" \
  "ds_write_b32 v9, v81 offset:1176\n" \
  "v_mul_u32_u24 v5, 911, %[t]\n" \
  "v_lshrrev_b32 v5, 16, v5\n" \
  "v_add_lshl_u32 v5, v5, %[t], 4\n" \
  "v_lshlrev_b32 v6, 4, %[t]\n" \
  "s_mul_i32 s50, %[b], 18432\n" \
  "s_add_u32 s48, s48, s50\n" \
  "s_addc_u32 s49, s49, 0\n" \
  "v_add_u32 v7, 9216, v6\n" \
  "s_waitcnt lgkmcnt(0)\n" \
  "s_barrier\n" \
  "s_cmp_ge_u32 %[w], 9\n" \
  "s_cbranch_scc1 Mlz%=\n" \
  "ds_read_b128 v[16:19], v5\n" \
  "ds_read_b128 v[20:23], v5 offset:9344\n" \
  "s_waitcnt lgkmcnt(1)\n" \
  "global_store_dwordx4 v6, v[16:19], s[48:49]" OFLAG "\n" \
  "s_waitcnt lgkmcnt(0)\n" \
  "global_store_dwordx4 v7, v[20:23], s[48:49]" OFLAG "\n" \
  "s_branch Mlz%=\n" \
  "Mxs%=:\n" \
  "s_movk_i32 s97, 0x7fff\n" \
  "Mxr%=:\n" \
  LSTAMPC \
  "s_mov_b64 exec, vcc\n" \
  "buffer_load_dwordx4 v[96:99], v94, s[40:43], s58 offen sc1\n" \
  "buffer_load_dwordx4 v[100:103], v94, s[40:43], s59 offen sc1\n" \
  "s_mov_b64 exec, -1\n" \
  "s_waitcnt vmcnt(0)\n" \
  "v_bitop3_b32 v14, v99, v103, s44 bitop3:0x7e\n" \
  "v_cmp_ne_u32 vcc, 0, v14\n" \
  "s_cbranch_vccz Mxb%=\n" \
  "s_sub_u32 s97, s97, 1\n" \
  "s_cbranch_scc0 Mxr%=\n" \
  "s_branch Mxb%=\n" \
  "Mps0%=:\n" \
  LSTAMPD \
  "s_sub_u32 s97, s97, 1\n" \
  "s_cbranch_scc1 Mpk0%=\n" \
  "s_mov_b32 s51, 17920\n" \
  "buffer_load_dwordx4 v[12:15], v6, s[40:43], s51 offen sc1\n" \
  "s_mov_b32 s51, 45568\n" \
  "buffer_load_dwordx4 v[16:19], v6, s[40:43], s51 offen sc1\n" \
  "s_mov_b32 s51, 73216\n" \
  "buffer_load_dwordx4 v[20:23], v6, s[40:43], s51 offen sc1\n" \
  "s_mov_b32 s51, 22528\n" \
  "buffer_load_dwordx4 v[24:27], v6, s[40:43], s51 offen sc1\n" \
  "s_mov_b32 s51, 50176\n" \
  "buffer_load_dwordx4 v[28:31], v6, s[40:43], s51 offen sc1\n" \
  "s_mov_b32 s51, 77824\n" \
  "buffer_load_dwordx4 v[32:35], v6, s[40:43], s51 offen sc1\n" \
  "s_mov_b32 s51, 27136\n" \
  "buffer_load_dwordx4 v[36:39], v6, s[40:43], s51 offen sc1\n" \
  "s_mov_b32 s51, 54784\n" \
  "buffer_load_dwordx4 v[40:43], v6, s[40:43], s51 offen sc1\n" \
  "s_mov_b32 s51, 82432\n" \
  "buffer_load_dwordx4 v[44:47], v6, s[40:43], s51 offen sc1\n" \
  "s_waitcnt vmcnt(0)\n" \
  "s_branch Mpr0%=\n" \
  "Mps1%=:\n" \
  LSTAMPD \
  "s_sub_u32 s97, s97, 1\n" \
  "s_cbranch_scc1 Mpk1%=\n" \
  "s_mov_b32 s51, 17920\n" \
  "buffer_load_dwordx4 v[48:51], v6, s[40:43], s51 offen offset:768 sc1\n" \
  "s_mov_b32 s51, 45568\n" \
  "buffer_load_dwordx4 v[52:55], v6, s[40:43], s51 offen offset:768 sc1\n" \
  "s_mov_b32 s51, 73216\n" \
  "buffer_load_dwordx4 v[56:59], v6, s[40:43], s51 offen offset:768 sc1\n" \
  "s_mov_b32 s51, 22528\n" \
  "buffer_load_dwordx4 v[60:63], v6, s[40:43], s51 offen offset:768 sc1\n" \
  "s_mov_b32 s51, 50176\n" \
  "buffer_load_dwordx4 v[64:67], v6, s[40:43], s51 offen offset:768 sc1\n" \
  "s_mov_b32 s51, 77824\n" \
  "buffer_load_dwordx4 v[68:71], v6, s[40:43], s51 offen offset:768 sc1\n" \
  "s_mov_b32 s51, 27136\n" \
  "buffer_load_dwordx4 v[72:75], v6, s[40:43], s51 offen offset:768 sc1\n" \
  "s_mov_b32 s51, 54784\n" \
  "buffer_load_dwordx4 v[76:79], v6, s[40:43], s51 offen offset:768 sc1\n" \
  "s_mov_b32 s51, 82432\n" \
  "buffer_load_dwordx4 v[80:83], v6, s[40:43], s51 offen offset:768 sc1\n" \
  "s_waitcnt vmcnt(0)\n" \
  "s_branch Mpr1%=\n" \
  "Mls%=:\n" \
  "s_lshr_b32 s50, %[b], 4\n" \
  "s_cmp_lt_u32 s50, " STR(X1T) "\n" \
  "s_cbranch_scc1 Mld%=\n" \
  "s_sleep " STR(X1SLP) "\n" \
  "s_movk_i32 s97, 64\n" \
  "Myw%=:\n" \
  "s_memtime s[52:53]\n" \
  "s_waitcnt lgkmcnt(0)\n" \
  "s_sub_u32 s52, s52, s58\n" \
  "s_cmp_gt_u32 s52, " STR(YT) "\n" \
  "s_cbranch_scc1 Mld%=\n" \
  "s_sleep 1\n" \
  "s_sub_u32 s97, s97, 1\n" \
  "s_cbranch_scc0 Myw%=\n" \
  "Mld%=:\n" \
  "s_cmp_eq_u32 %[w], 15\n" \
  "s_cbranch_scc1 Mlk%=\n" \
  "s_barrier\n" \
  "s_barrier\n" \
  "s_barrier\n" \
  "s_barrier\n" \
  "s_barrier\n" \
  "s_barrier\n" \
  "s_branch Mlz%=\n" \
  "Mlk%=:\n" \
  "s_barrier\n" \
  "s_barrier\n" \
  "s_setprio 3\n" \
  "ds_read_b128 v[16:19], v1 offset:32\n" \
  "ds_read_b128 v[20:23], v1 offset:48\n" \
  "ds_read_b128 v[24:27], v1 offset:64\n" \
  "v_lshrrev_b32 v5, 2, v4\n" \
  "v_mul_u32_u24 v6, 208, v5\n" \
  "v_and_b32 v7, 3, v4\n" \
  "v_mul_u32_u24 v8, 48, v7\n" \
  "s_mov_b32 s50, 120864\n" \
  "v_add3_u32 v6, v6, v8, s50\n" \
  "v_lshl_add_u32 v9, v5, 3, v1\n" \
  "s_lshl_b32 s50, %[b], 4\n" \
  "v_add_u32 v10, s50, v5\n" \
  "v_lshlrev_b32 v11, 3, v10\n" \
  "v_sub_u32 v10, 2047, v10\n" \
  "v_mov_b32 v13, s44\n" \
  "v_mul_u32_u24 v14, 48, v5\n" \
  "v_mul_u32_u24 v7, 12, v7\n" \
  "s_mov_b32 s50, 124192\n" \
  "v_add3_u32 v14, v14, v7, s50\n" \
  "s_mov_b32 s50, 87040\n" \
  "s_mov_b32 s51, 103424\n" \
  "s_waitcnt lgkmcnt(0)\n" \
  "v_pk_add_f32 v[16:17], v[16:17], v[18:19]\n" \
  "v_pk_add_f32 v[20:21], v[20:21], v[22:23]\n" \
  "v_pk_add_f32 v[24:25], v[24:25], v[26:27]\n" \
  "v_pk_add_f32 v[16:17], v[16:17], v[20:21]\n" \
  "v_pk_add_f32 v[16:17], v[16:17], v[24:25]\n" \
  "v_add_f32 v15, v16, v17\n" \
  "s_barrier\n" \
  "ds_read_b128 v[16:19], v6\n" \
  "ds_read_b128 v[20:23], v6 offset:16\n" \
  "ds_read_b128 v[24:27], v6 offset:32\n" \
  "s_waitcnt lgkmcnt(1)\n" \
  "v_pk_add_f32 v[16:17], v[16:17], v[20:21]\n" \
  "v_pk_add_f32 v[18:19], v[18:19], v[22:23]\n" \
  "s_waitcnt lgkmcnt(0)\n" \
  "v_pk_add_f32 v[16:17], v[16:17], v[24:25]\n" \
  "v_pk_add_f32 v[18:19], v[18:19], v[26:27]\n" \
  "v_add_f32 v18, v18, v19\n" \
  "v_add_f32_dpp v16, v16, v16 quad_perm:[1,0,3,2] row_mask:0xf bank_mask:0xf bound_ctrl:1\n" \
  "v_add_f32_dpp v17, v17, v17 quad_perm:[1,0,3,2] row_mask:0xf bank_mask:0xf bound_ctrl:1\n" \
  "v_add_f32_dpp v18, v18, v18 quad_perm:[1,0,3,2] row_mask:0xf bank_mask:0xf bound_ctrl:1\n" \
  "v_add_f32_dpp v16, v16, v16 quad_perm:[2,3,0,1] row_mask:0xf bank_mask:0xf bound_ctrl:1\n" \
  "v_add_f32_dpp v17, v17, v17 quad_perm:[2,3,0,1] row_mask:0xf bank_mask:0xf bound_ctrl:1\n" \
  "v_add_f32_dpp v18, v18, v18 quad_perm:[2,3,0,1] row_mask:0xf bank_mask:0xf bound_ctrl:1\n" \
  "v_mul_f32 v16, 0xbae38e39, v16\n" \
  "v_mul_f32 v17, 0x3ae38e39, v17\n" \
  "v_fma_f32 v17, -v16, v16, v17\n" \
  "v_max_f32 v17, 0, v17\n" \
  "v_cvt_pk_bf16_f32 v17, 0, v17\n" \
  "v_add_f32 v17, 0x3727c5ac, v17\n" \
  "v_cvt_pk_bf16_f32 v17, 0, v17\n" \
  "v_sqrt_f32 v17, v17\n" \
  "v_cvt_pk_bf16_f32 v16, 0, v16\n" \
  "v_cvt_pk_bf16_f32 v17, 0, v17\n" \
  "v_rcp_f32 v17, v17\n" \
  "v_fma_f32 v12, v16, v15, v18\n" \
  "v_mul_f32 v12, v12, v17\n" \
  "ds_write_b64 v9, v[16:17] offset:128\n" \
  "v_and_b32 v18, 0x7ff, v12\n" \
  "v_add_u32 v18, 0x7ff, v18\n" \
  "v_or_b32 v12, v12, v18\n" \
  "v_ashrrev_i32 v18, 31, v12\n" \
  "v_or_b32 v18, 0x80000000, v18\n" \
  "v_xor_b32 v12, v12, v18\n" \
  "v_and_b32 v12, 0xfffff800, v12\n" \
  "v_or_b32 v12, v12, v10\n" \
  "s_mov_b32 exec_lo, 0x11111111\n" \
  "s_mov_b32 exec_hi, 0x11111111\n" \
  "buffer_store_dwordx2 v[12:13], v11, s[40:43], s50 offen sc1\n" \
  "s_mov_b64 exec, -1\n" \
  LSTAMP13 \
  "s_waitcnt lgkmcnt(0)\n" \
  "s_barrier\n" \
  "s_barrier\n" \
  "ds_read2_b32 v[16:17], v14 offset1:1\n" \
  "ds_read_b32 v18, v14 offset:8\n" \
  "s_waitcnt lgkmcnt(0)\n" \
  "v_add_f32 v12, v16, v17\n" \
  "v_add_f32 v12, v12, v18\n" \
  "s_nop 1\n" \
  "v_add_f32_dpp v12, v12, v12 quad_perm:[1,0,3,2] row_mask:0xf bank_mask:0xf bound_ctrl:1\n" \
  "s_nop 1\n" \
  "v_add_f32_dpp v12, v12, v12 quad_perm:[2,3,0,1] row_mask:0xf bank_mask:0xf bound_ctrl:1\n" \
  "v_and_b32 v18, 0x7ff, v12\n" \
  "v_add_u32 v18, 0x7ff, v18\n" \
  "v_or_b32 v12, v12, v18\n" \
  "v_ashrrev_i32 v18, 31, v12\n" \
  "v_or_b32 v18, 0x80000000, v18\n" \
  "v_xor_b32 v12, v12, v18\n" \
  "v_and_b32 v12, 0xfffff800, v12\n" \
  "v_or_b32 v12, v12, v10\n" \
  "s_mov_b32 exec_lo, 0x11111111\n" \
  "s_mov_b32 exec_hi, 0x11111111\n" \
  "buffer_store_dwordx2 v[12:13], v11, s[40:43], s51 offen sc1\n" \
  "s_mov_b64 exec, -1\n" \
  LSTAMP14 \
  "s_barrier\n" \
  "Mlz%=:\n" \
  "s_waitcnt vmcnt(0)\n" \
  "s_barrier\n" \
  "s_cmp_lg_u32 %[w], 15\n" \
  "s_cbranch_scc1 Mly%=\n" \
  "s_lshl2_add_u32 s50, %[b], 136192\n" \
  "s_mov_b64 exec, 1\n" \
  "buffer_store_dword v13, off, s[40:43], s50 sc1\n" \
  "s_mov_b64 exec, -1\n" \
  "Mly%=:\n"

#define EW_HEAD \
  "s_load_dwordx4 s[52:55], %[k], 0x58\n" \
  "s_load_dwordx2 s[40:41], %[k], 0x70\n" \
  "s_lshr_b32 s50, %[w], 2\n" \
  "s_mov_b32 s42, 10403840\n" \
  "s_mov_b32 s43, 0x20000\n" \
  "s_cmp_eq_u32 s50, 3\n" \
  "s_cbranch_scc1 Eez%=\n" \
  "s_cmpk_ge_u32 %[b], 246\n" \
  "s_cbranch_scc1 Een%=\n" \
  "s_sub_u32 s51, %[b], 192\n" \
  "s_mul_i32 s56, s51, 43\n" \
  "s_lshr_b32 s56, s56, 7\n" \
  "s_mul_i32 s57, s56, 3\n" \
  "s_sub_u32 s57, s51, s57\n" \
  "s_mul_i32 s51, s57, 192\n" \
  "s_cmp_eq_u32 s50, 2\n" \
  "s_cbranch_scc1 Eec%=\n" \
  "v_bfe_u32 v1, %[t], 3, 5\n" \
  "v_add_u32 v2, s51, v1\n" \
  "v_mul_u32_u24 v2, 6912, v2\n" \
  "v_and_b32 v3, 2, v1\n" \
  "v_lshrrev_b32 v1, 1, v1\n" \
  "v_and_or_b32 v3, v1, 4, v3\n" \
  "v_xor_b32 v3, v3, %[t]\n" \
  "v_and_b32 v3, 7, v3\n" \
  "s_lshl_b32 s58, s56, 7\n" \
  "v_lshl_add_u32 v2, v3, 4, v2\n" \
  "v_add_u32 v2, s58, v2\n" \
  "s_cmp_lg_u32 s50, 0\n" \
  "s_cbranch_scc1 Eeb%=\n" \
  "s_lshl_b32 s51, %[w], 10\n" \
  "s_add_u32 m0, s51, 73728\n" \
  GSTAMP1 \
  "s_waitcnt lgkmcnt(0)\n" \
  "s_memtime s[92:93]\n" \
  "global_load_lds_dwordx4 v2, s[54:55]\n" \
  "s_add_u32 m0, m0, 4096\n" \
  "v_add_u32 v2, 221184, v2\n" \
  "global_load_lds_dwordx4 v2, s[54:55]\n" \
  "s_add_u32 m0, m0, 4096\n" \
  "v_add_u32 v2, 221184, v2\n" \
  "global_load_lds_dwordx4 v2, s[54:55]\n" \
  "s_add_u32 m0, m0, 4096\n" \
  "v_add_u32 v2, 221184, v2\n" \
  "global_load_lds_dwordx4 v2, s[54:55]\n" \
  "s_add_u32 m0, m0, 4096\n" \
  "v_add_u32 v2, 221184, v2\n" \
  "global_load_lds_dwordx4 v2, s[54:55]\n" \
  "s_add_u32 m0, m0, 4096\n" \
  "v_add_u32 v2, 221184, v2\n" \
  "global_load_lds_dwordx4 v2, s[54:55]\n" \
  "s_branch Eeb%=\n" \
  "Een%=:\n" \
  "s_mov_b32 s51, 0\n" \
  "s_cmp_eq_u32 s50, 2\n" \
  "s_cbranch_scc1 Eec%=\n" \
  "s_cmpk_lt_u32 %[b], 248\n" \
  "s_cbranch_scc1 Eeb%=\n" \
  "s_cmp_lg_u32 s50, 0\n" \
  "s_cbranch_scc1 Eeb%=\n" \
  "s_getpc_b64 s[48:49]\n" \
  "Epc%=:\n" \
  "v_lshlrev_b32 v1, 7, %[t]\n" \
  "v_min_u32 v1, Ez%=-Epc%=, v1\n" \
  "s_mov_b32 m0, 160032\n" \
  "s_nop 0\n" \
  "global_load_lds_dword v1, s[48:49]\n" \
  "s_branch Eeb%=\n" \
  "Eez%=:\n" \
  "v_and_b32 v5, 63, %[t]\n" \
  "v_mov_b32 v6, 159488\n" \
  "v_lshl_add_u32 v6, v5, 2, v6\n" \
  "v_mov_b32 v8, 0\n" \
  "ds_write_b32 v6, v8\n" \
  "s_branch Eeb%=\n" \
  "Eec%=:\n" \
  "v_and_b32 v5, 255, %[t]\n" \
  "v_min_u32 v5, 191, v5\n" \
  "v_add_lshl_u32 v6, v5, s51, 1\n" \
  GSTAMP1 \
  "s_waitcnt lgkmcnt(0)\n" \
  "global_load_short_d16_hi v7, v6, s[52:53]\n" \
  "buffer_load_dword v8, off, s[40:43], 0 sc1\n" \
  "Eeb%=:\n"

#define EW_BODY \
  "v_and_b32 v10, 15, %[t]\n" \
  "v_bfe_u32 v11, %[t], 4, 2\n" \
  "v_and_b32 v12, 63, %[t]\n" \
  "s_cmp_lg_u32 s50, 2\n" \
  "s_cbranch_scc1 Em0%=\n" \
  "s_setprio 2\n" \
  "v_bfe_u32 v1, %[t], 2, 2\n" \
  "v_lshl_add_u32 v1, v11, 3, v1\n" \
  "v_lshlrev_b32 v1, 7, v1\n" \
  "v_bfe_u32 v2, %[t], 3, 2\n" \
  "v_xor_b32 v2, %[w], v2\n" \
  "v_and_b32 v2, 3, v2\n" \
  "v_lshl_add_u32 v1, v2, 5, v1\n" \
  "v_and_b32 v2, 3, %[t]\n" \
  "v_lshl_add_u32 v1, v2, 3, v1\n" \
  "v_add_u32 v1, 73728, v1\n" \
  "v_mov_b32 v9, 160416\n" \
  "v_lshl_add_u32 v5, v5, 1, v9\n" \
  "v_lshl_add_u32 v9, v11, 4, v9\n" \
  "v_lshlrev_b32 v3, 3, v10\n" \
  "s_mul_i32 s58, s57, 27648\n" \
  "s_lshl_b32 s59, s56, 9\n" \
  "s_add_u32 s58, s58, s59\n" \
  "s_lshl_b32 s59, %[w], 7\n" \
  "s_add_u32 s58, s58, s59\n" \
  "s_add_u32 s58, s58, 3072\n" \
  "s_cmp_ge_u32 s56, 9\n" \
  "s_cselect_b32 s59, 4608, 0\n" \
  "v_mov_b32 v4, 158208\n" \
  "s_waitcnt vmcnt(0)\n" \
  GSTAMP2 \
  "v_and_b32 v7, 0xffff0000, v7\n" \
  "v_mul_f32 v6, 0xbfb8aa3b, v7\n" \
  "v_exp_f32 v6, v6\n" \
  "v_add_u32 v8, 1, v8\n" \
  "v_add_f32 v6, 1.0, v6\n" \
  "v_rcp_f32 v6, v6\n" \
  "v_readfirstlane_b32 s44, v8\n" \
  "v_mul_f32 v6, v6, v7\n" \
  "v_cvt_pk_bf16_f32 v6, 0, v6\n" \
  "ds_write_b32 v4, v8\n" \
  "s_cmpk_ge_u32 %[b], 246\n" \
  "s_cbranch_scc1 Ew0%=\n" \
  "ds_write_b16_d16_hi v5, v6\n" \
  "s_waitcnt lgkmcnt(0)\n" \
  "s_barrier\n" \
  GSTAMP3 \
  "ds_read_b64_tr_b16 v[40:41], v1 offset:0\n" \
  "ds_read_b64_tr_b16 v[42:43], v1 offset:512\n" \
  "ds_read_b64_tr_b16 v[44:45], v1 offset:4096\n" \
  "ds_read_b64_tr_b16 v[46:47], v1 offset:4608\n" \
  "ds_read_b64_tr_b16 v[48:49], v1 offset:8192\n" \
  "ds_read_b64_tr_b16 v[50:51], v1 offset:8704\n" \
  "ds_read_b64_tr_b16 v[52:53], v1 offset:12288\n" \
  "ds_read_b64_tr_b16 v[54:55], v1 offset:12800\n" \
  "ds_read_b64_tr_b16 v[56:57], v1 offset:16384\n" \
  "ds_read_b64_tr_b16 v[58:59], v1 offset:16896\n" \
  "ds_read_b64_tr_b16 v[60:61], v1 offset:20480\n" \
  "ds_read_b64_tr_b16 v[62:63], v1 offset:20992\n" \
  "ds_read_b128 v[64:67], v9 offset:0\n" \
  "ds_read_b128 v[68:71], v9 offset:64\n" \
  "ds_read_b128 v[72:75], v9 offset:128\n" \
  "ds_read_b128 v[76:79], v9 offset:192\n" \
  "ds_read_b128 v[80:83], v9 offset:256\n" \
  "ds_read_b128 v[84:87], v9 offset:320\n" \
  "s_mov_b64 s[46:47], 0xffff\n" \
  "s_waitcnt lgkmcnt(0)\n" \
  "v_mfma_f32_16x16x32_bf16 v[16:19], v[64:67], v[40:43], 0\n" \
  "v_mfma_f32_16x16x32_bf16 v[16:19], v[68:71], v[44:47], v[16:19]\n" \
  "v_mfma_f32_16x16x32_bf16 v[16:19], v[72:75], v[48:51], v[16:19]\n" \
  "v_mfma_f32_16x16x32_bf16 v[16:19], v[76:79], v[52:55], v[16:19]\n" \
  "v_mfma_f32_16x16x32_bf16 v[16:19], v[80:83], v[56:59], v[16:19]\n" \
  "v_mfma_f32_16x16x32_bf16 v[16:19], v[84:87], v[60:63], v[16:19]\n" \
  "s_nop 7\n" \
  "s_mov_b64 exec, s[46:47]\n" \
  "v_mov_b32 v17, s44\n" \
  "buffer_store_dwordx2 v[16:17], v3, s[40:43], s58 offen sc1\n" \
  "s_mov_b64 exec, -1\n" \
  GSTAMP4 \
  "s_add_u32 s58, s58, 9216\n" \
  "s_add_u32 s58, s58, s59\n" \
  "s_barrier\n" \
  "s_barrier\n" \
  "ds_read_b64_tr_b16 v[40:41], v1 offset:24576\n" \
  "ds_read_b64_tr_b16 v[42:43], v1 offset:25088\n" \
  "ds_read_b64_tr_b16 v[44:45], v1 offset:28672\n" \
  "ds_read_b64_tr_b16 v[46:47], v1 offset:29184\n" \
  "ds_read_b64_tr_b16 v[48:49], v1 offset:32768\n" \
  "ds_read_b64_tr_b16 v[50:51], v1 offset:33280\n" \
  "ds_read_b64_tr_b16 v[52:53], v1 offset:36864\n" \
  "ds_read_b64_tr_b16 v[54:55], v1 offset:37376\n" \
  "ds_read_b64_tr_b16 v[56:57], v1 offset:40960\n" \
  "ds_read_b64_tr_b16 v[58:59], v1 offset:41472\n" \
  "ds_read_b64_tr_b16 v[60:61], v1 offset:45056\n" \
  "ds_read_b64_tr_b16 v[62:63], v1 offset:45568\n" \
  "s_mov_b64 s[46:47], 0xffff\n" \
  "s_waitcnt lgkmcnt(0)\n" \
  "v_mfma_f32_16x16x32_bf16 v[16:19], v[64:67], v[40:43], 0\n" \
  "v_mfma_f32_16x16x32_bf16 v[16:19], v[68:71], v[44:47], v[16:19]\n" \
  "v_mfma_f32_16x16x32_bf16 v[16:19], v[72:75], v[48:51], v[16:19]\n" \
  "v_mfma_f32_16x16x32_bf16 v[16:19], v[76:79], v[52:55], v[16:19]\n" \
  "v_mfma_f32_16x16x32_bf16 v[16:19], v[80:83], v[56:59], v[16:19]\n" \
  "v_mfma_f32_16x16x32_bf16 v[16:19], v[84:87], v[60:63], v[16:19]\n" \
  "s_nop 7\n" \
  "s_mov_b64 exec, s[46:47]\n" \
  "v_mov_b32 v17, s44\n" \
  "buffer_store_dwordx2 v[16:17], v3, s[40:43], s58 offen sc1\n" \
  "s_mov_b64 exec, -1\n" \
  GSTAMP5 \
  "s_add_u32 s58, s58, 4608\n" \
  "v_add_u32 v1, 49152, v1\n" \
  "s_barrier\n" \
  "ds_read_b64_tr_b16 v[40:41], v1 offset:0\n" \
  "ds_read_b64_tr_b16 v[42:43], v1 offset:512\n" \
  "ds_read_b64_tr_b16 v[44:45], v1 offset:4096\n" \
  "ds_read_b64_tr_b16 v[46:47], v1 offset:4608\n" \
  "ds_read_b64_tr_b16 v[48:49], v1 offset:8192\n" \
  "ds_read_b64_tr_b16 v[50:51], v1 offset:8704\n" \
  "ds_read_b64_tr_b16 v[52:53], v1 offset:12288\n" \
  "ds_read_b64_tr_b16 v[54:55], v1 offset:12800\n" \
  "ds_read_b64_tr_b16 v[56:57], v1 offset:16384\n" \
  "ds_read_b64_tr_b16 v[58:59], v1 offset:16896\n" \
  "ds_read_b64_tr_b16 v[60:61], v1 offset:20480\n" \
  "ds_read_b64_tr_b16 v[62:63], v1 offset:20992\n" \
  "s_mov_b64 s[46:47], 0xffff\n" \
  "s_waitcnt lgkmcnt(0)\n" \
  "v_mfma_f32_16x16x32_bf16 v[16:19], v[64:67], v[40:43], 0\n" \
  "v_mfma_f32_16x16x32_bf16 v[16:19], v[68:71], v[44:47], v[16:19]\n" \
  "v_mfma_f32_16x16x32_bf16 v[16:19], v[72:75], v[48:51], v[16:19]\n" \
  "v_mfma_f32_16x16x32_bf16 v[16:19], v[76:79], v[52:55], v[16:19]\n" \
  "v_mfma_f32_16x16x32_bf16 v[16:19], v[80:83], v[56:59], v[16:19]\n" \
  "v_mfma_f32_16x16x32_bf16 v[16:19], v[84:87], v[60:63], v[16:19]\n" \
  "s_nop 7\n" \
  "s_mov_b64 exec, s[46:47]\n" \
  "v_mov_b32 v17, s44\n" \
  "buffer_store_dwordx2 v[16:17], v3, s[40:43], s58 offen sc1\n" \
  "s_mov_b64 exec, -1\n" \
  GSTAMP6 \
  "s_branch Ew0%=\n" \
  "Em0%=:\n" \
  "s_cmpk_ge_u32 %[b], 246\n" \
  "s_cbranch_scc1 Em2%=\n" \
  "s_cmp_eq_u32 s50, 3\n" \
  "s_cbranch_scc1 Em1%=\n" \
  "s_cmp_ge_u32 s56, 9\n" \
  "s_cselect_b32 s59, 1152, 0\n" \
  "s_cmp_eq_u32 s50, 0\n" \
  "s_cselect_b32 s58, -1102464, 0\n" \
  "s_cselect_b32 s51, 0, 2304\n" \
  "s_add_u32 s58, s58, s51\n" \
  "s_add_u32 s58, s58, s59\n" \
  "v_add_u32 v2, s58, v2\n" \
  "s_mul_i32 s51, s50, -28672\n" \
  "s_lshl_b32 s58, %[w], 10\n" \
  "s_add_u32 s51, s51, s58\n" \
  "s_add_u32 m0, s51, 122880\n" \
  "s_waitcnt vmcnt(0) lgkmcnt(0)\n" \
  GSTAMP3 \
  "s_cmp_eq_u32 s50, 0\n" \
  "s_cbranch_scc1 Em3%=\n" \
  "s_memtime s[92:93]\n" \
  "s_sleep " STR(P1D) "\n" \
  "global_load_lds_dwordx4 v2, s[54:55]\n" \
  "s_add_u32 m0, m0, 4096\n" \
  "v_add_u32 v2, 221184, v2\n" \
  "global_load_lds_dwordx4 v2, s[54:55]\n" \
  "s_add_u32 m0, m0, 4096\n" \
  "v_add_u32 v2, 221184, v2\n" \
  "global_load_lds_dwordx4 v2, s[54:55]\n" \
  "s_add_u32 m0, m0, 4096\n" \
  "v_add_u32 v2, 221184, v2\n" \
  "global_load_lds_dwordx4 v2, s[54:55]\n" \
  "s_add_u32 m0, m0, 4096\n" \
  "v_add_u32 v2, 221184, v2\n" \
  "global_load_lds_dwordx4 v2, s[54:55]\n" \
  "s_add_u32 m0, m0, 4096\n" \
  "v_add_u32 v2, 221184, v2\n" \
  "global_load_lds_dwordx4 v2, s[54:55]\n" \
  "s_load_dwordx4 s[52:55], %[k], 0x40\n" \
  "s_load_dwordx2 s[48:49], %[k], 0x50\n" \
  "s_load_dwordx2 s[84:85], %[k], 0x68\n" \
  "s_barrier\n" \
  "s_barrier\n" \
  "s_waitcnt lgkmcnt(0)\n" \
  "s_and_b32 s56, %[b], 7\n" \
  "s_bfe_u32 s57, %[b], 0x30003\n" \
  "s_lshr_b32 s58, s52, 7\n" \
  "s_add_u32 s58, s58, s56\n" \
  "s_sub_u32 s58, s57, s58\n" \
  "s_add_u32 s58, s58, 4\n" \
  "s_and_b32 s58, s58, 7\n" \
  "s_mul_i32 s59, s56, 9\n" \
  "s_add_u32 s58, s58, s59\n" \
  "s_lshl_b32 s58, s58, 6\n" \
  "s_add_u32 s59, s59, 8\n" \
  "s_lshl_b32 s59, s59, 6\n" \
  "s_lshl3_add_u32 s59, s57, s59\n" \
  "s_lshl3_add_u32 s57, s56, s57\n" \
  "v_mov_b32 v2, s52\n" \
  "v_mov_b32 v3, s53\n" \
  "v_mov_b32 v4, s54\n" \
  "v_mov_b32 v5, s55\n" \
  "s_mov_b32 s46, 294912\n" \
  "s_mov_b32 s47, 0\n" \
  "v_bfe_u32 v7, %[t], 2, 2\n" \
  "s_mov_b32 vcc_lo, 0xff00ff00\n" \
  "s_mov_b32 vcc_hi, 0xff00ff00\n" \
  "v_cndmask_b32 v2, v2, v4, vcc\n" \
  "v_cndmask_b32 v3, v3, v5, vcc\n" \
  "v_lshrrev_b32 v4, 4, %[t]\n" \
  "v_mul_u32_u24 v4, 9216, v4\n" \
  "s_lshl_b32 s51, %[w], 1\n" \
  "s_and_b32 s51, s51, 4\n" \
  "v_or_b32 v5, s51, v11\n" \
  "v_xor_b32 v5, v5, %[t]\n" \
  "v_and_b32 v5, 7, v5\n" \
  "v_lshl_add_u32 v4, v5, 4, v4\n" \
  "s_lshl_b32 s51, s58, 1\n" \
  "v_add_u32 v4, s51, v4\n" \
  "v_mov_b32 v5, 0\n" \
  "v_lshl_add_u64 v[2:3], v[2:3], 0, v[4:5]\n" \
  "s_lshl_b32 m0, %[w], 10\n" \
  "s_waitcnt vmcnt(0)\n" \
  GSTAMP5 \
  "s_barrier\n" \
  "s_sleep " STR(GED) "\n" \
  "global_load_lds_dwordx4 v[2:3], off" WNT "\n" \
  "s_add_u32 m0, m0, 8192\n" \
  "v_lshl_add_u64 v[2:3], v[2:3], 0, s[46:47]\n" \
  "global_load_lds_dwordx4 v[2:3], off" WNT "\n" \
  "s_add_u32 m0, m0, 8192\n" \
  "v_lshl_add_u64 v[2:3], v[2:3], 0, s[46:47]\n" \
  "global_load_lds_dwordx4 v[2:3], off" WNT "\n" \
  "s_add_u32 m0, m0, 8192\n" \
  "v_lshl_add_u64 v[2:3], v[2:3], 0, s[46:47]\n" \
  "global_load_lds_dwordx4 v[2:3], off" WNT "\n" \
  "s_add_u32 m0, m0, 8192\n" \
  "v_lshl_add_u64 v[2:3], v[2:3], 0, s[46:47]\n" \
  "global_load_lds_dwordx4 v[2:3], off" WNT "\n" \
  "s_add_u32 m0, m0, 8192\n" \
  "v_lshl_add_u64 v[2:3], v[2:3], 0, s[46:47]\n" \
  "global_load_lds_dwordx4 v[2:3], off" WNT "\n" \
  "s_add_u32 m0, m0, 8192\n" \
  "v_lshl_add_u64 v[2:3], v[2:3], 0, s[46:47]\n" \
  "s_barrier\n" \
  "s_barrier\n" \
  "v_mov_b32 v8, 158208\n" \
  "ds_read_b32 v8, v8\n" \
  "s_waitcnt lgkmcnt(0)\n" \
  "v_readfirstlane_b32 s44, v8\n" \
  "s_branch Ew1%=\n" \
  "Em3%=:\n" \
  "s_barrier\n" \
  "s_barrier\n" \
  "global_load_lds_dwordx4 v2, s[54:55]\n" \
  "s_add_u32 m0, m0, 4096\n" \
  "v_add_u32 v2, 221184, v2\n" \
  "global_load_lds_dwordx4 v2, s[54:55]\n" \
  "s_add_u32 m0, m0, 4096\n" \
  "v_add_u32 v2, 221184, v2\n" \
  "global_load_lds_dwordx4 v2, s[54:55]\n" \
  "s_add_u32 m0, m0, 4096\n" \
  "v_add_u32 v2, 221184, v2\n" \
  "global_load_lds_dwordx4 v2, s[54:55]\n" \
  "s_add_u32 m0, m0, 4096\n" \
  "v_add_u32 v2, 221184, v2\n" \
  "global_load_lds_dwordx4 v2, s[54:55]\n" \
  "s_add_u32 m0, m0, 4096\n" \
  "v_add_u32 v2, 221184, v2\n" \
  "global_load_lds_dwordx4 v2, s[54:55]\n" \
  "s_barrier\n" \
  "s_waitcnt vmcnt(0)\n" \
  GSTAMP5 \
  "s_barrier\n" \
  "s_branch Ew0%=\n" \
  "Em1%=:\n" \
  "s_waitcnt lgkmcnt(0)\n" \
  "s_barrier\n" \
  "s_barrier\n" \
  "s_barrier\n" \
  "s_barrier\n" \
  "s_branch Ew0%=\n" \
  "Em2%=:\n" \
  "s_waitcnt lgkmcnt(0)\n" \
  "s_memtime s[92:93]\n" \
  "s_sleep " STR(WDLY) "\n" \
  "s_cmpk_lt_u32 %[b], 248\n" \
  "s_cbranch_scc1 Ew0%=\n" \
  "s_sleep " STR(WSLOW) "\n" \
  "Ew0%=:\n" \
  "s_setprio 0\n" \
  "s_load_dwordx4 s[52:55], %[k], 0x40\n" \
  "s_load_dwordx2 s[48:49], %[k], 0x50\n" \
  "s_load_dwordx2 s[84:85], %[k], 0x68\n" \
  "s_waitcnt lgkmcnt(0)\n" \
  "s_barrier\n" \
  "s_and_b32 s56, %[b], 7\n" \
  "s_bfe_u32 s57, %[b], 0x30003\n" \
  "s_lshr_b32 s58, s52, 7\n" \
  "s_add_u32 s58, s58, s56\n" \
  "s_sub_u32 s58, s57, s58\n" \
  "s_add_u32 s58, s58, 4\n" \
  "s_and_b32 s58, s58, 7\n" \
  "s_mul_i32 s59, s56, 9\n" \
  "s_add_u32 s58, s58, s59\n" \
  "s_lshl_b32 s58, s58, 6\n" \
  "s_add_u32 s59, s59, 8\n" \
  "s_lshl_b32 s59, s59, 6\n" \
  "s_lshl3_add_u32 s59, s57, s59\n" \
  "s_lshl3_add_u32 s57, s56, s57\n" \
  "v_mov_b32 v8, 158208\n" \
  "ds_read_b32 v8, v8\n" \
  "s_waitcnt lgkmcnt(0)\n" \
  "v_readfirstlane_b32 s44, v8\n" \
  "s_cmp_gt_u32 %[w], 8\n" \
  "s_cbranch_scc1 Ewd%=\n" \
  "v_mov_b32 v2, s52\n" \
  "v_mov_b32 v3, s53\n" \
  "v_mov_b32 v4, s54\n" \
  "v_mov_b32 v5, s55\n" \
  "s_mov_b32 s46, 294912\n" \
  "s_mov_b32 s47, 0\n" \
  "v_bfe_u32 v7, %[t], 2, 2\n" \
  "s_cmp_eq_u32 %[w], 8\n" \
  "s_cbranch_scc1 Ewx%=\n" \
  "s_mov_b32 vcc_lo, 0xff00ff00\n" \
  "s_mov_b32 vcc_hi, 0xff00ff00\n" \
  "v_cndmask_b32 v2, v2, v4, vcc\n" \
  "v_cndmask_b32 v3, v3, v5, vcc\n" \
  "v_lshrrev_b32 v4, 4, %[t]\n" \
  "v_mul_u32_u24 v4, 9216, v4\n" \
  "s_lshl_b32 s51, %[w], 1\n" \
  "s_and_b32 s51, s51, 4\n" \
  "v_or_b32 v5, s51, v11\n" \
  "v_xor_b32 v5, v5, %[t]\n" \
  "v_and_b32 v5, 7, v5\n" \
  "v_lshl_add_u32 v4, v5, 4, v4\n" \
  "s_lshl_b32 s51, s58, 1\n" \
  "v_add_u32 v4, s51, v4\n" \
  "v_mov_b32 v5, 0\n" \
  "v_lshl_add_u64 v[2:3], v[2:3], 0, v[4:5]\n" \
  "s_lshl_b32 m0, %[w], 10\n" \
  "s_nop 0\n" \
  "global_load_lds_dwordx4 v[2:3], off" WNT "\n" \
  "s_add_u32 m0, m0, 8192\n" \
  "v_lshl_add_u64 v[2:3], v[2:3], 0, s[46:47]\n" \
  "global_load_lds_dwordx4 v[2:3], off" WNT "\n" \
  "s_add_u32 m0, m0, 8192\n" \
  "v_lshl_add_u64 v[2:3], v[2:3], 0, s[46:47]\n" \
  "global_load_lds_dwordx4 v[2:3], off" WNT "\n" \
  "s_add_u32 m0, m0, 8192\n" \
  "v_lshl_add_u64 v[2:3], v[2:3], 0, s[46:47]\n" \
  "global_load_lds_dwordx4 v[2:3], off" WNT "\n" \
  "s_add_u32 m0, m0, 8192\n" \
  "v_lshl_add_u64 v[2:3], v[2:3], 0, s[46:47]\n" \
  "global_load_lds_dwordx4 v[2:3], off" WNT "\n" \
  "s_add_u32 m0, m0, 8192\n" \
  "v_lshl_add_u64 v[2:3], v[2:3], 0, s[46:47]\n" \
  "global_load_lds_dwordx4 v[2:3], off" WNT "\n" \
  "s_add_u32 m0, m0, 8192\n" \
  "v_lshl_add_u64 v[2:3], v[2:3], 0, s[46:47]\n" \
  "Ew1%=:\n" \
  "global_load_lds_dwordx4 v[2:3], off" WNT "\n" \
  "s_add_u32 m0, m0, 8192\n" \
  "v_lshl_add_u64 v[2:3], v[2:3], 0, s[46:47]\n" \
  "global_load_lds_dwordx4 v[2:3], off" WNT "\n" \
  "s_add_u32 m0, m0, 8192\n" \
  "v_lshl_add_u64 v[2:3], v[2:3], 0, s[46:47]\n" \
  "global_load_lds_dwordx4 v[2:3], off" WNT "\n" \
  "s_add_u32 m0, m0, 8192\n" \
  "v_lshl_add_u64 v[2:3], v[2:3], 0, s[46:47]\n" \
  "s_barrier\n" \
  "global_load_lds_dwordx4 v[2:3], off" WNT "\n" \
  "s_add_u32 m0, m0, 8192\n" \
  "v_lshl_add_u64 v[2:3], v[2:3], 0, s[46:47]\n" \
  "global_load_lds_dwordx4 v[2:3], off" WNT "\n" \
  "s_add_u32 m0, m0, 8192\n" \
  "v_lshl_add_u64 v[2:3], v[2:3], 0, s[46:47]\n" \
  "global_load_lds_dwordx4 v[2:3], off" WNT "\n" \
  "s_add_u32 m0, m0, 8192\n" \
  "v_lshl_add_u64 v[2:3], v[2:3], 0, s[46:47]\n" \
  "global_load_lds_dwordx4 v[2:3], off" WNT "\n" \
  "s_add_u32 m0, m0, 8192\n" \
  "v_lshl_add_u64 v[2:3], v[2:3], 0, s[46:47]\n" \
  "global_load_lds_dwordx4 v[2:3], off" WNT "\n" \
  "s_add_u32 m0, m0, 8192\n" \
  "v_lshl_add_u64 v[2:3], v[2:3], 0, s[46:47]\n" \
  "s_waitcnt vmcnt(5)\n" \
  "s_barrier\n" \
  "global_load_lds_dwordx4 v[2:3], off" WNT "\n" \
  "s_add_u32 m0, m0, 8192\n" \
  "v_lshl_add_u64 v[2:3], v[2:3], 0, s[46:47]\n" \
  "global_load_lds_dwordx4 v[2:3], off" WNT "\n" \
  "s_add_u32 m0, m0, 8192\n" \
  "v_lshl_add_u64 v[2:3], v[2:3], 0, s[46:47]\n" \
  "global_load_lds_dwordx4 v[2:3], off" WNT "\n" \
  "s_add_u32 m0, m0, 8192\n" \
  "v_lshl_add_u64 v[2:3], v[2:3], 0, s[46:47]\n" \
  "global_load_lds_dwordx4 v[2:3], off" WNT "\n" \
  "v_lshl_add_u32 v1, v11, 3, v7\n" \
  "v_lshlrev_b32 v1, 8, v1\n" \
  "v_and_b32 v4, 2, %[t]\n" \
  "v_lshl_add_u32 v1, v4, 6, v1\n" \
  "v_and_b32 v4, 1, v11\n" \
  "v_lshl_or_b32 v4, v4, 2, v7\n" \
  "v_xor_b32 v4, %[w], v4\n" \
  "v_lshl_add_u32 v1, v4, 4, v1\n" \
  "v_and_b32 v4, 1, %[t]\n" \
  "v_lshl_add_u32 v1, v4, 3, v1\n" \
  "ds_read_b64_tr_b16 v[40:41], v1 offset:0\n" \
  "ds_read_b64_tr_b16 v[42:43], v1 offset:1024\n" \
  "ds_read_b64_tr_b16 v[44:45], v1 offset:8192\n" \
  "ds_read_b64_tr_b16 v[46:47], v1 offset:9216\n" \
  "ds_read_b64_tr_b16 v[48:49], v1 offset:16384\n" \
  "ds_read_b64_tr_b16 v[50:51], v1 offset:17408\n" \
  "ds_read_b64_tr_b16 v[52:53], v1 offset:24576\n" \
  "ds_read_b64_tr_b16 v[54:55], v1 offset:25600\n" \
  "ds_read_b64_tr_b16 v[56:57], v1 offset:32768\n" \
  "ds_read_b64_tr_b16 v[58:59], v1 offset:33792\n" \
  "ds_read_b64_tr_b16 v[60:61], v1 offset:40960\n" \
  "ds_read_b64_tr_b16 v[62:63], v1 offset:41984\n" \
  "ds_read_b64_tr_b16 v[64:65], v1 offset:49152\n" \
  "ds_read_b64_tr_b16 v[66:67], v1 offset:50176\n" \
  "ds_read_b64_tr_b16 v[68:69], v1 offset:57344\n" \
  "ds_read_b64_tr_b16 v[70:71], v1 offset:58368\n" \
  "v_add_u32 v1, 0x10000, v1\n" \
  "ds_read_b64_tr_b16 v[72:73], v1\n" \
  "ds_read_b64_tr_b16 v[74:75], v1 offset:1024\n" \
  "v_add_u32 v1, 0x2000, v1\n" \
  "s_waitcnt vmcnt(0) lgkmcnt(0)\n" \
  GSTAMP6 \
  "s_barrier\n" \
  "ds_read_b64_tr_b16 v[76:77], v1 offset:0\n" \
  "ds_read_b64_tr_b16 v[78:79], v1 offset:1024\n" \
  "ds_read_b64_tr_b16 v[80:81], v1 offset:8192\n" \
  "ds_read_b64_tr_b16 v[82:83], v1 offset:9216\n" \
  "ds_read_b64_tr_b16 v[84:85], v1 offset:16384\n" \
  "ds_read_b64_tr_b16 v[86:87], v1 offset:17408\n" \
  "ds_read_b64_tr_b16 v[88:89], v1 offset:24576\n" \
  "ds_read_b64_tr_b16 v[90:91], v1 offset:25600\n" \
  "ds_read_b64_tr_b16 v[92:93], v1 offset:32768\n" \
  "ds_read_b64_tr_b16 v[94:95], v1 offset:33792\n" \
  "ds_read_b64_tr_b16 v[96:97], v1 offset:40960\n" \
  "ds_read_b64_tr_b16 v[98:99], v1 offset:41984\n" \
  "ds_read_b64_tr_b16 v[100:101], v1 offset:49152\n" \
  "ds_read_b64_tr_b16 v[102:103], v1 offset:50176\n" \
  "ds_read_b64_tr_b16 v[104:105], v1 offset:57344\n" \
  "ds_read_b64_tr_b16 v[106:107], v1 offset:58368\n" \
  "v_add_u32 v1, 0x10000, v1\n" \
  "ds_read_b64_tr_b16 v[108:109], v1\n" \
  "ds_read_b64_tr_b16 v[110:111], v1 offset:1024\n" \
  "v_add_u32 v1, 0x2000, v1\n" \
  "s_waitcnt lgkmcnt(0)\n" \
  "s_barrier\n" \
  GSTAMP7 \
  "s_branch Ek0%=\n" \
  "Ewx%=:\n" \
  "s_mov_b32 vcc_lo, 0xaaaaaaaa\n" \
  "s_mov_b32 vcc_hi, 0xaaaaaaaa\n" \
  "v_cndmask_b32 v2, v2, v4, vcc\n" \
  "v_cndmask_b32 v3, v3, v5, vcc\n" \
  "v_lshrrev_b32 v4, 1, v12\n" \
  "v_mul_u32_u24 v4, 9216, v4\n" \
  "s_lshl_b32 s51, s59, 1\n" \
  "v_add_u32 v4, s51, v4\n" \
  "v_mov_b32 v5, 0\n" \
  "v_lshl_add_u64 v[2:3], v[2:3], 0, v[4:5]\n" \
  "s_mov_b32 m0, 46080\n" \
  "v_lshlrev_b32 v1, 3, v10\n" \
  "v_lshl_add_u32 v1, v11, 8, v1\n" \
  "s_barrier\n" \
  "s_barrier\n" \
  "s_barrier\n" \
  "global_load_lds_dwordx4 v[2:3], off\n" \
  "s_add_u32 m0, m0, 1024\n" \
  "v_lshl_add_u64 v[2:3], v[2:3], 0, s[46:47]\n" \
  "global_load_lds_dwordx4 v[2:3], off\n" \
  "s_add_u32 m0, m0, 1024\n" \
  "v_lshl_add_u64 v[2:3], v[2:3], 0, s[46:47]\n" \
  "global_load_lds_dwordx4 v[2:3], off\n" \
  "s_add_u32 m0, m0, 1024\n" \
  "v_lshl_add_u64 v[2:3], v[2:3], 0, s[46:47]\n" \
  "global_load_lds_dwordx4 v[2:3], off\n" \
  "s_add_u32 m0, m0, 1024\n" \
  "v_lshl_add_u64 v[2:3], v[2:3], 0, s[46:47]\n" \
  "global_load_lds_dwordx4 v[2:3], off\n" \
  "s_add_u32 m0, m0, 1024\n" \
  "v_lshl_add_u64 v[2:3], v[2:3], 0, s[46:47]\n" \
  "global_load_lds_dwordx4 v[2:3], off\n" \
  "s_add_u32 m0, m0, 1024\n" \
  "v_lshl_add_u64 v[2:3], v[2:3], 0, s[46:47]\n" \
  "global_load_lds_dwordx4 v[2:3], off\n" \
  "s_add_u32 m0, m0, 1024\n" \
  "v_lshl_add_u64 v[2:3], v[2:3], 0, s[46:47]\n" \
  "global_load_lds_dwordx4 v[2:3], off\n" \
  "s_add_u32 m0, m0, 1024\n" \
  "v_lshl_add_u64 v[2:3], v[2:3], 0, s[46:47]\n" \
  "global_load_lds_dwordx4 v[2:3], off\n" \
  "s_add_u32 m0, m0, 1024\n" \
  "v_lshl_add_u64 v[2:3], v[2:3], 0, s[46:47]\n" \
  "s_barrier\n" \
  "global_load_lds_dwordx4 v[2:3], off\n" \
  "s_add_u32 m0, m0, 1024\n" \
  "v_lshl_add_u64 v[2:3], v[2:3], 0, s[46:47]\n" \
  "global_load_lds_dwordx4 v[2:3], off\n" \
  "s_add_u32 m0, m0, 1024\n" \
  "v_lshl_add_u64 v[2:3], v[2:3], 0, s[46:47]\n" \
  "global_load_lds_dwordx4 v[2:3], off\n" \
  "s_add_u32 m0, m0, 1024\n" \
  "v_lshl_add_u64 v[2:3], v[2:3], 0, s[46:47]\n" \
  "global_load_lds_dwordx4 v[2:3], off\n" \
  "s_add_u32 m0, m0, 1024\n" \
  "v_lshl_add_u64 v[2:3], v[2:3], 0, s[46:47]\n" \
  "global_load_lds_dwordx4 v[2:3], off\n" \
  "s_add_u32 m0, m0, 1024\n" \
  "v_lshl_add_u64 v[2:3], v[2:3], 0, s[46:47]\n" \
  "global_load_lds_dwordx4 v[2:3], off\n" \
  "s_add_u32 m0, m0, 1024\n" \
  "v_lshl_add_u64 v[2:3], v[2:3], 0, s[46:47]\n" \
  "global_load_lds_dwordx4 v[2:3], off\n" \
  "s_add_u32 m0, m0, 1024\n" \
  "v_lshl_add_u64 v[2:3], v[2:3], 0, s[46:47]\n" \
  "global_load_lds_dwordx4 v[2:3], off\n" \
  "s_add_u32 m0, m0, 1024\n" \
  "v_lshl_add_u64 v[2:3], v[2:3], 0, s[46:47]\n" \
  "global_load_lds_dwordx4 v[2:3], off\n" \
  "s_add_u32 m0, m0, 1024\n" \
  "v_lshl_add_u64 v[2:3], v[2:3], 0, s[46:47]\n" \
  GSTAMP6 \
  "s_waitcnt vmcnt(17)\n" \
  "ds_read_b64_tr_b16 v[40:41], v1 offset:46080\n" \
  "ds_read_b64_tr_b16 v[42:43], v1 offset:46208\n" \
  "s_waitcnt vmcnt(16)\n" \
  "ds_read_b64_tr_b16 v[44:45], v1 offset:47104\n" \
  "ds_read_b64_tr_b16 v[46:47], v1 offset:47232\n" \
  "s_waitcnt vmcnt(15)\n" \
  "ds_read_b64_tr_b16 v[48:49], v1 offset:48128\n" \
  "ds_read_b64_tr_b16 v[50:51], v1 offset:48256\n" \
  "s_waitcnt vmcnt(14)\n" \
  "ds_read_b64_tr_b16 v[52:53], v1 offset:49152\n" \
  "ds_read_b64_tr_b16 v[54:55], v1 offset:49280\n" \
  "s_waitcnt vmcnt(13)\n" \
  "ds_read_b64_tr_b16 v[56:57], v1 offset:50176\n" \
  "ds_read_b64_tr_b16 v[58:59], v1 offset:50304\n" \
  "s_waitcnt vmcnt(12)\n" \
  "ds_read_b64_tr_b16 v[60:61], v1 offset:51200\n" \
  "ds_read_b64_tr_b16 v[62:63], v1 offset:51328\n" \
  "s_waitcnt vmcnt(11)\n" \
  "ds_read_b64_tr_b16 v[64:65], v1 offset:52224\n" \
  "ds_read_b64_tr_b16 v[66:67], v1 offset:52352\n" \
  "s_waitcnt vmcnt(10)\n" \
  "ds_read_b64_tr_b16 v[68:69], v1 offset:53248\n" \
  "ds_read_b64_tr_b16 v[70:71], v1 offset:53376\n" \
  "s_waitcnt vmcnt(9)\n" \
  "ds_read_b64_tr_b16 v[72:73], v1 offset:54272\n" \
  "ds_read_b64_tr_b16 v[74:75], v1 offset:54400\n" \
  "s_waitcnt vmcnt(8)\n" \
  "ds_read_b64_tr_b16 v[76:77], v1 offset:55296\n" \
  "ds_read_b64_tr_b16 v[78:79], v1 offset:55424\n" \
  "s_waitcnt vmcnt(7)\n" \
  "ds_read_b64_tr_b16 v[80:81], v1 offset:56320\n" \
  "ds_read_b64_tr_b16 v[82:83], v1 offset:56448\n" \
  "s_waitcnt vmcnt(6)\n" \
  "ds_read_b64_tr_b16 v[84:85], v1 offset:57344\n" \
  "ds_read_b64_tr_b16 v[86:87], v1 offset:57472\n" \
  "s_waitcnt vmcnt(5)\n" \
  "ds_read_b64_tr_b16 v[88:89], v1 offset:58368\n" \
  "ds_read_b64_tr_b16 v[90:91], v1 offset:58496\n" \
  "s_waitcnt vmcnt(4)\n" \
  "ds_read_b64_tr_b16 v[92:93], v1 offset:59392\n" \
  "ds_read_b64_tr_b16 v[94:95], v1 offset:59520\n" \
  "s_waitcnt vmcnt(3)\n" \
  "ds_read_b64_tr_b16 v[96:97], v1 offset:60416\n" \
  "ds_read_b64_tr_b16 v[98:99], v1 offset:60544\n" \
  "s_waitcnt vmcnt(2)\n" \
  "ds_read_b64_tr_b16 v[100:101], v1 offset:61440\n" \
  "ds_read_b64_tr_b16 v[102:103], v1 offset:61568\n" \
  "s_waitcnt vmcnt(1)\n" \
  "ds_read_b64_tr_b16 v[104:105], v1 offset:62464\n" \
  "ds_read_b64_tr_b16 v[106:107], v1 offset:62592\n" \
  "s_waitcnt vmcnt(0)\n" \
  "ds_read_b64_tr_b16 v[108:109], v1 offset:63488\n" \
  "ds_read_b64_tr_b16 v[110:111], v1 offset:63616\n" \
  GSTAMP7 \
  "v_mul_u32_u24 v1, 1184, v10\n" \
  "v_lshl_add_u32 v1, v11, 4, v1\n" \
  "v_add_u32 v1, 120832, v1\n" \
  "v_lshrrev_b32 v2, 3, v12\n" \
  "v_mul_u32_u24 v2, 320, v2\n" \
  "v_and_b32 v4, 7, %[t]\n" \
  "v_lshl_add_u32 v2, v4, 1, v2\n" \
  "s_lshl4_add_u32 s46, %[w], 141312\n" \
  "v_add_u32 v2, s46, v2\n" \
  "s_barrier\n" \
  "s_barrier\n" \
  "s_barrier\n" \
  "s_branch Eg0%=\n" \
  "Ewd%=:\n" \
  "s_cmp_eq_u32 %[w], 15\n" \
  "s_cbranch_scc1 Ewz%=\n" \
  "s_sub_u32 s56, %[w], 9\n" \
  "v_lshrrev_b32 v4, 2, v12\n" \
  "v_lshrrev_b32 v5, 4, v12\n" \
  "v_and_b32 v5, 2, v5\n" \
  "v_xor_b32 v5, v5, %[t]\n" \
  "v_and_b32 v5, 3, v5\n" \
  "s_mul_i32 s51, s56, 192\n" \
  "v_lshl_add_u32 v5, v5, 4, s51\n" \
  "v_add_u32 v2, s58, v4\n" \
  "v_mul_u32_u24 v2, 1152, v2\n" \
  "v_add_u32 v2, v2, v5\n" \
  "v_and_b32 v4, 7, v4\n" \
  "v_add_u32 v3, s59, v4\n" \
  "v_mul_u32_u24 v3, 1152, v3\n" \
  "v_add_u32 v3, v3, v5\n" \
  "s_mul_i32 s51, s56, 15360\n" \
  "s_cmp_ge_u32 s56, 3\n" \
  "s_cselect_b32 s46, 27648, 0\n" \
  "s_add_u32 s51, s51, s46\n" \
  "s_mov_b32 m0, s51\n" \
  "v_and_b32 v4, 1, v11\n" \
  "v_bfe_u32 v7, %[t], 2, 2\n" \
  "v_lshl_add_u32 v1, v4, 3, v7\n" \
  "v_lshlrev_b32 v1, 6, v1\n" \
  "v_lshrrev_b32 v5, 1, v11\n" \
  "v_xor_b32 v5, v5, v4\n" \
  "v_lshl_add_u32 v1, v5, 5, v1\n" \
  "v_and_b32 v5, 3, %[t]\n" \
  "v_lshl_add_u32 v1, v5, 3, v1\n" \
  "v_add_u32 v1, s51, v1\n" \
  "s_barrier\n" \
  "s_barrier\n" \
  "s_barrier\n" \
  "s_barrier\n" \
  "global_load_lds_dwordx4 v2, s[48:49]" DNTF "\n" \
  "s_add_u32 m0, m0, 1024\n" \
  "v_add_u32 v2, 64, v2\n" \
  "global_load_lds_dwordx4 v2, s[48:49]" DNTF "\n" \
  "s_add_u32 m0, m0, 1024\n" \
  "v_add_u32 v2, 64, v2\n" \
  "global_load_lds_dwordx4 v2, s[48:49]" DNTF "\n" \
  "s_add_u32 m0, m0, 1024\n" \
  "v_add_u32 v2, 18304, v2\n" \
  "global_load_lds_dwordx4 v2, s[48:49]" DNTF "\n" \
  "s_add_u32 m0, m0, 1024\n" \
  "v_add_u32 v2, 64, v2\n" \
  "global_load_lds_dwordx4 v2, s[48:49]" DNTF "\n" \
  "s_add_u32 m0, m0, 1024\n" \
  "v_add_u32 v2, 64, v2\n" \
  "global_load_lds_dwordx4 v2, s[48:49]" DNTF "\n" \
  "s_add_u32 m0, m0, 1024\n" \
  "v_add_u32 v2, 18304, v2\n" \
  "global_load_lds_dwordx4 v2, s[48:49]" DNTF "\n" \
  "s_add_u32 m0, m0, 1024\n" \
  "v_add_u32 v2, 64, v2\n" \
  "global_load_lds_dwordx4 v2, s[48:49]" DNTF "\n" \
  "s_add_u32 m0, m0, 1024\n" \
  "v_add_u32 v2, 64, v2\n" \
  "global_load_lds_dwordx4 v2, s[48:49]" DNTF "\n" \
  "s_add_u32 m0, m0, 1024\n" \
  "v_add_u32 v2, 18304, v2\n" \
  "global_load_lds_dwordx4 v2, s[48:49]" DNTF "\n" \
  "s_add_u32 m0, m0, 1024\n" \
  "v_add_u32 v2, 64, v2\n" \
  "global_load_lds_dwordx4 v2, s[48:49]" DNTF "\n" \
  "s_add_u32 m0, m0, 1024\n" \
  "v_add_u32 v2, 64, v2\n" \
  "global_load_lds_dwordx4 v2, s[48:49]" DNTF "\n" \
  "s_add_u32 m0, m0, 1024\n" \
  "v_add_u32 v2, 18304, v2\n" \
  "global_load_lds_dwordx4 v3, s[48:49]" DNTF "\n" \
  "s_add_u32 m0, m0, 1024\n" \
  "v_add_u32 v3, 64, v3\n" \
  "global_load_lds_dwordx4 v3, s[48:49]" DNTF "\n" \
  "s_add_u32 m0, m0, 1024\n" \
  "v_add_u32 v3, 64, v3\n" \
  "global_load_lds_dwordx4 v3, s[48:49]" DNTF "\n" \
  "s_add_u32 m0, m0, 1024\n" \
  "v_add_u32 v3, 18304, v3\n" \
  GSTAMP6 \
  "s_barrier\n" \
  "s_barrier\n" \
  "s_barrier\n" \
  "s_waitcnt vmcnt(14)\n" \
  "ds_read_b64_tr_b16 v[40:41], v1 offset:0\n" \
  "ds_read_b64_tr_b16 v[42:43], v1 offset:256\n" \
  "s_waitcnt vmcnt(13)\n" \
  "ds_read_b64_tr_b16 v[44:45], v1 offset:1024\n" \
  "ds_read_b64_tr_b16 v[46:47], v1 offset:1280\n" \
  "s_waitcnt vmcnt(12)\n" \
  "ds_read_b64_tr_b16 v[48:49], v1 offset:2048\n" \
  "ds_read_b64_tr_b16 v[50:51], v1 offset:2304\n" \
  "s_waitcnt vmcnt(11)\n" \
  "ds_read_b64_tr_b16 v[52:53], v1 offset:3072\n" \
  "ds_read_b64_tr_b16 v[54:55], v1 offset:3328\n" \
  "s_waitcnt vmcnt(10)\n" \
  "ds_read_b64_tr_b16 v[56:57], v1 offset:4096\n" \
  "ds_read_b64_tr_b16 v[58:59], v1 offset:4352\n" \
  "s_waitcnt vmcnt(9)\n" \
  "ds_read_b64_tr_b16 v[60:61], v1 offset:5120\n" \
  "ds_read_b64_tr_b16 v[62:63], v1 offset:5376\n" \
  "s_waitcnt vmcnt(8)\n" \
  "ds_read_b64_tr_b16 v[64:65], v1 offset:6144\n" \
  "ds_read_b64_tr_b16 v[66:67], v1 offset:6400\n" \
  "s_waitcnt vmcnt(7)\n" \
  "ds_read_b64_tr_b16 v[68:69], v1 offset:7168\n" \
  "ds_read_b64_tr_b16 v[70:71], v1 offset:7424\n" \
  "s_waitcnt vmcnt(6)\n" \
  "ds_read_b64_tr_b16 v[72:73], v1 offset:8192\n" \
  "ds_read_b64_tr_b16 v[74:75], v1 offset:8448\n" \
  "s_waitcnt vmcnt(5)\n" \
  "ds_read_b64_tr_b16 v[76:77], v1 offset:9216\n" \
  "ds_read_b64_tr_b16 v[78:79], v1 offset:9472\n" \
  "s_waitcnt vmcnt(4)\n" \
  "ds_read_b64_tr_b16 v[80:81], v1 offset:10240\n" \
  "ds_read_b64_tr_b16 v[82:83], v1 offset:10496\n" \
  "s_waitcnt vmcnt(3)\n" \
  "ds_read_b64_tr_b16 v[84:85], v1 offset:11264\n" \
  "ds_read_b64_tr_b16 v[86:87], v1 offset:11520\n" \
  "s_waitcnt vmcnt(2)\n" \
  "ds_read_b64_tr_b16 v[88:89], v1 offset:12288\n" \
  "ds_read_b64_tr_b16 v[90:91], v1 offset:12544\n" \
  "s_waitcnt vmcnt(1)\n" \
  "ds_read_b64_tr_b16 v[92:93], v1 offset:13312\n" \
  "ds_read_b64_tr_b16 v[94:95], v1 offset:13568\n" \
  "s_waitcnt vmcnt(0)\n" \
  "ds_read_b64_tr_b16 v[96:97], v1 offset:14336\n" \
  "ds_read_b64_tr_b16 v[98:99], v1 offset:14592\n" \
  "s_lshl_b32 s51, s56, 5\n" \
  "v_lshrrev_b32 v5, 1, v11\n" \
  "v_lshl_add_u32 v5, v5, 4, v10\n" \
  "v_add_u32 v5, s51, v5\n" \
  "v_mul_u32_u24 v6, 171, v5\n" \
  "v_lshrrev_b32 v6, 11, v6\n" \
  "v_mul_u32_u24 v6, 12096, v6\n" \
  "v_lshl_add_u32 v6, v5, 4, v6\n" \
  "v_mul_u32_u24 v4, 393216, v4\n" \
  "s_mul_i32 s51, s57, 192\n" \
  "v_add3_u32 v38, v6, v4, s51\n" \
  "s_mov_b32 s86, 6455296\n" \
  "s_mov_b32 s87, 6651904\n" \
  "v_mov_b32 v31, s44\n" \
  "v_mov_b32 v35, s44\n" \
  "v_mov_b32 v3, 159488\n" \
  GSTAMP7 \
  "s_barrier\n" \
  "s_branch Ed0%=\n" \
  "Ewz%=:\n" \
  "v_mov_b32 v4, 147456\n" \
  "v_lshl_add_u32 v4, v12, 4, v4\n" \
  "v_mov_b64 v[16:17], 0\n" \
  "v_mov_b64 v[18:19], 0\n" \
  "ds_write_b128 v4, v[16:19] offset:0\n" \
  "ds_write_b128 v4, v[16:19] offset:1024\n" \
  "ds_write_b128 v4, v[16:19] offset:2048\n" \
  "ds_write_b128 v4, v[16:19] offset:3072\n" \
  "ds_write_b128 v4, v[16:19] offset:4096\n" \
  "ds_write_b128 v4, v[16:19] offset:5120\n" \
  "ds_write_b128 v4, v[16:19] offset:6144\n" \
  "ds_write_b128 v4, v[16:19] offset:7168\n" \
  "ds_write_b128 v4, v[16:19] offset:8192\n" \
  "s_barrier\n" \
  "s_barrier\n" \
  "s_barrier\n" \
  "s_barrier\n" \
  "v_add_u32 v4, -6144, v4\n" \
  "ds_write_b128 v4, v[16:19] offset:0\n" \
  "ds_write_b128 v4, v[16:19] offset:1024\n" \
  "ds_write_b128 v4, v[16:19] offset:2048\n" \
  "v_mov_b32 v3, 159488\n" \
  "v_mov_b32 v13, 160800\n" \
  "s_waitcnt lgkmcnt(0)\n" \
  "s_barrier\n" \
  "s_barrier\n" \
  "s_barrier\n" \
  "s_branch Eh1%=\n" \
  "Ek0%=:\n" \
  "v_lshlrev_b32 v1, 4, %[t]\n" \
  "s_mov_b32 s51, 87040\n" \
  "s_mov_b32 s52, 95232\n" \
  "v_and_b32 v2, 7, %[t]\n" \
  "v_mov_b32 v3, 159488\n" \
  "v_lshl_add_u32 v4, v2, 2, v3\n" \
  "v_and_b32 v5, 3, %[t]\n" \
  "v_lshl_add_u32 v5, v5, 4, v3\n" \
  "v_mov_b32 v6, 1\n" \
  "v_mov_b32 v7, 147456\n" \
  "v_lshrrev_b32 v8, 3, %[t]\n" \
  "v_lshl_add_u32 v8, v8, 2, v7\n" \
  "v_lshl_add_u32 v9, v2, 4, v7\n" \
  "s_lshl_b32 s56, %[w], 3\n" \
  "v_mov_b32 v13, 160800\n" \
  "v_lshrrev_b32 v14, 5, %[t]\n" \
  "v_and_b32 v15, 31, %[t]\n" \
  "v_mul_u32_u24 v36, 1184, v14\n" \
  "v_mad_u32_u24 v36, v15, 12, v36\n" \
  "v_add_u32 v36, 120832, v36\n" \
  "v_lshl_add_u32 v14, v14, 2, v13\n" \
  "v_lshlrev_b32 v15, 4, v15\n" \
  "v_add_u32 v15, 6453760, v15\n" \
  "s_mov_b32 s45, -1536\n" \
  "s_movk_i32 s97, 128\n" \
  "Ekt%=:\n" \
  "s_memtime s[46:47]\n" \
  "s_waitcnt lgkmcnt(0)\n" \
  "s_sub_u32 s46, s46, s92\n" \
  "s_cmp_gt_u32 s46, " STR(KT) "\n" \
  "s_cbranch_scc1 Eku%=\n" \
  "s_sleep 2\n" \
  "s_sub_u32 s97, s97, 1\n" \
  "s_cbranch_scc0 Ekt%=\n" \
  "Eku%=:\n" \
  "buffer_load_dwordx4 v[20:23], v1, s[40:43], s51 offen sc1\n" \
  "buffer_load_dwordx4 v[24:27], v1, s[40:43], s52 offen sc1\n" \
  "s_waitcnt vmcnt(1)\n" \
  "v_bitop3_b32 v28, v21, v23, s44 bitop3:0x7e\n" \
  "v_cmp_ne_u32 vcc, 0, v28\n" \
  "s_cbranch_vccz Ekax%=\n" \
  "s_movk_i32 s97, 0x7fff\n" \
  "Ekar%=:\n" \
  "buffer_load_dwordx4 v[20:23], v1, s[40:43], s51 offen sc1\n" \
  "s_waitcnt vmcnt(0)\n" \
  "v_bitop3_b32 v28, v21, v23, s44 bitop3:0x7e\n" \
  "v_cmp_ne_u32 vcc, 0, v28\n" \
  "s_cbranch_vccz Ekax%=\n" \
  "s_sleep " STR(KSLP) "\n" \
  "s_sub_u32 s97, s97, 1\n" \
  "s_cbranch_scc0 Ekar%=\n" \
  "Ekax%=:\n" \
  "ds_max_u32 v4, v20 offset:96\n" \
  "ds_max_u32 v4, v22 offset:128\n" \
  "s_waitcnt vmcnt(0)\n" \
  "v_bitop3_b32 v28, v25, v27, s44 bitop3:0x7e\n" \
  "v_cmp_ne_u32 vcc, 0, v28\n" \
  "s_cbranch_vccz Ekbx%=\n" \
  "s_movk_i32 s97, 0x7fff\n" \
  "Ekbr%=:\n" \
  "buffer_load_dwordx4 v[24:27], v1, s[40:43], s52 offen sc1\n" \
  "s_waitcnt vmcnt(0)\n" \
  "v_bitop3_b32 v28, v25, v27, s44 bitop3:0x7e\n" \
  "v_cmp_ne_u32 vcc, 0, v28\n" \
  "s_cbranch_vccz Ekbx%=\n" \
  "s_sleep " STR(KSLP) "\n" \
  "s_sub_u32 s97, s97, 1\n" \
  "s_cbranch_scc0 Ekbr%=\n" \
  "Ekbx%=:\n" \
  GSTAMP8 \
  "ds_max_u32 v4, v24 offset:96\n" \
  "ds_max_u32 v4, v26 offset:128\n" \
  "s_waitcnt lgkmcnt(0)\n" \
  "s_barrier\n" \
  GSTAMP2 \
  "ds_read_b128 v[28:31], v5 offset:96\n" \
  "s_waitcnt lgkmcnt(0)\n" \
  "v_min3_u32 v28, v28, v29, v30\n" \
  "v_min_u32 v28, v28, v31\n" \
  "s_nop 1\n" \
  "v_min_u32_dpp v28, v28, v28 quad_perm:[1,0,3,2] row_mask:0xf bank_mask:0xf\n" \
  "s_nop 1\n" \
  "v_min_u32_dpp v28, v28, v28 quad_perm:[2,3,0,1] row_mask:0xf bank_mask:0xf\n" \
  "v_cmp_ge_u32 s[88:89], v20, v28\n" \
  "v_cmp_ge_u32 s[90:91], v22, v28\n" \
  "v_cmp_ge_u32 s[94:95], v24, v28\n" \
  "v_cmp_ge_u32 s[96:97], v26, v28\n" \
  "s_mov_b64 exec, s[88:89]\n" \
  "ds_add_rtn_u32 v21, v3, v6 offset:24\n" \
  "s_mov_b64 exec, s[90:91]\n" \
  "ds_add_rtn_u32 v23, v3, v6 offset:24\n" \
  "s_mov_b64 exec, s[94:95]\n" \
  "ds_add_rtn_u32 v25, v3, v6 offset:24\n" \
  "s_mov_b64 exec, s[96:97]\n" \
  "ds_add_rtn_u32 v27, v3, v6 offset:24\n" \
  "s_waitcnt lgkmcnt(0)\n" \
  "v_lshl_add_u32 v27, v27, 2, v7\n" \
  "ds_write_b32 v27, v26\n" \
  "s_mov_b64 exec, s[94:95]\n" \
  "v_lshl_add_u32 v25, v25, 2, v7\n" \
  "ds_write_b32 v25, v24\n" \
  "s_mov_b64 exec, s[90:91]\n" \
  "v_lshl_add_u32 v23, v23, 2, v7\n" \
  "ds_write_b32 v23, v22\n" \
  "s_mov_b64 exec, s[88:89]\n" \
  "v_lshl_add_u32 v21, v21, 2, v7\n" \
  "ds_write_b32 v21, v20\n" \
  "s_mov_b64 exec, -1\n" \
  "s_waitcnt lgkmcnt(0)\n" \
  "s_barrier\n" \
  GSTAMP4 \
  "ds_read_b32 v18, v3 offset:24\n" \
  "Ec1%=:\n" \
  "ds_read_b32 v17, v8\n" \
  "ds_read_b128 v[20:23], v9\n" \
  "ds_read_b128 v[24:27], v9 offset:128\n" \
  "v_mov_b32 v16, 0\n" \
  "s_waitcnt lgkmcnt(3)\n" \
  "v_readfirstlane_b32 s46, v18\n" \
  "s_cmp_ge_u32 s56, s46\n" \
  "s_cbranch_scc1 Ec4%=\n" \
  "s_mov_b32 s47, 64\n" \
  "v_mov_b32 v19, v9\n" \
  "Ec2%=:\n" \
  "s_waitcnt lgkmcnt(0)\n" \
  "v_cmp_gt_u32 s[88:89], v20, v17\n" \
  "v_cmp_gt_u32 s[90:91], v21, v17\n" \
  "v_cmp_gt_u32 s[94:95], v22, v17\n" \
  "v_cmp_gt_u32 s[96:97], v23, v17\n" \
  "v_addc_co_u32 v16, vcc, 0, v16, s[88:89]\n" \
  "v_addc_co_u32 v16, vcc, 0, v16, s[90:91]\n" \
  "v_addc_co_u32 v16, vcc, 0, v16, s[94:95]\n" \
  "v_addc_co_u32 v16, vcc, 0, v16, s[96:97]\n" \
  "v_cmp_gt_u32 s[88:89], v24, v17\n" \
  "v_cmp_gt_u32 s[90:91], v25, v17\n" \
  "v_cmp_gt_u32 s[94:95], v26, v17\n" \
  "v_cmp_gt_u32 s[96:97], v27, v17\n" \
  "v_addc_co_u32 v16, vcc, 0, v16, s[88:89]\n" \
  "v_addc_co_u32 v16, vcc, 0, v16, s[90:91]\n" \
  "v_addc_co_u32 v16, vcc, 0, v16, s[94:95]\n" \
  "v_addc_co_u32 v16, vcc, 0, v16, s[96:97]\n" \
  "s_cmp_ge_u32 s47, s46\n" \
  "s_cbranch_scc1 Ec3%=\n" \
  "s_add_u32 s47, s47, 64\n" \
  "v_add_u32 v19, 256, v19\n" \
  "ds_read_b128 v[20:23], v19\n" \
  "ds_read_b128 v[24:27], v19 offset:128\n" \
  "s_branch Ec2%=\n" \
  "Ec3%=:\n" \
  "v_add_u32_dpp v16, v16, v16 quad_perm:[1,0,3,2] row_mask:0xf bank_mask:0xf\n" \
  "s_nop 1\n" \
  "v_add_u32_dpp v16, v16, v16 quad_perm:[2,3,0,1] row_mask:0xf bank_mask:0xf\n" \
  "s_addk_i32 s56, 64\n" \
  "s_cmp_lt_u32 s56, s46\n" \
  "v_add_u32_dpp v16, v16, v16 row_half_mirror row_mask:0xf bank_mask:0xf\n" \
  "v_min_u32 v16, 16, v16\n" \
  "v_lshl_add_u32 v16, v16, 2, v13\n" \
  "ds_write_b32 v16, v17\n" \
  "s_cbranch_scc0 Ec4%=\n" \
  "v_add_u32 v8, 256, v8\n" \
  "s_branch Ec1%=\n" \
  "Ec4%=:\n" \
  "s_waitcnt lgkmcnt(0)\n" \
  "s_barrier\n" \
  GSTAMP9 \
  "ds_read_b32 v16, v14\n" \
  "s_waitcnt lgkmcnt(0)\n" \
  "v_and_b32 v16, 0x7ff, v16\n" \
  "v_mad_i32_i24 v15, v16, s45, v15\n" \
  "s_movk_i32 s97, 0x7fff\n" \
  "Ehr%=:\n" \
  "buffer_load_dwordx4 v[20:23], v15, s[40:43], 0 offen offset:0 sc1\n" \
  "buffer_load_dwordx4 v[24:27], v15, s[40:43], 0 offen offset:512 sc1\n" \
  "buffer_load_dwordx4 v[28:31], v15, s[40:43], 0 offen offset:1024 sc1\n" \
  "v_mul_u32_u24 v1, 1184, v10\n" \
  "v_lshl_add_u32 v1, v11, 4, v1\n" \
  "v_add_u32 v1, 120832, v1\n" \
  "v_lshrrev_b32 v2, 3, v12\n" \
  "v_mul_u32_u24 v2, 320, v2\n" \
  "v_and_b32 v4, 7, %[t]\n" \
  "v_lshl_add_u32 v2, v4, 1, v2\n" \
  "s_lshl4_add_u32 s46, %[w], 141312\n" \
  "v_add_u32 v2, s46, v2\n" \
  "s_waitcnt vmcnt(0)\n" \
  "v_bitop3_b32 v16, v23, v27, s44 bitop3:0x7e\n" \
  "v_xor_b32 v31, s44, v31\n" \
  "v_or_b32 v16, v16, v31\n" \
  "v_cmp_ne_u32 vcc, 0, v16\n" \
  "s_cbranch_vccz Ehx%=\n" \
  "s_sub_u32 s97, s97, 1\n" \
  "s_cbranch_scc0 Ehr%=\n" \
  "Ehx%=:\n" \
  GSTAMP10 \
  "ds_write2_b32 v36, v20, v24 offset0:0 offset1:96\n" \
  "ds_write2_b32 v36, v21, v25 offset0:1 offset1:97\n" \
  "ds_write2_b32 v36, v22, v26 offset0:2 offset1:98\n" \
  "ds_write2_b32 v36, v28, v29 offset0:192 offset1:193\n" \
  "ds_write_b32 v36, v30 offset:776\n" \
  "Eg0%=:\n" \
  "s_waitcnt lgkmcnt(0)\n" \
  "s_barrier\n" \
  GSTAMP5 \
  "ds_read_b128 v[112:115], v1 offset:0\n" \
  "ds_read_b128 v[116:119], v1 offset:64\n" \
  "ds_read_b128 v[120:123], v1 offset:128\n" \
  "ds_read_b128 v[124:127], v1 offset:192\n" \
  "ds_read_b128 v[20:23], v1 offset:256\n" \
  "ds_read_b128 v[24:27], v1 offset:320\n" \
  "ds_read_b128 v[28:31], v1 offset:384\n" \
  "ds_read_b128 v[32:35], v1 offset:448\n" \
  "s_waitcnt lgkmcnt(7)\n" \
  "v_mfma_f32_16x16x32_bf16 v[16:19], v[112:115], v[40:43], 0\n" \
  "ds_read_b128 v[112:115], v1 offset:512\n" \
  "s_waitcnt lgkmcnt(7)\n" \
  "v_mfma_f32_16x16x32_bf16 v[16:19], v[116:119], v[44:47], v[16:19]\n" \
  "ds_read_b128 v[116:119], v1 offset:576\n" \
  "s_waitcnt lgkmcnt(7)\n" \
  "v_mfma_f32_16x16x32_bf16 v[16:19], v[120:123], v[48:51], v[16:19]\n" \
  "ds_read_b128 v[120:123], v1 offset:640\n" \
  "s_waitcnt lgkmcnt(7)\n" \
  "v_mfma_f32_16x16x32_bf16 v[16:19], v[124:127], v[52:55], v[16:19]\n" \
  "ds_read_b128 v[124:127], v1 offset:704\n" \
  "s_waitcnt lgkmcnt(7)\n" \
  "v_mfma_f32_16x16x32_bf16 v[16:19], v[20:23], v[56:59], v[16:19]\n" \
  "ds_read_b128 v[20:23], v1 offset:768\n" \
  "s_waitcnt lgkmcnt(7)\n" \
  "v_mfma_f32_16x16x32_bf16 v[16:19], v[24:27], v[60:63], v[16:19]\n" \
  "ds_read_b128 v[24:27], v1 offset:832\n" \
  "s_waitcnt lgkmcnt(7)\n" \
  "v_mfma_f32_16x16x32_bf16 v[16:19], v[28:31], v[64:67], v[16:19]\n" \
  "ds_read_b128 v[28:31], v1 offset:896\n" \
  "s_waitcnt lgkmcnt(7)\n" \
  "v_mfma_f32_16x16x32_bf16 v[16:19], v[32:35], v[68:71], v[16:19]\n" \
  "ds_read_b128 v[32:35], v1 offset:960\n" \
  "s_waitcnt lgkmcnt(7)\n" \
  "v_mfma_f32_16x16x32_bf16 v[16:19], v[112:115], v[72:75], v[16:19]\n" \
  "ds_read_b128 v[112:115], v1 offset:1024\n" \
  "s_waitcnt lgkmcnt(7)\n" \
  "v_mfma_f32_16x16x32_bf16 v[16:19], v[116:119], v[76:79], v[16:19]\n" \
  "ds_read_b128 v[116:119], v1 offset:1088\n" \
  "s_waitcnt lgkmcnt(7)\n" \
  "v_mfma_f32_16x16x32_bf16 v[16:19], v[120:123], v[80:83], v[16:19]\n" \
  "s_waitcnt lgkmcnt(6)\n" \
  "v_mfma_f32_16x16x32_bf16 v[16:19], v[124:127], v[84:87], v[16:19]\n" \
  "s_waitcnt lgkmcnt(5)\n" \
  "v_mfma_f32_16x16x32_bf16 v[16:19], v[20:23], v[88:91], v[16:19]\n" \
  "s_waitcnt lgkmcnt(4)\n" \
  "v_mfma_f32_16x16x32_bf16 v[16:19], v[24:27], v[92:95], v[16:19]\n" \
  "s_waitcnt lgkmcnt(3)\n" \
  "v_mfma_f32_16x16x32_bf16 v[16:19], v[28:31], v[96:99], v[16:19]\n" \
  "s_waitcnt lgkmcnt(2)\n" \
  "v_mfma_f32_16x16x32_bf16 v[16:19], v[32:35], v[100:103], v[16:19]\n" \
  "s_waitcnt lgkmcnt(1)\n" \
  "v_mfma_f32_16x16x32_bf16 v[16:19], v[112:115], v[104:107], v[16:19]\n" \
  "s_waitcnt lgkmcnt(0)\n" \
  "v_mfma_f32_16x16x32_bf16 v[16:19], v[116:119], v[108:111], v[16:19]\n" \
  "s_nop 7\n" \
  "v_mov_b64 v[20:21], v[16:17]\n" \
  "v_mov_b32_dpp v16, v18 row_ror:8 row_mask:0xf bank_mask:0xc\n" \
  "v_mov_b32_dpp v17, v19 row_ror:8 row_mask:0xf bank_mask:0xc\n" \
  "v_mov_b32_dpp v18, v20 row_ror:8 row_mask:0xf bank_mask:0x3\n" \
  "v_mov_b32_dpp v19, v21 row_ror:8 row_mask:0xf bank_mask:0x3\n" \
  "v_cvt_pk_bf16_f32 v16, 0, v16\n" \
  "v_cvt_pk_bf16_f32 v17, 0, v17\n" \
  "v_mul_f32 v20, 0xbfb8aa3b, v16\n" \
  "v_mul_f32 v21, 0xbfb8aa3b, v17\n" \
  "v_exp_f32 v20, v20\n" \
  "v_exp_f32 v21, v21\n" \
  "v_cvt_pk_bf16_f32 v18, 0, v18\n" \
  "v_pk_add_f32 v[20:21], v[20:21], 1.0 op_sel_hi:[1,0]\n" \
  "v_rcp_f32 v20, v20\n" \
  "v_rcp_f32 v21, v21\n" \
  "v_cvt_pk_bf16_f32 v19, 0, v19\n" \
  "v_pk_mul_f32 v[16:17], v[16:17], v[20:21]\n" \
  "v_cvt_pk_bf16_f32 v16, 0, v16\n" \
  "v_cvt_pk_bf16_f32 v17, 0, v17\n" \
  "v_pk_mul_f32 v[16:17], v[16:17], v[18:19]\n" \
  "v_cvt_pk_bf16_f32 v16, v16, v17\n" \
  "ds_write_b16 v2, v16\n" \
  "ds_write_b16_d16_hi v2, v16 offset:160\n" \
  GSTAMP11 \
  "s_waitcnt lgkmcnt(0)\n" \
  "s_barrier\n" \
  "s_cmp_gt_u32 %[w], 2\n" \
  "s_cbranch_scc1 Ez%=\n" \
  "s_lshr_b32 s46, s57, 4\n" \
  "v_bfe_u32 v1, %[t], 3, 1\n" \
  "v_lshl_add_u32 v1, v1, 3, v3\n" \
  "s_lshl_b32 s46, s46, 4\n" \
  "v_add_u32 v1, s46, v1\n" \
  "ds_read_b64 v[4:5], v1 offset:32\n" \
  "s_and_b32 s46, s57, 15\n" \
  "s_mul_i32 s46, s46, 12\n" \
  "s_lshl2_add_u32 s46, %[w], s46\n" \
  "v_and_b32 v2, 3, %[t]\n" \
  "v_add_u32 v6, s46, v2\n" \
  "v_lshrrev_b32 v7, 5, v6\n" \
  "v_lshl_add_u32 v6, v7, 6, v6\n" \
  "v_min_u32 v7, 2, v11\n" \
  "v_lshl_add_u32 v6, v7, 5, v6\n" \
  "v_mul_u32_u24 v7, 683, v6\n" \
  "v_lshrrev_b32 v7, 11, v7\n" \
  "v_mul_u32_u24 v8, 3, v7\n" \
  "v_sub_u32 v8, v6, v8\n" \
  "v_and_b32 v9, 1, v7\n" \
  "v_sub_u32 v8, v8, v9\n" \
  "v_min_u32 v8, 2, v8\n" \
  "v_mul_u32_u24 v7, 16512, v7\n" \
  "v_lshlrev_b32 v9, 3, v6\n" \
  "s_waitcnt lgkmcnt(0)\n" \
  "v_not_b32 v13, v5\n" \
  "v_and_b32 v13, 0x7ff, v13\n" \
  "v_lshrrev_b32 v14, 1, v13\n" \
  "v_lshl_add_u32 v7, v14, 4, v7\n" \
  "v_lshrrev_b32 v14, 4, v13\n" \
  "v_lshlrev_b32 v14, 2, v14\n" \
  "v_mul_u32_u24 v4, 1184, v4\n" \
  "v_lshl_add_u32 v4, v6, 1, v4\n" \
  "v_add_u32 v4, 101888, v4\n" \
  "v_mov_b32 v15, 0\n" \
  "ds_read_u16_d16_hi v15, v4\n" \
  "s_mov_b32 s90, 139264\n" \
  "s_mov_b32 s91, 136192\n" \
  "s_mov_b32 s86, 27136\n" \
  "s_mov_b32 s87, 54784\n" \
  "s_mov_b32 s88, 82432\n" \
  "buffer_load_dwordx4 v[16:19], v7, s[40:43], s90 offen sc1\n" \
  "buffer_load_dword v20, v14, s[40:43], s91 offen sc1\n" \
  "buffer_load_dwordx2 v[22:23], v9, s[40:43], s86 offen sc1\n" \
  "buffer_load_dwordx2 v[24:25], v9, s[40:43], s87 offen sc1\n" \
  "buffer_load_dwordx2 v[26:27], v9, s[40:43], s88 offen sc1\n" \
  "v_lshrrev_b32 v28, 2, v12\n" \
  "v_mul_u32_u24 v28, 192, v28\n" \
  "v_lshl_add_u32 v28, v2, 4, v28\n" \
  "s_mul_i32 s46, s57, 12288\n" \
  "s_lshl_b32 s47, %[w], 6\n" \
  "s_add_u32 s46, s46, s47\n" \
  "s_add_u32 s46, s46, 6455296\n" \
  "s_add_u32 s47, s46, 6144\n" \
  ".if " STR(FSLP) "\n" \
  "s_sleep " STR(FSLP) "\n" \
  ".endif\n" \
  GSTAMP12 \
  "buffer_load_dwordx4 v[40:43], v28, s[40:43], s46 offen sc1\n" \
  "buffer_load_dwordx4 v[44:47], v28, s[40:43], s46 offen offset:3072 sc1\n" \
  "buffer_load_dwordx4 v[48:51], v28, s[40:43], s47 offen sc1\n" \
  "buffer_load_dwordx4 v[52:55], v28, s[40:43], s47 offen offset:3072 sc1\n" \
  "s_waitcnt vmcnt(4)\n" \
  "v_bitop3_b32 v21, v19, v20, s44 bitop3:0x7e\n" \
  "v_bitop3_b32 v30, v23, v25, s44 bitop3:0x7e\n" \
  "v_xor_b32 v29, s44, v27\n" \
  "v_or3_b32 v21, v21, v30, v29\n" \
  "v_cmp_ne_u32 vcc, 0, v21\n" \
  "s_cbranch_vccz Efx%=\n" \
  "s_movk_i32 s97, 0x7fff\n" \
  "Efr%=:\n" \
  "buffer_load_dwordx4 v[16:19], v7, s[40:43], s90 offen sc1\n" \
  "buffer_load_dword v20, v14, s[40:43], s91 offen sc1\n" \
  "buffer_load_dwordx2 v[22:23], v9, s[40:43], s86 offen sc1\n" \
  "buffer_load_dwordx2 v[24:25], v9, s[40:43], s87 offen sc1\n" \
  "buffer_load_dwordx2 v[26:27], v9, s[40:43], s88 offen sc1\n" \
  "s_waitcnt vmcnt(0)\n" \
  "v_bitop3_b32 v21, v19, v20, s44 bitop3:0x7e\n" \
  "v_bitop3_b32 v30, v23, v25, s44 bitop3:0x7e\n" \
  "v_xor_b32 v29, s44, v27\n" \
  "v_or3_b32 v21, v21, v30, v29\n" \
  "v_cmp_ne_u32 vcc, 0, v21\n" \
  "s_cbranch_vccz Efx%=\n" \
  "s_sub_u32 s97, s97, 1\n" \
  "s_cbranch_scc0 Efr%=\n" \
  "Efx%=:\n" \
  "v_add_f32 v22, v22, v24\n" \
  "v_add_f32 v22, v22, v26\n" \
  "v_cvt_pk_bf16_f32 v22, 0, v22\n" \
  "v_and_b32 v20, 1, v13\n" \
  "v_cmp_eq_u32 vcc, 1, v20\n" \
  "v_cmp_eq_u32 s[94:95], 2, v8\n" \
  "v_mul_u32_u24 v13, 1152, v13\n" \
  "v_cndmask_b32 v16, v16, v17, vcc\n" \
  "v_cndmask_b32 v8, v8, v20, s[94:95]\n" \
  "v_cndmask_b32 v16, v16, v18, s[94:95]\n" \
  "v_lshlrev_b32 v8, 4, v8\n" \
  "v_lshrrev_b32 v16, v8, v16\n" \
  "v_lshlrev_b32 v16, 16, v16\n" \
  "v_lshl_add_u32 v13, v6, 1, v13\n" \
  "v_ashrrev_i32 v17, 31, v5\n" \
  "v_not_b32 v17, v17\n" \
  "v_or_b32 v17, 0x80000000, v17\n" \
  "v_xor_b32 v5, v5, v17\n" \
  "v_and_b32 v5, 0xfffff800, v5\n" \
  "v_cvt_pk_bf16_f32 v5, 0, v5\n" \
  "v_mul_f32 v5, 0xbfb8aa3b, v5\n" \
  "v_exp_f32 v5, v5\n" \
  "v_and_b32 v17, 2, v10\n" \
  "v_add_f32 v5, 1.0, v5\n" \
  "v_rcp_f32 v5, v5\n" \
  "v_cmp_eq_u32 vcc, 0, v17\n" \
  "v_cvt_pk_bf16_f32 v5, 0, v5\n" \
  "v_and_b32 v17, 1, v10\n" \
  "v_lshlrev_b32 v17, 4, v17\n" \
  "v_mov_b32 v18, 0x3f80\n" \
  "v_lshlrev_b32 v17, v17, v18\n" \
  "v_cndmask_b32 v17, 0, v17, vcc\n" \
  "v_lshrrev_b32 v18, 2, v10\n" \
  "v_cmp_eq_u32 vcc, 0, v18\n" \
  "s_nop 1\n" \
  "v_cndmask_b32 v32, 0, v17, vcc\n" \
  "v_cmp_eq_u32 vcc, 1, v18\n" \
  "s_nop 1\n" \
  "v_cndmask_b32 v33, 0, v17, vcc\n" \
  "v_cmp_eq_u32 vcc, 2, v18\n" \
  "s_nop 1\n" \
  "v_cndmask_b32 v34, 0, v17, vcc\n" \
  "v_mov_b32 v35, 0\n" \
  "s_waitcnt vmcnt(0) lgkmcnt(0)\n" \
  "v_xor_b32 v43, s44, v43\n" \
  "v_xor_b32 v47, s44, v47\n" \
  "v_xor_b32 v51, s44, v51\n" \
  "v_xor_b32 v55, s44, v55\n" \
  "v_or3_b32 v17, v43, v47, v51\n" \
  "v_or_b32 v17, v17, v55\n" \
  "v_cmp_ne_u32 vcc, 0, v17\n" \
  "s_cbranch_vccnz Eps%=\n" \
  "Epb%=:\n" \
  GSTAMP13 \
  "v_mfma_f32_16x16x32_bf16 v[24:27], v[32:35], v[40:43], 0\n" \
  "v_mfma_f32_16x16x32_bf16 v[24:27], v[32:35], v[44:47], v[24:27]\n" \
  "v_mfma_f32_16x16x32_bf16 v[24:27], v[32:35], v[48:51], v[24:27]\n" \
  "v_mfma_f32_16x16x32_bf16 v[24:27], v[32:35], v[52:55], v[24:27]\n" \
  "s_mov_b32 s46, 0x0f0f0f0f\n" \
  "s_mov_b32 s47, 0x0f0f\n" \
  "s_nop 5\n" \
  "v_add_f32_dpp v17, v24, v24 row_ror:8 row_mask:0xf bank_mask:0x3\n" \
  "v_add_f32_dpp v17, v25, v25 row_ror:8 row_mask:0xf bank_mask:0xc\n" \
  "s_nop 1\n" \
  "v_add_f32_dpp v17, v17, v17 row_shl:4 row_mask:0xf bank_mask:0xf bound_ctrl:1\n" \
  "v_cvt_pk_bf16_f32 v17, 0, v17\n" \
  "v_mul_f32 v17, v17, v5\n" \
  "v_cvt_pk_bf16_f32 v17, 0, v17\n" \
  "v_add_f32 v17, v17, v15\n" \
  "v_cvt_pk_bf16_f32 v17, 0, v17\n" \
  "v_mul_f32 v17, v17, v22\n" \
  "v_cvt_pk_bf16_f32 v17, 0, v17\n" \
  "v_add_f32 v17, v17, v16\n" \
  "v_cvt_pk_bf16_f32 v17, 0, v17\n" \
  "s_mov_b64 exec, s[46:47]\n" \
  "global_store_short_d16_hi v13, v17, s[84:85]\n" \
  "s_or_b32 s46, s57, %[w]\n" \
  "s_cbranch_scc1 Ez%=\n" \
  "s_mov_b64 exec, 1\n" \
  "v_mov_b32 v17, s44\n" \
  "buffer_store_dword v17, off, s[40:43], 0 sc1\n" \
  "s_branch Ez%=\n" \
  "Eps%=:\n" \
  "s_movk_i32 s97, 0x7fff\n" \
  "Epr%=:\n" \
  GSTAMPC \
  "s_mov_b64 exec, vcc\n" \
  "buffer_load_dwordx4 v[40:43], v28, s[40:43], s46 offen sc1\n" \
  "buffer_load_dwordx4 v[44:47], v28, s[40:43], s46 offen offset:3072 sc1\n" \
  "buffer_load_dwordx4 v[48:51], v28, s[40:43], s47 offen sc1\n" \
  "buffer_load_dwordx4 v[52:55], v28, s[40:43], s47 offen offset:3072 sc1\n" \
  "s_waitcnt vmcnt(0)\n" \
  "v_xor_b32 v43, s44, v43\n" \
  "v_xor_b32 v47, s44, v47\n" \
  "v_xor_b32 v51, s44, v51\n" \
  "v_xor_b32 v55, s44, v55\n" \
  "v_or3_b32 v17, v43, v47, v51\n" \
  "v_or_b32 v17, v17, v55\n" \
  "v_cmp_ne_u32 vcc, 0, v17\n" \
  "s_mov_b64 exec, -1\n" \
  "s_cbranch_vccz Epb%=\n" \
  "s_sub_u32 s97, s97, 1\n" \
  "s_cbranch_scc0 Epr%=\n" \
  "s_branch Epb%=\n" \
  "Eh1%=:\n" \
  "s_setprio 3\n" \
  "v_lshl_add_u32 v1, v10, 2, v13\n" \
  "ds_read_b32 v16, v1\n" \
  "s_mov_b32 s46, 103424\n" \
  "v_lshl_add_u32 v4, v10, 2, v3\n" \
  "v_lshl_add_u32 v5, v11, 4, v3\n" \
  "v_add_u32 v18, 16, v10\n" \
  "s_waitcnt lgkmcnt(0)\n" \
  "v_not_b32 v16, v16\n" \
  "v_and_b32 v16, 0x7ff, v16\n" \
  "v_lshlrev_b32 v1, 3, v16\n" \
  "buffer_load_dwordx2 v[16:17], v1, s[40:43], s46 offen sc1\n" \
  "s_barrier\n" \
  "s_waitcnt vmcnt(0)\n" \
  "v_cmp_ne_u32 vcc, s44, v17\n" \
  "s_cbranch_vccz Eqx%=\n" \
  "s_movk_i32 s97, 0x7fff\n" \
  "Eqr%=:\n" \
  "buffer_load_dwordx2 v[16:17], v1, s[40:43], s46 offen sc1\n" \
  "s_waitcnt vmcnt(0)\n" \
  "v_cmp_ne_u32 vcc, s44, v17\n" \
  "s_cbranch_vccz Eqx%=\n" \
  "s_sub_u32 s97, s97, 1\n" \
  "s_cbranch_scc0 Eqr%=\n" \
  "Eqx%=:\n" \
  "ds_write_b32 v4, v16 offset:96\n" \
  "ds_read_b128 v[20:23], v5 offset:96\n" \
  "v_mov_b32 v17, v16\n" \
  "v_mov_b32 v19, v16\n" \
  "v_mov_b32 v16, 0\n" \
  "s_waitcnt lgkmcnt(0)\n" \
  "v_cmp_gt_u32 s[88:89], v20, v17\n" \
  "v_cmp_gt_u32 s[90:91], v21, v17\n" \
  "v_cmp_gt_u32 s[94:95], v22, v17\n" \
  "v_cmp_gt_u32 s[96:97], v23, v17\n" \
  "v_addc_co_u32 v16, vcc, 0, v16, s[88:89]\n" \
  "v_addc_co_u32 v16, vcc, 0, v16, s[90:91]\n" \
  "v_addc_co_u32 v16, vcc, 0, v16, s[94:95]\n" \
  "v_addc_co_u32 v16, vcc, 0, v16, s[96:97]\n" \
  "v_mov_b32 v17, v16\n" \
  "s_nop 1\n" \
  "v_permlane16_swap_b32 v16, v17\n" \
  "v_add_u32 v16, v16, v17\n" \
  "v_mov_b32 v17, v16\n" \
  "s_nop 1\n" \
  "v_permlane32_swap_b32 v16, v17\n" \
  "v_add_u32 v16, v16, v17\n" \
  "v_cmp_gt_u32 vcc, 8, v16\n" \
  "v_lshl_add_u32 v16, v16, 3, v3\n" \
  "s_mov_b64 exec, vcc\n" \
  "ds_write_b64 v16, v[18:19] offset:32\n" \
  "s_mov_b64 exec, -1\n" \
  GSTAMP11 \
  "s_waitcnt lgkmcnt(0)\n" \
  "s_barrier\n" \
  "s_branch Ez%=\n" \
  "Ed0%=:\n" \
  "v_and_b32 v2, 7, %[t]\n" \
  "v_lshl_add_u32 v2, v2, 3, v3\n" \
  "s_mov_b32 s97, 0x1fffff\n" \
  "Ed1%=:\n" \
  "ds_read_b32 v1, v2 offset:32\n" \
  "s_waitcnt lgkmcnt(0)\n" \
  "v_cmp_eq_u32 vcc, 0, v1\n" \
  "s_cbranch_vccz Ed2%=\n" \
  "s_sleep 1\n" \
  "s_sub_u32 s97, s97, 1\n" \
  "s_cbranch_scc0 Ed1%=\n" \
  "Ed2%=:\n" \
  "s_mov_b32 vcc_lo, 0x00ff00ff\n" \
  "s_mov_b32 vcc_hi, 0xff00ff00\n" \
  "v_cndmask_b32 v1, 32, v1, vcc\n" \
  "v_mul_u32_u24 v1, 160, v1\n" \
  "v_and_b32 v2, 16, %[t]\n" \
  "v_add_u32 v2, 138752, v2\n" \
  "v_add_u32 v1, v1, v2\n" \
  "s_setprio 3\n" \
  GSTAMP11 \
  "s_barrier\n" \
  "ds_read_b128 v[100:103], v1 offset:0\n" \
  "ds_read_b128 v[104:107], v1 offset:32\n" \
  "ds_read_b128 v[108:111], v1 offset:64\n" \
  "ds_read_b128 v[112:115], v1 offset:96\n" \
  "ds_read_b128 v[116:119], v1 offset:128\n" \
  "s_waitcnt lgkmcnt(4)\n" \
  "v_mfma_f32_16x16x32_bf16 v[16:19], v[100:103], v[40:43], 0\n" \
  "v_mfma_f32_16x16x32_bf16 v[20:23], v[100:103], v[44:47], 0\n" \
  "v_mfma_f32_16x16x32_bf16 v[24:27], v[100:103], v[48:51], 0\n" \
  "s_waitcnt lgkmcnt(3)\n" \
  "v_mfma_f32_16x16x32_bf16 v[16:19], v[104:107], v[52:55], v[16:19]\n" \
  "v_mfma_f32_16x16x32_bf16 v[20:23], v[104:107], v[56:59], v[20:23]\n" \
  "v_mfma_f32_16x16x32_bf16 v[24:27], v[104:107], v[60:63], v[24:27]\n" \
  "s_waitcnt lgkmcnt(2)\n" \
  "v_mfma_f32_16x16x32_bf16 v[16:19], v[108:111], v[64:67], v[16:19]\n" \
  "v_mfma_f32_16x16x32_bf16 v[20:23], v[108:111], v[68:71], v[20:23]\n" \
  "v_mfma_f32_16x16x32_bf16 v[24:27], v[108:111], v[72:75], v[24:27]\n" \
  "s_waitcnt lgkmcnt(1)\n" \
  "v_mfma_f32_16x16x32_bf16 v[16:19], v[112:115], v[76:79], v[16:19]\n" \
  "v_mfma_f32_16x16x32_bf16 v[20:23], v[112:115], v[80:83], v[20:23]\n" \
  "v_mfma_f32_16x16x32_bf16 v[24:27], v[112:115], v[84:87], v[24:27]\n" \
  "s_waitcnt lgkmcnt(0)\n" \
  "v_mfma_f32_16x16x32_bf16 v[16:19], v[116:119], v[88:91], v[16:19]\n" \
  "v_mfma_f32_16x16x32_bf16 v[20:23], v[116:119], v[92:95], v[20:23]\n" \
  "v_mfma_f32_16x16x32_bf16 v[24:27], v[116:119], v[96:99], v[24:27]\n" \
  "s_nop 5\n" \
  "v_cvt_pk_bf16_f32 v28, v16, v17\n" \
  "v_cvt_pk_bf16_f32 v32, v18, v19\n" \
  "v_cvt_pk_bf16_f32 v29, v20, v21\n" \
  "v_cvt_pk_bf16_f32 v33, v22, v23\n" \
  "v_cvt_pk_bf16_f32 v30, v24, v25\n" \
  "v_cvt_pk_bf16_f32 v34, v26, v27\n" \
  "buffer_store_dwordx4 v[28:31], v38, s[40:43], s86 offen sc1\n" \
  "buffer_store_dwordx4 v[32:35], v38, s[40:43], s87 offen sc1\n" \
  GSTAMP12 \
  "Ez%=:\n" \
  "s_mov_b64 exec, -1\n"

#define V4(a) "v" #a "0", "v" #a "1", "v" #a "2", "v" #a "3", "v" #a "4", "v" #a "5", "v" #a "6", "v" #a "7", "v" #a "8", "v" #a "9"
#define S4(a) "s" #a "0", "s" #a "1", "s" #a "2", "s" #a "3", "s" #a "4", "s" #a "5", "s" #a "6", "s" #a "7", "s" #a "8", "s" #a "9"
#define WAVE_CLOB "v1", "v2", "v3", "v4", "v5", "v6", "v7", "v8", "v9", V4(1), V4(2), V4(3), V4(4), V4(5), V4(6), V4(7), V4(8), V4(9), V4(10), V4(11), \
                  "v120", "v121", "v122", "v123", "v124", "v125", "v126", "v127", S4(4), S4(5), "s84", "s85", "s86", "s87", "s88", "s89", "s90", "s91", "s92", "s93", "s94", "s95", "s96", "s97", "s98", "s99", "vcc", "scc", "memory"

__global__ void __launch_bounds__(1024) __attribute__((aligned(KALIGN))) mega(const u16*, const u16*, const u16*, const u16*, const u16*, const u16*, const u16*, const u16*, const u16*, const u16*, const u16*,
                                             const u16*, const u16*, u16*, char* ws) {
  TS(0);
  int tid = threadIdx.x, blk = blockIdx.x, wave;
  const auto ka = __builtin_amdgcn_kernarg_segment_ptr();
  asm volatile("s_load_dword s99, %[k], 0x0\n\ts_load_dword s99, %[k], 0x40\n\tv_readfirstlane_b32 %[w], %[t]\n\ts_lshr_b32 %[w], %[w], 6\n\ts_cmpk_lt_u32 %[b], 192\n\ts_cbranch_scc1 Lcc%=\n" EW_HEAD "s_branch Lcm%=\n"
               "Lcc%=:\n.if " STR(ROT) "\nv_add_u32 v0, 768, v0\nv_and_b32 v0, 1023, v0\ns_add_u32 %[w], %[w], 12\ns_and_b32 %[w], %[w], 15\n.endif\n\ts_cmp_lt_u32 %[w], 12\n\ts_cbranch_scc1 Lcg%=\n" SW_HEAD "s_branch Lcs%=\nLcg%=:\n" GW_HEAD GW_BODY "s_branch Lcl%=\nLcs%=:\n" SW_BODY
               "Lcl%=:\n\ts_cmpk_ge_u32 %[b], 128\n\ts_cbranch_scc1 Lce%=\n" LW_ASM "s_branch Lce%=\nLcm%=:\n" EW_BODY
               "Lce%=:\n\ts_waitcnt lgkmcnt(0)"
               : [w] "=&s"(wave) STAMP_IO : [t] "{v0}"(tid), [b] "s"(blk), [k] "s"(ka) : WAVE_CLOB);
  if (blk >= 192) TS(15);
}

extern "C" void kernel_launch(void* const* d_in, const int* in_sizes, int n_in, void* d_out, int out_size, void* d_ws, size_t ws_size, hipStream_t stream) {
  (void)hipFuncSetAttribute((const void*)mega, hipFuncAttributeMaxDynamicSharedMemorySize, LDS_BYTES);
  mega<<<256, 1024, LDS_BYTES, stream>>>((const u16*)d_in[0], (const u16*)d_in[1], (const u16*)d_in[2], (const u16*)d_in[3], (const u16*)d_in[4],
                                         (const u16*)d_in[5], (const u16*)d_in[6], (const u16*)d_in[8], (const u16*)d_in[9], (const u16*)d_in[10],
                                         (const u16*)d_in[11], (const u16*)d_in[12], (const u16*)d_in[13], (u16*)d_out, (char*)d_ws);
}
```

```cpp
#include <hip/hip_runtime.h>
#include <stdint.h>

typedef unsigned short u16;
typedef unsigned int u32;
typedef unsigned long long u64;

#define WNT " nt"
#define DNTF ""
#define PRN 3
#define PRF 2
#define PRG 2
#define PRH 3
#define PRE_ 1
#define GPRIO 1
#define XDLY 0
#define X1T 4
#define X1SLP 14
#define PSLP 0
#define KSLP 1
#define FSLP 18
#define OFLAG " sc1"
#define WARM 0
#define PAD(k) do { if (WARM) asm volatile("Lpad" #k ":\n\ts_cbranch_execz Lwret"); } while (0)
#define PADS(k) ""
#define STR_(x) #x
#define STR(x) STR_(x)
#define PB(k) "s_branch Lpad" #k "\n\t"
#define PN "s_branch Lwret\n\t"
#define NPADC 24
#define PADTAB PB(0) PB(1) PB(2) PB(3) PB(4) PB(5) PB(6) PB(7) PB(8) PB(9) PB(10) PB(11) PB(12) PB(13) PB(14) PB(15) \
               PB(16) PB(17) PB(18) PB(19) PB(20) PB(21) PB(22) PB(23) PN PN PN PN PN PN PN PN \
               PB(32) PB(33) PB(34) PB(35) PB(36) PB(37) PB(38) PB(39) PB(40) PB(41) PB(42) PB(43) PB(44) PB(45) PB(46) PB(47)
#define CODE_LINES 218
#define OLATE 1
#define MSLP 1
#define GSLP 10
#define XT 4200
#define JDLY 30
#define DT 9600
#define PFG 0x40000
#define PFT ""
#define PFD 4
#define P1D 8
#define GUW 8
#define GAE 2
#define KBAR 1
#define NOC 0
#define GED 6
#define WSLOW 0
#define YT 0
#define TLBW 0
#define NOPRM 0
#define B0D 2
#define KT 15000
#define QSLP 20
#define P2D 22
#define KALIGN 4096
#define ROT 0
#define GEAR 2
#define SDLY 1
#define PRK 0
#define PRP 1
#define PWT 0
#define DTH 2
#define ASLP 0
#define B0T 2600
#define B0L 3300
#define OSLP 0
#define PW2 0
#define CT 9000
#define D2D 12
#define KPR 2
#define CTH 3
#define KSL0 2
#define KD2 0
#define XD2 0
#define WD2 0
#define WDLY 16
#define PFM 0
#define PFW 1

constexpr int LDS_BYTES = 163840;

#define TS(i) do {} while (0)
#define CNT(i) do {} while (0)

#define STA(i, r) ""
#define STAMP_IO
#define LSTAMP_IO
#define GSTAMP1 STA(1, "s[60:61]")
#define SSTAMP1 GSTAMP1
#define GSTAMP2 STA(2, "s[62:63]")
#define SSTAMP2 GSTAMP2
#define GSTAMP3 STA(3, "s[64:65]")
#define SSTAMP3 GSTAMP3
#define GSTAMP4 STA(4, "s[66:67]")
#define SSTAMP4 GSTAMP4
#define GSTAMP5 STA(5, "s[68:69]")
#define SSTAMP5 GSTAMP5
#define GSTAMP6 STA(6, "s[70:71]")
#define SSTAMP6 GSTAMP6
#define GSTAMP7 STA(7, "s[72:73]")
#define SSTAMP7 GSTAMP7
#define GSTAMP8 STA(8, "s[74:75]")
#define SSTAMP8 GSTAMP8
#define GSTAMP9 STA(9, "s[76:77]")
#define SSTAMP9 GSTAMP9
#define GSTAMP10 STA(10, "s[78:79]")
#define SSTAMP10 GSTAMP10
#define GSTAMP11 STA(11, "s[80:81]")
#define SSTAMP11 GSTAMP11
#define STC(r) ""
#define SSTAMPCm STC("s60")
#define SSTAMPCu0 STC("s62")
#define SSTAMPCu1 STC("s62")
#define SSTAMPCu2 STC("s66")
#define SSTAMPCu3 STC("s66")
#define SSTAMPCa STC("s64")
#define LSTAMPC STC("s38")
#define LSTAMPD STC("s36")
#define LSTAMP8 GSTAMP8
#define LSTAMP9 GSTAMP9
#define LSTAMP10 GSTAMP10
#define LSTAMP11 GSTAMP11
#define LSTAMP13 STA(13, "s[36:37]")
#define LSTAMP14 STA(14, "s[38:39]")
#define GSTAMP12 SSTAMPE1
#define GSTAMP13 LSTAMP13
#define GSTAMPC LSTAMPC
#define GSTAMPK STC("s60")
#define SSTAMPE0 GSTAMP6
#define SSTAMPE1 STA(12, "s[82:83]")
#define SSTAMPE2 ""
#define GW_HEAD \
  "s_load_dwordx8 s[40:47], %[k], 0x8\n" \
  "s_load_dwordx2 s[48:49], %[k], 0x28\n" \
  "s_setprio 1\n" \
  "s_lshr_b32 s50, %[w], 2\n" \
  "s_and_b32 s54, %[b], 1\n" \
  "s_add_u32 s54, s54, s50\n" \
  "s_cmp_eq_u32 s54, 3\n" \
  "s_cselect_b32 s54, 0, s54\n" \
  "s_mul_i32 s53, %[b], 3\n" \
  "s_add_u32 s53, s53, s54\n" \
  "s_add_u32 s52, %[w], s50\n" \
  "s_lshl_b32 s55, s52, 7\n" \
  "s_and_b32 s55, s55, 128\n" \
  "s_lshl_b32 s54, s53, 8\n" \
  "s_add_u32 s54, s54, s55\n" \
  "v_and_b32 v9, 63, %[t]\n" \
  "v_lshl_add_u32 v17, v9, 1, s54\n" \
  "v_mov_b64 v[108:109], 0\n" \
  "v_mov_b64 v[110:111], 0\n" \
  "s_mul_i32 s57, s50, 32096\n" \
  "s_lshl1_add_u32 m0, s55, s57\n" \
  "s_lshl_b32 s84, s53, 1\n" \
  "s_and_b32 s84, s84, -4\n" \
  "s_cmp_ge_u32 s50, " STR(GEAR) "\n" \
  "s_cbranch_scc1 Lnc%=\n" \
  "s_setprio 3\n" \
  "s_bitcmp1_b32 s52, 1\n" \
  GSTAMP1 \
  "s_waitcnt lgkmcnt(0)\n" \
  "global_load_short_d16_hi v108, v17, s[40:41]\n" \
  "global_load_short_d16_hi v109, v17, s[42:43]\n" \
  "s_load_dword s98, s[48:49], s84\n" \
  "s_cbranch_scc1 Lnd%=\n" \
  "global_load_short_d16_hi v110, v17, s[44:45]\n" \
  "global_load_short_d16_hi v111, v17, s[46:47]\n" \
  "Lnd%=:\n" \
  "s_setprio 1\n" \
  "Lnc%=:\n" \
  "s_and_b32 s51, %[w], 3\n" \
  "s_lshl_b32 s58, s50, 13\n" \
  "s_add_u32 s58, s58, 96288\n" \
  "v_and_b32 v1, 15, %[t]\n" \
  "v_and_b32 v2, 48, %[t]\n" \
  "v_mov_b64 v[12:13], 0\n" \
  "v_mov_b64 v[14:15], 0\n" \
  "v_and_b32 v5, 255, %[t]\n" \
  "v_lshl_add_u32 v5, v5, 4, s58\n" \
  "ds_write_b128 v5, v[12:15] offset:4096\n" \
  "v_min_u32 v4, 1, v9\n" \
  "v_mul_u32_u24 v4, 40, v4\n" \
  "v_add_u32 v4, v4, v9\n" \
  "v_mov_b32 v5, 159488\n" \
  "v_lshl_add_u32 v4, v4, 2, v5\n" \
  "s_movk_i32 s54, 544\n" \
  "v_mad_u32_u24 v5, v1, s54, v2\n" \
  "s_and_b32 s54, s51, 1\n" \
  "s_mul_i32 s55, s54, 8704\n" \
  "s_add_u32 s55, s55, 14688\n" \
  "s_cmp_lt_u32 s51, 2\n" \
  "s_cselect_b32 s56, 0, s55\n" \
  "s_cselect_b32 s55, s55, 8704\n" \
  "s_cselect_b32 s85, 16, 64\n" \
  "s_cselect_b32 s86, 512, 16\n" \
  "s_cselect_b32 s87, 8, 5\n" \
  "s_cselect_b32 s88, -2912, 0\n" \
  "v_add_u32 v5, s57, v5\n" \
  "v_add_u32 v6, s55, v5\n" \
  "v_add_u32 v5, s56, v5\n" \
  "s_lshl_b32 s54, s54, s87\n" \
  "s_add_u32 s54, s54, s88\n" \
  "s_add_u32 s54, s54, s58\n" \
  "v_lshrrev_b32 v7, 1, v2\n" \
  "v_and_b32 v7, 8, v7\n" \
  "v_mad_u32_u24 v7, v1, s85, v7\n" \
  "v_lshrrev_b32 v9, 5, v9\n" \
  "v_mad_u32_u24 v7, v9, s86, v7\n" \
  "v_add_u32 v7, s54, v7\n" \
  "s_lshl_b32 s59, s50, 3\n" \
  "s_lshl_b32 s59, 1, s59\n" \
  "v_mov_b32 v16, s59\n" \
  "s_not_b32 s53, s53\n" \
  "s_lshl_b32 s53, s53, 4\n" \
  "s_cmp_eq_u32 s50, 0\n" \
  "s_cbranch_scc0 Lp1%=\n" \
  "s_setprio 2\n" \
  "Lp1%=:\n" \
  "s_bitcmp1_b32 s52, 1\n" \
  "s_cbranch_scc1 Lgb%=\n" \
  "s_barrier\n"

#define GW_BODY \
  GSTAMP2 \
  "s_cmp_lt_u32 s50, " STR(GEAR) "\n" \
  "s_cbranch_scc1 Lne1%=\n" \
  "s_waitcnt lgkmcnt(0)\n" \
  "global_load_short_d16_hi v108, v17, s[40:41]\n" \
  "global_load_short_d16_hi v109, v17, s[42:43]\n" \
  "s_load_dword s98, s[48:49], s84\n" \
  "global_load_short_d16_hi v110, v17, s[44:45]\n" \
  "global_load_short_d16_hi v111, v17, s[46:47]\n" \
  "Lne1%=:\n" \
  "v_lshrrev_b32 v3, 1, v2\n" \
  "v_add3_u32 v3, v3, v1, 1\n" \
  "v_and_b32 v8, 1, v3\n" \
  "v_mul_u32_u24 v8, 1470, v8\n" \
  "v_lshl_add_u32 v3, v3, 1, v8\n" \
  "s_add_u32 s55, s58, 4544\n" \
  "v_add_u32 v3, s55, v3\n" \
  "v_lshlrev_b32 v8, 5, v1\n" \
  "v_sub_u32 v10, v2, v8\n" \
  "s_add_u32 s55, s58, 480\n" \
  "v_add_u32 v10, s55, v10\n" \
  "v_mul_u32_u24 v11, 20, v2\n" \
  "v_lshl_add_u32 v11, v1, 4, v11\n" \
  "v_add_u32 v11, s57, v11\n" \
  "v_lshlrev_b32 v8, 2, v1\n" \
  "v_sub_u32 v8, v8, v2\n" \
  "s_add_u32 s55, s58, 7540\n" \
  "v_add_u32 v8, s55, v8\n" \
  "s_bitcmp0_b32 s51, 0\n" \
  "s_cbranch_scc1 Lo1%=\n" \
  "s_lshr_b32 s55, s51, 1\n" \
  "s_mul_i32 s55, s55, 1470\n" \
  "s_add_u32 s55, s55, s58\n" \
  "v_lshrrev_b32 v19, 3, v2\n" \
  "v_mul_u32_u24 v19, 29, v19\n" \
  "v_lshl_add_u32 v19, v1, 1, v19\n" \
  "v_add_u32 v19, s55, v19\n" \
  "v_mov_b32 v20, 0x3c00\n" \
  "s_mov_b32 exec_lo, 0x001f0007\n" \
  "s_mov_b32 exec_hi, 0x01ff007f\n" \
  "ds_write_b16 v19, v20 offset:5798\n" \
  "s_mov_b64 exec, -1\n" \
  "Lo1%=:\n" \
  "s_lshl_b32 s54, s59, 2\n" \
  "v_mov_b32 v9, s57\n" \
  "s_sub_u32 s55, s51, s50\n" \
  "s_cmp_eq_u32 s51, 3\n" \
  "s_cselect_b32 s55, 3, s55\n" \
  "s_waitcnt vmcnt(0) lgkmcnt(0)\n" \
  "s_lshl_b32 s98, s98, s53\n" \
  "s_and_b32 s98, s98, 0xffff0000\n" \
  "v_mul_f32 v108, 0x3fb8aa3b, v108\n" \
  "v_mov_b32 v112, s98\n" \
  "v_mul_f32 v112, 0x3fb8aa3b, v112\n" \
  "v_exp_f32 v108, v108\n" \
  "v_exp_f32 v112, v112\n" \
  "v_max_f32 v122, |v110|, |v111|\n" \
  "v_cvt_pk_bf16_f32 v108, 0, v108\n" \
  "v_cvt_pk_bf16_f32 v112, 0, v112\n" \
  "v_max_u32_dpp v122, v122, v122 quad_perm:[1,0,3,2] row_mask:0xf bank_mask:0xf\n" \
  "v_pk_mul_f32 v[108:109], v[108:109], v[112:113] op_sel_hi:[1,0]\n" \
  "v_mul_f32 v115, 0x3e22f983, v109\n" \
  "v_max_u32_dpp v122, v122, v122 quad_perm:[2,3,0,1] row_mask:0xf bank_mask:0xf\n" \
  "v_mul_f32 v117, 0x4222f983, v109\n" \
  "v_mul_f32 v114, 0xbfb8aa3b, v108\n" \
  "v_max_u32_dpp v122, v122, v122 row_half_mirror row_mask:0xf bank_mask:0xf\n" \
  "v_mul_f32 v116, 0xc3b8aa3b, v108\n" \
  "v_fract_f32 v115, v115\n" \
  "v_max_u32_dpp v122, v122, v122 row_mirror row_mask:0xf bank_mask:0xf\n" \
  "v_fract_f32 v117, v117\n" \
  "v_exp_f32 v114, v114\n" \
  "v_max_u32_dpp v122, v122, v122 row_bcast:15 row_mask:0xa bank_mask:0xf\n" \
  "v_cos_f32 v118, v115\n" \
  "v_sin_f32 v119, v115\n" \
  "v_max_u32_dpp v122, v122, v122 row_bcast:31 row_mask:0xc bank_mask:0xf\n" \
  "v_exp_f32 v116, v116\n" \
  "v_cos_f32 v120, v117\n" \
  "v_readlane_b32 s94, v122, 63\n" \
  "v_sin_f32 v121, v117\n" \
  "v_pk_mul_f32 v[118:119], v[118:119], v[114:115] op_sel_hi:[1,0]\n" \
  "s_lshr_b32 s94, s94, 23\n" \
  "v_pk_mul_f32 v[120:121], v[120:121], v[116:117] op_sel_hi:[1,0]\n" \
  "s_max_u32 s94, s94, 26\n" \
  "s_min_u32 s94, s94, 226\n" \
  "v_mov_b32 v113, s94\n" \
  "s_sub_u32 s94, 266, s94\n" \
  "s_lshl_b32 s94, s94, 23\n" \
  "ds_write_addtid_b32 v113 offset:8704\n" \
  "ds_write_addtid_b32 v112 offset:10336\n" \
  "v_pk_mul_f32 v[110:111], v[110:111], s[94:95] op_sel_hi:[1,0]\n" \
  "v_pk_mul_f32 v[104:105], v[110:111], v[118:119] op_sel:[1,1] op_sel_hi:[1,0] neg_lo:[0,1]\n" \
  "v_pk_mul_f32 v[106:107], v[110:111], v[120:121] op_sel:[1,1] op_sel_hi:[1,0] neg_lo:[0,1]\n" \
  "v_cvt_pk_f16_f32 v112, v110, v111\n" \
  "v_pk_fma_f32 v[114:115], v[110:111], v[118:119], v[104:105] op_sel_hi:[0,1,1]\n" \
  "v_pk_fma_f32 v[116:117], v[110:111], v[120:121], v[106:107] op_sel_hi:[0,1,1]\n" \
  "ds_write_addtid_b32 v112 offset:0\n" \
  "v_pk_mul_f32 v[104:105], v[114:115], v[118:119] op_sel:[1,1] op_sel_hi:[1,0] neg_lo:[0,1]\n" \
  "v_cvt_pk_f16_f32 v112, v114, v115\n" \
  "v_cvt_pk_f16_f32 v103, v116, v117\n" \
  "v_pk_fma_f32 v[114:115], v[114:115], v[118:119], v[104:105] op_sel_hi:[0,1,1]\n" \
  "ds_write_addtid_b32 v112 offset:544\n" \
  "ds_write_addtid_b32 v103 offset:4352\n" \
  "ds_write_addtid_b32 v103 offset:4896\n" \
  "v_pk_mul_f32 v[104:105], v[114:115], v[118:119] op_sel:[1,1] op_sel_hi:[1,0] neg_lo:[0,1]\n" \
  "v_pk_mul_f32 v[106:107], v[114:115], v[120:121] op_sel:[1,1] op_sel_hi:[1,0] neg_lo:[0,1]\n" \
  "v_cvt_pk_f16_f32 v112, v114, v115\n" \
  "v_pk_fma_f32 v[122:123], v[114:115], v[118:119], v[104:105] op_sel_hi:[0,1,1]\n" \
  "v_pk_fma_f32 v[116:117], v[114:115], v[120:121], v[106:107] op_sel_hi:[0,1,1]\n" \
  "ds_write_addtid_b32 v112 offset:1088\n" \
  "v_pk_mul_f32 v[104:105], v[122:123], v[118:119] op_sel:[1,1] op_sel_hi:[1,0] neg_lo:[0,1]\n" \
  "v_cvt_pk_f16_f32 v112, v122, v123\n" \
  "v_cvt_pk_f16_f32 v103, v116, v117\n" \
  "v_pk_fma_f32 v[114:115], v[122:123], v[118:119], v[104:105] op_sel_hi:[0,1,1]\n" \
  "ds_write_addtid_b32 v112 offset:1632\n" \
  "ds_write_addtid_b32 v103 offset:5440\n" \
  "v_pk_mul_f32 v[104:105], v[114:115], v[118:119] op_sel:[1,1] op_sel_hi:[1,0] neg_lo:[0,1]\n" \
  "v_pk_mul_f32 v[106:107], v[114:115], v[120:121] op_sel:[1,1] op_sel_hi:[1,0] neg_lo:[0,1]\n" \
  "v_cvt_pk_f16_f32 v112, v114, v115\n" \
  "v_pk_fma_f32 v[122:123], v[114:115], v[118:119], v[104:105] op_sel_hi:[0,1,1]\n" \
  "v_pk_fma_f32 v[116:117], v[114:115], v[120:121], v[106:107] op_sel_hi:[0,1,1]\n" \
  "ds_write_addtid_b32 v112 offset:2176\n" \
  "v_pk_mul_f32 v[104:105], v[122:123], v[118:119] op_sel:[1,1] op_sel_hi:[1,0] neg_lo:[0,1]\n" \
  "v_pk_mul_f32 v[106:107], v[116:117], v[120:121] op_sel:[1,1] op_sel_hi:[1,0] neg_lo:[0,1]\n" \
  "v_cvt_pk_f16_f32 v112, v122, v123\n" \
  "v_cvt_pk_f16_f32 v103, v116, v117\n" \
  "v_pk_fma_f32 v[114:115], v[122:123], v[118:119], v[104:105] op_sel_hi:[0,1,1]\n" \
  "v_pk_fma_f32 v[116:117], v[116:117], v[120:121], v[106:107] op_sel_hi:[0,1,1]\n" \
  "ds_write_addtid_b32 v112 offset:2720\n" \
  "ds_write_addtid_b32 v103 offset:5984\n" \
  "ds_write_addtid_b32 v103 offset:6528\n" \
  "ds_write_addtid_b32 v103 offset:7072\n" \
  "v_pk_mul_f32 v[104:105], v[114:115], v[118:119] op_sel:[1,1] op_sel_hi:[1,0] neg_lo:[0,1]\n" \
  "v_pk_mul_f32 v[106:107], v[116:117], v[120:121] op_sel:[1,1] op_sel_hi:[1,0] neg_lo:[0,1]\n" \
  "v_cvt_pk_f16_f32 v112, v114, v115\n" \
  "v_cvt_pk_f16_f32 v103, v116, v117\n" \
  "v_pk_fma_f32 v[122:123], v[114:115], v[118:119], v[104:105] op_sel_hi:[0,1,1]\n" \
  "v_pk_fma_f32 v[116:117], v[116:117], v[120:121], v[106:107] op_sel_hi:[0,1,1]\n" \
  "ds_write_addtid_b32 v112 offset:3264\n" \
  "ds_write_addtid_b32 v103 offset:9248\n" \
  "v_pk_mul_f32 v[104:105], v[122:123], v[118:119] op_sel:[1,1] op_sel_hi:[1,0] neg_lo:[0,1]\n" \
  "v_pk_mul_f32 v[106:107], v[114:115], v[120:121] op_sel:[1,1] op_sel_hi:[1,0] neg_lo:[0,1]\n" \
  "v_cvt_pk_f16_f32 v112, v122, v123\n" \
  "v_cvt_pk_f16_f32 v103, v116, v117\n" \
  "v_pk_fma_f32 v[122:123], v[122:123], v[118:119], v[104:105] op_sel_hi:[0,1,1]\n" \
  "v_pk_fma_f32 v[116:117], v[114:115], v[120:121], v[106:107] op_sel_hi:[0,1,1]\n" \
  "ds_write_addtid_b32 v112 offset:3808\n" \
  "ds_write_addtid_b32 v103 offset:9792\n" \
  "v_pk_mul_f32 v[104:105], v[122:123], v[120:121] op_sel:[1,1] op_sel_hi:[1,0] neg_lo:[0,1]\n" \
  "v_pk_mul_f32 v[106:107], v[120:121], v[120:121] op_sel:[1,1] op_sel_hi:[1,0] neg_lo:[0,1]\n" \
  "v_cvt_pk_f16_f32 v112, v116, v117\n" \
  "v_pk_fma_f32 v[114:115], v[122:123], v[120:121], v[104:105] op_sel_hi:[0,1,1]\n" \
  "v_pk_fma_f32 v[116:117], v[120:121], v[120:121], v[106:107] op_sel_hi:[0,1,1]\n" \
  "ds_write_addtid_b32 v112 offset:7616\n" \
  "v_pk_mul_f32 v[104:105], v[122:123], v[116:117] op_sel:[1,1] op_sel_hi:[1,0] neg_lo:[0,1]\n" \
  "v_pk_mul_f32 v[106:107], v[114:115], v[116:117] op_sel:[1,1] op_sel_hi:[1,0] neg_lo:[0,1]\n" \
  "v_cvt_pk_f16_f32 v112, v114, v115\n" \
  "v_pk_fma_f32 v[122:123], v[122:123], v[116:117], v[104:105] op_sel_hi:[0,1,1]\n" \
  "v_pk_fma_f32 v[114:115], v[114:115], v[116:117], v[106:107] op_sel_hi:[0,1,1]\n" \
  "ds_write_addtid_b32 v112 offset:8160\n" \
  "v_pk_mul_f32 v[104:105], v[122:123], v[116:117] op_sel:[1,1] op_sel_hi:[1,0] neg_lo:[0,1]\n" \
  "v_pk_mul_f32 v[106:107], v[114:115], v[116:117] op_sel:[1,1] op_sel_hi:[1,0] neg_lo:[0,1]\n" \
  "v_cvt_pk_f16_f32 v112, v122, v123\n" \
  "v_cvt_pk_f16_f32 v103, v114, v115\n" \
  "v_pk_fma_f32 v[122:123], v[122:123], v[116:117], v[104:105] op_sel_hi:[0,1,1]\n" \
  "v_pk_fma_f32 v[114:115], v[114:115], v[116:117], v[106:107] op_sel_hi:[0,1,1]\n" \
  "ds_write_addtid_b32 v112 offset:10880\n" \
  "ds_write_addtid_b32 v103 offset:11424\n" \
  "v_pk_mul_f32 v[104:105], v[122:123], v[116:117] op_sel:[1,1] op_sel_hi:[1,0] neg_lo:[0,1]\n" \
  "v_pk_mul_f32 v[106:107], v[114:115], v[116:117] op_sel:[1,1] op_sel_hi:[1,0] neg_lo:[0,1]\n" \
  "v_cvt_pk_f16_f32 v112, v122, v123\n" \
  "v_cvt_pk_f16_f32 v103, v114, v115\n" \
  "v_pk_fma_f32 v[122:123], v[122:123], v[116:117], v[104:105] op_sel_hi:[0,1,1]\n" \
  "v_pk_fma_f32 v[114:115], v[114:115], v[116:117], v[106:107] op_sel_hi:[0,1,1]\n" \
  "ds_write_addtid_b32 v112 offset:12512\n" \
  "ds_write_addtid_b32 v103 offset:13056\n" \
  "v_cvt_pk_f16_f32 v112, v122, v123\n" \
  "v_cvt_pk_f16_f32 v103, v114, v115\n" \
  "ds_write_addtid_b32 v112 offset:13600\n" \
  "ds_write_addtid_b32 v103 offset:14144\n" \
  "s_branch Lgk%=\n" \
  "Lgb%=:\n" \
  "s_barrier\n" \
  GSTAMP2 \
  "s_cmp_lt_u32 s50, " STR(GEAR) "\n" \
  "s_cbranch_scc1 Lne8%=\n" \
  "s_waitcnt lgkmcnt(0)\n" \
  "global_load_short_d16_hi v108, v17, s[40:41]\n" \
  "global_load_short_d16_hi v109, v17, s[42:43]\n" \
  "s_load_dword s98, s[48:49], s84\n" \
  "Lne8%=:\n" \
  "v_lshrrev_b32 v3, 1, v2\n" \
  "v_add3_u32 v3, v3, v1, 1\n" \
  "v_and_b32 v8, 1, v3\n" \
  "v_mul_u32_u24 v8, 1470, v8\n" \
  "v_lshl_add_u32 v3, v3, 1, v8\n" \
  "s_add_u32 s55, s58, 4544\n" \
  "v_add_u32 v3, s55, v3\n" \
  "v_lshlrev_b32 v8, 5, v1\n" \
  "v_sub_u32 v10, v2, v8\n" \
  "s_add_u32 s55, s58, 480\n" \
  "v_add_u32 v10, s55, v10\n" \
  "v_mul_u32_u24 v11, 20, v2\n" \
  "v_lshl_add_u32 v11, v1, 4, v11\n" \
  "v_add_u32 v11, s57, v11\n" \
  "v_lshlrev_b32 v8, 2, v1\n" \
  "v_sub_u32 v8, v8, v2\n" \
  "s_add_u32 s55, s58, 7540\n" \
  "v_add_u32 v8, s55, v8\n" \
  "s_bitcmp0_b32 s51, 0\n" \
  "s_cbranch_scc1 Lo8%=\n" \
  "s_lshr_b32 s55, s51, 1\n" \
  "s_mul_i32 s55, s55, 1470\n" \
  "s_add_u32 s55, s55, s58\n" \
  "v_lshrrev_b32 v19, 3, v2\n" \
  "v_mul_u32_u24 v19, 29, v19\n" \
  "v_lshl_add_u32 v19, v1, 1, v19\n" \
  "v_add_u32 v19, s55, v19\n" \
  "v_mov_b32 v20, 0x3c00\n" \
  "s_mov_b32 exec_lo, 0x001f0007\n" \
  "s_mov_b32 exec_hi, 0x01ff007f\n" \
  "ds_write_b16 v19, v20 offset:5798\n" \
  "s_mov_b64 exec, -1\n" \
  "Lo8%=:\n" \
  "s_lshl_b32 s54, s59, 2\n" \
  "v_mov_b32 v9, s57\n" \
  "s_sub_u32 s55, s51, s50\n" \
  "s_cmp_eq_u32 s51, 3\n" \
  "s_cselect_b32 s55, 3, s55\n" \
  "s_waitcnt vmcnt(0) lgkmcnt(0)\n" \
  "s_lshl_b32 s98, s98, s53\n" \
  "s_and_b32 s98, s98, 0xffff0000\n" \
  "v_mul_f32 v108, 0x3fb8aa3b, v108\n" \
  "v_mov_b32 v112, s98\n" \
  "v_mul_f32 v112, 0x3fb8aa3b, v112\n" \
  "v_exp_f32 v108, v108\n" \
  "v_exp_f32 v112, v112\n" \
  "v_mov_b32 v103, 0x3c00\n" \
  "v_cvt_pk_bf16_f32 v108, 0, v108\n" \
  "v_cvt_pk_bf16_f32 v112, 0, v112\n" \
  "v_pk_mul_f32 v[108:109], v[108:109], v[112:113] op_sel_hi:[1,0]\n" \
  "v_mul_f32 v115, 0xbfa2f983, v109\n" \
  "v_mul_f32 v117, 0xc1a2f983, v109\n" \
  "v_mul_f32 v114, 0xc138aa3b, v108\n" \
  "v_mul_f32 v116, 0xc338aa3b, v108\n" \
  "v_fract_f32 v115, v115\n" \
  "v_fract_f32 v117, v117\n" \
  "v_exp_f32 v114, v114\n" \
  "v_cos_f32 v118, v115\n" \
  "v_sin_f32 v119, v115\n" \
  "v_exp_f32 v116, v116\n" \
  "v_cos_f32 v120, v117\n" \
  "v_sin_f32 v121, v117\n" \
  "v_pk_mul_f32 v[118:119], v[118:119], v[114:115] op_sel_hi:[1,0]\n" \
  "v_pk_mul_f32 v[120:121], v[120:121], v[116:117] op_sel_hi:[1,0]\n" \
  "ds_write_addtid_b32 v103 offset:14688\n" \
  "v_pk_mul_f32 v[104:105], v[118:119], v[118:119] op_sel:[1,1] op_sel_hi:[1,0] neg_lo:[0,1]\n" \
  "v_pk_mul_f32 v[106:107], v[120:121], v[118:119] op_sel:[1,1] op_sel_hi:[1,0] neg_lo:[0,1]\n" \
  "v_cvt_pk_f16_f32 v112, v118, v119\n" \
  "v_cvt_pk_f16_f32 v103, v120, v121\n" \
  "v_pk_fma_f32 v[110:111], v[118:119], v[118:119], v[104:105] op_sel_hi:[0,1,1]\n" \
  "v_pk_fma_f32 v[122:123], v[120:121], v[118:119], v[106:107] op_sel_hi:[0,1,1]\n" \
  "ds_write_addtid_b32 v112 offset:15232\n" \
  "ds_write_addtid_b32 v103 offset:23392\n" \
  "v_pk_mul_f32 v[104:105], v[110:111], v[118:119] op_sel:[1,1] op_sel_hi:[1,0] neg_lo:[0,1]\n" \
  "v_pk_mul_f32 v[106:107], v[122:123], v[118:119] op_sel:[1,1] op_sel_hi:[1,0] neg_lo:[0,1]\n" \
  "v_cvt_pk_f16_f32 v112, v110, v111\n" \
  "v_cvt_pk_f16_f32 v103, v122, v123\n" \
  "v_pk_fma_f32 v[110:111], v[110:111], v[118:119], v[104:105] op_sel_hi:[0,1,1]\n" \
  "v_pk_fma_f32 v[122:123], v[122:123], v[118:119], v[106:107] op_sel_hi:[0,1,1]\n" \
  "ds_write_addtid_b32 v112 offset:15776\n" \
  "ds_write_addtid_b32 v103 offset:23936\n" \
  "v_pk_mul_f32 v[104:105], v[110:111], v[118:119] op_sel:[1,1] op_sel_hi:[1,0] neg_lo:[0,1]\n" \
  "v_pk_mul_f32 v[106:107], v[122:123], v[118:119] op_sel:[1,1] op_sel_hi:[1,0] neg_lo:[0,1]\n" \
  "v_cvt_pk_f16_f32 v112, v110, v111\n" \
  "v_cvt_pk_f16_f32 v103, v122, v123\n" \
  "v_pk_fma_f32 v[110:111], v[110:111], v[118:119], v[104:105] op_sel_hi:[0,1,1]\n" \
  "v_pk_fma_f32 v[122:123], v[122:123], v[118:119], v[106:107] op_sel_hi:[0,1,1]\n" \
  "ds_write_addtid_b32 v112 offset:16320\n" \
  "ds_write_addtid_b32 v103 offset:24480\n" \
  "v_pk_mul_f32 v[104:105], v[110:111], v[118:119] op_sel:[1,1] op_sel_hi:[1,0] neg_lo:[0,1]\n" \
  "v_pk_mul_f32 v[106:107], v[122:123], v[118:119] op_sel:[1,1] op_sel_hi:[1,0] neg_lo:[0,1]\n" \
  "v_cvt_pk_f16_f32 v112, v110, v111\n" \
  "v_cvt_pk_f16_f32 v103, v122, v123\n" \
  "v_pk_fma_f32 v[110:111], v[110:111], v[118:119], v[104:105] op_sel_hi:[0,1,1]\n" \
  "v_pk_fma_f32 v[122:123], v[122:123], v[118:119], v[106:107] op_sel_hi:[0,1,1]\n" \
  "ds_write_addtid_b32 v112 offset:16864\n" \
  "ds_write_addtid_b32 v103 offset:25024\n" \
  "v_pk_mul_f32 v[104:105], v[110:111], v[118:119] op_sel:[1,1] op_sel_hi:[1,0] neg_lo:[0,1]\n" \
  "v_pk_mul_f32 v[106:107], v[122:123], v[118:119] op_sel:[1,1] op_sel_hi:[1,0] neg_lo:[0,1]\n" \
  "v_cvt_pk_f16_f32 v112, v110, v111\n" \
  "v_cvt_pk_f16_f32 v103, v122, v123\n" \
  "v_pk_fma_f32 v[110:111], v[110:111], v[118:119], v[104:105] op_sel_hi:[0,1,1]\n" \
  "v_pk_fma_f32 v[122:123], v[122:123], v[118:119], v[106:107] op_sel_hi:[0,1,1]\n" \
  "ds_write_addtid_b32 v112 offset:17408\n" \
  "ds_write_addtid_b32 v103 offset:25568\n" \
  "v_pk_mul_f32 v[104:105], v[110:111], v[118:119] op_sel:[1,1] op_sel_hi:[1,0] neg_lo:[0,1]\n" \
  "v_pk_mul_f32 v[106:107], v[122:123], v[118:119] op_sel:[1,1] op_sel_hi:[1,0] neg_lo:[0,1]\n" \
  "v_cvt_pk_f16_f32 v112, v110, v111\n" \
  "v_cvt_pk_f16_f32 v103, v122, v123\n" \
  "v_pk_fma_f32 v[110:111], v[110:111], v[118:119], v[104:105] op_sel_hi:[0,1,1]\n" \
  "v_pk_fma_f32 v[122:123], v[122:123], v[118:119], v[106:107] op_sel_hi:[0,1,1]\n" \
  "ds_write_addtid_b32 v112 offset:17952\n" \
  "ds_write_addtid_b32 v103 offset:26112\n" \
  "v_pk_mul_f32 v[104:105], v[110:111], v[118:119] op_sel:[1,1] op_sel_hi:[1,0] neg_lo:[0,1]\n" \
  "v_pk_mul_f32 v[106:107], v[122:123], v[118:119] op_sel:[1,1] op_sel_hi:[1,0] neg_lo:[0,1]\n" \
  "v_cvt_pk_f16_f32 v112, v110, v111\n" \
  "v_cvt_pk_f16_f32 v103, v122, v123\n" \
  "v_pk_fma_f32 v[110:111], v[110:111], v[118:119], v[104:105] op_sel_hi:[0,1,1]\n" \
  "v_pk_fma_f32 v[122:123], v[122:123], v[118:119], v[106:107] op_sel_hi:[0,1,1]\n" \
  "ds_write_addtid_b32 v112 offset:18496\n" \
  "ds_write_addtid_b32 v103 offset:26656\n" \
  "v_pk_mul_f32 v[104:105], v[110:111], v[118:119] op_sel:[1,1] op_sel_hi:[1,0] neg_lo:[0,1]\n" \
  "v_pk_mul_f32 v[106:107], v[122:123], v[118:119] op_sel:[1,1] op_sel_hi:[1,0] neg_lo:[0,1]\n" \
  "v_cvt_pk_f16_f32 v112, v110, v111\n" \
  "v_cvt_pk_f16_f32 v103, v122, v123\n" \
  "v_pk_fma_f32 v[110:111], v[110:111], v[118:119], v[104:105] op_sel_hi:[0,1,1]\n" \
  "v_pk_fma_f32 v[122:123], v[122:123], v[118:119], v[106:107] op_sel_hi:[0,1,1]\n" \
  "ds_write_addtid_b32 v112 offset:19040\n" \
  "ds_write_addtid_b32 v103 offset:27200\n" \
  "v_pk_mul_f32 v[104:105], v[110:111], v[118:119] op_sel:[1,1] op_sel_hi:[1,0] neg_lo:[0,1]\n" \
  "v_pk_mul_f32 v[106:107], v[122:123], v[118:119] op_sel:[1,1] op_sel_hi:[1,0] neg_lo:[0,1]\n" \
  "v_cvt_pk_f16_f32 v112, v110, v111\n" \
  "v_cvt_pk_f16_f32 v103, v122, v123\n" \
  "v_pk_fma_f32 v[110:111], v[110:111], v[118:119], v[104:105] op_sel_hi:[0,1,1]\n" \
  "v_pk_fma_f32 v[122:123], v[122:123], v[118:119], v[106:107] op_sel_hi:[0,1,1]\n" \
  "ds_write_addtid_b32 v112 offset:19584\n" \
  "ds_write_addtid_b32 v103 offset:27744\n" \
  "v_pk_mul_f32 v[104:105], v[110:111], v[118:119] op_sel:[1,1] op_sel_hi:[1,0] neg_lo:[0,1]\n" \
  "v_pk_mul_f32 v[106:107], v[122:123], v[118:119] op_sel:[1,1] op_sel_hi:[1,0] neg_lo:[0,1]\n" \
  "v_cvt_pk_f16_f32 v112, v110, v111\n" \
  "v_cvt_pk_f16_f32 v103, v122, v123\n" \
  "v_pk_fma_f32 v[110:111], v[110:111], v[118:119], v[104:105] op_sel_hi:[0,1,1]\n" \
  "v_pk_fma_f32 v[122:123], v[122:123], v[118:119], v[106:107] op_sel_hi:[0,1,1]\n" \
  "ds_write_addtid_b32 v112 offset:20128\n" \
  "ds_write_addtid_b32 v103 offset:28288\n" \
  "v_pk_mul_f32 v[104:105], v[110:111], v[118:119] op_sel:[1,1] op_sel_hi:[1,0] neg_lo:[0,1]\n" \
  "v_pk_mul_f32 v[106:107], v[122:123], v[118:119] op_sel:[1,1] op_sel_hi:[1,0] neg_lo:[0,1]\n" \
  "v_cvt_pk_f16_f32 v112, v110, v111\n" \
  "v_cvt_pk_f16_f32 v103, v122, v123\n" \
  "v_pk_fma_f32 v[110:111], v[110:111], v[118:119], v[104:105] op_sel_hi:[0,1,1]\n" \
  "v_pk_fma_f32 v[122:123], v[122:123], v[118:119], v[106:107] op_sel_hi:[0,1,1]\n" \
  "ds_write_addtid_b32 v112 offset:20672\n" \
  "ds_write_addtid_b32 v103 offset:28832\n" \
  "v_pk_mul_f32 v[104:105], v[110:111], v[118:119] op_sel:[1,1] op_sel_hi:[1,0] neg_lo:[0,1]\n" \
  "v_pk_mul_f32 v[106:107], v[122:123], v[118:119] op_sel:[1,1] op_sel_hi:[1,0] neg_lo:[0,1]\n" \
  "v_cvt_pk_f16_f32 v112, v110, v111\n" \
  "v_cvt_pk_f16_f32 v103, v122, v123\n" \
  "v_pk_fma_f32 v[110:111], v[110:111], v[118:119], v[104:105] op_sel_hi:[0,1,1]\n" \
  "v_pk_fma_f32 v[122:123], v[122:123], v[118:119], v[106:107] op_sel_hi:[0,1,1]\n" \
  "ds_write_addtid_b32 v112 offset:21216\n" \
  "ds_write_addtid_b32 v103 offset:29376\n" \
  "v_pk_mul_f32 v[104:105], v[110:111], v[118:119] op_sel:[1,1] op_sel_hi:[1,0] neg_lo:[0,1]\n" \
  "v_pk_mul_f32 v[106:107], v[122:123], v[118:119] op_sel:[1,1] op_sel_hi:[1,0] neg_lo:[0,1]\n" \
  "v_cvt_pk_f16_f32 v112, v110, v111\n" \
  "v_cvt_pk_f16_f32 v103, v122, v123\n" \
  "v_pk_fma_f32 v[110:111], v[110:111], v[118:119], v[104:105] op_sel_hi:[0,1,1]\n" \
  "v_pk_fma_f32 v[122:123], v[122:123], v[118:119], v[106:107] op_sel_hi:[0,1,1]\n" \
  "ds_write_addtid_b32 v112 offset:21760\n" \
  "ds_write_addtid_b32 v103 offset:29920\n" \
  "v_pk_mul_f32 v[104:105], v[110:111], v[118:119] op_sel:[1,1] op_sel_hi:[1,0] neg_lo:[0,1]\n" \
  "v_pk_mul_f32 v[106:107], v[122:123], v[118:119] op_sel:[1,1] op_sel_hi:[1,0] neg_lo:[0,1]\n" \
  "v_cvt_pk_f16_f32 v112, v110, v111\n" \
  "v_cvt_pk_f16_f32 v103, v122, v123\n" \
  "v_pk_fma_f32 v[110:111], v[110:111], v[118:119], v[104:105] op_sel_hi:[0,1,1]\n" \
  "v_pk_fma_f32 v[122:123], v[122:123], v[118:119], v[106:107] op_sel_hi:[0,1,1]\n" \
  "ds_write_addtid_b32 v112 offset:22304\n" \
  "ds_write_addtid_b32 v103 offset:30464\n" \
  "v_pk_mul_f32 v[104:105], v[122:123], v[118:119] op_sel:[1,1] op_sel_hi:[1,0] neg_lo:[0,1]\n" \
  "v_cvt_pk_f16_f32 v112, v110, v111\n" \
  "v_cvt_pk_f16_f32 v103, v122, v123\n" \
  "v_pk_fma_f32 v[122:123], v[122:123], v[118:119], v[104:105] op_sel_hi:[0,1,1]\n" \
  "ds_write_addtid_b32 v112 offset:22848\n" \
  "ds_write_addtid_b32 v103 offset:31008\n" \
  "v_cvt_pk_f16_f32 v112, v122, v123\n" \
  "ds_write_addtid_b32 v112 offset:31552\n" \
  "Lgk%=:\n" \
  "ds_add_u32 v4, v16 offset:8\n" \
  GSTAMP3 \
  "s_mov_b32 s97, 0x1fffff\n" \
  "Lkp%=:\n" \
  "ds_read_b32 v17, v4 offset:8\n" \
  "s_waitcnt lgkmcnt(0)\n" \
  "v_readfirstlane_b32 s96, v17\n" \
  "s_and_b32 s96, s96, s54\n" \
  "s_cmp_eq_u32 s96, s54\n" \
  "s_cbranch_scc1 Lkp%=x\n" \
  "s_sleep 1\n" \
  "s_sub_u32 s97, s97, 1\n" \
  "s_cbranch_scc0 Lkp%=\n" \
  "Lkp%=x:\n" \
  GSTAMP4 \
  "ds_read2st64_b32 v[18:19], v9 offset0:34 offset1:35\n" \
  "ds_read_b128 v[24:27], v5 offset:0\n" \
  "ds_read_b128 v[28:31], v6 offset:0\n" \
  "ds_read_b128 v[56:59], v5 offset:256\n" \
  "ds_read_b128 v[60:63], v6 offset:256\n" \
  "ds_read_b128 v[32:35], v5 offset:64\n" \
  "ds_read_b128 v[36:39], v6 offset:64\n" \
  "ds_read_b128 v[64:67], v5 offset:320\n" \
  "ds_read_b128 v[68:71], v6 offset:320\n" \
  "ds_read_b128 v[40:43], v5 offset:128\n" \
  "ds_read_b128 v[44:47], v6 offset:128\n" \
  "ds_read_b128 v[72:75], v5 offset:384\n" \
  "ds_read_b128 v[76:79], v6 offset:384\n" \
  "ds_read_b128 v[48:51], v5 offset:192\n" \
  "ds_read_b128 v[52:55], v6 offset:192\n" \
  "ds_read_b128 v[80:83], v5 offset:448\n" \
  "ds_read_b128 v[84:87], v6 offset:448\n" \
  "s_waitcnt lgkmcnt(14)\n" \
  "v_max_u32 v17, v18, v19\n" \
  "v_sub_u32 v20, 121, v17\n" \
  "s_waitcnt lgkmcnt(14)\n" \
  "v_mfma_f32_16x16x32_f16 v[88:91], v[24:27], v[28:31], 0\n" \
  "v_add_u32 v18, v18, v20\n" \
  "v_add_u32 v19, v19, v20\n" \
  "s_waitcnt lgkmcnt(12)\n" \
  "v_mfma_f32_16x16x32_f16 v[92:95], v[56:59], v[60:63], 0\n" \
  "v_max_i32 v18, 0, v18\n" \
  "v_max_i32 v19, 0, v19\n" \
  "s_waitcnt lgkmcnt(10)\n" \
  "v_mfma_f32_16x16x32_f16 v[88:91], v[32:35], v[36:39], v[88:91]\n" \
  "v_lshlrev_b32 v18, 23, v18\n" \
  "v_lshlrev_b32 v19, 23, v19\n" \
  "s_waitcnt lgkmcnt(8)\n" \
  "v_mfma_f32_16x16x32_f16 v[92:95], v[64:67], v[68:71], v[92:95]\n" \
  "s_waitcnt lgkmcnt(6)\n" \
  "v_mfma_f32_16x16x32_f16 v[88:91], v[40:43], v[44:47], v[88:91]\n" \
  "s_waitcnt lgkmcnt(4)\n" \
  "v_mfma_f32_16x16x32_f16 v[92:95], v[72:75], v[76:79], v[92:95]\n" \
  "s_waitcnt lgkmcnt(2)\n" \
  "v_mfma_f32_16x16x32_f16 v[88:91], v[48:51], v[52:55], v[88:91]\n" \
  "s_waitcnt lgkmcnt(0)\n" \
  "v_mfma_f32_16x16x32_f16 v[92:95], v[80:83], v[84:87], v[92:95]\n" \
  "s_cmp_lt_u32 s51, 2\n" \
  "v_mov_b32 v21, 1\n" \
  "s_nop 3\n" \
  "v_pk_mul_f32 v[88:89], v[88:89], v[18:19] op_sel_hi:[1,0]\n" \
  "v_pk_mul_f32 v[90:91], v[90:91], v[18:19] op_sel_hi:[1,0]\n" \
  "v_pk_fma_f32 v[88:89], v[92:93], v[18:19], v[88:89] op_sel:[0,1,0]\n" \
  "v_pk_fma_f32 v[90:91], v[94:95], v[18:19], v[90:91] op_sel:[0,1,0]\n" \
  "v_cvt_pk_f16_f32 v88, v88, v89\n" \
  "v_cvt_pk_f16_f32 v89, v90, v91\n" \
  "s_cbranch_scc0 Lkf%=\n" \
  "ds_write_b64 v7, v[88:89] offset:7488\n" \
  "v_alignbit_b32 v90, v89, v88, 16\n" \
  "ds_write_b16 v7, v88 offset:8958\n" \
  "ds_write_b16_d16_hi v7, v89 offset:8964\n" \
  "ds_write_b32 v7, v90 offset:8960\n" \
  "s_cmp_eq_u32 s51, 0\n" \
  "s_cbranch_scc1 Lkd%=\n" \
  "s_mov_b32 exec_lo, 0\n" \
  "s_brev_b32 exec_hi, 1\n" \
  "ds_write_b16_d16_hi v7, v89 offset:7496\n" \
  "ds_write_b16_d16_hi v7, v89 offset:7498\n" \
  "ds_write_b16_d16_hi v7, v89 offset:7500\n" \
  "ds_write_b16_d16_hi v7, v89 offset:8966\n" \
  "ds_write_b16_d16_hi v7, v89 offset:8968\n" \
  "ds_write_b16_d16_hi v7, v89 offset:8970\n" \
  "s_branch Lke%=\n" \
  "Lkf%=:\n" \
  "s_mov_b32 exec_lo, 0x07b607b6\n" \
  "s_mov_b32 exec_hi, 0x07b607b6\n" \
  "ds_write_b64 v7, v[88:89] offset:7488\n" \
  "s_cmp_eq_u32 s51, 2\n" \
  "s_cbranch_scc1 Lk2%=\n" \
  "s_mov_b32 exec_lo, 0\n" \
  "s_mov_b32 exec_hi, 0x200000\n" \
  "ds_write_b16_d16_hi v7, v89 offset:5358\n" \
  "ds_write_b16_d16_hi v7, v89 offset:5360\n" \
  "ds_write_b16_d16_hi v7, v89 offset:6828\n" \
  "ds_write_b16_d16_hi v7, v89 offset:6830\n" \
  "s_branch Lke%=\n" \
  "Lk2%=:\n" \
  "s_mov_b64 exec, -1\n" \
  "ds_add_u32 v4, v16 offset:12\n" \
  "ds_read_b32 v18, v9 offset:10336\n" \
  "v_add_u32 v17, -5, v17\n" \
  "v_lshlrev_b32 v17, 23, v17\n" \
  "s_lshl2_add_u32 s56, s50, 158224\n" \
  "v_mov_b32 v19, s56\n" \
  "s_waitcnt lgkmcnt(0)\n" \
  "v_mul_f32 v17, v17, v18\n" \
  "ds_write_b32 v19, v17\n" \
  "s_branch Lkg%=\n" \
  "Lke%=:\n" \
  "s_mov_b64 exec, -1\n" \
  "Lkd%=:\n" \
  "ds_add_u32 v4, v16 offset:12\n" \
  "Lkg%=:\n" \
  "ds_add_u32 v4, v21 offset:4\n" \
  GSTAMP5 \
  "s_setprio " STR(PRK) "\n" \
  "s_cmp_eq_u32 s55, 2\n" \
  "s_cbranch_scc1 Lr4%=\n" \
  "s_cmp_eq_u32 s55, -1\n" \
  "s_cbranch_scc1 Lr4%=\n" \
  "s_mov_b32 s97, 0x1fffff\n" \
  "Lkq%=:\n" \
  "ds_read_b32 v17, v4 offset:12\n" \
  "s_waitcnt lgkmcnt(0)\n" \
  "v_readfirstlane_b32 s96, v17\n" \
  "s_and_b32 s96, s96, s54\n" \
  "s_cmp_eq_u32 s96, s54\n" \
  "s_cbranch_scc1 Lkq%=x\n" \
  "s_sleep 1\n" \
  "s_sub_u32 s97, s97, 1\n" \
  "s_cbranch_scc0 Lkq%=\n" \
  "Lkq%=x:\n" \
  GSTAMP6 \
  "s_cmp_eq_u32 s55, 3\n" \
  "s_cbranch_scc1 Lr1%=\n" \
  "s_cmp_lg_u32 s55, 0\n" \
  "s_cbranch_scc1 Lr2%=\n" \
  "ds_read2_b32 v[60:61], v3 offset0:0 offset1:1\n" \
  "ds_read2_b32 v[62:63], v3 offset0:2 offset1:3\n" \
  "ds_read2_b32 v[92:93], v3 offset0:128 offset1:129\n" \
  "ds_read2_b32 v[94:95], v3 offset0:130 offset1:131\n" \
  "ds_read2_b32 v[64:65], v3 offset0:16 offset1:17\n" \
  "ds_read2_b32 v[66:67], v3 offset0:18 offset1:19\n" \
  "ds_read2_b32 v[96:97], v3 offset0:144 offset1:145\n" \
  "ds_read2_b32 v[98:99], v3 offset0:146 offset1:147\n" \
  "ds_read2_b32 v[68:69], v3 offset0:32 offset1:33\n" \
  "ds_read2_b32 v[70:71], v3 offset0:34 offset1:35\n" \
  "ds_read2_b32 v[100:101], v3 offset0:160 offset1:161\n" \
  "ds_read2_b32 v[102:103], v3 offset0:162 offset1:163\n" \
  "ds_read2_b32 v[72:73], v3 offset0:48 offset1:49\n" \
  "ds_read2_b32 v[74:75], v3 offset0:50 offset1:51\n" \
  "ds_read2_b32 v[104:105], v3 offset0:176 offset1:177\n" \
  "ds_read2_b32 v[106:107], v3 offset0:178 offset1:179\n" \
  "ds_read2_b32 v[76:77], v3 offset0:64 offset1:65\n" \
  "ds_read2_b32 v[78:79], v3 offset0:66 offset1:67\n" \
  "ds_read2_b32 v[108:109], v3 offset0:192 offset1:193\n" \
  "ds_read2_b32 v[110:111], v3 offset0:194 offset1:195\n" \
  "ds_read2_b32 v[80:81], v3 offset0:80 offset1:81\n" \
  "ds_read2_b32 v[82:83], v3 offset0:82 offset1:83\n" \
  "ds_read2_b32 v[112:113], v3 offset0:208 offset1:209\n" \
  "ds_read2_b32 v[114:115], v3 offset0:210 offset1:211\n" \
  "ds_read2_b32 v[84:85], v3 offset0:96 offset1:97\n" \
  "ds_read2_b32 v[86:87], v3 offset0:98 offset1:99\n" \
  "ds_read2_b32 v[116:117], v3 offset0:224 offset1:225\n" \
  "ds_read2_b32 v[118:119], v3 offset0:226 offset1:227\n" \
  "ds_read2_b32 v[88:89], v3 offset0:112 offset1:113\n" \
  "ds_read2_b32 v[90:91], v3 offset0:114 offset1:115\n" \
  "ds_read2_b32 v[120:121], v3 offset0:240 offset1:241\n" \
  "ds_read2_b32 v[122:123], v3 offset0:242 offset1:243\n" \
  "v_add_u32 v9, 1024, v3\n" \
  "ds_read2_b32 v[124:125], v9 offset0:0 offset1:1\n" \
  "ds_read2_b32 v[126:127], v9 offset0:2 offset1:3\n" \
  "s_mov_b32 s97, 0x1fffff\n" \
  "Lnp%=:\n" \
  "ds_read_b32 v8, v4 offset:4\n" \
  "s_waitcnt lgkmcnt(0)\n" \
  "v_readfirstlane_b32 s96, v8\n" \
  "s_and_b32 s96, s96, 0xff\n" \
  "s_cmp_eq_u32 s96, 16\n" \
  "s_cbranch_scc1 Lnp%=x\n" \
  "s_sleep 1\n" \
  "s_sub_u32 s97, s97, 1\n" \
  "s_cbranch_scc0 Lnp%=\n" \
  "Lnp%=x:\n" \
  "s_setprio " STR(PRN) "\n" \
  "ds_read_b128 v[28:31], v10 offset:3584\n" \
  "ds_read_b128 v[32:35], v10 offset:3648\n" \
  "ds_read_b128 v[36:39], v10 offset:3712\n" \
  "ds_read_b128 v[40:43], v10 offset:3776\n" \
  "ds_read_b128 v[44:47], v10 offset:3840\n" \
  "ds_read_b128 v[48:51], v10 offset:3904\n" \
  "ds_read_b128 v[52:55], v10 offset:3968\n" \
  "ds_read_b128 v[56:59], v10 offset:4032\n" \
  "s_waitcnt lgkmcnt(6)\n" \
  "v_mfma_f32_16x16x32_f16 v[12:15], v[60:63], v[28:31], 0\n" \
  "v_mfma_f32_16x16x32_f16 v[16:19], v[92:95], v[28:31], 0\n" \
  "v_mfma_f32_16x16x32_f16 v[20:23], v[124:127], v[28:31], 0\n" \
  "ds_read_b128 v[28:31], v10 offset:3072\n" \
  "v_mfma_f32_16x16x32_f16 v[12:15], v[64:67], v[32:35], v[12:15]\n" \
  "v_mfma_f32_16x16x32_f16 v[16:19], v[96:99], v[32:35], v[16:19]\n" \
  "ds_read_b128 v[32:35], v10 offset:3136\n" \
  "s_waitcnt lgkmcnt(6)\n" \
  "v_mfma_f32_16x16x32_f16 v[12:15], v[68:71], v[36:39], v[12:15]\n" \
  "v_mfma_f32_16x16x32_f16 v[16:19], v[100:103], v[36:39], v[16:19]\n" \
  "ds_read_b128 v[36:39], v10 offset:3200\n" \
  "v_mfma_f32_16x16x32_f16 v[12:15], v[72:75], v[40:43], v[12:15]\n" \
  "v_mfma_f32_16x16x32_f16 v[16:19], v[104:107], v[40:43], v[16:19]\n" \
  "ds_read_b128 v[40:43], v10 offset:3264\n" \
  "s_waitcnt lgkmcnt(6)\n" \
  "v_mfma_f32_16x16x32_f16 v[12:15], v[76:79], v[44:47], v[12:15]\n" \
  "v_mfma_f32_16x16x32_f16 v[16:19], v[108:111], v[44:47], v[16:19]\n" \
  "ds_read_b128 v[44:47], v10 offset:3328\n" \
  "v_mfma_f32_16x16x32_f16 v[12:15], v[80:83], v[48:51], v[12:15]\n" \
  "v_mfma_f32_16x16x32_f16 v[16:19], v[112:115], v[48:51], v[16:19]\n" \
  "ds_read_b128 v[48:51], v10 offset:3392\n" \
  "s_waitcnt lgkmcnt(6)\n" \
  "v_mfma_f32_16x16x32_f16 v[12:15], v[84:87], v[52:55], v[12:15]\n" \
  "v_mfma_f32_16x16x32_f16 v[16:19], v[116:119], v[52:55], v[16:19]\n" \
  "ds_read_b128 v[52:55], v10 offset:3456\n" \
  "v_mfma_f32_16x16x32_f16 v[12:15], v[88:91], v[56:59], v[12:15]\n" \
  "v_mfma_f32_16x16x32_f16 v[16:19], v[120:123], v[56:59], v[16:19]\n" \
  "ds_read_b128 v[56:59], v10 offset:3520\n" \
  "s_waitcnt lgkmcnt(6)\n" \
  "v_mfma_f32_16x16x32_f16 v[16:19], v[60:63], v[28:31], v[16:19]\n" \
  "v_mfma_f32_16x16x32_f16 v[20:23], v[92:95], v[28:31], v[20:23]\n" \
  "v_mfma_f32_16x16x32_f16 v[24:27], v[124:127], v[28:31], 0\n" \
  "ds_read_b128 v[28:31], v10 offset:2560\n" \
  "v_mfma_f32_16x16x32_f16 v[16:19], v[64:67], v[32:35], v[16:19]\n" \
  "v_mfma_f32_16x16x32_f16 v[20:23], v[96:99], v[32:35], v[20:23]\n" \
  "ds_read_b128 v[32:35], v10 offset:2624\n" \
  "s_waitcnt lgkmcnt(6)\n" \
  "v_mfma_f32_16x16x32_f16 v[16:19], v[68:71], v[36:39], v[16:19]\n" \
  "v_mov_b32 v9, 0x1\n" \
  "ds_write_b128 v11, v[12:15] offset:0\n" \
  "ds_add_u32 v4, v9 offset:0\n" \
  "v_mfma_f32_16x16x32_f16 v[20:23], v[100:103], v[36:39], v[20:23]\n" \
  "ds_read_b128 v[36:39], v10 offset:2688\n" \
  "v_mfma_f32_16x16x32_f16 v[16:19], v[72:75], v[40:43], v[16:19]\n" \
  "v_mfma_f32_16x16x32_f16 v[20:23], v[104:107], v[40:43], v[20:23]\n" \
  "ds_read_b128 v[40:43], v10 offset:2752\n" \
  "s_waitcnt lgkmcnt(8)\n" \
  "v_mfma_f32_16x16x32_f16 v[16:19], v[76:79], v[44:47], v[16:19]\n" \
  "v_mfma_f32_16x16x32_f16 v[20:23], v[108:111], v[44:47], v[20:23]\n" \
  "ds_read_b128 v[44:47], v10 offset:2816\n" \
  "v_mfma_f32_16x16x32_f16 v[16:19], v[80:83], v[48:51], v[16:19]\n" \
  "v_mfma_f32_16x16x32_f16 v[20:23], v[112:115], v[48:51], v[20:23]\n" \
  "ds_read_b128 v[48:51], v10 offset:2880\n" \
  "s_waitcnt lgkmcnt(8)\n" \
  "v_mfma_f32_16x16x32_f16 v[16:19], v[84:87], v[52:55], v[16:19]\n" \
  "v_mfma_f32_16x16x32_f16 v[20:23], v[116:119], v[52:55], v[20:23]\n" \
  "ds_read_b128 v[52:55], v10 offset:2944\n" \
  "v_mfma_f32_16x16x32_f16 v[16:19], v[88:91], v[56:59], v[16:19]\n" \
  "v_mfma_f32_16x16x32_f16 v[20:23], v[120:123], v[56:59], v[20:23]\n" \
  "ds_read_b128 v[56:59], v10 offset:3008\n" \
  "s_waitcnt lgkmcnt(8)\n" \
  "v_mfma_f32_16x16x32_f16 v[20:23], v[60:63], v[28:31], v[20:23]\n" \
  "v_mfma_f32_16x16x32_f16 v[24:27], v[92:95], v[28:31], v[24:27]\n" \
  "ds_read_b128 v[28:31], v10 offset:2048\n" \
  "v_mfma_f32_16x16x32_f16 v[20:23], v[64:67], v[32:35], v[20:23]\n" \
  "v_mfma_f32_16x16x32_f16 v[24:27], v[96:99], v[32:35], v[24:27]\n" \
  "ds_read_b128 v[32:35], v10 offset:2112\n" \
  "s_waitcnt lgkmcnt(6)\n" \
  "v_mfma_f32_16x16x32_f16 v[20:23], v[68:71], v[36:39], v[20:23]\n" \
  "v_mov_b32 v9, 0x10\n" \
  "ds_write_b128 v11, v[16:19] offset:1280\n" \
  "ds_add_u32 v4, v9 offset:0\n" \
  "v_mfma_f32_16x16x32_f16 v[24:27], v[100:103], v[36:39], v[24:27]\n" \
  "ds_read_b128 v[36:39], v10 offset:2176\n" \
  "v_mfma_f32_16x16x32_f16 v[20:23], v[72:75], v[40:43], v[20:23]\n" \
  "v_mfma_f32_16x16x32_f16 v[24:27], v[104:107], v[40:43], v[24:27]\n" \
  "ds_read_b128 v[40:43], v10 offset:2240\n" \
  "s_waitcnt lgkmcnt(8)\n" \
  "v_mfma_f32_16x16x32_f16 v[20:23], v[76:79], v[44:47], v[20:23]\n" \
  "v_mfma_f32_16x16x32_f16 v[24:27], v[108:111], v[44:47], v[24:27]\n" \
  "ds_read_b128 v[44:47], v10 offset:2304\n" \
  "v_mfma_f32_16x16x32_f16 v[20:23], v[80:83], v[48:51], v[20:23]\n" \
  "v_mfma_f32_16x16x32_f16 v[24:27], v[112:115], v[48:51], v[24:27]\n" \
  "ds_read_b128 v[48:51], v10 offset:2368\n" \
  "s_waitcnt lgkmcnt(8)\n" \
  "v_mfma_f32_16x16x32_f16 v[20:23], v[84:87], v[52:55], v[20:23]\n" \
  "v_mfma_f32_16x16x32_f16 v[24:27], v[116:119], v[52:55], v[24:27]\n" \
  "ds_read_b128 v[52:55], v10 offset:2432\n" \
  "v_mfma_f32_16x16x32_f16 v[20:23], v[88:91], v[56:59], v[20:23]\n" \
  "v_mfma_f32_16x16x32_f16 v[24:27], v[120:123], v[56:59], v[24:27]\n" \
  "ds_read_b128 v[56:59], v10 offset:2496\n" \
  "s_waitcnt lgkmcnt(8)\n" \
  "v_mfma_f32_16x16x32_f16 v[24:27], v[60:63], v[28:31], v[24:27]\n" \
  "v_mfma_f32_16x16x32_f16 v[24:27], v[64:67], v[32:35], v[24:27]\n" \
  "s_waitcnt lgkmcnt(4)\n" \
  "v_mfma_f32_16x16x32_f16 v[24:27], v[68:71], v[36:39], v[24:27]\n" \
  "v_mov_b32 v9, 0x100\n" \
  "ds_write_b128 v11, v[20:23] offset:2560\n" \
  "ds_add_u32 v4, v9 offset:0\n" \
  "v_mfma_f32_16x16x32_f16 v[24:27], v[72:75], v[40:43], v[24:27]\n" \
  "s_waitcnt lgkmcnt(4)\n" \
  "v_mfma_f32_16x16x32_f16 v[24:27], v[76:79], v[44:47], v[24:27]\n" \
  "v_mfma_f32_16x16x32_f16 v[24:27], v[80:83], v[48:51], v[24:27]\n" \
  "s_waitcnt lgkmcnt(2)\n" \
  "v_mfma_f32_16x16x32_f16 v[24:27], v[84:87], v[52:55], v[24:27]\n" \
  "v_mfma_f32_16x16x32_f16 v[24:27], v[88:91], v[56:59], v[24:27]\n" \
  "v_mov_b32 v9, 0x1000\n" \
  "s_nop 6\n" \
  "ds_write_b128 v11, v[24:27] offset:3840\n" \
  "ds_add_u32 v4, v9 offset:0\n" \
  "s_branch Lr3%=\n" \
  "Lr1%=:\n" \
  "v_add_u32 v9, 1024, v3\n" \
  "ds_read2_b32 v[12:13], v9 offset0:48 offset1:49\n" \
  "ds_read2_b32 v[14:15], v9 offset0:50 offset1:51\n" \
  "ds_read2_b32 v[16:17], v9 offset0:44 offset1:45\n" \
  "ds_read2_b32 v[18:19], v9 offset0:46 offset1:47\n" \
  "ds_read2_b32 v[20:21], v9 offset0:61 offset1:62\n" \
  "ds_read2_b32 v[22:23], v9 offset0:63 offset1:64\n" \
  "ds_read2_b32 v[24:25], v8 offset0:0 offset1:1\n" \
  "ds_read2_b32 v[26:27], v8 offset0:2 offset1:3\n" \
  "ds_read2_b32 v[36:37], v8 offset0:48 offset1:49\n" \
  "ds_read2_b32 v[38:39], v8 offset0:50 offset1:51\n" \
  "ds_read2_b32 v[28:29], v8 offset0:16 offset1:17\n" \
  "ds_read2_b32 v[30:31], v8 offset0:18 offset1:19\n" \
  "ds_read2_b32 v[40:41], v8 offset0:64 offset1:65\n" \
  "ds_read2_b32 v[42:43], v8 offset0:66 offset1:67\n" \
  "ds_read2_b32 v[32:33], v8 offset0:32 offset1:33\n" \
  "ds_read2_b32 v[34:35], v8 offset0:34 offset1:35\n" \
  "ds_read2_b32 v[44:45], v8 offset0:80 offset1:81\n" \
  "ds_read2_b32 v[46:47], v8 offset0:82 offset1:83\n" \
  "s_mov_b32 s97, 0x1fffff\n" \
  "Lfp%=:\n" \
  "ds_read_b32 v8, v4 offset:4\n" \
  "s_waitcnt lgkmcnt(0)\n" \
  "v_readfirstlane_b32 s96, v8\n" \
  "s_and_b32 s96, s96, 0xff\n" \
  "s_cmp_eq_u32 s96, 16\n" \
  "s_cbranch_scc1 Lfp%=x\n" \
  "s_sleep 1\n" \
  "s_sub_u32 s97, s97, 1\n" \
  "s_cbranch_scc0 Lfp%=\n" \
  "Lfp%=x:\n" \
  "s_setprio " STR(PRF) "\n" \
  "ds_read_b128 v[48:51], v10 offset:3584\n" \
  "ds_read_b128 v[52:55], v10 offset:3600\n" \
  "ds_read_b128 v[56:59], v10 offset:3072\n" \
  "ds_read_b128 v[60:63], v10 offset:3088\n" \
  "s_waitcnt lgkmcnt(3)\n" \
  "v_mfma_f32_16x16x32_f16 v[64:67], v[48:51], v[12:15], 0\n" \
  "v_mfma_f32_16x16x32_f16 v[68:71], v[48:51], v[16:19], 0\n" \
  "v_mfma_f32_16x16x32_f16 v[72:75], v[48:51], v[20:23], 0\n" \
  "s_waitcnt lgkmcnt(2)\n" \
  "v_mfma_f32_16x16x32_f16 v[76:79], v[52:55], v[20:23], 0\n" \
  "ds_read_b128 v[48:51], v10 offset:2560\n" \
  "ds_read_b128 v[52:55], v10 offset:2576\n" \
  "s_nop 2\n" \
  "v_cvt_pk_f16_f32 v80, v67, v71\n" \
  "v_cvt_pk_f16_f32 v81, v66, v70\n" \
  "v_cvt_pk_f16_f32 v82, v65, v69\n" \
  "v_cvt_pk_f16_f32 v83, v64, v68\n" \
  "v_cvt_pk_f16_f32 v84, v75, v79\n" \
  "v_cvt_pk_f16_f32 v85, v74, v78\n" \
  "v_cvt_pk_f16_f32 v86, v73, v77\n" \
  "v_cvt_pk_f16_f32 v87, v72, v76\n" \
  "s_waitcnt lgkmcnt(3)\n" \
  "v_mfma_f32_16x16x32_f16 v[64:67], v[56:59], v[12:15], 0\n" \
  "v_mfma_f32_16x16x32_f16 v[68:71], v[56:59], v[16:19], 0\n" \
  "v_mfma_f32_16x16x32_f16 v[72:75], v[56:59], v[20:23], 0\n" \
  "s_waitcnt lgkmcnt(2)\n" \
  "v_mfma_f32_16x16x32_f16 v[76:79], v[60:63], v[20:23], 0\n" \
  "ds_read_b128 v[56:59], v10 offset:2048\n" \
  "ds_read_b128 v[60:63], v10 offset:2064\n" \
  "v_mfma_f32_16x16x32_f16 v[104:107], v[80:83], v[24:27], 0\n" \
  "v_mfma_f32_16x16x32_f16 v[108:111], v[80:83], v[28:31], 0\n" \
  "s_nop 0\n" \
  "v_cvt_pk_f16_f32 v88, v67, v71\n" \
  "v_cvt_pk_f16_f32 v89, v66, v70\n" \
  "v_mfma_f32_16x16x32_f16 v[104:107], v[84:87], v[36:39], v[104:107]\n" \
  "v_cvt_pk_f16_f32 v90, v65, v69\n" \
  "v_cvt_pk_f16_f32 v91, v64, v68\n" \
  "v_mfma_f32_16x16x32_f16 v[96:99], v[80:83], v[32:35], 0\n" \
  "v_cvt_pk_f16_f32 v92, v75, v79\n" \
  "v_cvt_pk_f16_f32 v93, v74, v78\n" \
  "v_mfma_f32_16x16x32_f16 v[108:111], v[84:87], v[40:43], v[108:111]\n" \
  "v_cvt_pk_f16_f32 v94, v73, v77\n" \
  "v_cvt_pk_f16_f32 v95, v72, v76\n" \
  "v_mfma_f32_16x16x32_f16 v[96:99], v[84:87], v[44:47], v[96:99]\n" \
  "s_waitcnt lgkmcnt(3)\n" \
  "v_mfma_f32_16x16x32_f16 v[64:67], v[48:51], v[12:15], 0\n" \
  "v_mfma_f32_16x16x32_f16 v[68:71], v[48:51], v[16:19], 0\n" \
  "v_mfma_f32_16x16x32_f16 v[72:75], v[48:51], v[20:23], 0\n" \
  "s_waitcnt lgkmcnt(2)\n" \
  "v_mfma_f32_16x16x32_f16 v[76:79], v[52:55], v[20:23], 0\n" \
  "s_mov_b32 s97, 0x1fffff\n" \
  "Lfq%=:\n" \
  "ds_read_b32 v8, v4 offset:4\n" \
  "s_waitcnt lgkmcnt(0)\n" \
  "v_readfirstlane_b32 s96, v8\n" \
  "s_and_b32 s96, s96, 0x400\n" \
  "s_cmp_eq_u32 s96, 0x400\n" \
  "s_cbranch_scc1 Lfq%=x\n" \
  "s_sub_u32 s97, s97, 1\n" \
  "s_cbranch_scc0 Lfq%=\n" \
  "Lfq%=x:\n" \
  "ds_read_b128 v[48:51], v10 offset:1536\n" \
  "ds_read_b128 v[52:55], v10 offset:1552\n" \
  "v_mov_b32 v9, 0x100\n" \
  "ds_write_b128 v11, v[104:107] offset:10240\n" \
  "ds_add_u32 v4, v9 offset:0\n" \
  "v_mfma_f32_16x16x32_f16 v[108:111], v[88:91], v[24:27], v[108:111]\n" \
  "v_mfma_f32_16x16x32_f16 v[96:99], v[88:91], v[28:31], v[96:99]\n" \
  "v_cvt_pk_f16_f32 v80, v67, v71\n" \
  "v_cvt_pk_f16_f32 v81, v66, v70\n" \
  "v_mfma_f32_16x16x32_f16 v[108:111], v[92:95], v[36:39], v[108:111]\n" \
  "v_cvt_pk_f16_f32 v82, v65, v69\n" \
  "v_cvt_pk_f16_f32 v83, v64, v68\n" \
  "v_mfma_f32_16x16x32_f16 v[100:103], v[88:91], v[32:35], 0\n" \
  "v_cvt_pk_f16_f32 v84, v75, v79\n" \
  "v_cvt_pk_f16_f32 v85, v74, v78\n" \
  "v_mfma_f32_16x16x32_f16 v[96:99], v[92:95], v[40:43], v[96:99]\n" \
  "v_cvt_pk_f16_f32 v86, v73, v77\n" \
  "v_cvt_pk_f16_f32 v87, v72, v76\n" \
  "v_mfma_f32_16x16x32_f16 v[100:103], v[92:95], v[44:47], v[100:103]\n" \
  "v_mfma_f32_16x16x32_f16 v[64:67], v[56:59], v[12:15], 0\n" \
  "v_mfma_f32_16x16x32_f16 v[68:71], v[56:59], v[16:19], 0\n" \
  "v_mfma_f32_16x16x32_f16 v[72:75], v[56:59], v[20:23], 0\n" \
  "v_mfma_f32_16x16x32_f16 v[76:79], v[60:63], v[20:23], 0\n" \
  "ds_read_b128 v[56:59], v10 offset:1024\n" \
  "ds_read_b128 v[60:63], v10 offset:1040\n" \
  "v_mov_b32 v9, 0x1000\n" \
  "ds_write_b128 v11, v[108:111] offset:11520\n" \
  "ds_add_u32 v4, v9 offset:0\n" \
  "v_mfma_f32_16x16x32_f16 v[96:99], v[80:83], v[24:27], v[96:99]\n" \
  "v_mfma_f32_16x16x32_f16 v[100:103], v[80:83], v[28:31], v[100:103]\n" \
  "v_cvt_pk_f16_f32 v88, v67, v71\n" \
  "v_cvt_pk_f16_f32 v89, v66, v70\n" \
  "v_mfma_f32_16x16x32_f16 v[96:99], v[84:87], v[36:39], v[96:99]\n" \
  "v_cvt_pk_f16_f32 v90, v65, v69\n" \
  "v_cvt_pk_f16_f32 v91, v64, v68\n" \
  "v_mfma_f32_16x16x32_f16 v[104:107], v[80:83], v[32:35], 0\n" \
  "v_cvt_pk_f16_f32 v92, v75, v79\n" \
  "v_cvt_pk_f16_f32 v93, v74, v78\n" \
  "v_mfma_f32_16x16x32_f16 v[100:103], v[84:87], v[40:43], v[100:103]\n" \
  "v_cvt_pk_f16_f32 v94, v73, v77\n" \
  "v_cvt_pk_f16_f32 v95, v72, v76\n" \
  "v_mfma_f32_16x16x32_f16 v[104:107], v[84:87], v[44:47], v[104:107]\n" \
  "s_waitcnt lgkmcnt(7)\n" \
  "v_mfma_f32_16x16x32_f16 v[64:67], v[48:51], v[12:15], 0\n" \
  "v_mfma_f32_16x16x32_f16 v[68:71], v[48:51], v[16:19], 0\n" \
  "v_mfma_f32_16x16x32_f16 v[72:75], v[48:51], v[20:23], 0\n" \
  "s_waitcnt lgkmcnt(6)\n" \
  "v_mfma_f32_16x16x32_f16 v[76:79], v[52:55], v[20:23], 0\n" \
  "v_mov_b32 v9, 0x10000\n" \
  "ds_write_b128 v11, v[96:99] offset:12800\n" \
  "ds_add_u32 v4, v9 offset:0\n" \
  "v_mfma_f32_16x16x32_f16 v[100:103], v[88:91], v[24:27], v[100:103]\n" \
  "v_mfma_f32_16x16x32_f16 v[104:107], v[88:91], v[28:31], v[104:107]\n" \
  "v_cvt_pk_f16_f32 v80, v67, v71\n" \
  "v_cvt_pk_f16_f32 v81, v66, v70\n" \
  "v_mfma_f32_16x16x32_f16 v[100:103], v[92:95], v[36:39], v[100:103]\n" \
  "v_cvt_pk_f16_f32 v82, v65, v69\n" \
  "v_cvt_pk_f16_f32 v83, v64, v68\n" \
  "v_mfma_f32_16x16x32_f16 v[108:111], v[88:91], v[32:35], 0\n" \
  "v_cvt_pk_f16_f32 v84, v75, v79\n" \
  "v_cvt_pk_f16_f32 v85, v74, v78\n" \
  "v_mfma_f32_16x16x32_f16 v[104:107], v[92:95], v[40:43], v[104:107]\n" \
  "v_cvt_pk_f16_f32 v86, v73, v77\n" \
  "v_cvt_pk_f16_f32 v87, v72, v76\n" \
  "v_mfma_f32_16x16x32_f16 v[108:111], v[92:95], v[44:47], v[108:111]\n" \
  "s_waitcnt lgkmcnt(5)\n" \
  "v_mfma_f32_16x16x32_f16 v[64:67], v[56:59], v[12:15], 0\n" \
  "v_mfma_f32_16x16x32_f16 v[68:71], v[56:59], v[16:19], 0\n" \
  "v_mfma_f32_16x16x32_f16 v[72:75], v[56:59], v[20:23], 0\n" \
  "s_waitcnt lgkmcnt(4)\n" \
  "v_mfma_f32_16x16x32_f16 v[76:79], v[60:63], v[20:23], 0\n" \
  "v_mov_b32 v9, 0x100000\n" \
  "ds_write_b128 v11, v[100:103] offset:14080\n" \
  "ds_add_u32 v4, v9 offset:0\n" \
  "v_mfma_f32_16x16x32_f16 v[104:107], v[80:83], v[24:27], v[104:107]\n" \
  "s_nop 0\n" \
  "v_cvt_pk_f16_f32 v88, v67, v71\n" \
  "v_cvt_pk_f16_f32 v89, v66, v70\n" \
  "v_mfma_f32_16x16x32_f16 v[108:111], v[80:83], v[28:31], v[108:111]\n" \
  "v_cvt_pk_f16_f32 v90, v65, v69\n" \
  "v_cvt_pk_f16_f32 v91, v64, v68\n" \
  "v_mfma_f32_16x16x32_f16 v[104:107], v[84:87], v[36:39], v[104:107]\n" \
  "v_cvt_pk_f16_f32 v92, v75, v79\n" \
  "v_cvt_pk_f16_f32 v93, v74, v78\n" \
  "v_mfma_f32_16x16x32_f16 v[108:111], v[84:87], v[40:43], v[108:111]\n" \
  "v_cvt_pk_f16_f32 v94, v73, v77\n" \
  "v_cvt_pk_f16_f32 v95, v72, v76\n" \
  "v_mov_b32 v9, 0x1000000\n" \
  "s_nop 1\n" \
  "ds_write_b128 v11, v[104:107] offset:15360\n" \
  "ds_add_u32 v4, v9 offset:0\n" \
  "v_mfma_f32_16x16x32_f16 v[108:111], v[88:91], v[24:27], v[108:111]\n" \
  "v_mfma_f32_16x16x32_f16 v[108:111], v[92:95], v[36:39], v[108:111]\n" \
  "v_mov_b32 v9, 0x10000000\n" \
  "s_nop 6\n" \
  "ds_write_b128 v11, v[108:111] offset:16640\n" \
  "ds_add_u32 v4, v9 offset:0\n" \
  "s_branch Lr3%=\n" \
  "Lr2%=:\n" \
  "ds_read2_b32 v[24:25], v8 offset0:96 offset1:97\n" \
  "ds_read2_b32 v[26:27], v8 offset0:98 offset1:99\n" \
  "ds_read2_b32 v[28:29], v8 offset0:112 offset1:113\n" \
  "ds_read2_b32 v[30:31], v8 offset0:114 offset1:115\n" \
  "ds_read2_b32 v[32:33], v8 offset0:128 offset1:129\n" \
  "ds_read2_b32 v[34:35], v8 offset0:130 offset1:131\n" \
  "ds_read2_b32 v[36:37], v8 offset0:144 offset1:145\n" \
  "ds_read2_b32 v[38:39], v8 offset0:146 offset1:147\n" \
  "v_add_u32 v9, 1024, v3\n" \
  "ds_read2_b32 v[124:125], v9 offset0:0 offset1:1\n" \
  "ds_read2_b32 v[126:127], v9 offset0:2 offset1:3\n" \
  "ds_read2_b32 v[20:21], v9 offset0:32 offset1:33\n" \
  "ds_read2_b32 v[22:23], v9 offset0:34 offset1:35\n" \
  "ds_read2_b32 v[12:13], v9 offset0:76 offset1:77\n" \
  "ds_read2_b32 v[14:15], v9 offset0:78 offset1:79\n" \
  "ds_read2_b32 v[16:17], v9 offset0:91 offset1:92\n" \
  "ds_read2_b32 v[18:19], v9 offset0:93 offset1:94\n" \
  "ds_read2_b32 v[40:41], v3 offset0:112 offset1:113\n" \
  "ds_read2_b32 v[42:43], v3 offset0:114 offset1:115\n" \
  "ds_read2_b32 v[44:45], v3 offset0:128 offset1:129\n" \
  "ds_read2_b32 v[46:47], v3 offset0:130 offset1:131\n" \
  "ds_read2_b32 v[48:49], v3 offset0:144 offset1:145\n" \
  "ds_read2_b32 v[50:51], v3 offset0:146 offset1:147\n" \
  "ds_read2_b32 v[52:53], v3 offset0:160 offset1:161\n" \
  "ds_read2_b32 v[54:55], v3 offset0:162 offset1:163\n" \
  "ds_read2_b32 v[56:57], v3 offset0:176 offset1:177\n" \
  "ds_read2_b32 v[58:59], v3 offset0:178 offset1:179\n" \
  "ds_read2_b32 v[60:61], v3 offset0:192 offset1:193\n" \
  "ds_read2_b32 v[62:63], v3 offset0:194 offset1:195\n" \
  "ds_read2_b32 v[64:65], v3 offset0:208 offset1:209\n" \
  "ds_read2_b32 v[66:67], v3 offset0:210 offset1:211\n" \
  "ds_read2_b32 v[68:69], v3 offset0:224 offset1:225\n" \
  "ds_read2_b32 v[70:71], v3 offset0:226 offset1:227\n" \
  "ds_read2_b32 v[72:73], v3 offset0:240 offset1:241\n" \
  "ds_read2_b32 v[74:75], v3 offset0:242 offset1:243\n" \
  "s_mov_b32 s97, 0x1fffff\n" \
  "Lgp%=:\n" \
  "ds_read_b32 v8, v4 offset:4\n" \
  "s_waitcnt lgkmcnt(0)\n" \
  "v_readfirstlane_b32 s96, v8\n" \
  "s_and_b32 s96, s96, 0xff\n" \
  "s_cmp_eq_u32 s96, 16\n" \
  "s_cbranch_scc1 Lgp%=x\n" \
  "s_sleep 1\n" \
  "s_sub_u32 s97, s97, 1\n" \
  "s_cbranch_scc0 Lgp%=\n" \
  "Lgp%=x:\n" \
  "s_setprio " STR(PRG) "\n" \
  "ds_read_b128 v[76:79], v10 offset:3584\n" \
  "ds_read_b128 v[80:83], v10 offset:3600\n" \
  "ds_read_b128 v[84:87], v10 offset:3072\n" \
  "ds_read_b128 v[88:91], v10 offset:3088\n" \
  "s_waitcnt lgkmcnt(3)\n" \
  "v_mfma_f32_16x16x32_f16 v[92:95], v[76:79], v[12:15], 0\n" \
  "s_waitcnt lgkmcnt(2)\n" \
  "v_mfma_f32_16x16x32_f16 v[96:99], v[80:83], v[16:19], 0\n" \
  "v_mfma_f32_16x16x32_f16 v[108:111], v[20:23], v[76:79], 0\n" \
  "ds_read_b128 v[76:79], v10 offset:2560\n" \
  "ds_read_b128 v[80:83], v10 offset:2576\n" \
  "s_nop 4\n" \
  "v_cvt_pk_f16_f32 v100, v95, v99\n" \
  "v_cvt_pk_f16_f32 v101, v94, v98\n" \
  "v_cvt_pk_f16_f32 v102, v93, v97\n" \
  "v_cvt_pk_f16_f32 v103, v92, v96\n" \
  "s_waitcnt lgkmcnt(3)\n" \
  "v_mfma_f32_16x16x32_f16 v[92:95], v[84:87], v[12:15], 0\n" \
  "s_waitcnt lgkmcnt(2)\n" \
  "v_mfma_f32_16x16x32_f16 v[96:99], v[88:91], v[16:19], 0\n" \
  "v_mfma_f32_16x16x32_f16 v[112:115], v[20:23], v[84:87], 0\n" \
  "ds_read_b128 v[84:87], v10 offset:2048\n" \
  "ds_read_b128 v[88:91], v10 offset:2064\n" \
  "v_mfma_f32_16x16x32_f16 v[108:111], v[100:103], v[24:27], v[108:111]\n" \
  "v_mfma_f32_16x16x32_f16 v[112:115], v[100:103], v[28:31], v[112:115]\n" \
  "v_mfma_f32_16x16x32_f16 v[116:119], v[100:103], v[32:35], 0\n" \
  "v_mfma_f32_16x16x32_f16 v[120:123], v[100:103], v[36:39], 0\n" \
  "ds_read2_b32 v[36:37], v3 offset0:96 offset1:97\n" \
  "ds_read2_b32 v[38:39], v3 offset0:98 offset1:99\n" \
  "v_cvt_pk_f16_f32 v104, v95, v99\n" \
  "v_cvt_pk_f16_f32 v105, v94, v98\n" \
  "v_cvt_pk_f16_f32 v106, v93, v97\n" \
  "v_cvt_pk_f16_f32 v107, v92, v96\n" \
  "s_waitcnt lgkmcnt(5)\n" \
  "v_mfma_f32_16x16x32_f16 v[92:95], v[76:79], v[12:15], 0\n" \
  "s_waitcnt lgkmcnt(4)\n" \
  "v_mfma_f32_16x16x32_f16 v[96:99], v[80:83], v[16:19], 0\n" \
  "v_mfma_f32_16x16x32_f16 v[116:119], v[20:23], v[76:79], v[116:119]\n" \
  "v_mfma_f32_16x16x32_f16 v[108:111], v[124:127], v[76:79], v[108:111]\n" \
  "ds_read_b128 v[76:79], v10 offset:2048\n" \
  "ds_read_b128 v[80:83], v10 offset:2112\n" \
  "v_mfma_f32_16x16x32_f16 v[112:115], v[104:107], v[24:27], v[112:115]\n" \
  "v_mfma_f32_16x16x32_f16 v[116:119], v[104:107], v[28:31], v[116:119]\n" \
  "v_mfma_f32_16x16x32_f16 v[120:123], v[104:107], v[32:35], v[120:123]\n" \
  "ds_read2_b32 v[32:33], v3 offset0:80 offset1:81\n" \
  "ds_read2_b32 v[34:35], v3 offset0:82 offset1:83\n" \
  "v_cvt_pk_f16_f32 v100, v95, v99\n" \
  "v_cvt_pk_f16_f32 v101, v94, v98\n" \
  "v_cvt_pk_f16_f32 v102, v93, v97\n" \
  "v_cvt_pk_f16_f32 v103, v92, v96\n" \
  "s_waitcnt lgkmcnt(7)\n" \
  "v_mfma_f32_16x16x32_f16 v[92:95], v[84:87], v[12:15], 0\n" \
  "s_waitcnt lgkmcnt(6)\n" \
  "v_mfma_f32_16x16x32_f16 v[96:99], v[88:91], v[16:19], 0\n" \
  "v_mfma_f32_16x16x32_f16 v[120:123], v[20:23], v[84:87], v[120:123]\n" \
  "ds_read_b128 v[84:87], v10 offset:2176\n" \
  "ds_read_b128 v[88:91], v10 offset:2240\n" \
  "ds_read2_b32 v[12:13], v3 offset0:0 offset1:1\n" \
  "ds_read2_b32 v[14:15], v3 offset0:2 offset1:3\n" \
  "ds_read2_b32 v[16:17], v3 offset0:16 offset1:17\n" \
  "ds_read2_b32 v[18:19], v3 offset0:18 offset1:19\n" \
  "ds_read2_b32 v[20:21], v3 offset0:32 offset1:33\n" \
  "ds_read2_b32 v[22:23], v3 offset0:34 offset1:35\n" \
  "v_mfma_f32_16x16x32_f16 v[116:119], v[100:103], v[24:27], v[116:119]\n" \
  "v_mfma_f32_16x16x32_f16 v[120:123], v[100:103], v[28:31], v[120:123]\n" \
  "ds_read2_b32 v[28:29], v3 offset0:64 offset1:65\n" \
  "ds_read2_b32 v[30:31], v3 offset0:66 offset1:67\n" \
  "v_cvt_pk_f16_f32 v104, v95, v99\n" \
  "v_cvt_pk_f16_f32 v105, v94, v98\n" \
  "v_cvt_pk_f16_f32 v106, v93, v97\n" \
  "v_cvt_pk_f16_f32 v107, v92, v96\n" \
  "s_mov_b32 s97, 0x1fffff\n" \
  "Lgq%=:\n" \
  "ds_read_b32 v8, v4 offset:4\n" \
  "s_waitcnt lgkmcnt(0)\n" \
  "v_readfirstlane_b32 s96, v8\n" \
  "s_and_b32 s96, s96, 0x400\n" \
  "s_cmp_eq_u32 s96, 0x400\n" \
  "s_cbranch_scc1 Lgq%=x\n" \
  "s_sub_u32 s97, s97, 1\n" \
  "s_cbranch_scc0 Lgq%=\n" \
  "Lgq%=x:\n" \
  "ds_read_b128 v[92:95], v10 offset:2304\n" \
  "ds_read_b128 v[96:99], v10 offset:2368\n" \
  "ds_read_b128 v[100:103], v10 offset:2432\n" \
  "v_mfma_f32_16x16x32_f16 v[120:123], v[104:107], v[24:27], v[120:123]\n" \
  "ds_read2_b32 v[24:25], v3 offset0:48 offset1:49\n" \
  "ds_read2_b32 v[26:27], v3 offset0:50 offset1:51\n" \
  "ds_read_b128 v[104:107], v10 offset:2496\n" \
  "v_mfma_f32_16x16x32_f16 v[108:111], v[44:47], v[76:79], v[108:111]\n" \
  "v_mfma_f32_16x16x32_f16 v[112:115], v[124:127], v[76:79], v[112:115]\n" \
  "ds_read_b128 v[76:79], v10 offset:1536\n" \
  "v_mfma_f32_16x16x32_f16 v[108:111], v[48:51], v[80:83], v[108:111]\n" \
  "ds_read_b128 v[80:83], v10 offset:1600\n" \
  "v_mfma_f32_16x16x32_f16 v[108:111], v[52:55], v[84:87], v[108:111]\n" \
  "ds_read_b128 v[84:87], v10 offset:1664\n" \
  "v_mfma_f32_16x16x32_f16 v[108:111], v[56:59], v[88:91], v[108:111]\n" \
  "ds_read_b128 v[88:91], v10 offset:1728\n" \
  "s_waitcnt lgkmcnt(8)\n" \
  "v_mfma_f32_16x16x32_f16 v[108:111], v[60:63], v[92:95], v[108:111]\n" \
  "ds_read_b128 v[92:95], v10 offset:1792\n" \
  "v_mfma_f32_16x16x32_f16 v[108:111], v[64:67], v[96:99], v[108:111]\n" \
  "ds_read_b128 v[96:99], v10 offset:1856\n" \
  "s_waitcnt lgkmcnt(6)\n" \
  "v_mfma_f32_16x16x32_f16 v[108:111], v[68:71], v[100:103], v[108:111]\n" \
  "ds_read_b128 v[100:103], v10 offset:1920\n" \
  "v_mfma_f32_16x16x32_f16 v[108:111], v[72:75], v[104:107], v[108:111]\n" \
  "ds_read_b128 v[104:107], v10 offset:1984\n" \
  "s_waitcnt lgkmcnt(6)\n" \
  "v_mfma_f32_16x16x32_f16 v[108:111], v[12:15], v[76:79], v[108:111]\n" \
  "v_mfma_f32_16x16x32_f16 v[112:115], v[44:47], v[76:79], v[112:115]\n" \
  "v_mfma_f32_16x16x32_f16 v[116:119], v[124:127], v[76:79], v[116:119]\n" \
  "ds_read_b128 v[76:79], v10 offset:1024\n" \
  "v_mfma_f32_16x16x32_f16 v[108:111], v[16:19], v[80:83], v[108:111]\n" \
  "v_mfma_f32_16x16x32_f16 v[112:115], v[48:51], v[80:83], v[112:115]\n" \
  "ds_read_b128 v[80:83], v10 offset:1088\n" \
  "s_waitcnt lgkmcnt(6)\n" \
  "v_mfma_f32_16x16x32_f16 v[108:111], v[20:23], v[84:87], v[108:111]\n" \
  "v_mfma_f32_16x16x32_f16 v[112:115], v[52:55], v[84:87], v[112:115]\n" \
  "ds_read_b128 v[84:87], v10 offset:1152\n" \
  "v_mfma_f32_16x16x32_f16 v[108:111], v[24:27], v[88:91], v[108:111]\n" \
  "v_mfma_f32_16x16x32_f16 v[112:115], v[56:59], v[88:91], v[112:115]\n" \
  "ds_read_b128 v[88:91], v10 offset:1216\n" \
  "s_waitcnt lgkmcnt(6)\n" \
  "v_mfma_f32_16x16x32_f16 v[108:111], v[28:31], v[92:95], v[108:111]\n" \
  "v_mfma_f32_16x16x32_f16 v[112:115], v[60:63], v[92:95], v[112:115]\n" \
  "ds_read_b128 v[92:95], v10 offset:1280\n" \
  "v_mfma_f32_16x16x32_f16 v[108:111], v[32:35], v[96:99], v[108:111]\n" \
  "v_mfma_f32_16x16x32_f16 v[112:115], v[64:67], v[96:99], v[112:115]\n" \
  "ds_read_b128 v[96:99], v10 offset:1344\n" \
  "s_waitcnt lgkmcnt(6)\n" \
  "v_mfma_f32_16x16x32_f16 v[108:111], v[36:39], v[100:103], v[108:111]\n" \
  "v_mfma_f32_16x16x32_f16 v[112:115], v[68:71], v[100:103], v[112:115]\n" \
  "ds_read_b128 v[100:103], v10 offset:1408\n" \
  "v_mfma_f32_16x16x32_f16 v[108:111], v[40:43], v[104:107], v[108:111]\n" \
  "v_mfma_f32_16x16x32_f16 v[112:115], v[72:75], v[104:107], v[112:115]\n" \
  "ds_read_b128 v[104:107], v10 offset:1472\n" \
  "s_waitcnt lgkmcnt(6)\n" \
  "v_mfma_f32_16x16x32_f16 v[112:115], v[12:15], v[76:79], v[112:115]\n" \
  "v_mfma_f32_16x16x32_f16 v[116:119], v[44:47], v[76:79], v[116:119]\n" \
  "v_mfma_f32_16x16x32_f16 v[120:123], v[124:127], v[76:79], v[120:123]\n" \
  "ds_read_b128 v[76:79], v10 offset:512\n" \
  "v_mfma_f32_16x16x32_f16 v[112:115], v[16:19], v[80:83], v[112:115]\n" \
  "v_mfma_f32_16x16x32_f16 v[116:119], v[48:51], v[80:83], v[116:119]\n" \
  "ds_read_b128 v[80:83], v10 offset:576\n" \
  "s_waitcnt lgkmcnt(6)\n" \
  "v_mfma_f32_16x16x32_f16 v[112:115], v[20:23], v[84:87], v[112:115]\n" \
  "v_mov_b32 v9, 0x10000\n" \
  "ds_write_b128 v11, v[108:111] offset:5120\n" \
  "ds_add_u32 v4, v9 offset:0\n" \
  "v_mfma_f32_16x16x32_f16 v[116:119], v[52:55], v[84:87], v[116:119]\n" \
  "ds_read_b128 v[84:87], v10 offset:640\n" \
  "v_mfma_f32_16x16x32_f16 v[112:115], v[24:27], v[88:91], v[112:115]\n" \
  "v_mfma_f32_16x16x32_f16 v[116:119], v[56:59], v[88:91], v[116:119]\n" \
  "ds_read_b128 v[88:91], v10 offset:704\n" \
  "s_waitcnt lgkmcnt(8)\n" \
  "v_mfma_f32_16x16x32_f16 v[112:115], v[28:31], v[92:95], v[112:115]\n" \
  "v_mfma_f32_16x16x32_f16 v[116:119], v[60:63], v[92:95], v[116:119]\n" \
  "ds_read_b128 v[92:95], v10 offset:768\n" \
  "v_mfma_f32_16x16x32_f16 v[112:115], v[32:35], v[96:99], v[112:115]\n" \
  "v_mfma_f32_16x16x32_f16 v[116:119], v[64:67], v[96:99], v[116:119]\n" \
  "ds_read_b128 v[96:99], v10 offset:832\n" \
  "s_waitcnt lgkmcnt(8)\n" \
  "v_mfma_f32_16x16x32_f16 v[112:115], v[36:39], v[100:103], v[112:115]\n" \
  "v_mfma_f32_16x16x32_f16 v[116:119], v[68:71], v[100:103], v[116:119]\n" \
  "ds_read_b128 v[100:103], v10 offset:896\n" \
  "v_mfma_f32_16x16x32_f16 v[112:115], v[40:43], v[104:107], v[112:115]\n" \
  "v_mfma_f32_16x16x32_f16 v[116:119], v[72:75], v[104:107], v[116:119]\n" \
  "ds_read_b128 v[104:107], v10 offset:960\n" \
  "s_waitcnt lgkmcnt(8)\n" \
  "v_mfma_f32_16x16x32_f16 v[116:119], v[12:15], v[76:79], v[116:119]\n" \
  "v_mfma_f32_16x16x32_f16 v[120:123], v[44:47], v[76:79], v[120:123]\n" \
  "ds_read_b128 v[76:79], v10 offset:0\n" \
  "v_mfma_f32_16x16x32_f16 v[116:119], v[16:19], v[80:83], v[116:119]\n" \
  "v_mfma_f32_16x16x32_f16 v[120:123], v[48:51], v[80:83], v[120:123]\n" \
  "ds_read_b128 v[80:83], v10 offset:64\n" \
  "s_waitcnt lgkmcnt(6)\n" \
  "v_mfma_f32_16x16x32_f16 v[116:119], v[20:23], v[84:87], v[116:119]\n" \
  "v_mov_b32 v9, 0x100000\n" \
  "ds_write_b128 v11, v[112:115] offset:6400\n" \
  "ds_add_u32 v4, v9 offset:0\n" \
  "v_mfma_f32_16x16x32_f16 v[120:123], v[52:55], v[84:87], v[120:123]\n" \
  "ds_read_b128 v[84:87], v10 offset:128\n" \
  "v_mfma_f32_16x16x32_f16 v[116:119], v[24:27], v[88:91], v[116:119]\n" \
  "v_mfma_f32_16x16x32_f16 v[120:123], v[56:59], v[88:91], v[120:123]\n" \
  "ds_read_b128 v[88:91], v10 offset:192\n" \
  "s_waitcnt lgkmcnt(8)\n" \
  "v_mfma_f32_16x16x32_f16 v[116:119], v[28:31], v[92:95], v[116:119]\n" \
  "v_mfma_f32_16x16x32_f16 v[120:123], v[60:63], v[92:95], v[120:123]\n" \
  "ds_read_b128 v[92:95], v10 offset:256\n" \
  "v_mfma_f32_16x16x32_f16 v[116:119], v[32:35], v[96:99], v[116:119]\n" \
  "v_mfma_f32_16x16x32_f16 v[120:123], v[64:67], v[96:99], v[120:123]\n" \
  "ds_read_b128 v[96:99], v10 offset:320\n" \
  "s_waitcnt lgkmcnt(8)\n" \
  "v_mfma_f32_16x16x32_f16 v[116:119], v[36:39], v[100:103], v[116:119]\n" \
  "v_mfma_f32_16x16x32_f16 v[120:123], v[68:71], v[100:103], v[120:123]\n" \
  "ds_read_b128 v[100:103], v10 offset:384\n" \
  "v_mfma_f32_16x16x32_f16 v[116:119], v[40:43], v[104:107], v[116:119]\n" \
  "v_mfma_f32_16x16x32_f16 v[120:123], v[72:75], v[104:107], v[120:123]\n" \
  "ds_read_b128 v[104:107], v10 offset:448\n" \
  "s_waitcnt lgkmcnt(8)\n" \
  "v_mfma_f32_16x16x32_f16 v[120:123], v[12:15], v[76:79], v[120:123]\n" \
  "v_mfma_f32_16x16x32_f16 v[120:123], v[16:19], v[80:83], v[120:123]\n" \
  "s_waitcnt lgkmcnt(4)\n" \
  "v_mfma_f32_16x16x32_f16 v[120:123], v[20:23], v[84:87], v[120:123]\n" \
  "v_mov_b32 v9, 0x1000000\n" \
  "ds_write_b128 v11, v[116:119] offset:7680\n" \
  "ds_add_u32 v4, v9 offset:0\n" \
  "v_mfma_f32_16x16x32_f16 v[120:123], v[24:27], v[88:91], v[120:123]\n" \
  "s_waitcnt lgkmcnt(4)\n" \
  "v_mfma_f32_16x16x32_f16 v[120:123], v[28:31], v[92:95], v[120:123]\n" \
  "v_mfma_f32_16x16x32_f16 v[120:123], v[32:35], v[96:99], v[120:123]\n" \
  "s_waitcnt lgkmcnt(2)\n" \
  "v_mfma_f32_16x16x32_f16 v[120:123], v[36:39], v[100:103], v[120:123]\n" \
  "v_mfma_f32_16x16x32_f16 v[120:123], v[40:43], v[104:107], v[120:123]\n" \
  "v_mov_b32 v9, 0x10000000\n" \
  "s_nop 6\n" \
  "ds_write_b128 v11, v[120:123] offset:8960\n" \
  "ds_add_u32 v4, v9 offset:0\n" \
  "s_branch Lr3%=\n" \
  "Lr4%=:\n" \
  "s_load_dwordx4 s[84:87], %[k], 0x40\n" \
  "s_load_dwordx2 s[88:89], %[k], 0x50\n" \
  "s_and_b32 s46, %[b], 7\n" \
  "s_lshr_b32 s47, %[b], 3\n" \
  "s_mul_i32 s47, s47, 3\n" \
  "s_add_u32 s47, s47, s50\n" \
  "v_and_b32 v9, 63, %[t]\n" \
  "s_mul_i32 s48, s46, 72\n" \
  "s_add_u32 s48, s48, s47\n" \
  "s_mul_i32 s48, s48, 72\n" \
  "v_min_u32 v5, 35, v9\n" \
  "v_add_lshl_u32 v121, v5, s48, 7\n" \
  "v_add_u32 v122, 4608, v121\n" \
  "s_mul_i32 s48, s47, 0\n" \
  "v_add_u32 v6, s48, v9\n" \
  "v_mul_u32_u24 v7, 7282, v6\n" \
  "v_lshrrev_b32 v7, 16, v7\n" \
  "v_mul_u32_u24 v7, 8064, v7\n" \
  "v_lshl_add_u32 v6, v6, 7, v7\n" \
  "v_add_u32 v6, 5308416, v6\n" \
  "v_cmp_gt_u32 vcc, 0, v9\n" \
  "v_subrev_u32 v9, 0, v9\n" \
  "v_min_u32 v9, 15, v9\n" \
  "s_mul_i32 s47, s47, 8\n" \
  "v_lshrrev_b32 v7, 1, v9\n" \
  "v_add_u32 v7, s47, v7\n" \
  "v_mul_u32_u24 v7, 9216, v7\n" \
  "v_and_b32 v9, 1, v9\n" \
  "s_waitcnt lgkmcnt(0)\n" \
  "s_lshr_b32 s47, s84, 7\n" \
  "s_add_u32 s47, s47, s46\n" \
  "s_sub_u32 s47, 3, s47\n" \
  "s_and_b32 s47, s47, 7\n" \
  "s_sub_u32 s48, 8, s47\n" \
  "v_mul_u32_u24 v9, s48, v9\n" \
  "v_add_lshl_u32 v9, v9, s47, 7\n" \
  "v_add_u32 v7, v7, v9\n" \
  "v_cndmask_b32 v6, v7, v6, vcc\n" \
  "s_mul_i32 s46, s46, 1152\n" \
  "v_add_u32 v120, s46, v6\n" \
  "s_setprio 3\n" \
  "s_mov_b32 s97, 0x1fffff\n" \
  "Lkr%=:\n" \
  "ds_read_b32 v17, v4 offset:4\n" \
  "s_waitcnt lgkmcnt(0)\n" \
  "v_readfirstlane_b32 s96, v17\n" \
  "s_and_b32 s96, s96, " STR(PFG) "\n" \
  "s_cmp_eq_u32 s96, " STR(PFG) "\n" \
  "s_cbranch_scc1 Lkr%=x\n" \
  "s_sleep 2\n" \
  "s_sub_u32 s97, s97, 1\n" \
  "s_cbranch_scc0 Lkr%=\n" \
  "Lkr%=x:\n" \
  "s_lshl_b64 exec, -1, 0\n" \
  "global_load_dword v126, v120, s[84:85]\n" \
  "global_load_dword v126, v120, s[86:87]\n" \
  "s_mov_b64 exec, -1\n" \
  "global_load_dword v126, v121, s[88:89]\n" \
  "global_load_dword v126, v122, s[88:89]\n" \
  "Lr3%=:\n" \
  GSTAMP7 \
  "s_setprio 0\n" \
  "Lr5%=:\n"

#define SW_HEAD \
  "s_load_dwordx2 s[46:47], %[k], 0x0\n" \
  "s_load_dwordx2 s[48:49], %[k], 0x30\n" \
  "s_load_dwordx2 s[40:41], %[k], 0x70\n" \
  "s_setprio 3\n" \
  "s_and_b32 s50, %[w], 3\n" \
  "s_mul_i32 s51, %[b], 9216\n" \
  "s_mul_i32 s52, s50, 288\n" \
  "s_add_u32 s51, s51, s52\n" \
  "v_bfe_u32 v1, %[t], 3, 3\n" \
  "v_and_b32 v2, 7, %[t]\n" \
  "v_mul_u32_u24 v3, 1152, v1\n" \
  "v_mul_u32_u24 v4, 12, v2\n" \
  "v_add3_u32 v3, v3, v4, s51\n" \
  "v_and_b32 v5, 255, %[t]\n" \
  "v_mov_b32 v6, 158464\n" \
  "v_lshl_add_u32 v6, v5, 3, v6\n" \
  "v_mov_b64 v[8:9], 0\n" \
  "ds_write_b64 v6, v[8:9]\n" \
  "s_mov_b32 s42, 10668032\n" \
  "s_mov_b32 s43, 0x20000\n" \
  "s_mul_i32 s52, %[b], 6\n" \
  "s_and_b32 s52, s52, -4\n" \
  "s_cmp_lt_u32 %[b], 128\n" \
  SSTAMP1 \
  "s_waitcnt lgkmcnt(0)\n" \
  "s_memtime s[58:59]\n" \
  ".if " STR(SDLY) "\n" \
  "s_sleep " STR(SDLY) "\n" \
  ".endif\n" \
  "global_load_dwordx3 v[16:18], v3, s[46:47] offset:0\n" \
  "global_load_dwordx3 v[20:22], v3, s[46:47] offset:96\n" \
  "global_load_dwordx3 v[24:26], v3, s[46:47] offset:192\n" \
  "buffer_load_dword v7, off, s[40:43], 0 sc1\n" \
  "s_load_dwordx2 s[54:55], s[48:49], s52\n" \
  "s_cbranch_scc1 Lone%=\n" \
  "v_add_u32 v3, 589824, v3\n" \
  "global_load_dwordx3 v[28:30], v3, s[46:47] offset:0\n" \
  "global_load_dwordx3 v[32:34], v3, s[46:47] offset:96\n" \
  "global_load_dwordx3 v[36:38], v3, s[46:47] offset:192\n" \
  "Lone%=:\n" \
  "s_barrier\n"

#define SW_BODY \
  "s_mul_i32 s51, s50, 3200\n" \
  "s_add_u32 s51, s51, 125088\n" \
  "v_mul_u32_u24 v3, 400, v1\n" \
  "v_lshlrev_b32 v4, 4, v2\n" \
  "v_add3_u32 v3, v3, v4, s51\n" \
  "v_and_b32 v10, 3, %[t]\n" \
  "v_mul_u32_u24 v4, 800, v10\n" \
  "v_lshl_add_u32 v4, v1, 4, v4\n" \
  "v_add_u32 v4, s51, v4\n" \
  "v_and_b32 v11, 4, %[t]\n" \
  "v_lshl_add_u32 v6, v11, 1, v4\n" \
  "v_mul_u32_u24 v12, 8421504, v11\n" \
  "v_lshrrev_b32 v13, 1, v11\n" \
  "v_add_u32 v12, v12, v13\n" \
  "v_add_u32 v12, 0x05040100, v12\n" \
  "s_lshr_b32 s51, %[b], 1\n" \
  "s_mul_i32 s51, s51, 24704\n" \
  "s_and_b32 s52, %[b], 1\n" \
  "s_lshl_b32 s52, s52, 6\n" \
  "s_add_u32 s51, s51, s52\n" \
  "s_mul_i32 s52, s50, 6144\n" \
  "s_add_u32 s51, s51, s52\n" \
  "s_add_u32 s51, s51, 7241728\n" \
  "v_bfe_u32 v13, %[t], 2, 4\n" \
  "v_lshlrev_b32 v13, 7, v13\n" \
  "v_lshl_add_u32 v13, v10, 4, v13\n" \
  "v_add_u32 v13, s51, v13\n" \
  "s_lshl4_add_u32 s51, s50, 158464\n" \
  "v_lshl_add_u32 v14, v1, 6, s51\n" \
  "v_mov_b32 v42, 1\n" \
  "s_mov_b32 s53, 3\n" \
  "s_movk_i32 s56, 0x1000\n" \
  "s_mov_b32 s57, 0\n" \
  "v_and_b32 v15, 63, %[t]\n" \
  "v_min_u32 v111, 1, v15\n" \
  "v_mul_u32_u24 v111, 40, v111\n" \
  "v_add_u32 v111, v111, v15\n" \
  "v_mov_b32 v8, 159488\n" \
  "v_lshl_add_u32 v111, v111, 2, v8\n" \
  "v_lshlrev_b32 v8, 2, v5\n" \
  "v_sub_u32 v105, 97308, v8\n" \
  "v_lshrrev_b32 v8, 3, v5\n" \
  "v_mul_u32_u24 v106, 24704, v8\n" \
  "v_lshl_add_u32 v106, v2, 4, v106\n" \
  "s_lshl_b32 s51, %[b], 7\n" \
  "s_add_u32 s51, s51, 7241728\n" \
  "v_add_u32 v106, s51, v106\n" \
  "v_lshlrev_b32 v107, 4, v5\n" \
  "s_mul_i32 s51, %[b], 16512\n" \
  "s_add_u32 s51, s51, 139264\n" \
  "v_add_u32 v117, s51, v107\n" \
  "s_and_b32 s51, %[b], 0\n" \
  "s_mul_i32 s51, s51, 16512\n" \
  "s_add_u32 s51, s51, 10535936\n" \
  "v_add_u32 v107, s51, v107\n" \
  "v_and_b32 v9, 4, v1\n" \
  "v_add_u32 v9, v9, v10\n" \
  "v_mul_u32_u24 v122, 16512, v9\n" \
  "v_and_b32 v9, 3, v1\n" \
  "v_lshl_add_u32 v122, v9, 4, v122\n" \
  "v_and_b32 v8, 15, v8\n" \
  "v_bfe_u32 v9, v5, 1, 2\n" \
  "v_mul_u32_u24 v9, 320, v9\n" \
  "v_lshl_add_u32 v115, v8, 4, v9\n" \
  "v_and_b32 v9, 1, v5\n" \
  "v_lshl_add_u32 v115, v9, 3, v115\n" \
  "s_lshr_b32 s84, s50, 1\n" \
  "s_mul_i32 s51, s84, 1280\n" \
  "v_add_u32 v115, s51, v115\n" \
  "v_add_u32 v116, 64192, v115\n" \
  "s_lshl_b32 s84, s84, 2\n" \
  "s_and_b32 s51, %[b], 1\n" \
  "v_min_u32 v8, 2, v1\n" \
  "v_add_u32 v8, s51, v8\n" \
  "v_cmp_ne_u32 vcc, 3, v8\n" \
  "v_min_u32 v9, 2, v10\n" \
  "v_mul_u32_u24 v9, 27648, v9\n" \
  "v_cndmask_b32 v8, 0, v8, vcc\n" \
  "v_lshl_add_u32 v9, v8, 3, v9\n" \
  "s_mul_i32 s52, %[b], 24\n" \
  "s_add_u32 s52, s52, 4096\n" \
  "v_add_u32 v9, s52, v9\n" \
  "v_mul_u32_u24 v108, 1152, v11\n" \
  "v_add_u32 v108, v108, v9\n" \
  "v_add_u32 v114, 9216, v9\n" \
  "v_add_lshl_u32 v109, v8, s51, 4\n" \
  "v_cmp_ne_u32 vcc, 3, v10\n" \
  "s_nop 1\n" \
  "v_cndmask_b32 v110, 0, 1.0, vcc\n" \
  "s_waitcnt vmcnt(0) lgkmcnt(0)\n" \
  SSTAMP2 \
  "v_readfirstlane_b32 s44, v7\n" \
  "Lun%=:\n" \
  "ds_write_b96 v3, v[16:18] offset:0\n" \
  "ds_write_b96 v3, v[20:22] offset:128\n" \
  "ds_write_b96 v3, v[24:26] offset:256\n" \
  "ds_read2_b32 v[44:45], v6 offset0:0 offset1:100\n" \
  "ds_read2_b32 v[56:57], v4 offset0:1 offset1:101\n" \
  "ds_read2_b32 v[48:49], v6 offset0:32 offset1:132\n" \
  "ds_read2_b32 v[58:59], v4 offset0:33 offset1:133\n" \
  "ds_read2_b32 v[52:53], v6 offset0:64 offset1:164\n" \
  "ds_read2_b32 v[60:61], v4 offset0:65 offset1:165\n" \
  "v_mov_b64 v[40:41], 0\n" \
  "s_add_u32 s45, s44, 1\n" \
  "v_dot2c_f32_bf16 v40, 0x3f803f80, v16\n" \
  "v_dot2c_f32_bf16 v41, v16, v16\n" \
  "v_dot2c_f32_bf16 v40, 0x3f803f80, v17\n" \
  "v_dot2c_f32_bf16 v41, v17, v17\n" \
  "v_dot2c_f32_bf16 v40, 0x3f803f80, v18\n" \
  "v_dot2c_f32_bf16 v41, v18, v18\n" \
  "v_dot2c_f32_bf16 v40, 0x3f803f80, v20\n" \
  "v_dot2c_f32_bf16 v41, v20, v20\n" \
  "v_dot2c_f32_bf16 v40, 0x3f803f80, v21\n" \
  "v_dot2c_f32_bf16 v41, v21, v21\n" \
  "v_dot2c_f32_bf16 v40, 0x3f803f80, v22\n" \
  "v_dot2c_f32_bf16 v41, v22, v22\n" \
  "v_dot2c_f32_bf16 v40, 0x3f803f80, v24\n" \
  "v_dot2c_f32_bf16 v41, v24, v24\n" \
  "v_dot2c_f32_bf16 v40, 0x3f803f80, v25\n" \
  "v_dot2c_f32_bf16 v41, v25, v25\n" \
  "v_dot2c_f32_bf16 v40, 0x3f803f80, v26\n" \
  "v_dot2c_f32_bf16 v41, v26, v26\n" \
  "v_mov_b32 v47, s45\n" \
  "v_mov_b32 v51, s45\n" \
  "v_mov_b32 v55, s45\n" \
  "s_waitcnt lgkmcnt(0)\n" \
  "v_perm_b32 v46, v57, v56, v12\n" \
  "v_add_f32_dpp v40, v40, v40 quad_perm:[1,0,3,2] row_mask:0xf bank_mask:0xf bound_ctrl:1\n" \
  "v_add_f32_dpp v41, v41, v41 quad_perm:[1,0,3,2] row_mask:0xf bank_mask:0xf bound_ctrl:1\n" \
  "buffer_store_dwordx4 v[44:47], v13, s[40:43], 0 offen sc1\n" \
  "v_perm_b32 v50, v59, v58, v12\n" \
  "v_add_f32_dpp v40, v40, v40 quad_perm:[2,3,0,1] row_mask:0xf bank_mask:0xf bound_ctrl:1\n" \
  "v_add_f32_dpp v41, v41, v41 quad_perm:[2,3,0,1] row_mask:0xf bank_mask:0xf bound_ctrl:1\n" \
  "buffer_store_dwordx4 v[48:51], v13, s[40:43], 0 offen offset:2048 sc1\n" \
  "v_perm_b32 v54, v61, v60, v12\n" \
  "v_add_f32_dpp v40, v40, v40 row_half_mirror row_mask:0xf bank_mask:0xf bound_ctrl:1\n" \
  "v_add_f32_dpp v41, v41, v41 row_half_mirror row_mask:0xf bank_mask:0xf bound_ctrl:1\n" \
  "buffer_store_dwordx4 v[52:55], v13, s[40:43], s56 offen sc1\n" \
  "ds_write_b96 v14, v[40:42]\n" \
  SSTAMP3 \
  "s_cmp_lg_u32 s50, s53\n" \
  "s_cbranch_scc1 Lsn%=\n" \
  "v_and_b32 v15, 31, %[t]\n" \
  "s_add_u32 s51, s57, 158464\n" \
  "v_lshl_add_u32 v15, v15, 4, s51\n" \
  "v_mov_b32 v43, 158208\n" \
  "ds_write_b32 v43, v55\n" \
  "s_movk_i32 s97, 0x7fff\n" \
  "Lsp%=:\n" \
  "ds_read_b96 v[56:58], v15\n" \
  "s_waitcnt lgkmcnt(0)\n" \
  "v_cmp_ne_u32 vcc, 1, v58\n" \
  "s_cbranch_vccz Lsq%=\n" \
  "s_sub_u32 s97, s97, 1\n" \
  "s_cbranch_scc0 Lsp%=\n" \
  "Lsq%=:\n" \
  "v_add_f32_dpp v56, v56, v56 quad_perm:[1,0,3,2] row_mask:0xf bank_mask:0xf bound_ctrl:1\n" \
  "v_add_f32_dpp v57, v57, v57 quad_perm:[1,0,3,2] row_mask:0xf bank_mask:0xf bound_ctrl:1\n" \
  "s_lshl_b32 s51, %[b], 6\n" \
  "v_add_f32_dpp v56, v56, v56 quad_perm:[2,3,0,1] row_mask:0xf bank_mask:0xf bound_ctrl:1\n" \
  "v_add_f32_dpp v57, v57, v57 quad_perm:[2,3,0,1] row_mask:0xf bank_mask:0xf bound_ctrl:1\n" \
  "v_mul_f32 v56, 0x3ae38e39, v56\n" \
  "v_mul_f32 v57, 0x3ae38e39, v57\n" \
  "v_fma_f32 v57, -v56, v56, v57\n" \
  "v_max_f32 v57, 0, v57\n" \
  "v_cvt_pk_bf16_f32 v57, 0, v57\n" \
  "v_add_f32 v57, 0x3727c5ac, v57\n" \
  "v_cvt_pk_bf16_f32 v57, 0, v57\n" \
  "v_sqrt_f32 v57, v57\n" \
  "v_mov_b32_dpp v58, v56 row_shl:4 row_mask:0xf bank_mask:0xf bound_ctrl:1\n" \
  "v_cvt_pk_bf16_f32 v57, 0, v57\n" \
  "v_rcp_f32 v60, v57\n" \
  "v_cvt_pk_bf16_f32 v62, v56, v58\n" \
  "v_mov_b32 v63, s45\n" \
  "s_lshl3_add_u32 s51, s57, s51\n" \
  "v_mov_b32_dpp v61, v60 row_shl:4 row_mask:0xf bank_mask:0xf bound_ctrl:1\n" \
  "s_add_u32 s51, s51, 10535936\n" \
  "s_mov_b32 exec_lo, 16843009\n" \
  "s_mov_b32 exec_hi, 0\n" \
  "buffer_store_dwordx4 v[60:63], v122, s[40:43], s51 offen sc1\n" \
  "s_mov_b64 exec, -1\n" \
  "Lsn%=:\n" \
  "s_cmp_lt_u32 %[b], 128\n" \
  "s_cbranch_scc1 Lsd%=\n" \
  "s_cmp_eq_u32 s53, 2\n" \
  "s_cbranch_scc1 Lsd%=\n" \
  "s_mov_b32 s53, 2\n" \
  "s_movk_i32 s57, 512\n" \
  "v_add_u32 v13, 790528, v13\n" \
  "v_add_u32 v14, 512, v14\n" \
  "v_mov_b64 v[16:17], v[28:29]\n" \
  "v_mov_b32 v18, v30\n" \
  "v_mov_b64 v[20:21], v[32:33]\n" \
  "v_mov_b32 v22, v34\n" \
  "v_mov_b64 v[24:25], v[36:37]\n" \
  "v_mov_b32 v26, v38\n" \
  "s_branch Lun%=\n" \
  "Lsd%=:\n" \
  SSTAMP4 \
  "s_mov_b32 s44, s45\n" \
  "s_mov_b32 s45, s84\n" \
  "v_lshrrev_b64 v[8:9], v109, s[54:55]\n" \
  "v_lshlrev_b32 v109, 16, v8\n" \
  "s_movk_i32 s97, 64\n" \
  "Lxw%=:\n" \
  "s_memtime s[46:47]\n" \
  "s_waitcnt lgkmcnt(0)\n" \
  "s_sub_u32 s46, s46, s58\n" \
  "s_cmp_gt_u32 s46, " STR(XT) "\n" \
  "s_cbranch_scc1 Lxv%=\n" \
  "s_sleep 1\n" \
  "s_sub_u32 s97, s97, 1\n" \
  "s_cbranch_scc0 Lxw%=\n" \
  "Lxv%=:\n" \
  "s_setprio " STR(PRH) "\n" \
  SSTAMP5 \
  "buffer_load_dwordx2 v[102:103], v108, s[40:43], 0 offen sc1\n" \
  "s_mov_b32 s94, 0\n" \
  "s_mov_b32 s95, 0\n" \
  "buffer_load_dwordx4 v[64:67], v106, s[40:43], s94 offen sc1\n" \
  "buffer_load_dwordx4 v[68:71], v107, s[40:43], s95 offen sc1\n" \
  "s_mov_b32 s94, 790528\n" \
  "s_mov_b32 s95, 4096\n" \
  "buffer_load_dwordx4 v[72:75], v106, s[40:43], s94 offen sc1\n" \
  "buffer_load_dwordx4 v[76:79], v107, s[40:43], s95 offen sc1\n" \
  "s_waitcnt vmcnt(4)\n" \
  SSTAMP6 \
  "v_cmp_ne_u32 vcc, s44, v103\n" \
  "s_cmp_lg_u64 vcc, 0\n" \
  "s_cbranch_scc1 Lms%=\n" \
  "Lmb%=:\n" \
  "v_mul_f32 v102, v102, v110\n" \
  "v_mov_b32 v104, 226\n" \
  "s_mov_b32 s94, 0x42000000\n" \
  "v_add_f32_dpp v102, v102, v102 quad_perm:[1,0,3,2] row_mask:0xf bank_mask:0xf bound_ctrl:1\n" \
  "v_mov_b32 v112, 1\n" \
  "s_nop 0\n" \
  "v_add_f32_dpp v102, v102, v102 quad_perm:[2,3,0,1] row_mask:0xf bank_mask:0xf bound_ctrl:1\n" \
  "v_cvt_pk_bf16_f32 v102, 0, v102\n" \
  "s_nop 1\n" \
  "v_mov_b32_dpp v103, v102 row_shl:4 row_mask:0xf bank_mask:0xf bound_ctrl:1\n" \
  "v_add_f32 v103, 1.0, v103\n" \
  "v_cvt_pk_bf16_f32 v103, 0, v103\n" \
  "v_fma_f32 v96, |v103|, s94, |v102|\n" \
  "v_lshrrev_b32 v96, 23, v96\n" \
  "v_med3_u32 v96, v96, 26, v104\n" \
  "v_lshlrev_b32 v96, 23, v96\n" \
  "v_sub_u32 v97, 0x84000000, v96\n" \
  "v_add_u32 v104, 0xfb000000, v96\n" \
  "v_mul_f32 v103, v103, v97\n" \
  "v_mul_f32 v102, v102, v97\n" \
  "v_mul_f32 v113, v109, v104\n" \
  "v_readlane_b32 s84, v103, 0\n" \
  "v_readlane_b32 s87, v102, 0\n" \
  "v_readlane_b32 s85, v103, 8\n" \
  "v_readlane_b32 s88, v102, 8\n" \
  "v_readlane_b32 s86, v103, 16\n" \
  "v_readlane_b32 s89, v102, 16\n" \
  "v_mov_b32 v96, s84\n" \
  "v_mov_b32 v97, s87\n" \
  "v_mov_b32 v98, s85\n" \
  "v_mov_b32 v99, s88\n" \
  "v_mov_b32 v100, s86\n" \
  "v_mov_b32 v101, s89\n" \
  "v_readlane_b32 s84, v113, 0\n" \
  "v_readlane_b32 s87, v104, 0\n" \
  "v_readlane_b32 s85, v113, 8\n" \
  "v_readlane_b32 s88, v104, 8\n" \
  "v_readlane_b32 s86, v113, 16\n" \
  "v_readlane_b32 s89, v104, 16\n" \
  "s_waitcnt vmcnt(2)\n" \
  "v_cmp_ne_u32 vcc, s44, v67\n" \
  "v_cmp_ne_u32 s[94:95], s44, v71\n" \
  "s_or_b64 vcc, vcc, s[94:95]\n" \
  "s_cbranch_scc1 Lu0s%=\n" \
  "Lu0b%=:\n" \
  "v_lshlrev_b32 v102, 16, v70\n" \
  "v_and_b32 v103, 0xffff0000, v70\n" \
  "v_lshlrev_b32 v16, 16, v64\n" \
  "v_lshlrev_b32 v17, 16, v65\n" \
  "v_and_b32 v18, 0xffff0000, v64\n" \
  "v_and_b32 v19, 0xffff0000, v65\n" \
  "v_lshlrev_b32 v20, 16, v66\n" \
  "v_and_b32 v21, 0xffff0000, v66\n" \
  "v_pk_add_f32 v[40:41], v[16:17], v[102:103] neg_lo:[0,1] neg_hi:[0,1]\n" \
  "v_pk_add_f32 v[42:43], v[18:19], v[102:103] neg_lo:[0,1] neg_hi:[0,1]\n" \
  "v_pk_add_f32 v[44:45], v[20:21], v[102:103] neg_lo:[0,1] neg_hi:[0,1]\n" \
  "v_cvt_pk_bf16_f32 v40, 0, v40\n" \
  "v_cvt_pk_bf16_f32 v41, 0, v41\n" \
  "v_cvt_pk_bf16_f32 v42, 0, v42\n" \
  "v_cvt_pk_bf16_f32 v43, 0, v43\n" \
  "v_cvt_pk_bf16_f32 v44, 0, v44\n" \
  "v_cvt_pk_bf16_f32 v45, 0, v45\n" \
  "v_pk_mul_f32 v[40:41], v[40:41], v[68:69]\n" \
  "v_pk_mul_f32 v[42:43], v[42:43], v[68:69]\n" \
  "v_pk_mul_f32 v[44:45], v[44:45], v[68:69]\n" \
  "v_cvt_pk_bf16_f32 v40, 0, v40\n" \
  "v_cvt_pk_bf16_f32 v41, 0, v41\n" \
  "v_cvt_pk_bf16_f32 v42, 0, v42\n" \
  "v_cvt_pk_bf16_f32 v43, 0, v43\n" \
  "v_cvt_pk_bf16_f32 v44, 0, v44\n" \
  "v_cvt_pk_bf16_f32 v45, 0, v45\n" \
  "v_pk_mul_f32 v[40:41], v[40:41], v[96:97] op_sel_hi:[1,0]\n" \
  "v_pk_mul_f32 v[42:43], v[42:43], v[98:99] op_sel_hi:[1,0]\n" \
  "v_pk_mul_f32 v[44:45], v[44:45], v[100:101] op_sel_hi:[1,0]\n" \
  "v_cvt_pk_bf16_f32 v40, 0, v40\n" \
  "v_cvt_pk_bf16_f32 v41, 0, v41\n" \
  "v_cvt_pk_bf16_f32 v42, 0, v42\n" \
  "v_cvt_pk_bf16_f32 v43, 0, v43\n" \
  "v_cvt_pk_bf16_f32 v44, 0, v44\n" \
  "v_cvt_pk_bf16_f32 v45, 0, v45\n" \
  "v_pk_add_f32 v[40:41], v[40:41], v[96:97] op_sel:[0,1]\n" \
  "v_pk_add_f32 v[42:43], v[42:43], v[98:99] op_sel:[0,1]\n" \
  "v_pk_add_f32 v[44:45], v[44:45], v[100:101] op_sel:[0,1]\n" \
  "v_cvt_pk_bf16_f32 v40, 0, v40\n" \
  "v_cvt_pk_bf16_f32 v41, 0, v41\n" \
  "v_cvt_pk_bf16_f32 v42, 0, v42\n" \
  "v_cvt_pk_bf16_f32 v43, 0, v43\n" \
  "v_cvt_pk_bf16_f32 v44, 0, v44\n" \
  "v_cvt_pk_bf16_f32 v45, 0, v45\n" \
  "v_cvt_pkrtz_f16_f32 v102, v41, v40\n" \
  "v_cvt_pkrtz_f16_f32 v103, v43, v42\n" \
  "v_cvt_pkrtz_f16_f32 v104, v45, v44\n" \
  "ds_write_b32 v105, v102 offset:3072\n" \
  "ds_write_b32 v105, v103 offset:11264\n" \
  "ds_write_b32 v105, v104 offset:19456\n" \
  SSTAMP7 \
  "s_mov_b32 s94, 1581056\n" \
  "s_mov_b32 s95, 8192\n" \
  "buffer_load_dwordx4 v[80:83], v106, s[40:43], s94 offen sc1\n" \
  "buffer_load_dwordx4 v[84:87], v107, s[40:43], s95 offen sc1\n" \
  "s_mov_b32 s94, 2371584\n" \
  "s_mov_b32 s95, 12288\n" \
  "buffer_load_dwordx4 v[88:91], v106, s[40:43], s94 offen sc1\n" \
  "buffer_load_dwordx4 v[92:95], v107, s[40:43], s95 offen sc1\n" \
  "buffer_load_dwordx2 v[118:119], v114, s[40:43], 0 offen sc1\n" \
  "v_mov_b32 v102, 0x10000\n" \
  "ds_add_u32 v111, v102 offset:4\n" \
  "s_waitcnt vmcnt(5)\n" \
  "v_cmp_ne_u32 vcc, s44, v75\n" \
  "v_cmp_ne_u32 s[94:95], s44, v79\n" \
  "s_or_b64 vcc, vcc, s[94:95]\n" \
  "s_cbranch_scc1 Lu1s%=\n" \
  "Lu1b%=:\n" \
  "v_lshlrev_b32 v102, 16, v78\n" \
  "v_and_b32 v103, 0xffff0000, v78\n" \
  "v_lshlrev_b32 v22, 16, v72\n" \
  "v_lshlrev_b32 v23, 16, v73\n" \
  "v_and_b32 v24, 0xffff0000, v72\n" \
  "v_and_b32 v25, 0xffff0000, v73\n" \
  "v_lshlrev_b32 v26, 16, v74\n" \
  "v_and_b32 v27, 0xffff0000, v74\n" \
  "v_pk_add_f32 v[46:47], v[22:23], v[102:103] neg_lo:[0,1] neg_hi:[0,1]\n" \
  "v_pk_add_f32 v[48:49], v[24:25], v[102:103] neg_lo:[0,1] neg_hi:[0,1]\n" \
  "v_pk_add_f32 v[50:51], v[26:27], v[102:103] neg_lo:[0,1] neg_hi:[0,1]\n" \
  "v_cvt_pk_bf16_f32 v46, 0, v46\n" \
  "v_cvt_pk_bf16_f32 v47, 0, v47\n" \
  "v_cvt_pk_bf16_f32 v48, 0, v48\n" \
  "v_cvt_pk_bf16_f32 v49, 0, v49\n" \
  "v_cvt_pk_bf16_f32 v50, 0, v50\n" \
  "v_cvt_pk_bf16_f32 v51, 0, v51\n" \
  "v_pk_mul_f32 v[46:47], v[46:47], v[76:77]\n" \
  "v_pk_mul_f32 v[48:49], v[48:49], v[76:77]\n" \
  "v_pk_mul_f32 v[50:51], v[50:51], v[76:77]\n" \
  "v_cvt_pk_bf16_f32 v46, 0, v46\n" \
  "v_cvt_pk_bf16_f32 v47, 0, v47\n" \
  "v_cvt_pk_bf16_f32 v48, 0, v48\n" \
  "v_cvt_pk_bf16_f32 v49, 0, v49\n" \
  "v_cvt_pk_bf16_f32 v50, 0, v50\n" \
  "v_cvt_pk_bf16_f32 v51, 0, v51\n" \
  "v_pk_mul_f32 v[46:47], v[46:47], v[96:97] op_sel_hi:[1,0]\n" \
  "v_pk_mul_f32 v[48:49], v[48:49], v[98:99] op_sel_hi:[1,0]\n" \
  "v_pk_mul_f32 v[50:51], v[50:51], v[100:101] op_sel_hi:[1,0]\n" \
  "v_cvt_pk_bf16_f32 v46, 0, v46\n" \
  "v_cvt_pk_bf16_f32 v47, 0, v47\n" \
  "v_cvt_pk_bf16_f32 v48, 0, v48\n" \
  "v_cvt_pk_bf16_f32 v49, 0, v49\n" \
  "v_cvt_pk_bf16_f32 v50, 0, v50\n" \
  "v_cvt_pk_bf16_f32 v51, 0, v51\n" \
  "v_pk_add_f32 v[46:47], v[46:47], v[96:97] op_sel:[0,1]\n" \
  "v_pk_add_f32 v[48:49], v[48:49], v[98:99] op_sel:[0,1]\n" \
  "v_pk_add_f32 v[50:51], v[50:51], v[100:101] op_sel:[0,1]\n" \
  "v_cvt_pk_bf16_f32 v46, 0, v46\n" \
  "v_cvt_pk_bf16_f32 v47, 0, v47\n" \
  "v_cvt_pk_bf16_f32 v48, 0, v48\n" \
  "v_cvt_pk_bf16_f32 v49, 0, v49\n" \
  "v_cvt_pk_bf16_f32 v50, 0, v50\n" \
  "v_cvt_pk_bf16_f32 v51, 0, v51\n" \
  "v_cvt_pkrtz_f16_f32 v102, v47, v46\n" \
  "v_cvt_pkrtz_f16_f32 v103, v49, v48\n" \
  "v_cvt_pkrtz_f16_f32 v104, v51, v50\n" \
  "ds_write_b32 v105, v102 offset:2048\n" \
  "ds_write_b32 v105, v103 offset:10240\n" \
  "ds_write_b32 v105, v104 offset:18432\n" \
  "ds_add_u32 v111, v112 offset:4\n" \
  "v_mov_b32 v112, 0x100\n" \
  SSTAMP8 \
  "s_waitcnt vmcnt(0)\n" \
  "v_cmp_ne_u32 vcc, s44, v83\n" \
  "v_cmp_ne_u32 s[94:95], s44, v87\n" \
  "s_or_b64 vcc, vcc, s[94:95]\n" \
  "s_cbranch_scc1 Lu2s%=\n" \
  "Lu2b%=:\n" \
  "v_cmp_ne_u32 vcc, s44, v91\n" \
  "v_cmp_ne_u32 s[94:95], s44, v95\n" \
  "s_or_b64 vcc, vcc, s[94:95]\n" \
  "s_cbranch_scc1 Lu3s%=\n" \
  "Lu3b%=:\n" \
  "v_cmp_ne_u32 vcc, s44, v119\n" \
  "s_cmp_lg_u64 vcc, 0\n" \
  "s_cbranch_scc1 Las%=\n" \
  "Lab%=:\n" \
  "v_lshlrev_b32 v102, 16, v86\n" \
  "v_and_b32 v103, 0xffff0000, v86\n" \
  "v_lshlrev_b32 v28, 16, v80\n" \
  "v_lshlrev_b32 v29, 16, v81\n" \
  "v_and_b32 v30, 0xffff0000, v80\n" \
  "v_and_b32 v31, 0xffff0000, v81\n" \
  "v_lshlrev_b32 v32, 16, v82\n" \
  "v_and_b32 v33, 0xffff0000, v82\n" \
  "v_sub_f32 v52, v28, v102\n" \
  "v_sub_f32 v53, v29, v103\n" \
  "v_sub_f32 v54, v30, v102\n" \
  "v_sub_f32 v55, v31, v103\n" \
  "v_sub_f32 v56, v32, v102\n" \
  "v_sub_f32 v57, v33, v103\n" \
  "v_cvt_pk_bf16_f32 v52, 0, v52\n" \
  "v_cvt_pk_bf16_f32 v53, 0, v53\n" \
  "v_cvt_pk_bf16_f32 v54, 0, v54\n" \
  "v_cvt_pk_bf16_f32 v55, 0, v55\n" \
  "v_cvt_pk_bf16_f32 v56, 0, v56\n" \
  "v_cvt_pk_bf16_f32 v57, 0, v57\n" \
  "v_mul_f32 v52, v52, v84\n" \
  "v_mul_f32 v53, v53, v85\n" \
  "v_mul_f32 v54, v54, v84\n" \
  "v_mul_f32 v55, v55, v85\n" \
  "v_mul_f32 v56, v56, v84\n" \
  "v_mul_f32 v57, v57, v85\n" \
  "v_cvt_pk_bf16_f32 v52, 0, v52\n" \
  "v_cvt_pk_bf16_f32 v53, 0, v53\n" \
  "v_cvt_pk_bf16_f32 v54, 0, v54\n" \
  "v_cvt_pk_bf16_f32 v55, 0, v55\n" \
  "v_cvt_pk_bf16_f32 v56, 0, v56\n" \
  "v_cvt_pk_bf16_f32 v57, 0, v57\n" \
  "v_mul_f32 v52, v52, v96\n" \
  "v_mul_f32 v53, v53, v96\n" \
  "v_mul_f32 v54, v54, v98\n" \
  "v_mul_f32 v55, v55, v98\n" \
  "v_mul_f32 v56, v56, v100\n" \
  "v_mul_f32 v57, v57, v100\n" \
  "v_cvt_pk_bf16_f32 v52, 0, v52\n" \
  "v_cvt_pk_bf16_f32 v53, 0, v53\n" \
  "v_cvt_pk_bf16_f32 v54, 0, v54\n" \
  "v_cvt_pk_bf16_f32 v55, 0, v55\n" \
  "v_cvt_pk_bf16_f32 v56, 0, v56\n" \
  "v_cvt_pk_bf16_f32 v57, 0, v57\n" \
  "v_add_f32 v52, v52, v97\n" \
  "v_add_f32 v53, v53, v97\n" \
  "v_add_f32 v54, v54, v99\n" \
  "v_add_f32 v55, v55, v99\n" \
  "v_add_f32 v56, v56, v101\n" \
  "v_add_f32 v57, v57, v101\n" \
  "v_cvt_pk_bf16_f32 v52, 0, v52\n" \
  "v_cvt_pk_bf16_f32 v53, 0, v53\n" \
  "v_cvt_pk_bf16_f32 v54, 0, v54\n" \
  "v_cvt_pk_bf16_f32 v55, 0, v55\n" \
  "v_cvt_pk_bf16_f32 v56, 0, v56\n" \
  "v_cvt_pk_bf16_f32 v57, 0, v57\n" \
  "v_cvt_pkrtz_f16_f32 v102, v53, v52\n" \
  "v_cvt_pkrtz_f16_f32 v103, v55, v54\n" \
  "v_cvt_pkrtz_f16_f32 v104, v57, v56\n" \
  "ds_write_b32 v105, v102 offset:1024\n" \
  "ds_write_b32 v105, v103 offset:9216\n" \
  "ds_write_b32 v105, v104 offset:17408\n" \
  "v_lshlrev_b32 v102, 16, v94\n" \
  "v_and_b32 v103, 0xffff0000, v94\n" \
  "v_lshlrev_b32 v34, 16, v88\n" \
  "v_lshlrev_b32 v35, 16, v89\n" \
  "v_and_b32 v36, 0xffff0000, v88\n" \
  "v_and_b32 v37, 0xffff0000, v89\n" \
  "v_lshlrev_b32 v38, 16, v90\n" \
  "v_and_b32 v39, 0xffff0000, v90\n" \
  "v_sub_f32 v58, v34, v102\n" \
  "v_sub_f32 v59, v35, v103\n" \
  "v_sub_f32 v60, v36, v102\n" \
  "v_sub_f32 v61, v37, v103\n" \
  "v_sub_f32 v62, v38, v102\n" \
  "v_sub_f32 v63, v39, v103\n" \
  "v_cvt_pk_bf16_f32 v58, 0, v58\n" \
  "v_cvt_pk_bf16_f32 v59, 0, v59\n" \
  "v_cvt_pk_bf16_f32 v60, 0, v60\n" \
  "v_cvt_pk_bf16_f32 v61, 0, v61\n" \
  "v_cvt_pk_bf16_f32 v62, 0, v62\n" \
  "v_cvt_pk_bf16_f32 v63, 0, v63\n" \
  "v_mul_f32 v58, v58, v92\n" \
  "v_mul_f32 v59, v59, v93\n" \
  "v_mul_f32 v60, v60, v92\n" \
  "v_mul_f32 v61, v61, v93\n" \
  "v_mul_f32 v62, v62, v92\n" \
  "v_mul_f32 v63, v63, v93\n" \
  "v_cvt_pk_bf16_f32 v58, 0, v58\n" \
  "v_cvt_pk_bf16_f32 v59, 0, v59\n" \
  "v_cvt_pk_bf16_f32 v60, 0, v60\n" \
  "v_cvt_pk_bf16_f32 v61, 0, v61\n" \
  "v_cvt_pk_bf16_f32 v62, 0, v62\n" \
  "v_cvt_pk_bf16_f32 v63, 0, v63\n" \
  "v_mul_f32 v58, v58, v96\n" \
  "v_mul_f32 v59, v59, v96\n" \
  "v_mul_f32 v60, v60, v98\n" \
  "v_mul_f32 v61, v61, v98\n" \
  "v_mul_f32 v62, v62, v100\n" \
  "v_mul_f32 v63, v63, v100\n" \
  "v_cvt_pk_bf16_f32 v58, 0, v58\n" \
  "v_cvt_pk_bf16_f32 v59, 0, v59\n" \
  "v_cvt_pk_bf16_f32 v60, 0, v60\n" \
  "v_cvt_pk_bf16_f32 v61, 0, v61\n" \
  "v_cvt_pk_bf16_f32 v62, 0, v62\n" \
  "v_cvt_pk_bf16_f32 v63, 0, v63\n" \
  "v_add_f32 v58, v58, v97\n" \
  "v_add_f32 v59, v59, v97\n" \
  "v_add_f32 v60, v60, v99\n" \
  "v_add_f32 v61, v61, v99\n" \
  "v_add_f32 v62, v62, v101\n" \
  "v_add_f32 v63, v63, v101\n" \
  "v_cvt_pk_bf16_f32 v58, 0, v58\n" \
  "v_cvt_pk_bf16_f32 v59, 0, v59\n" \
  "v_cvt_pk_bf16_f32 v60, 0, v60\n" \
  "v_cvt_pk_bf16_f32 v61, 0, v61\n" \
  "v_cvt_pk_bf16_f32 v62, 0, v62\n" \
  "v_cvt_pk_bf16_f32 v63, 0, v63\n" \
  "v_cvt_pkrtz_f16_f32 v102, v59, v58\n" \
  "v_cvt_pkrtz_f16_f32 v103, v61, v60\n" \
  "v_cvt_pkrtz_f16_f32 v104, v63, v62\n" \
  "ds_write_b32 v105, v102 offset:0\n" \
  "ds_write_b32 v105, v103 offset:8192\n" \
  "ds_write_b32 v105, v104 offset:16384\n" \
  "ds_add_u32 v111, v112 offset:4\n" \
  SSTAMP9 \
  "s_setprio " STR(PRE_) "\n" \
  "v_mul_f32 v118, v118, v110\n" \
  "v_mov_b32 v90, 158224\n" \
  "v_mov_b32 v85, s44\n" \
  "v_add_f32_dpp v118, v118, v118 quad_perm:[1,0,3,2] row_mask:0xf bank_mask:0xf bound_ctrl:1\n" \
  "s_nop 1\n" \
  "v_add_f32_dpp v118, v118, v118 quad_perm:[2,3,0,1] row_mask:0xf bank_mask:0xf bound_ctrl:1\n" \
  "v_cvt_pk_bf16_f32 v118, 0, v118\n" \
  "s_nop 0\n" \
  "v_readlane_b32 s94, v118, 0\n" \
  "v_readlane_b32 s95, v118, 8\n" \
  "v_readlane_b32 s96, v118, 16\n" \
  "v_mov_b32 v77, s94\n" \
  "v_mov_b32 v79, s95\n" \
  "v_mov_b32 v81, s96\n" \
  "ds_read_b32 v86, v111\n" \
  "ds_read_b64 v[64:65], v115 offset:0\n" \
  "ds_read_b64 v[66:67], v115 offset:32096\n" \
  "ds_read_b64 v[68:69], v116 offset:0\n" \
  "ds_read_b128 v[92:95], v90\n" \
  "v_mul_f32 v40, s84, v40\n" \
  "v_mul_f32 v41, s84, v41\n" \
  "v_mul_f32 v42, s85, v42\n" \
  "v_mul_f32 v43, s85, v43\n" \
  "v_mul_f32 v44, s86, v44\n" \
  "v_mul_f32 v45, s86, v45\n" \
  "v_cvt_pk_bf16_f32 v40, 0, v40\n" \
  "v_cvt_pk_bf16_f32 v41, 0, v41\n" \
  "v_cvt_pk_bf16_f32 v42, 0, v42\n" \
  "v_cvt_pk_bf16_f32 v43, 0, v43\n" \
  "v_cvt_pk_bf16_f32 v44, 0, v44\n" \
  "v_cvt_pk_bf16_f32 v45, 0, v45\n" \
  "s_waitcnt lgkmcnt(0)\n" \
  "v_readfirstlane_b32 s94, v86\n" \
  "s_lshr_b32 s94, s94, s45\n" \
  "s_bfe_u32 s94, s94, 0x40000\n" \
  "s_cmp_lg_u32 s94, 3\n" \
  "s_cbranch_scc1 Le0s%=\n" \
  "Le0b%=:\n" \
  "v_mul_f32 v76, s87, v92\n" \
  "v_mul_f32 v78, s88, v93\n" \
  "v_mul_f32 v80, s89, v94\n" \
  "v_mul_f32 v64, v64, v76\n" \
  "v_mul_f32 v65, v65, v76\n" \
  "v_mul_f32 v66, v66, v78\n" \
  "v_mul_f32 v67, v67, v78\n" \
  "v_mul_f32 v68, v68, v80\n" \
  "v_mul_f32 v69, v69, v80\n" \
  "v_cvt_pk_bf16_f32 v64, 0, v64\n" \
  "v_cvt_pk_bf16_f32 v65, 0, v65\n" \
  "v_cvt_pk_bf16_f32 v66, 0, v66\n" \
  "v_cvt_pk_bf16_f32 v67, 0, v67\n" \
  "v_cvt_pk_bf16_f32 v68, 0, v68\n" \
  "v_cvt_pk_bf16_f32 v69, 0, v69\n" \
  "v_add_f32 v64, v64, v40\n" \
  "v_add_f32 v65, v65, v41\n" \
  "v_add_f32 v66, v66, v42\n" \
  "v_add_f32 v67, v67, v43\n" \
  "v_add_f32 v68, v68, v44\n" \
  "v_add_f32 v69, v69, v45\n" \
  "v_cvt_pk_bf16_f32 v64, 0, v64\n" \
  "v_cvt_pk_bf16_f32 v65, 0, v65\n" \
  "v_cvt_pk_bf16_f32 v66, 0, v66\n" \
  "v_cvt_pk_bf16_f32 v67, 0, v67\n" \
  "v_cvt_pk_bf16_f32 v68, 0, v68\n" \
  "v_cvt_pk_bf16_f32 v69, 0, v69\n" \
  "v_mul_f32 v64, v64, v77\n" \
  "v_mul_f32 v65, v65, v77\n" \
  "v_mul_f32 v66, v66, v79\n" \
  "v_mul_f32 v67, v67, v79\n" \
  "v_mul_f32 v68, v68, v81\n" \
  "v_mul_f32 v69, v69, v81\n" \
  "v_cvt_pk_bf16_f32 v64, 0, v64\n" \
  "v_cvt_pk_bf16_f32 v65, 0, v65\n" \
  "v_cvt_pk_bf16_f32 v66, 0, v66\n" \
  "v_cvt_pk_bf16_f32 v67, 0, v67\n" \
  "v_cvt_pk_bf16_f32 v68, 0, v68\n" \
  "v_cvt_pk_bf16_f32 v69, 0, v69\n" \
  "v_add_f32 v64, v64, v16\n" \
  "v_add_f32 v65, v65, v17\n" \
  "v_add_f32 v66, v66, v18\n" \
  "v_add_f32 v67, v67, v19\n" \
  "v_add_f32 v68, v68, v20\n" \
  "v_add_f32 v69, v69, v21\n" \
  "v_cvt_pk_bf16_f32 v82, v64, v66\n" \
  "v_cvt_pk_bf16_f32 v83, v65, v67\n" \
  "v_cvt_pk_bf16_f32 v84, v68, v69\n" \
  "s_movk_i32 s94, 0\n" \
  "buffer_store_dwordx4 v[82:85], v117, s[40:43], s94 offen sc1\n" \
  "ds_read_b32 v86, v111\n" \
  "ds_read_b64 v[64:65], v115 offset:2560\n" \
  "ds_read_b64 v[70:71], v115 offset:10240\n" \
  "ds_read_b64 v[66:67], v115 offset:34656\n" \
  "ds_read_b64 v[72:73], v115 offset:42336\n" \
  "ds_read_b64 v[68:69], v116 offset:2560\n" \
  "ds_read_b64 v[74:75], v116 offset:10240\n" \
  "v_mul_f32 v46, s84, v46\n" \
  "v_mul_f32 v47, s84, v47\n" \
  "v_mul_f32 v48, s85, v48\n" \
  "v_mul_f32 v49, s85, v49\n" \
  "v_mul_f32 v50, s86, v50\n" \
  "v_mul_f32 v51, s86, v51\n" \
  "v_cvt_pk_bf16_f32 v46, 0, v46\n" \
  "v_cvt_pk_bf16_f32 v47, 0, v47\n" \
  "v_cvt_pk_bf16_f32 v48, 0, v48\n" \
  "v_cvt_pk_bf16_f32 v49, 0, v49\n" \
  "v_cvt_pk_bf16_f32 v50, 0, v50\n" \
  "v_cvt_pk_bf16_f32 v51, 0, v51\n" \
  "s_waitcnt lgkmcnt(0)\n" \
  "v_readfirstlane_b32 s94, v86\n" \
  "s_lshr_b32 s94, s94, s45\n" \
  "s_bfe_u32 s94, s94, 0x40008\n" \
  "s_cmp_lg_u32 s94, 6\n" \
  "s_cbranch_scc1 Le1s%=\n" \
  "Le1b%=:\n" \
  "v_add_f32 v64, v64, v70\n" \
  "v_add_f32 v65, v65, v71\n" \
  "v_add_f32 v66, v66, v72\n" \
  "v_add_f32 v67, v67, v73\n" \
  "v_add_f32 v68, v68, v74\n" \
  "v_add_f32 v69, v69, v75\n" \
  "v_mul_f32 v64, v64, v76\n" \
  "v_mul_f32 v65, v65, v76\n" \
  "v_mul_f32 v66, v66, v78\n" \
  "v_mul_f32 v67, v67, v78\n" \
  "v_mul_f32 v68, v68, v80\n" \
  "v_mul_f32 v69, v69, v80\n" \
  "v_cvt_pk_bf16_f32 v64, 0, v64\n" \
  "v_cvt_pk_bf16_f32 v65, 0, v65\n" \
  "v_cvt_pk_bf16_f32 v66, 0, v66\n" \
  "v_cvt_pk_bf16_f32 v67, 0, v67\n" \
  "v_cvt_pk_bf16_f32 v68, 0, v68\n" \
  "v_cvt_pk_bf16_f32 v69, 0, v69\n" \
  "v_add_f32 v64, v64, v46\n" \
  "v_add_f32 v65, v65, v47\n" \
  "v_add_f32 v66, v66, v48\n" \
  "v_add_f32 v67, v67, v49\n" \
  "v_add_f32 v68, v68, v50\n" \
  "v_add_f32 v69, v69, v51\n" \
  "v_cvt_pk_bf16_f32 v64, 0, v64\n" \
  "v_cvt_pk_bf16_f32 v65, 0, v65\n" \
  "v_cvt_pk_bf16_f32 v66, 0, v66\n" \
  "v_cvt_pk_bf16_f32 v67, 0, v67\n" \
  "v_cvt_pk_bf16_f32 v68, 0, v68\n" \
  "v_cvt_pk_bf16_f32 v69, 0, v69\n" \
  "v_mul_f32 v64, v64, v77\n" \
  "v_mul_f32 v65, v65, v77\n" \
  "v_mul_f32 v66, v66, v79\n" \
  "v_mul_f32 v67, v67, v79\n" \
  "v_mul_f32 v68, v68, v81\n" \
  "v_mul_f32 v69, v69, v81\n" \
  "v_cvt_pk_bf16_f32 v64, 0, v64\n" \
  "v_cvt_pk_bf16_f32 v65, 0, v65\n" \
  "v_cvt_pk_bf16_f32 v66, 0, v66\n" \
  "v_cvt_pk_bf16_f32 v67, 0, v67\n" \
  "v_cvt_pk_bf16_f32 v68, 0, v68\n" \
  "v_cvt_pk_bf16_f32 v69, 0, v69\n" \
  "v_add_f32 v64, v64, v22\n" \
  "v_add_f32 v65, v65, v23\n" \
  "v_add_f32 v66, v66, v24\n" \
  "v_add_f32 v67, v67, v25\n" \
  "v_add_f32 v68, v68, v26\n" \
  "v_add_f32 v69, v69, v27\n" \
  "v_cvt_pk_bf16_f32 v82, v64, v66\n" \
  "v_cvt_pk_bf16_f32 v83, v65, v67\n" \
  "v_cvt_pk_bf16_f32 v84, v68, v69\n" \
  "s_movk_i32 s94, 4096\n" \
  "buffer_store_dwordx4 v[82:85], v117, s[40:43], s94 offen sc1\n" \
  SSTAMPE1 \
  "ds_read_b32 v86, v111\n" \
  "ds_read_b64 v[64:65], v115 offset:5120\n" \
  "ds_read_b64 v[70:71], v115 offset:12800\n" \
  "ds_read_b64 v[66:67], v115 offset:37216\n" \
  "ds_read_b64 v[72:73], v115 offset:44896\n" \
  "ds_read_b64 v[68:69], v116 offset:5120\n" \
  "ds_read_b64 v[74:75], v116 offset:12800\n" \
  "v_mul_f32 v52, s84, v52\n" \
  "v_mul_f32 v53, s84, v53\n" \
  "v_mul_f32 v54, s85, v54\n" \
  "v_mul_f32 v55, s85, v55\n" \
  "v_mul_f32 v56, s86, v56\n" \
  "v_mul_f32 v57, s86, v57\n" \
  "v_cvt_pk_bf16_f32 v52, 0, v52\n" \
  "v_cvt_pk_bf16_f32 v53, 0, v53\n" \
  "v_cvt_pk_bf16_f32 v54, 0, v54\n" \
  "v_cvt_pk_bf16_f32 v55, 0, v55\n" \
  "v_cvt_pk_bf16_f32 v56, 0, v56\n" \
  "v_cvt_pk_bf16_f32 v57, 0, v57\n" \
  "s_waitcnt lgkmcnt(0)\n" \
  "v_readfirstlane_b32 s94, v86\n" \
  "s_lshr_b32 s94, s94, s45\n" \
  "s_bfe_u32 s94, s94, 0x40010\n" \
  "s_cmp_lg_u32 s94, 6\n" \
  "s_cbranch_scc1 Le2s%=\n" \
  "Le2b%=:\n" \
  "v_add_f32 v64, v64, v70\n" \
  "v_add_f32 v65, v65, v71\n" \
  "v_add_f32 v66, v66, v72\n" \
  "v_add_f32 v67, v67, v73\n" \
  "v_add_f32 v68, v68, v74\n" \
  "v_add_f32 v69, v69, v75\n" \
  "v_mul_f32 v64, v64, v76\n" \
  "v_mul_f32 v65, v65, v76\n" \
  "v_mul_f32 v66, v66, v78\n" \
  "v_mul_f32 v67, v67, v78\n" \
  "v_mul_f32 v68, v68, v80\n" \
  "v_mul_f32 v69, v69, v80\n" \
  "v_cvt_pk_bf16_f32 v64, 0, v64\n" \
  "v_cvt_pk_bf16_f32 v65, 0, v65\n" \
  "v_cvt_pk_bf16_f32 v66, 0, v66\n" \
  "v_cvt_pk_bf16_f32 v67, 0, v67\n" \
  "v_cvt_pk_bf16_f32 v68, 0, v68\n" \
  "v_cvt_pk_bf16_f32 v69, 0, v69\n" \
  "v_add_f32 v64, v64, v52\n" \
  "v_add_f32 v65, v65, v53\n" \
  "v_add_f32 v66, v66, v54\n" \
  "v_add_f32 v67, v67, v55\n" \
  "v_add_f32 v68, v68, v56\n" \
  "v_add_f32 v69, v69, v57\n" \
  "v_cvt_pk_bf16_f32 v64, 0, v64\n" \
  "v_cvt_pk_bf16_f32 v65, 0, v65\n" \
  "v_cvt_pk_bf16_f32 v66, 0, v66\n" \
  "v_cvt_pk_bf16_f32 v67, 0, v67\n" \
  "v_cvt_pk_bf16_f32 v68, 0, v68\n" \
  "v_cvt_pk_bf16_f32 v69, 0, v69\n" \
  "v_mul_f32 v64, v64, v77\n" \
  "v_mul_f32 v65, v65, v77\n" \
  "v_mul_f32 v66, v66, v79\n" \
  "v_mul_f32 v67, v67, v79\n" \
  "v_mul_f32 v68, v68, v81\n" \
  "v_mul_f32 v69, v69, v81\n" \
  "v_cvt_pk_bf16_f32 v64, 0, v64\n" \
  "v_cvt_pk_bf16_f32 v65, 0, v65\n" \
  "v_cvt_pk_bf16_f32 v66, 0, v66\n" \
  "v_cvt_pk_bf16_f32 v67, 0, v67\n" \
  "v_cvt_pk_bf16_f32 v68, 0, v68\n" \
  "v_cvt_pk_bf16_f32 v69, 0, v69\n" \
  "v_add_f32 v64, v64, v28\n" \
  "v_add_f32 v65, v65, v29\n" \
  "v_add_f32 v66, v66, v30\n" \
  "v_add_f32 v67, v67, v31\n" \
  "v_add_f32 v68, v68, v32\n" \
  "v_add_f32 v69, v69, v33\n" \
  "v_cvt_pk_bf16_f32 v82, v64, v66\n" \
  "v_cvt_pk_bf16_f32 v83, v65, v67\n" \
  "v_cvt_pk_bf16_f32 v84, v68, v69\n" \
  "s_movk_i32 s94, 8192\n" \
  "buffer_store_dwordx4 v[82:85], v117, s[40:43], s94 offen sc1\n" \
  "ds_read_b32 v86, v111\n" \
  "ds_read_b64 v[64:65], v115 offset:7680\n" \
  "ds_read_b64 v[70:71], v115 offset:15360\n" \
  "ds_read_b64 v[66:67], v115 offset:39776\n" \
  "ds_read_b64 v[72:73], v115 offset:47456\n" \
  "ds_read_b64 v[68:69], v116 offset:7680\n" \
  "ds_read_b64 v[74:75], v116 offset:15360\n" \
  "v_mul_f32 v58, s84, v58\n" \
  "v_mul_f32 v59, s84, v59\n" \
  "v_mul_f32 v60, s85, v60\n" \
  "v_mul_f32 v61, s85, v61\n" \
  "v_mul_f32 v62, s86, v62\n" \
  "v_mul_f32 v63, s86, v63\n" \
  "v_cvt_pk_bf16_f32 v58, 0, v58\n" \
  "v_cvt_pk_bf16_f32 v59, 0, v59\n" \
  "v_cvt_pk_bf16_f32 v60, 0, v60\n" \
  "v_cvt_pk_bf16_f32 v61, 0, v61\n" \
  "v_cvt_pk_bf16_f32 v62, 0, v62\n" \
  "v_cvt_pk_bf16_f32 v63, 0, v63\n" \
  "s_waitcnt lgkmcnt(0)\n" \
  "v_readfirstlane_b32 s94, v86\n" \
  "s_lshr_b32 s94, s94, s45\n" \
  "s_bfe_u32 s94, s94, 0x40018\n" \
  "s_cmp_lg_u32 s94, 6\n" \
  "s_cbranch_scc1 Le3s%=\n" \
  "Le3b%=:\n" \
  SSTAMP10 \
  "v_pk_add_f32 v[64:65], v[64:65], v[70:71]\n" \
  "v_pk_add_f32 v[66:67], v[66:67], v[72:73]\n" \
  "v_pk_add_f32 v[68:69], v[68:69], v[74:75]\n" \
  "v_pk_mul_f32 v[64:65], v[64:65], v[76:77] op_sel_hi:[1,0]\n" \
  "v_pk_mul_f32 v[66:67], v[66:67], v[78:79] op_sel_hi:[1,0]\n" \
  "v_pk_mul_f32 v[68:69], v[68:69], v[80:81] op_sel_hi:[1,0]\n" \
  "v_cvt_pk_bf16_f32 v64, 0, v64\n" \
  "v_cvt_pk_bf16_f32 v65, 0, v65\n" \
  "v_cvt_pk_bf16_f32 v66, 0, v66\n" \
  "v_cvt_pk_bf16_f32 v67, 0, v67\n" \
  "v_cvt_pk_bf16_f32 v68, 0, v68\n" \
  "v_cvt_pk_bf16_f32 v69, 0, v69\n" \
  "v_pk_add_f32 v[64:65], v[64:65], v[58:59]\n" \
  "v_pk_add_f32 v[66:67], v[66:67], v[60:61]\n" \
  "v_pk_add_f32 v[68:69], v[68:69], v[62:63]\n" \
  "v_cvt_pk_bf16_f32 v64, 0, v64\n" \
  "v_cvt_pk_bf16_f32 v65, 0, v65\n" \
  "v_cvt_pk_bf16_f32 v66, 0, v66\n" \
  "v_cvt_pk_bf16_f32 v67, 0, v67\n" \
  "v_cvt_pk_bf16_f32 v68, 0, v68\n" \
  "v_cvt_pk_bf16_f32 v69, 0, v69\n" \
  "v_pk_mul_f32 v[64:65], v[64:65], v[76:77] op_sel:[0,1]\n" \
  "v_pk_mul_f32 v[66:67], v[66:67], v[78:79] op_sel:[0,1]\n" \
  "v_pk_mul_f32 v[68:69], v[68:69], v[80:81] op_sel:[0,1]\n" \
  "v_cvt_pk_bf16_f32 v64, 0, v64\n" \
  "v_cvt_pk_bf16_f32 v65, 0, v65\n" \
  "v_cvt_pk_bf16_f32 v66, 0, v66\n" \
  "v_cvt_pk_bf16_f32 v67, 0, v67\n" \
  "v_cvt_pk_bf16_f32 v68, 0, v68\n" \
  "v_cvt_pk_bf16_f32 v69, 0, v69\n" \
  "v_pk_add_f32 v[64:65], v[64:65], v[34:35]\n" \
  "v_pk_add_f32 v[66:67], v[66:67], v[36:37]\n" \
  "v_pk_add_f32 v[68:69], v[68:69], v[38:39]\n" \
  "v_cvt_pk_bf16_f32 v82, v64, v66\n" \
  "v_cvt_pk_bf16_f32 v83, v65, v67\n" \
  "v_cvt_pk_bf16_f32 v84, v68, v69\n" \
  "s_movk_i32 s94, 12288\n" \
  "buffer_store_dwordx4 v[82:85], v117, s[40:43], s94 offen sc1\n" \
  "s_branch Lhe%=\n" \
  "Lms%=:\n" \
  "s_mov_b32 s97, 0x7fff\n" \
  "Lmr%=:\n" \
  SSTAMPCm \
  "s_mov_b64 exec, vcc\n" \
  "buffer_load_dwordx2 v[102:103], v108, s[40:43], 0 offen sc1\n" \
  "s_mov_b64 exec, -1\n" \
  "s_waitcnt vmcnt(0)\n" \
  "v_cmp_ne_u32 vcc, s44, v103\n" \
  "s_cmp_lg_u64 vcc, 0\n" \
  "s_cbranch_scc0 Lmb%=\n" \
  "s_sub_u32 s97, s97, 1\n" \
  "s_cbranch_scc0 Lmr%=\n" \
  "s_branch Lmb%=\n" \
  "Lu0s%=:\n" \
  "s_mov_b32 s97, 0x7fff\n" \
  "Lu0r%=:\n" \
  SSTAMPCu0 \
  "s_mov_b64 exec, vcc\n" \
  "s_mov_b32 s94, 0\n" \
  "s_mov_b32 s95, 0\n" \
  "buffer_load_dwordx4 v[64:67], v106, s[40:43], s94 offen sc1\n" \
  "buffer_load_dwordx4 v[68:71], v107, s[40:43], s95 offen sc1\n" \
  "s_mov_b64 exec, -1\n" \
  "s_waitcnt vmcnt(0)\n" \
  "v_cmp_ne_u32 vcc, s44, v67\n" \
  "v_cmp_ne_u32 s[94:95], s44, v71\n" \
  "s_or_b64 vcc, vcc, s[94:95]\n" \
  "s_cbranch_scc0 Lu0b%=\n" \
  "s_sub_u32 s97, s97, 1\n" \
  "s_cbranch_scc0 Lu0r%=\n" \
  "s_branch Lu0b%=\n" \
  "Lu1s%=:\n" \
  "s_mov_b32 s97, 0x7fff\n" \
  "Lu1r%=:\n" \
  SSTAMPCu1 \
  "s_mov_b64 exec, vcc\n" \
  "s_mov_b32 s94, 790528\n" \
  "s_mov_b32 s95, 4096\n" \
  "buffer_load_dwordx4 v[72:75], v106, s[40:43], s94 offen sc1\n" \
  "buffer_load_dwordx4 v[76:79], v107, s[40:43], s95 offen sc1\n" \
  "s_mov_b64 exec, -1\n" \
  "s_waitcnt vmcnt(0)\n" \
  "v_cmp_ne_u32 vcc, s44, v75\n" \
  "v_cmp_ne_u32 s[94:95], s44, v79\n" \
  "s_or_b64 vcc, vcc, s[94:95]\n" \
  "s_cbranch_scc0 Lu1b%=\n" \
  "s_sub_u32 s97, s97, 1\n" \
  "s_cbranch_scc0 Lu1r%=\n" \
  "s_branch Lu1b%=\n" \
  "Lu2s%=:\n" \
  "s_mov_b32 s97, 0x7fff\n" \
  "Lu2r%=:\n" \
  SSTAMPCu2 \
  "s_mov_b64 exec, vcc\n" \
  "s_mov_b32 s94, 1581056\n" \
  "s_mov_b32 s95, 8192\n" \
  "buffer_load_dwordx4 v[80:83], v106, s[40:43], s94 offen sc1\n" \
  "buffer_load_dwordx4 v[84:87], v107, s[40:43], s95 offen sc1\n" \
  "s_mov_b64 exec, -1\n" \
  "s_waitcnt vmcnt(0)\n" \
  "v_cmp_ne_u32 vcc, s44, v83\n" \
  "v_cmp_ne_u32 s[94:95], s44, v87\n" \
  "s_or_b64 vcc, vcc, s[94:95]\n" \
  "s_cbranch_scc0 Lu2b%=\n" \
  "s_sub_u32 s97, s97, 1\n" \
  "s_cbranch_scc0 Lu2r%=\n" \
  "s_branch Lu2b%=\n" \
  "Lu3s%=:\n" \
  "s_mov_b32 s97, 0x7fff\n" \
  "Lu3r%=:\n" \
  SSTAMPCu3 \
  "s_mov_b64 exec, vcc\n" \
  "s_mov_b32 s94, 2371584\n" \
  "s_mov_b32 s95, 12288\n" \
  "buffer_load_dwordx4 v[88:91], v106, s[40:43], s94 offen sc1\n" \
  "buffer_load_dwordx4 v[92:95], v107, s[40:43], s95 offen sc1\n" \
  "s_mov_b64 exec, -1\n" \
  "s_waitcnt vmcnt(0)\n" \
  "v_cmp_ne_u32 vcc, s44, v91\n" \
  "v_cmp_ne_u32 s[94:95], s44, v95\n" \
  "s_or_b64 vcc, vcc, s[94:95]\n" \
  "s_cbranch_scc0 Lu3b%=\n" \
  "s_sub_u32 s97, s97, 1\n" \
  "s_cbranch_scc0 Lu3r%=\n" \
  "s_branch Lu3b%=\n" \
  "Las%=:\n" \
  "s_mov_b32 s97, 0x7fff\n" \
  "Lar%=:\n" \
  SSTAMPCa \
  "s_mov_b64 exec, vcc\n" \
  "buffer_load_dwordx2 v[118:119], v114, s[40:43], 0 offen sc1\n" \
  "s_mov_b64 exec, -1\n" \
  "s_waitcnt vmcnt(0)\n" \
  "v_cmp_ne_u32 vcc, s44, v119\n" \
  "s_cmp_lg_u64 vcc, 0\n" \
  "s_cbranch_scc0 Lab%=\n" \
  "s_sub_u32 s97, s97, 1\n" \
  "s_cbranch_scc0 Lar%=\n" \
  "s_branch Lab%=\n" \
  "Le0s%=:\n" \
  "s_mov_b32 s97, 0x1fffff\n" \
  "Le0r%=:\n" \
  "s_sleep 1\n" \
  "ds_read_b32 v86, v111\n" \
  "s_waitcnt lgkmcnt(0)\n" \
  "v_readfirstlane_b32 s94, v86\n" \
  "s_lshr_b32 s94, s94, s45\n" \
  "s_bfe_u32 s94, s94, 0x40000\n" \
  "s_cmp_lg_u32 s94, 3\n" \
  "s_cbranch_scc0 Le0x%=\n" \
  "s_sub_u32 s97, s97, 1\n" \
  "s_cbranch_scc0 Le0r%=\n" \
  "Le0x%=:\n" \
  "ds_read_b64 v[64:65], v115 offset:0\n" \
  "ds_read_b64 v[66:67], v115 offset:32096\n" \
  "ds_read_b64 v[68:69], v116 offset:0\n" \
  "ds_read_b128 v[92:95], v90\n" \
  "s_waitcnt lgkmcnt(0)\n" \
  "s_branch Le0b%=\n" \
  "Le1s%=:\n" \
  "s_mov_b32 s97, 0x1fffff\n" \
  "Le1r%=:\n" \
  "s_sleep 1\n" \
  "ds_read_b32 v86, v111\n" \
  "s_waitcnt lgkmcnt(0)\n" \
  "v_readfirstlane_b32 s94, v86\n" \
  "s_lshr_b32 s94, s94, s45\n" \
  "s_bfe_u32 s94, s94, 0x40008\n" \
  "s_cmp_lg_u32 s94, 6\n" \
  "s_cbranch_scc0 Le1x%=\n" \
  "s_sub_u32 s97, s97, 1\n" \
  "s_cbranch_scc0 Le1r%=\n" \
  "Le1x%=:\n" \
  "ds_read_b64 v[64:65], v115 offset:2560\n" \
  "ds_read_b64 v[70:71], v115 offset:10240\n" \
  "ds_read_b64 v[66:67], v115 offset:34656\n" \
  "ds_read_b64 v[72:73], v115 offset:42336\n" \
  "ds_read_b64 v[68:69], v116 offset:2560\n" \
  "ds_read_b64 v[74:75], v116 offset:10240\n" \
  "s_waitcnt lgkmcnt(0)\n" \
  "s_branch Le1b%=\n" \
  "Le2s%=:\n" \
  "s_mov_b32 s97, 0x1fffff\n" \
  "Le2r%=:\n" \
  "s_sleep 1\n" \
  "ds_read_b32 v86, v111\n" \
  "s_waitcnt lgkmcnt(0)\n" \
  "v_readfirstlane_b32 s94, v86\n" \
  "s_lshr_b32 s94, s94, s45\n" \
  "s_bfe_u32 s94, s94, 0x40010\n" \
  "s_cmp_lg_u32 s94, 6\n" \
  "s_cbranch_scc0 Le2x%=\n" \
  "s_sub_u32 s97, s97, 1\n" \
  "s_cbranch_scc0 Le2r%=\n" \
  "Le2x%=:\n" \
  "ds_read_b64 v[64:65], v115 offset:5120\n" \
  "ds_read_b64 v[70:71], v115 offset:12800\n" \
  "ds_read_b64 v[66:67], v115 offset:37216\n" \
  "ds_read_b64 v[72:73], v115 offset:44896\n" \
  "ds_read_b64 v[68:69], v116 offset:5120\n" \
  "ds_read_b64 v[74:75], v116 offset:12800\n" \
  "s_waitcnt lgkmcnt(0)\n" \
  "s_branch Le2b%=\n" \
  "Le3s%=:\n" \
  "s_mov_b32 s97, 0x1fffff\n" \
  "Le3r%=:\n" \
  "ds_read_b32 v86, v111\n" \
  "ds_read_b64 v[64:65], v115 offset:7680\n" \
  "ds_read_b64 v[70:71], v115 offset:15360\n" \
  "ds_read_b64 v[66:67], v115 offset:39776\n" \
  "ds_read_b64 v[72:73], v115 offset:47456\n" \
  "ds_read_b64 v[68:69], v116 offset:7680\n" \
  "ds_read_b64 v[74:75], v116 offset:15360\n" \
  "s_waitcnt lgkmcnt(0)\n" \
  "v_readfirstlane_b32 s94, v86\n" \
  "s_lshr_b32 s94, s94, s45\n" \
  "s_bfe_u32 s94, s94, 0x40018\n" \
  "s_cmp_lg_u32 s94, 6\n" \
  "s_cbranch_scc0 Le3b%=\n" \
  "s_sub_u32 s97, s97, 1\n" \
  "s_cbranch_scc0 Le3r%=\n" \
  "s_branch Le3b%=\n" \
  "Lhe%=:\n" \
  SSTAMP11 \
  "s_setprio 0\n"

#define LW_ASM \
  "s_load_dwordx2 s[40:41], %[k], 0x70\n" \
  "s_load_dwordx2 s[46:47], %[k], 0x38\n" \
  "s_load_dwordx2 s[48:49], %[k], 0x68\n" \
  "v_mov_b32 v1, 158208\n" \
  "ds_read_b32 v2, v1\n" \
  "s_mov_b32 s42, 10668032\n" \
  "s_mov_b32 s43, 0x20000\n" \
  "v_and_b32 v3, 7, %[t]\n" \
  "v_and_b32 v4, 63, %[t]\n" \
  "s_waitcnt lgkmcnt(0)\n" \
  "v_readfirstlane_b32 s44, v2\n" \
  "s_cmp_ge_u32 %[w], 12\n" \
  "s_cbranch_scc1 Mls%=\n" \
  "s_lshr_b32 s50, 0x214, %[w]\n" \
  "s_bitcmp0_b32 s50, 0\n" \
  "s_cbranch_scc1 Mla%=\n" \
  "s_sleep " STR(PSLP) "\n" \
  "s_lshr_b32 s50, %[w], 2\n" \
  "s_mul_i32 s51, s50, 96\n" \
  "v_add_u32 v5, s51, v4\n" \
  "v_mov_b32 v92, 0\n" \
  "s_mov_b32 exec_hi, 0xffff\n" \
  "v_lshlrev_b32 v6, 4, v5\n" \
  "v_lshlrev_b32 v7, 2, v5\n" \
  "s_waitcnt vmcnt(" STR(PW2) ")\n" \
  "s_setprio " STR(PRP) "\n" \
  "global_load_dword v90, v7, s[46:47]\n" \
  "global_load_dword v93, v7, s[46:47] offset:192\n" \
  "s_mov_b32 s51, 17920\n" \
  "buffer_load_dwordx4 v[12:15], v6, s[40:43], s51 offen sc1\n" \
  "s_mov_b32 s51, 45568\n" \
  "buffer_load_dwordx4 v[16:19], v6, s[40:43], s51 offen sc1\n" \
  "s_mov_b32 s51, 73216\n" \
  "buffer_load_dwordx4 v[20:23], v6, s[40:43], s51 offen sc1\n" \
  "s_mov_b32 s51, 22528\n" \
  "buffer_load_dwordx4 v[24:27], v6, s[40:43], s51 offen sc1\n" \
  "s_mov_b32 s51, 50176\n" \
  "buffer_load_dwordx4 v[28:31], v6, s[40:43], s51 offen sc1\n" \
  "s_mov_b32 s51, 77824\n" \
  "buffer_load_dwordx4 v[32:35], v6, s[40:43], s51 offen sc1\n" \
  "s_mov_b32 s51, 27136\n" \
  "buffer_load_dwordx4 v[36:39], v6, s[40:43], s51 offen sc1\n" \
  "s_mov_b32 s51, 54784\n" \
  "buffer_load_dwordx4 v[40:43], v6, s[40:43], s51 offen sc1\n" \
  "s_mov_b32 s51, 82432\n" \
  "buffer_load_dwordx4 v[44:47], v6, s[40:43], s51 offen sc1\n" \
  "s_mov_b32 s51, 17920\n" \
  "buffer_load_dwordx4 v[48:51], v6, s[40:43], s51 offen offset:768 sc1\n" \
  "s_mov_b32 s51, 45568\n" \
  "buffer_load_dwordx4 v[52:55], v6, s[40:43], s51 offen offset:768 sc1\n" \
  "s_mov_b32 s51, 73216\n" \
  "buffer_load_dwordx4 v[56:59], v6, s[40:43], s51 offen offset:768 sc1\n" \
  "s_mov_b32 s51, 22528\n" \
  "buffer_load_dwordx4 v[60:63], v6, s[40:43], s51 offen offset:768 sc1\n" \
  "s_mov_b32 s51, 50176\n" \
  "buffer_load_dwordx4 v[64:67], v6, s[40:43], s51 offen offset:768 sc1\n" \
  "s_mov_b32 s51, 77824\n" \
  "buffer_load_dwordx4 v[68:71], v6, s[40:43], s51 offen offset:768 sc1\n" \
  "s_mov_b32 s51, 27136\n" \
  "buffer_load_dwordx4 v[72:75], v6, s[40:43], s51 offen offset:768 sc1\n" \
  "s_mov_b32 s51, 54784\n" \
  "buffer_load_dwordx4 v[76:79], v6, s[40:43], s51 offen offset:768 sc1\n" \
  "s_mov_b32 s51, 82432\n" \
  "buffer_load_dwordx4 v[80:83], v6, s[40:43], s51 offen offset:768 sc1\n" \
  "s_mov_b64 exec, -1\n" \
  "s_setprio 0\n" \
  "Mla%=:\n" \
  "v_lshrrev_b32 v95, 3, %[t]\n" \
  "v_mul_u32_u24 v94, 33024, v95\n" \
  "v_lshl_add_u32 v94, v3, 4, v94\n" \
  "s_lshl_b32 s58, %[b], 7\n" \
  "s_add_u32 s58, s58, 139264\n" \
  "s_add_u32 s59, s58, 16512\n" \
  LSTAMP8 \
  "s_barrier\n" \
  LSTAMP9 \
  "buffer_load_dwordx4 v[96:99], v94, s[40:43], s58 offen sc1\n" \
  "buffer_load_dwordx4 v[100:103], v94, s[40:43], s59 offen sc1\n" \
  "s_lshr_b32 s51, 0x214, %[w]\n" \
  "s_bitcmp0_b32 s51, 0\n" \
  "s_cbranch_scc1 Mlb%=\n" \
  "s_setprio 2\n" \
  "s_mov_b32 exec_hi, 0xffff\n" \
  "v_mov_b32 v8, 147456\n" \
  "v_lshl_add_u32 v7, v6, 1, v8\n" \
  "s_movk_i32 s97, 0x7fff\n" \
  "s_waitcnt vmcnt(11)\n" \
  "Mpr0%=:\n" \
  "v_bitop3_b32 v13, v13, v15, s44 bitop3:0x7e\n" \
  "v_bitop3_b32 v17, v17, v19, s44 bitop3:0x7e\n" \
  "v_bitop3_b32 v21, v21, v23, s44 bitop3:0x7e\n" \
  "v_bitop3_b32 v25, v25, v27, s44 bitop3:0x7e\n" \
  "v_bitop3_b32 v29, v29, v31, s44 bitop3:0x7e\n" \
  "v_bitop3_b32 v33, v33, v35, s44 bitop3:0x7e\n" \
  "v_bitop3_b32 v37, v37, v39, s44 bitop3:0x7e\n" \
  "v_bitop3_b32 v41, v41, v43, s44 bitop3:0x7e\n" \
  "v_bitop3_b32 v45, v45, v47, s44 bitop3:0x7e\n" \
  "v_or3_b32 v13, v13, v17, v21\n" \
  "v_or3_b32 v25, v25, v29, v33\n" \
  "v_or3_b32 v37, v37, v41, v45\n" \
  "v_or3_b32 v13, v13, v25, v37\n" \
  "v_cmp_ne_u32 vcc, 0, v13\n" \
  "s_cbranch_vccnz Mps0%=\n" \
  "Mpk0%=:\n" \
  "v_add_f32 v86, v12, v16\n" \
  "v_add_f32 v87, v14, v18\n" \
  "v_add_f32 v84, v24, v28\n" \
  "v_add_f32 v85, v26, v30\n" \
  "v_add_f32 v88, v36, v40\n" \
  "v_add_f32 v89, v38, v42\n" \
  "v_add_f32 v86, v86, v20\n" \
  "v_add_f32 v87, v87, v22\n" \
  "v_add_f32 v84, v84, v32\n" \
  "v_add_f32 v85, v85, v34\n" \
  "v_add_f32 v88, v88, v44\n" \
  "v_add_f32 v89, v89, v46\n" \
  "v_cvt_pk_bf16_f32 v84, 0, v84\n" \
  "v_cvt_pk_bf16_f32 v85, 0, v85\n" \
  "v_cvt_pk_bf16_f32 v86, 0, v86\n" \
  "v_cvt_pk_bf16_f32 v87, 0, v87\n" \
  "v_cvt_pk_bf16_f32 v88, v88, 0\n" \
  "v_cvt_pk_bf16_f32 v89, 0, v89\n" \
  "v_add_f32 v84, 1.0, v84\n" \
  "v_add_f32 v85, 1.0, v85\n" \
  "v_cvt_pk_bf16_f32 v84, 0, v84\n" \
  "v_cvt_pk_bf16_f32 v85, 0, v85\n" \
  "v_lshlrev_b32 v8, 16, v90\n" \
  "v_and_b32 v9, 0xffff0000, v90\n" \
  "v_mul_f32 v8, v8, v84\n" \
  "v_mul_f32 v9, v9, v85\n" \
  "v_cvt_pk_bf16_f32 v8, 0, v8\n" \
  "v_cvt_pk_bf16_f32 v9, 0, v9\n" \
  "v_add_f32 v92, v92, v8\n" \
  "v_add_f32 v92, v92, v9\n" \
  "v_lshrrev_b32 v8, 16, v8\n" \
  "v_or_b32 v91, v8, v9\n" \
  "ds_write_b128 v7, v[84:87] offset:0\n" \
  "ds_write_b128 v7, v[88:91] offset:16\n" \
  "s_movk_i32 s97, 0x7fff\n" \
  "s_waitcnt vmcnt(2)\n" \
  "Mpr1%=:\n" \
  "v_bitop3_b32 v49, v49, v51, s44 bitop3:0x7e\n" \
  "v_bitop3_b32 v53, v53, v55, s44 bitop3:0x7e\n" \
  "v_bitop3_b32 v57, v57, v59, s44 bitop3:0x7e\n" \
  "v_bitop3_b32 v61, v61, v63, s44 bitop3:0x7e\n" \
  "v_bitop3_b32 v65, v65, v67, s44 bitop3:0x7e\n" \
  "v_bitop3_b32 v69, v69, v71, s44 bitop3:0x7e\n" \
  "v_bitop3_b32 v73, v73, v75, s44 bitop3:0x7e\n" \
  "v_bitop3_b32 v77, v77, v79, s44 bitop3:0x7e\n" \
  "v_bitop3_b32 v81, v81, v83, s44 bitop3:0x7e\n" \
  "v_or3_b32 v49, v49, v53, v57\n" \
  "v_or3_b32 v61, v61, v65, v69\n" \
  "v_or3_b32 v73, v73, v77, v81\n" \
  "v_or3_b32 v49, v49, v61, v73\n" \
  "v_cmp_ne_u32 vcc, 0, v49\n" \
  "s_cbranch_vccnz Mps1%=\n" \
  "Mpk1%=:\n" \
  "v_add_f32 v86, v48, v52\n" \
  "v_add_f32 v87, v50, v54\n" \
  "v_add_f32 v84, v60, v64\n" \
  "v_add_f32 v85, v62, v66\n" \
  "v_add_f32 v88, v72, v76\n" \
  "v_add_f32 v89, v74, v78\n" \
  "v_add_f32 v86, v86, v56\n" \
  "v_add_f32 v87, v87, v58\n" \
  "v_add_f32 v84, v84, v68\n" \
  "v_add_f32 v85, v85, v70\n" \
  "v_add_f32 v88, v88, v80\n" \
  "v_add_f32 v89, v89, v82\n" \
  "v_mov_b32 v90, v93\n" \
  "v_cvt_pk_bf16_f32 v84, 0, v84\n" \
  "v_cvt_pk_bf16_f32 v85, 0, v85\n" \
  "v_cvt_pk_bf16_f32 v86, 0, v86\n" \
  "v_cvt_pk_bf16_f32 v87, 0, v87\n" \
  "v_cvt_pk_bf16_f32 v88, v88, 0\n" \
  "v_cvt_pk_bf16_f32 v89, 0, v89\n" \
  "v_add_f32 v84, 1.0, v84\n" \
  "v_add_f32 v85, 1.0, v85\n" \
  "v_cvt_pk_bf16_f32 v84, 0, v84\n" \
  "v_cvt_pk_bf16_f32 v85, 0, v85\n" \
  "v_lshlrev_b32 v8, 16, v90\n" \
  "v_and_b32 v9, 0xffff0000, v90\n" \
  "v_mul_f32 v8, v8, v84\n" \
  "v_mul_f32 v9, v9, v85\n" \
  "v_cvt_pk_bf16_f32 v8, 0, v8\n" \
  "v_cvt_pk_bf16_f32 v9, 0, v9\n" \
  "v_add_f32 v92, v92, v8\n" \
  "v_add_f32 v92, v92, v9\n" \
  "v_lshrrev_b32 v8, 16, v8\n" \
  "v_or_b32 v91, v8, v9\n" \
  "ds_write_b128 v7, v[84:87] offset:1536\n" \
  "ds_write_b128 v7, v[88:91] offset:1552\n" \
  "s_mov_b64 exec, -1\n" \
  "s_lshl_b32 s50, s50, 4\n" \
  "v_lshrrev_b32 v5, 2, v4\n" \
  "v_add_f32_dpp v92, v92, v92 quad_perm:[1,0,3,2] row_mask:0xf bank_mask:0xf bound_ctrl:1\n" \
  "v_and_b32 v5, 12, v5\n" \
  "v_add3_u32 v5, v5, v1, s50\n" \
  "v_add_f32_dpp v92, v92, v92 quad_perm:[2,3,0,1] row_mask:0xf bank_mask:0xf bound_ctrl:1\n" \
  "s_nop 1\n" \
  "v_add_f32_dpp v92, v92, v92 row_half_mirror row_mask:0xf bank_mask:0xf bound_ctrl:1\n" \
  "s_nop 1\n" \
  "v_add_f32_dpp v92, v92, v92 row_mirror row_mask:0xf bank_mask:0xf bound_ctrl:1\n" \
  "ds_write_b32 v5, v92 offset:32\n" \
  "s_setprio 0\n" \
  "Mlb%=:\n" \
  "v_mov_b32 v5, v95\n" \
  "v_mul_u32_u24 v7, 96, v5\n" \
  "v_add_u32 v7, 147456, v7\n" \
  "v_lshrrev_b32 v9, 5, v4\n" \
  "v_lshl_add_u32 v8, v3, 1, v9\n" \
  "v_mul_u32_u24 v10, 48, v8\n" \
  "s_lshl2_add_u32 s52, %[w], 124192\n" \
  "v_add_u32 v10, s52, v10\n" \
  "v_mul_u32_u24 v8, 208, v8\n" \
  "v_bfe_u32 v9, v4, 4, 1\n" \
  "v_lshl_add_u32 v8, v9, 2, v8\n" \
  "s_lshl4_add_u32 s52, %[w], 120864\n" \
  "v_add_u32 v8, s52, v8\n" \
  "v_mul_u32_u24 v9, 2336, v3\n" \
  "v_mul_u32_u24 v11, 12, v5\n" \
  "v_add_u32 v9, v9, v11\n" \
  "v_lshrrev_b32 v12, 3, v4\n" \
  "v_lshl_add_u32 v11, v3, 3, v12\n" \
  "v_lshlrev_b32 v11, 2, v11\n" \
  "v_mul_u32_u24 v12, 3072, v12\n" \
  "v_lshl_add_u32 v12, v3, 4, v12\n" \
  "s_mul_i32 s52, %[b], 24576\n" \
  "s_lshl_b32 s53, %[w], 7\n" \
  "s_add_u32 s52, s52, s53\n" \
  "s_add_u32 s52, s52, 3309568\n" \
  "v_lshl_add_u32 v13, v3, 4, v1\n" \
  "s_mov_b32 s54, 0x3f80\n" \
  "s_mov_b32 s55, 0x3f800000\n" \
  "v_mov_b64 v[48:49], 0\n" \
  "v_mov_b64 v[50:51], 0\n" \
  "v_mov_b64 v[52:53], 0\n" \
  "v_mov_b64 v[54:55], 0\n" \
  "s_mov_b32 s56, 0x05040100\n" \
  "s_mov_b32 s57, 0x07060302\n" \
  "s_waitcnt lgkmcnt(0)\n" \
  "s_barrier\n" \
  "ds_read_b128 v[24:27], v7 offset:0\n" \
  "ds_read_b128 v[28:31], v7 offset:16\n" \
  "ds_read_b128 v[32:35], v7 offset:32\n" \
  "ds_read_b128 v[36:39], v7 offset:48\n" \
  "ds_read_b128 v[40:43], v7 offset:64\n" \
  "ds_read_b128 v[44:47], v7 offset:80\n" \
  "s_waitcnt vmcnt(0)\n" \
  "v_bitop3_b32 v14, v99, v103, s44 bitop3:0x7e\n" \
  "v_cmp_ne_u32 vcc, 0, v14\n" \
  "s_cbranch_vccnz Mxs%=\n" \
  "Mxb%=:\n" \
  LSTAMP10 \
  "v_perm_b32 v99, v102, v98, s56\n" \
  "v_perm_b32 v103, v102, v98, s57\n" \
  "s_waitcnt lgkmcnt(0)\n" \
  "v_dot2c_f32_bf16 v48, 0x3f803f80, v96\n" \
  "v_dot2c_f32_bf16 v50, v96, v96\n" \
  "v_dot2c_f32_bf16 v52, v96, v31\n" \
  "v_dot2c_f32_bf16 v49, 0x3f803f80, v97\n" \
  "v_dot2c_f32_bf16 v51, v97, v97\n" \
  "v_dot2c_f32_bf16 v53, v97, v31\n" \
  "v_dot2c_f32_bf16 v48, 0x3f803f80, v100\n" \
  "v_dot2c_f32_bf16 v50, v100, v100\n" \
  "v_dot2c_f32_bf16 v52, v100, v47\n" \
  "v_dot2c_f32_bf16 v49, 0x3f803f80, v101\n" \
  "v_dot2c_f32_bf16 v51, v101, v101\n" \
  "v_dot2c_f32_bf16 v53, v101, v47\n" \
  "v_dot2c_f32_bf16 v48, 0x3f803f80, v99\n" \
  "v_dot2c_f32_bf16 v50, v99, v99\n" \
  "v_dot2c_f32_bf16 v52, v99, v39\n" \
  "v_dot2c_f32_bf16 v49, 0x3f803f80, v103\n" \
  "v_dot2c_f32_bf16 v51, v103, v103\n" \
  "v_dot2c_f32_bf16 v53, v103, v39\n" \
  "s_nop 0\n" \
  "v_permlane32_swap_b32 v48, v49\n" \
  "v_permlane32_swap_b32 v50, v51\n" \
  "v_permlane32_swap_b32 v52, v53\n" \
  "v_add_f32 v48, v48, v49\n" \
  "v_add_f32 v50, v50, v51\n" \
  "v_add_f32 v52, v52, v53\n" \
  "s_nop 0\n" \
  "v_permlane16_swap_b32 v48, v50\n" \
  "v_add_f32_dpp v52, v52, v52 row_ror:8 row_mask:0xf bank_mask:0xf bound_ctrl:1\n" \
  "v_add_f32 v48, v48, v50\n" \
  "s_nop 1\n" \
  "v_add_f32_dpp v48, v48, v48 row_ror:8 row_mask:0xf bank_mask:0xf bound_ctrl:1\n" \
  "ds_write2_b32 v8, v48, v52 offset1:2\n" \
  "s_waitcnt lgkmcnt(0)\n" \
  "s_barrier\n" \
  "s_barrier\n" \
  "ds_read_b128 v[56:59], v13 offset:128\n" \
  "s_waitcnt lgkmcnt(0)\n" \
  "v_dot2_f32_bf16 v64, v96, s54, v56\n" \
  "v_dot2_f32_bf16 v65, v96, s55, v56\n" \
  "v_dot2_f32_bf16 v66, v99, s54, v56\n" \
  "v_dot2_f32_bf16 v67, v99, s55, v56\n" \
  "v_dot2_f32_bf16 v68, v100, s54, v56\n" \
  "v_dot2_f32_bf16 v69, v100, s55, v56\n" \
  "v_dot2_f32_bf16 v70, v97, s54, v58\n" \
  "v_dot2_f32_bf16 v71, v97, s55, v58\n" \
  "v_dot2_f32_bf16 v72, v103, s54, v58\n" \
  "v_dot2_f32_bf16 v73, v103, s55, v58\n" \
  "v_dot2_f32_bf16 v74, v101, s54, v58\n" \
  "v_dot2_f32_bf16 v75, v101, s55, v58\n" \
  "v_cvt_pk_bf16_f32 v64, 0, v64\n" \
  "v_cvt_pk_bf16_f32 v65, 0, v65\n" \
  "v_cvt_pk_bf16_f32 v66, 0, v66\n" \
  "v_cvt_pk_bf16_f32 v67, 0, v67\n" \
  "v_cvt_pk_bf16_f32 v68, 0, v68\n" \
  "v_cvt_pk_bf16_f32 v69, 0, v69\n" \
  "v_cvt_pk_bf16_f32 v70, 0, v70\n" \
  "v_cvt_pk_bf16_f32 v71, 0, v71\n" \
  "v_cvt_pk_bf16_f32 v72, 0, v72\n" \
  "v_cvt_pk_bf16_f32 v73, 0, v73\n" \
  "v_cvt_pk_bf16_f32 v74, 0, v74\n" \
  "v_cvt_pk_bf16_f32 v75, 0, v75\n" \
  "v_pk_mul_f32 v[64:65], v[64:65], v[56:57] op_sel:[0,1]\n" \
  "v_pk_mul_f32 v[66:67], v[66:67], v[56:57] op_sel:[0,1]\n" \
  "v_pk_mul_f32 v[68:69], v[68:69], v[56:57] op_sel:[0,1]\n" \
  "v_pk_mul_f32 v[70:71], v[70:71], v[58:59] op_sel:[0,1]\n" \
  "v_pk_mul_f32 v[72:73], v[72:73], v[58:59] op_sel:[0,1]\n" \
  "v_pk_mul_f32 v[74:75], v[74:75], v[58:59] op_sel:[0,1]\n" \
  "v_cvt_pk_bf16_f32 v64, 0, v64\n" \
  "v_cvt_pk_bf16_f32 v65, 0, v65\n" \
  "v_cvt_pk_bf16_f32 v66, 0, v66\n" \
  "v_cvt_pk_bf16_f32 v67, 0, v67\n" \
  "v_cvt_pk_bf16_f32 v68, 0, v68\n" \
  "v_cvt_pk_bf16_f32 v69, 0, v69\n" \
  "v_cvt_pk_bf16_f32 v70, 0, v70\n" \
  "v_cvt_pk_bf16_f32 v71, 0, v71\n" \
  "v_cvt_pk_bf16_f32 v72, 0, v72\n" \
  "v_cvt_pk_bf16_f32 v73, 0, v73\n" \
  "v_cvt_pk_bf16_f32 v74, 0, v74\n" \
  "v_cvt_pk_bf16_f32 v75, 0, v75\n" \
  "v_pk_mul_f32 v[64:65], v[64:65], v[24:25]\n" \
  "v_pk_mul_f32 v[66:67], v[66:67], v[32:33]\n" \
  "v_pk_mul_f32 v[68:69], v[68:69], v[40:41]\n" \
  "v_pk_mul_f32 v[70:71], v[70:71], v[24:25]\n" \
  "v_pk_mul_f32 v[72:73], v[72:73], v[32:33]\n" \
  "v_pk_mul_f32 v[74:75], v[74:75], v[40:41]\n" \
  "v_cvt_pk_bf16_f32 v64, 0, v64\n" \
  "v_cvt_pk_bf16_f32 v65, 0, v65\n" \
  "v_cvt_pk_bf16_f32 v66, 0, v66\n" \
  "v_cvt_pk_bf16_f32 v67, 0, v67\n" \
  "v_cvt_pk_bf16_f32 v68, 0, v68\n" \
  "v_cvt_pk_bf16_f32 v69, 0, v69\n" \
  "v_cvt_pk_bf16_f32 v70, 0, v70\n" \
  "v_cvt_pk_bf16_f32 v71, 0, v71\n" \
  "v_cvt_pk_bf16_f32 v72, 0, v72\n" \
  "v_cvt_pk_bf16_f32 v73, 0, v73\n" \
  "v_cvt_pk_bf16_f32 v74, 0, v74\n" \
  "v_cvt_pk_bf16_f32 v75, 0, v75\n" \
  "v_pk_add_f32 v[64:65], v[64:65], v[26:27]\n" \
  "v_pk_add_f32 v[66:67], v[66:67], v[34:35]\n" \
  "v_pk_add_f32 v[68:69], v[68:69], v[42:43]\n" \
  "v_pk_add_f32 v[70:71], v[70:71], v[26:27]\n" \
  "v_pk_add_f32 v[72:73], v[72:73], v[34:35]\n" \
  "v_pk_add_f32 v[74:75], v[74:75], v[42:43]\n" \
  "v_cvt_pk_bf16_f32 v76, v64, v65\n" \
  "v_cvt_pk_bf16_f32 v77, v66, v67\n" \
  "v_cvt_pk_bf16_f32 v78, v68, v69\n" \
  "v_cvt_pk_bf16_f32 v79, v70, v71\n" \
  "v_cvt_pk_bf16_f32 v80, v72, v73\n" \
  "v_cvt_pk_bf16_f32 v81, v74, v75\n" \
  "ds_bpermute_b32 v84, v11, v76\n" \
  "ds_bpermute_b32 v85, v11, v77\n" \
  "ds_bpermute_b32 v86, v11, v78\n" \
  "ds_bpermute_b32 v88, v11, v79\n" \
  "ds_bpermute_b32 v89, v11, v80\n" \
  "ds_bpermute_b32 v90, v11, v81\n" \
  "v_dot2c_f32_bf16 v54, v76, v30\n" \
  "v_dot2c_f32_bf16 v55, v79, v30\n" \
  "v_dot2c_f32_bf16 v54, v77, v38\n" \
  "v_dot2c_f32_bf16 v55, v80, v38\n" \
  "v_dot2c_f32_bf16 v54, v78, v46\n" \
  "v_dot2c_f32_bf16 v55, v81, v46\n" \
  "v_mov_b32 v87, s44\n" \
  "v_mov_b32 v91, s44\n" \
  "s_nop 0\n" \
  "v_permlane32_swap_b32 v54, v55\n" \
  "v_add_f32 v54, v54, v55\n" \
  "v_mov_b32 v55, v54\n" \
  "s_waitcnt lgkmcnt(0)\n" \
  "buffer_store_dwordx4 v[84:87], v12, s[40:43], s52 offen sc1\n" \
  "v_permlane16_swap_b32 v54, v55\n" \
  "buffer_store_dwordx4 v[88:91], v12, s[40:43], s52 offen offset:1536 sc1\n" \
  "v_add_f32 v54, v54, v55\n" \
  "s_nop 1\n" \
  "v_add_f32_dpp v54, v54, v54 row_ror:8 row_mask:0xf bank_mask:0xf bound_ctrl:1\n" \
  "ds_write_b32 v10, v54\n" \
  LSTAMP11 \
  "s_waitcnt lgkmcnt(0)\n" \
  "s_barrier\n" \
  "v_dot2_f32_bf16 v64, v76, v28, 0\n" \
  "v_dot2_f32_bf16 v65, v76, v29, 0\n" \
  "v_dot2_f32_bf16 v66, v77, v36, 0\n" \
  "v_dot2_f32_bf16 v67, v77, v37, 0\n" \
  "v_dot2_f32_bf16 v68, v78, v44, 0\n" \
  "v_dot2_f32_bf16 v69, v78, v45, 0\n" \
  "v_dot2_f32_bf16 v70, v79, v28, 0\n" \
  "v_dot2_f32_bf16 v71, v79, v29, 0\n" \
  "v_dot2_f32_bf16 v72, v80, v36, 0\n" \
  "v_dot2_f32_bf16 v73, v80, v37, 0\n" \
  "v_dot2_f32_bf16 v74, v81, v44, 0\n" \
  "v_dot2_f32_bf16 v75, v81, v45, 0\n" \
  "v_cvt_pk_bf16_f32 v64, 0, v64\n" \
  "v_cvt_pk_bf16_f32 v65, 0, v65\n" \
  "v_cvt_pk_bf16_f32 v66, 0, v66\n" \
  "v_cvt_pk_bf16_f32 v67, 0, v67\n" \
  "v_cvt_pk_bf16_f32 v68, 0, v68\n" \
  "v_cvt_pk_bf16_f32 v69, 0, v69\n" \
  "v_cvt_pk_bf16_f32 v70, 0, v70\n" \
  "v_cvt_pk_bf16_f32 v71, 0, v71\n" \
  "v_cvt_pk_bf16_f32 v72, 0, v72\n" \
  "v_cvt_pk_bf16_f32 v73, 0, v73\n" \
  "v_cvt_pk_bf16_f32 v74, 0, v74\n" \
  "v_cvt_pk_bf16_f32 v75, 0, v75\n" \
  "v_dot2_f32_bf16 v64, v96, s54, v64\n" \
  "v_dot2_f32_bf16 v65, v96, s55, v65\n" \
  "v_dot2_f32_bf16 v66, v99, s54, v66\n" \
  "v_dot2_f32_bf16 v67, v99, s55, v67\n" \
  "v_dot2_f32_bf16 v68, v100, s54, v68\n" \
  "v_dot2_f32_bf16 v69, v100, s55, v69\n" \
  "v_dot2_f32_bf16 v70, v97, s54, v70\n" \
  "v_dot2_f32_bf16 v71, v97, s55, v71\n" \
  "v_dot2_f32_bf16 v72, v103, s54, v72\n" \
  "v_dot2_f32_bf16 v73, v103, s55, v73\n" \
  "v_dot2_f32_bf16 v74, v101, s54, v74\n" \
  "v_dot2_f32_bf16 v75, v101, s55, v75\n" \
  "v_cvt_pk_bf16_f32 v76, v64, v65\n" \
  "v_cvt_pk_bf16_f32 v77, v66, v67\n" \
  "v_cvt_pk_bf16_f32 v78, v68, v69\n" \
  "v_cvt_pk_bf16_f32 v79, v70, v71\n" \
  "v_cvt_pk_bf16_f32 v80, v72, v73\n" \
  "v_cvt_pk_bf16_f32 v81, v74, v75\n" \
  "ds_write2_b32 v9, v76, v77 offset1:1\n" \
  "ds_write_b32 v9, v78 offset:8\n" \
  "ds_write_b32 v9, v79 offset:1168\n" \
  "ds_write_b32 v9, v80 offset:1172\n" \
  "ds_write_b32 v9, v81 offset:1176\n" \
  "v_mul_u32_u24 v5, 911, %[t]\n" \
  "v_lshrrev_b32 v5, 16, v5\n" \
  "v_add_lshl_u32 v5, v5, %[t], 4\n" \
  "v_lshlrev_b32 v6, 4, %[t]\n" \
  "s_mul_i32 s50, %[b], 18432\n" \
  "s_add_u32 s48, s48, s50\n" \
  "s_addc_u32 s49, s49, 0\n" \
  "v_add_u32 v7, 9216, v6\n" \
  "s_waitcnt lgkmcnt(0)\n" \
  "s_barrier\n" \
  "s_cmp_ge_u32 %[w], 9\n" \
  "s_cbranch_scc1 Mlz%=\n" \
  ".if " STR(OSLP) "\n" \
  "s_sleep " STR(OSLP) "\n" \
  ".endif\n" \
  "ds_read_b128 v[16:19], v5\n" \
  "ds_read_b128 v[20:23], v5 offset:9344\n" \
  "s_waitcnt lgkmcnt(1)\n" \
  "global_store_dwordx4 v6, v[16:19], s[48:49]" OFLAG "\n" \
  "s_waitcnt lgkmcnt(0)\n" \
  "global_store_dwordx4 v7, v[20:23], s[48:49]" OFLAG "\n" \
  "s_branch Mlz%=\n" \
  "Mxs%=:\n" \
  "s_movk_i32 s97, 0x7fff\n" \
  "Mxr%=:\n" \
  LSTAMPC \
  "s_mov_b64 exec, vcc\n" \
  "buffer_load_dwordx4 v[96:99], v94, s[40:43], s58 offen sc1\n" \
  "buffer_load_dwordx4 v[100:103], v94, s[40:43], s59 offen sc1\n" \
  "s_mov_b64 exec, -1\n" \
  "s_waitcnt vmcnt(0)\n" \
  "v_bitop3_b32 v14, v99, v103, s44 bitop3:0x7e\n" \
  "v_cmp_ne_u32 vcc, 0, v14\n" \
  "s_cbranch_vccz Mxb%=\n" \
  "s_sub_u32 s97, s97, 1\n" \
  "s_cbranch_scc0 Mxr%=\n" \
  "s_branch Mxb%=\n" \
  "Mps0%=:\n" \
  LSTAMPD \
  "s_sub_u32 s97, s97, 1\n" \
  "s_cbranch_scc1 Mpk0%=\n" \
  "s_mov_b32 s51, 17920\n" \
  "buffer_load_dwordx4 v[12:15], v6, s[40:43], s51 offen sc1\n" \
  "s_mov_b32 s51, 45568\n" \
  "buffer_load_dwordx4 v[16:19], v6, s[40:43], s51 offen sc1\n" \
  "s_mov_b32 s51, 73216\n" \
  "buffer_load_dwordx4 v[20:23], v6, s[40:43], s51 offen sc1\n" \
  "s_mov_b32 s51, 22528\n" \
  "buffer_load_dwordx4 v[24:27], v6, s[40:43], s51 offen sc1\n" \
  "s_mov_b32 s51, 50176\n" \
  "buffer_load_dwordx4 v[28:31], v6, s[40:43], s51 offen sc1\n" \
  "s_mov_b32 s51, 77824\n" \
  "buffer_load_dwordx4 v[32:35], v6, s[40:43], s51 offen sc1\n" \
  "s_mov_b32 s51, 27136\n" \
  "buffer_load_dwordx4 v[36:39], v6, s[40:43], s51 offen sc1\n" \
  "s_mov_b32 s51, 54784\n" \
  "buffer_load_dwordx4 v[40:43], v6, s[40:43], s51 offen sc1\n" \
  "s_mov_b32 s51, 82432\n" \
  "buffer_load_dwordx4 v[44:47], v6, s[40:43], s51 offen sc1\n" \
  "s_waitcnt vmcnt(0)\n" \
  "s_branch Mpr0%=\n" \
  "Mps1%=:\n" \
  LSTAMPD \
  "s_sub_u32 s97, s97, 1\n" \
  "s_cbranch_scc1 Mpk1%=\n" \
  "s_mov_b32 s51, 17920\n" \
  "buffer_load_dwordx4 v[48:51], v6, s[40:43], s51 offen offset:768 sc1\n" \
  "s_mov_b32 s51, 45568\n" \
  "buffer_load_dwordx4 v[52:55], v6, s[40:43], s51 offen offset:768 sc1\n" \
  "s_mov_b32 s51, 73216\n" \
  "buffer_load_dwordx4 v[56:59], v6, s[40:43], s51 offen offset:768 sc1\n" \
  "s_mov_b32 s51, 22528\n" \
  "buffer_load_dwordx4 v[60:63], v6, s[40:43], s51 offen offset:768 sc1\n" \
  "s_mov_b32 s51, 50176\n" \
  "buffer_load_dwordx4 v[64:67], v6, s[40:43], s51 offen offset:768 sc1\n" \
  "s_mov_b32 s51, 77824\n" \
  "buffer_load_dwordx4 v[68:71], v6, s[40:43], s51 offen offset:768 sc1\n" \
  "s_mov_b32 s51, 27136\n" \
  "buffer_load_dwordx4 v[72:75], v6, s[40:43], s51 offen offset:768 sc1\n" \
  "s_mov_b32 s51, 54784\n" \
  "buffer_load_dwordx4 v[76:79], v6, s[40:43], s51 offen offset:768 sc1\n" \
  "s_mov_b32 s51, 82432\n" \
  "buffer_load_dwordx4 v[80:83], v6, s[40:43], s51 offen offset:768 sc1\n" \
  "s_waitcnt vmcnt(0)\n" \
  "s_branch Mpr1%=\n" \
  "Mls%=:\n" \
  "s_lshr_b32 s50, %[b], 4\n" \
  "s_cmp_lt_u32 s50, " STR(X1T) "\n" \
  "s_cbranch_scc1 Mld%=\n" \
  "s_sleep " STR(X1SLP) "\n" \
  "s_movk_i32 s97, 64\n" \
  "Myw%=:\n" \
  "s_memtime s[52:53]\n" \
  "s_waitcnt lgkmcnt(0)\n" \
  "s_sub_u32 s52, s52, s58\n" \
  "s_cmp_gt_u32 s52, " STR(YT) "\n" \
  "s_cbranch_scc1 Mld%=\n" \
  "s_sleep 1\n" \
  "s_sub_u32 s97, s97, 1\n" \
  "s_cbranch_scc0 Myw%=\n" \
  "Mld%=:\n" \
  "s_cmp_eq_u32 %[w], 15\n" \
  "s_cbranch_scc1 Mlk%=\n" \
  "s_barrier\n" \
  "s_barrier\n" \
  "s_barrier\n" \
  "s_barrier\n" \
  "s_barrier\n" \
  "s_barrier\n" \
  "s_branch Mlz%=\n" \
  "Mlk%=:\n" \
  "s_barrier\n" \
  "s_barrier\n" \
  "s_setprio 3\n" \
  "ds_read_b128 v[16:19], v1 offset:32\n" \
  "ds_read_b128 v[20:23], v1 offset:48\n" \
  "ds_read_b128 v[24:27], v1 offset:64\n" \
  "v_lshrrev_b32 v5, 2, v4\n" \
  "v_mul_u32_u24 v6, 208, v5\n" \
  "v_and_b32 v7, 3, v4\n" \
  "v_mul_u32_u24 v8, 48, v7\n" \
  "s_mov_b32 s50, 120864\n" \
  "v_add3_u32 v6, v6, v8, s50\n" \
  "v_lshl_add_u32 v9, v5, 3, v1\n" \
  "s_lshl_b32 s50, %[b], 4\n" \
  "v_add_u32 v10, s50, v5\n" \
  "v_lshlrev_b32 v11, 3, v10\n" \
  "v_sub_u32 v10, 2047, v10\n" \
  "v_mov_b32 v13, s44\n" \
  "v_mul_u32_u24 v14, 48, v5\n" \
  "v_mul_u32_u24 v7, 12, v7\n" \
  "s_mov_b32 s50, 124192\n" \
  "v_add3_u32 v14, v14, v7, s50\n" \
  "s_mov_b32 s50, 10403840\n" \
  "s_mov_b32 s51, 103424\n" \
  "s_mov_b32 s55, 10469888\n" \
  "v_and_b32 v28, 15, v4\n" \
  "v_lshrrev_b32 v7, 4, v4\n" \
  "v_mul_u32_u24 v7, 16512, v7\n" \
  "v_lshl_add_u32 v7, v28, 3, v7\n" \
  "s_lshl_b32 s56, %[b], 7\n" \
  "v_add_u32 v7, s56, v7\n" \
  "v_lshlrev_b32 v28, 4, v28\n" \
  "s_waitcnt lgkmcnt(0)\n" \
  "v_pk_add_f32 v[16:17], v[16:17], v[18:19]\n" \
  "v_pk_add_f32 v[20:21], v[20:21], v[22:23]\n" \
  "v_pk_add_f32 v[24:25], v[24:25], v[26:27]\n" \
  "v_pk_add_f32 v[16:17], v[16:17], v[20:21]\n" \
  "v_pk_add_f32 v[16:17], v[16:17], v[24:25]\n" \
  "v_add_f32 v15, v16, v17\n" \
  "s_barrier\n" \
  "ds_read_b128 v[16:19], v6\n" \
  "ds_read_b128 v[20:23], v6 offset:16\n" \
  "ds_read_b128 v[24:27], v6 offset:32\n" \
  "s_waitcnt lgkmcnt(1)\n" \
  "v_pk_add_f32 v[16:17], v[16:17], v[20:21]\n" \
  "v_pk_add_f32 v[18:19], v[18:19], v[22:23]\n" \
  "s_waitcnt lgkmcnt(0)\n" \
  "v_pk_add_f32 v[16:17], v[16:17], v[24:25]\n" \
  "v_pk_add_f32 v[18:19], v[18:19], v[26:27]\n" \
  "v_add_f32 v18, v18, v19\n" \
  "v_add_f32_dpp v16, v16, v16 quad_perm:[1,0,3,2] row_mask:0xf bank_mask:0xf bound_ctrl:1\n" \
  "v_add_f32_dpp v17, v17, v17 quad_perm:[1,0,3,2] row_mask:0xf bank_mask:0xf bound_ctrl:1\n" \
  "v_add_f32_dpp v18, v18, v18 quad_perm:[1,0,3,2] row_mask:0xf bank_mask:0xf bound_ctrl:1\n" \
  "v_add_f32_dpp v16, v16, v16 quad_perm:[2,3,0,1] row_mask:0xf bank_mask:0xf bound_ctrl:1\n" \
  "v_add_f32_dpp v17, v17, v17 quad_perm:[2,3,0,1] row_mask:0xf bank_mask:0xf bound_ctrl:1\n" \
  "v_add_f32_dpp v18, v18, v18 quad_perm:[2,3,0,1] row_mask:0xf bank_mask:0xf bound_ctrl:1\n" \
  "v_mul_f32 v16, 0xbae38e39, v16\n" \
  "v_mul_f32 v17, 0x3ae38e39, v17\n" \
  "v_fma_f32 v17, -v16, v16, v17\n" \
  "v_max_f32 v17, 0, v17\n" \
  "v_cvt_pk_bf16_f32 v17, 0, v17\n" \
  "v_add_f32 v17, 0x3727c5ac, v17\n" \
  "v_cvt_pk_bf16_f32 v17, 0, v17\n" \
  "v_sqrt_f32 v17, v17\n" \
  "v_cvt_pk_bf16_f32 v16, 0, v16\n" \
  "v_cvt_pk_bf16_f32 v17, 0, v17\n" \
  "v_rcp_f32 v17, v17\n" \
  "v_fma_f32 v12, v16, v15, v18\n" \
  "v_mul_f32 v12, v12, v17\n" \
  "ds_write_b64 v9, v[16:17] offset:128\n" \
  "v_and_b32 v18, 0x7ff, v12\n" \
  "v_add_u32 v18, 0x7ff, v18\n" \
  "v_or_b32 v12, v12, v18\n" \
  "v_ashrrev_i32 v18, 31, v12\n" \
  "v_or_b32 v18, 0x80000000, v18\n" \
  "v_xor_b32 v12, v12, v18\n" \
  "v_and_b32 v12, 0xfffff800, v12\n" \
  "v_or_b32 v12, v12, v10\n" \
  "ds_bpermute_b32 v12, v28, v12\n" \
  "s_waitcnt lgkmcnt(0)\n" \
  "buffer_store_dwordx2 v[12:13], v7, s[40:43], s50 offen sc1\n" \
  "buffer_store_dwordx2 v[12:13], v7, s[40:43], s55 offen sc1\n" \
  LSTAMP13 \
  "s_waitcnt lgkmcnt(0)\n" \
  "s_barrier\n" \
  "s_barrier\n" \
  "ds_read2_b32 v[16:17], v14 offset1:1\n" \
  "ds_read_b32 v18, v14 offset:8\n" \
  "s_waitcnt lgkmcnt(0)\n" \
  "v_add_f32 v12, v16, v17\n" \
  "v_add_f32 v12, v12, v18\n" \
  "s_nop 1\n" \
  "v_add_f32_dpp v12, v12, v12 quad_perm:[1,0,3,2] row_mask:0xf bank_mask:0xf bound_ctrl:1\n" \
  "s_nop 1\n" \
  "v_add_f32_dpp v12, v12, v12 quad_perm:[2,3,0,1] row_mask:0xf bank_mask:0xf bound_ctrl:1\n" \
  "v_and_b32 v18, 0x7ff, v12\n" \
  "v_add_u32 v18, 0x7ff, v18\n" \
  "v_or_b32 v12, v12, v18\n" \
  "v_ashrrev_i32 v18, 31, v12\n" \
  "v_or_b32 v18, 0x80000000, v18\n" \
  "v_xor_b32 v12, v12, v18\n" \
  "v_and_b32 v12, 0xfffff800, v12\n" \
  "v_or_b32 v12, v12, v10\n" \
  "s_mov_b32 exec_lo, 0x11111111\n" \
  "s_mov_b32 exec_hi, 0x11111111\n" \
  "buffer_store_dwordx2 v[12:13], v11, s[40:43], s51 offen sc1\n" \
  "s_mov_b64 exec, -1\n" \
  LSTAMP14 \
  "s_barrier\n" \
  "Mlz%=:\n" \
  "s_waitcnt vmcnt(0)\n" \
  "s_barrier\n" \
  "s_cmp_lg_u32 %[w], 15\n" \
  "s_cbranch_scc1 Mly%=\n" \
  "s_lshl2_add_u32 s50, %[b], 136192\n" \
  "s_mov_b64 exec, 1\n" \
  "buffer_store_dword v13, off, s[40:43], s50 sc1\n" \
  "s_mov_b64 exec, -1\n" \
  "Mly%=:\n"

#define EW_HEAD \
  "s_load_dwordx4 s[52:55], %[k], 0x58\n" \
  "s_load_dwordx2 s[40:41], %[k], 0x70\n" \
  "s_lshr_b32 s50, %[w], 2\n" \
  "s_mov_b32 s42, 10668032\n" \
  "s_mov_b32 s43, 0x20000\n" \
  "s_cmp_eq_u32 s50, 3\n" \
  "s_cbranch_scc1 Eez%=\n" \
  "s_cmpk_ge_u32 %[b], 246\n" \
  "s_cbranch_scc1 Een%=\n" \
  "s_sub_u32 s51, %[b], 192\n" \
  "s_mul_i32 s56, s51, 43\n" \
  "s_lshr_b32 s56, s56, 7\n" \
  "s_mul_i32 s57, s56, 3\n" \
  "s_sub_u32 s57, s51, s57\n" \
  "s_mul_i32 s51, s57, 192\n" \
  "s_cmp_eq_u32 s50, 2\n" \
  "s_cbranch_scc1 Eec%=\n" \
  "v_bfe_u32 v1, %[t], 3, 5\n" \
  "v_add_u32 v2, s51, v1\n" \
  "v_mul_u32_u24 v2, 6912, v2\n" \
  "v_and_b32 v3, 2, v1\n" \
  "v_lshrrev_b32 v1, 1, v1\n" \
  "v_and_or_b32 v3, v1, 4, v3\n" \
  "v_xor_b32 v3, v3, %[t]\n" \
  "v_and_b32 v3, 7, v3\n" \
  "s_lshl_b32 s58, s56, 7\n" \
  "v_lshl_add_u32 v2, v3, 4, v2\n" \
  "v_add_u32 v2, s58, v2\n" \
  "s_cmp_lg_u32 s50, 0\n" \
  "s_cbranch_scc1 Eeb%=\n" \
  "s_lshl_b32 s51, %[w], 10\n" \
  "s_add_u32 m0, s51, 73728\n" \
  GSTAMP1 \
  "s_waitcnt lgkmcnt(0)\n" \
  "s_memtime s[92:93]\n" \
  "global_load_lds_dwordx4 v2, s[54:55]\n" \
  "s_add_u32 m0, m0, 4096\n" \
  "v_add_u32 v2, 221184, v2\n" \
  "global_load_lds_dwordx4 v2, s[54:55]\n" \
  "s_add_u32 m0, m0, 4096\n" \
  "v_add_u32 v2, 221184, v2\n" \
  "global_load_lds_dwordx4 v2, s[54:55]\n" \
  "s_add_u32 m0, m0, 4096\n" \
  "v_add_u32 v2, 221184, v2\n" \
  "global_load_lds_dwordx4 v2, s[54:55]\n" \
  "s_add_u32 m0, m0, 4096\n" \
  "v_add_u32 v2, 221184, v2\n" \
  "global_load_lds_dwordx4 v2, s[54:55]\n" \
  "s_add_u32 m0, m0, 4096\n" \
  "v_add_u32 v2, 221184, v2\n" \
  "global_load_lds_dwordx4 v2, s[54:55]\n" \
  "s_branch Eea%=\n" \
  "Een%=:\n" \
  "s_mov_b32 s51, 0\n" \
  "s_cmp_eq_u32 s50, 2\n" \
  "s_cbranch_scc1 Eec%=\n" \
  "s_cmpk_lt_u32 %[b], 248\n" \
  "s_cbranch_scc1 Eeb%=\n" \
  "s_cmp_lg_u32 s50, 0\n" \
  "s_cbranch_scc1 Eeb%=\n" \
  "s_getpc_b64 s[48:49]\n" \
  "Epc%=:\n" \
  "v_lshlrev_b32 v1, 7, %[t]\n" \
  "v_min_u32 v1, Ez%=-Epc%=, v1\n" \
  "s_mov_b32 m0, 160032\n" \
  "s_nop 0\n" \
  "global_load_lds_dword v1, s[48:49]\n" \
  "s_branch Eeb%=\n" \
  "Eez%=:\n" \
  "v_and_b32 v5, 63, %[t]\n" \
  "v_mov_b32 v6, 159488\n" \
  "v_lshl_add_u32 v6, v5, 2, v6\n" \
  "v_mov_b32 v8, 0\n" \
  "ds_write_b32 v6, v8\n" \
  "s_branch Eeb%=\n" \
  "Eec%=:\n" \
  "v_and_b32 v5, 255, %[t]\n" \
  "v_min_u32 v5, 191, v5\n" \
  "v_add_lshl_u32 v6, v5, s51, 1\n" \
  GSTAMP1 \
  "s_waitcnt lgkmcnt(0)\n" \
  "global_load_short_d16_hi v7, v6, s[52:53]\n" \
  "buffer_load_dword v8, off, s[40:43], 0 sc1\n" \
  "s_branch Eea%=\n" \
  "Eeb%=:\n" \
  "s_waitcnt lgkmcnt(0)\n" \
  "s_memtime s[92:93]\n" \
  "Eea%=:\n"

#define EW_BODY \
  "v_and_b32 v10, 15, %[t]\n" \
  "v_bfe_u32 v11, %[t], 4, 2\n" \
  "v_and_b32 v12, 63, %[t]\n" \
  "s_cmp_lg_u32 s50, 2\n" \
  "s_cbranch_scc1 Em0%=\n" \
  "s_setprio 2\n" \
  "v_bfe_u32 v1, %[t], 2, 2\n" \
  "v_lshl_add_u32 v1, v11, 3, v1\n" \
  "v_lshlrev_b32 v1, 7, v1\n" \
  "v_bfe_u32 v2, %[t], 3, 2\n" \
  "v_xor_b32 v2, %[w], v2\n" \
  "v_and_b32 v2, 3, v2\n" \
  "v_lshl_add_u32 v1, v2, 5, v1\n" \
  "v_and_b32 v2, 3, %[t]\n" \
  "v_lshl_add_u32 v1, v2, 3, v1\n" \
  "v_add_u32 v1, 73728, v1\n" \
  "v_mov_b32 v9, 160416\n" \
  "v_lshl_add_u32 v5, v5, 1, v9\n" \
  "v_lshl_add_u32 v9, v11, 4, v9\n" \
  "v_lshlrev_b32 v3, 3, v10\n" \
  "s_mul_i32 s58, s57, 27648\n" \
  "s_lshl_b32 s59, s56, 9\n" \
  "s_add_u32 s58, s58, s59\n" \
  "s_lshl_b32 s59, %[w], 7\n" \
  "s_add_u32 s58, s58, s59\n" \
  "s_add_u32 s58, s58, 3072\n" \
  "s_cmp_ge_u32 s56, 9\n" \
  "s_cselect_b32 s59, 4608, 0\n" \
  "v_mov_b32 v4, 158208\n" \
  "s_waitcnt vmcnt(0)\n" \
  GSTAMP2 \
  "v_and_b32 v7, 0xffff0000, v7\n" \
  "v_mul_f32 v6, 0xbfb8aa3b, v7\n" \
  "v_exp_f32 v6, v6\n" \
  "v_add_u32 v8, 1, v8\n" \
  "v_add_f32 v6, 1.0, v6\n" \
  "v_rcp_f32 v6, v6\n" \
  "v_readfirstlane_b32 s44, v8\n" \
  "v_mul_f32 v6, v6, v7\n" \
  "v_cvt_pk_bf16_f32 v6, 0, v6\n" \
  "ds_write_b32 v4, v8\n" \
  "s_cmpk_ge_u32 %[b], 246\n" \
  "s_cbranch_scc1 Ew0%=\n" \
  "ds_write_b16_d16_hi v5, v6\n" \
  "s_waitcnt lgkmcnt(0)\n" \
  "s_barrier\n" \
  GSTAMP3 \
  "ds_read_b64_tr_b16 v[40:41], v1 offset:0\n" \
  "ds_read_b64_tr_b16 v[42:43], v1 offset:512\n" \
  "ds_read_b64_tr_b16 v[44:45], v1 offset:4096\n" \
  "ds_read_b64_tr_b16 v[46:47], v1 offset:4608\n" \
  "ds_read_b64_tr_b16 v[48:49], v1 offset:8192\n" \
  "ds_read_b64_tr_b16 v[50:51], v1 offset:8704\n" \
  "ds_read_b64_tr_b16 v[52:53], v1 offset:12288\n" \
  "ds_read_b64_tr_b16 v[54:55], v1 offset:12800\n" \
  "ds_read_b64_tr_b16 v[56:57], v1 offset:16384\n" \
  "ds_read_b64_tr_b16 v[58:59], v1 offset:16896\n" \
  "ds_read_b64_tr_b16 v[60:61], v1 offset:20480\n" \
  "ds_read_b64_tr_b16 v[62:63], v1 offset:20992\n" \
  "ds_read_b128 v[64:67], v9 offset:0\n" \
  "ds_read_b128 v[68:71], v9 offset:64\n" \
  "ds_read_b128 v[72:75], v9 offset:128\n" \
  "ds_read_b128 v[76:79], v9 offset:192\n" \
  "ds_read_b128 v[80:83], v9 offset:256\n" \
  "ds_read_b128 v[84:87], v9 offset:320\n" \
  "s_mov_b64 s[46:47], 0xffff\n" \
  "s_waitcnt lgkmcnt(0)\n" \
  "v_mfma_f32_16x16x32_bf16 v[16:19], v[64:67], v[40:43], 0\n" \
  "v_mfma_f32_16x16x32_bf16 v[16:19], v[68:71], v[44:47], v[16:19]\n" \
  "v_mfma_f32_16x16x32_bf16 v[16:19], v[72:75], v[48:51], v[16:19]\n" \
  "v_mfma_f32_16x16x32_bf16 v[16:19], v[76:79], v[52:55], v[16:19]\n" \
  "v_mfma_f32_16x16x32_bf16 v[16:19], v[80:83], v[56:59], v[16:19]\n" \
  "v_mfma_f32_16x16x32_bf16 v[16:19], v[84:87], v[60:63], v[16:19]\n" \
  "s_nop 7\n" \
  "s_mov_b64 exec, s[46:47]\n" \
  "v_mov_b32 v17, s44\n" \
  "buffer_store_dwordx2 v[16:17], v3, s[40:43], s58 offen sc1\n" \
  "s_mov_b64 exec, -1\n" \
  GSTAMP4 \
  "s_add_u32 s58, s58, 9216\n" \
  "s_add_u32 s58, s58, s59\n" \
  "s_barrier\n" \
  "s_barrier\n" \
  "ds_read_b64_tr_b16 v[40:41], v1 offset:24576\n" \
  "ds_read_b64_tr_b16 v[42:43], v1 offset:25088\n" \
  "ds_read_b64_tr_b16 v[44:45], v1 offset:28672\n" \
  "ds_read_b64_tr_b16 v[46:47], v1 offset:29184\n" \
  "ds_read_b64_tr_b16 v[48:49], v1 offset:32768\n" \
  "ds_read_b64_tr_b16 v[50:51], v1 offset:33280\n" \
  "ds_read_b64_tr_b16 v[52:53], v1 offset:36864\n" \
  "ds_read_b64_tr_b16 v[54:55], v1 offset:37376\n" \
  "ds_read_b64_tr_b16 v[56:57], v1 offset:40960\n" \
  "ds_read_b64_tr_b16 v[58:59], v1 offset:41472\n" \
  "ds_read_b64_tr_b16 v[60:61], v1 offset:45056\n" \
  "ds_read_b64_tr_b16 v[62:63], v1 offset:45568\n" \
  "s_mov_b64 s[46:47], 0xffff\n" \
  "s_waitcnt lgkmcnt(0)\n" \
  "v_mfma_f32_16x16x32_bf16 v[16:19], v[64:67], v[40:43], 0\n" \
  "v_mfma_f32_16x16x32_bf16 v[16:19], v[68:71], v[44:47], v[16:19]\n" \
  "v_mfma_f32_16x16x32_bf16 v[16:19], v[72:75], v[48:51], v[16:19]\n" \
  "v_mfma_f32_16x16x32_bf16 v[16:19], v[76:79], v[52:55], v[16:19]\n" \
  "v_mfma_f32_16x16x32_bf16 v[16:19], v[80:83], v[56:59], v[16:19]\n" \
  "v_mfma_f32_16x16x32_bf16 v[16:19], v[84:87], v[60:63], v[16:19]\n" \
  "s_nop 7\n" \
  "s_mov_b64 exec, s[46:47]\n" \
  "v_mov_b32 v17, s44\n" \
  "buffer_store_dwordx2 v[16:17], v3, s[40:43], s58 offen sc1\n" \
  "s_mov_b64 exec, -1\n" \
  GSTAMP5 \
  "s_add_u32 s58, s58, 4608\n" \
  "v_add_u32 v1, 49152, v1\n" \
  "s_barrier\n" \
  "ds_read_b64_tr_b16 v[40:41], v1 offset:0\n" \
  "ds_read_b64_tr_b16 v[42:43], v1 offset:512\n" \
  "ds_read_b64_tr_b16 v[44:45], v1 offset:4096\n" \
  "ds_read_b64_tr_b16 v[46:47], v1 offset:4608\n" \
  "ds_read_b64_tr_b16 v[48:49], v1 offset:8192\n" \
  "ds_read_b64_tr_b16 v[50:51], v1 offset:8704\n" \
  "ds_read_b64_tr_b16 v[52:53], v1 offset:12288\n" \
  "ds_read_b64_tr_b16 v[54:55], v1 offset:12800\n" \
  "ds_read_b64_tr_b16 v[56:57], v1 offset:16384\n" \
  "ds_read_b64_tr_b16 v[58:59], v1 offset:16896\n" \
  "ds_read_b64_tr_b16 v[60:61], v1 offset:20480\n" \
  "ds_read_b64_tr_b16 v[62:63], v1 offset:20992\n" \
  "s_mov_b64 s[46:47], 0xffff\n" \
  "s_waitcnt lgkmcnt(0)\n" \
  "v_mfma_f32_16x16x32_bf16 v[16:19], v[64:67], v[40:43], 0\n" \
  "v_mfma_f32_16x16x32_bf16 v[16:19], v[68:71], v[44:47], v[16:19]\n" \
  "v_mfma_f32_16x16x32_bf16 v[16:19], v[72:75], v[48:51], v[16:19]\n" \
  "v_mfma_f32_16x16x32_bf16 v[16:19], v[76:79], v[52:55], v[16:19]\n" \
  "v_mfma_f32_16x16x32_bf16 v[16:19], v[80:83], v[56:59], v[16:19]\n" \
  "v_mfma_f32_16x16x32_bf16 v[16:19], v[84:87], v[60:63], v[16:19]\n" \
  "s_nop 7\n" \
  "s_mov_b64 exec, s[46:47]\n" \
  "v_mov_b32 v17, s44\n" \
  "buffer_store_dwordx2 v[16:17], v3, s[40:43], s58 offen sc1\n" \
  "s_mov_b64 exec, -1\n" \
  GSTAMP6 \
  "s_branch Ew0%=\n" \
  "Em0%=:\n" \
  "s_cmpk_ge_u32 %[b], 246\n" \
  "s_cbranch_scc1 Em2%=\n" \
  "s_cmp_eq_u32 s50, 3\n" \
  "s_cbranch_scc1 Em1%=\n" \
  "s_cmp_ge_u32 s56, 9\n" \
  "s_cselect_b32 s59, 1152, 0\n" \
  "s_cmp_eq_u32 s50, 0\n" \
  "s_cselect_b32 s58, -1102464, 0\n" \
  "s_cselect_b32 s51, 0, 2304\n" \
  "s_add_u32 s58, s58, s51\n" \
  "s_add_u32 s58, s58, s59\n" \
  "v_add_u32 v2, s58, v2\n" \
  "s_mul_i32 s51, s50, -28672\n" \
  "s_lshl_b32 s58, %[w], 10\n" \
  "s_add_u32 s51, s51, s58\n" \
  "s_add_u32 m0, s51, 122880\n" \
  "s_waitcnt vmcnt(0) lgkmcnt(0)\n" \
  GSTAMP3 \
  "s_cmp_eq_u32 s50, 0\n" \
  "s_cbranch_scc1 Em3%=\n" \
  "s_sleep " STR(P1D) "\n" \
  "global_load_lds_dwordx4 v2, s[54:55]\n" \
  "s_add_u32 m0, m0, 4096\n" \
  "v_add_u32 v2, 221184, v2\n" \
  "global_load_lds_dwordx4 v2, s[54:55]\n" \
  "s_add_u32 m0, m0, 4096\n" \
  "v_add_u32 v2, 221184, v2\n" \
  "global_load_lds_dwordx4 v2, s[54:55]\n" \
  "s_add_u32 m0, m0, 4096\n" \
  "v_add_u32 v2, 221184, v2\n" \
  "global_load_lds_dwordx4 v2, s[54:55]\n" \
  "s_add_u32 m0, m0, 4096\n" \
  "v_add_u32 v2, 221184, v2\n" \
  "global_load_lds_dwordx4 v2, s[54:55]\n" \
  "s_add_u32 m0, m0, 4096\n" \
  "v_add_u32 v2, 221184, v2\n" \
  "global_load_lds_dwordx4 v2, s[54:55]\n" \
  "s_load_dwordx4 s[52:55], %[k], 0x40\n" \
  "s_load_dwordx2 s[48:49], %[k], 0x50\n" \
  "s_load_dwordx2 s[84:85], %[k], 0x68\n" \
  "s_barrier\n" \
  "s_barrier\n" \
  "s_waitcnt lgkmcnt(0)\n" \
  "s_and_b32 s56, %[b], 7\n" \
  "s_bfe_u32 s57, %[b], 0x30003\n" \
  "s_lshr_b32 s58, s52, 7\n" \
  "s_add_u32 s58, s58, s56\n" \
  "s_sub_u32 s58, s57, s58\n" \
  "s_add_u32 s58, s58, 4\n" \
  "s_and_b32 s58, s58, 7\n" \
  "s_mul_i32 s59, s56, 9\n" \
  "s_add_u32 s58, s58, s59\n" \
  "s_lshl_b32 s58, s58, 6\n" \
  "s_add_u32 s59, s59, 8\n" \
  "s_lshl_b32 s59, s59, 6\n" \
  "s_lshl3_add_u32 s59, s57, s59\n" \
  "s_lshl3_add_u32 s57, s56, s57\n" \
  "v_mov_b32 v2, s52\n" \
  "v_mov_b32 v3, s53\n" \
  "v_mov_b32 v4, s54\n" \
  "v_mov_b32 v5, s55\n" \
  "s_mov_b32 s46, 294912\n" \
  "s_mov_b32 s47, 0\n" \
  "v_bfe_u32 v7, %[t], 2, 2\n" \
  "s_mov_b32 vcc_lo, 0xff00ff00\n" \
  "s_mov_b32 vcc_hi, 0xff00ff00\n" \
  "v_cndmask_b32 v2, v2, v4, vcc\n" \
  "v_cndmask_b32 v3, v3, v5, vcc\n" \
  "v_lshrrev_b32 v4, 4, %[t]\n" \
  "v_mul_u32_u24 v4, 9216, v4\n" \
  "s_lshl_b32 s51, %[w], 1\n" \
  "s_and_b32 s51, s51, 4\n" \
  "v_or_b32 v5, s51, v11\n" \
  "v_xor_b32 v5, v5, %[t]\n" \
  "v_and_b32 v5, 7, v5\n" \
  "v_lshl_add_u32 v4, v5, 4, v4\n" \
  "s_lshl_b32 s51, s58, 1\n" \
  "v_add_u32 v4, s51, v4\n" \
  "v_mov_b32 v5, 0\n" \
  "v_lshl_add_u64 v[2:3], v[2:3], 0, v[4:5]\n" \
  "s_lshl_b32 m0, %[w], 10\n" \
  "s_waitcnt vmcnt(0)\n" \
  GSTAMP5 \
  "s_barrier\n" \
  "s_sleep " STR(GED) "\n" \
  "global_load_lds_dwordx4 v[2:3], off" WNT "\n" \
  "s_add_u32 m0, m0, 8192\n" \
  "v_lshl_add_u64 v[2:3], v[2:3], 0, s[46:47]\n" \
  "global_load_lds_dwordx4 v[2:3], off" WNT "\n" \
  "s_add_u32 m0, m0, 8192\n" \
  "v_lshl_add_u64 v[2:3], v[2:3], 0, s[46:47]\n" \
  "global_load_lds_dwordx4 v[2:3], off" WNT "\n" \
  "s_add_u32 m0, m0, 8192\n" \
  "v_lshl_add_u64 v[2:3], v[2:3], 0, s[46:47]\n" \
  "global_load_lds_dwordx4 v[2:3], off" WNT "\n" \
  "s_add_u32 m0, m0, 8192\n" \
  "v_lshl_add_u64 v[2:3], v[2:3], 0, s[46:47]\n" \
  "global_load_lds_dwordx4 v[2:3], off" WNT "\n" \
  "s_add_u32 m0, m0, 8192\n" \
  "v_lshl_add_u64 v[2:3], v[2:3], 0, s[46:47]\n" \
  "global_load_lds_dwordx4 v[2:3], off" WNT "\n" \
  "s_add_u32 m0, m0, 8192\n" \
  "v_lshl_add_u64 v[2:3], v[2:3], 0, s[46:47]\n" \
  "s_barrier\n" \
  "s_barrier\n" \
  "v_mov_b32 v8, 158208\n" \
  "ds_read_b32 v8, v8\n" \
  "s_waitcnt lgkmcnt(0)\n" \
  "v_readfirstlane_b32 s44, v8\n" \
  "s_branch Ew1%=\n" \
  "Em3%=:\n" \
  "s_barrier\n" \
  "s_barrier\n" \
  "global_load_lds_dwordx4 v2, s[54:55]\n" \
  "s_add_u32 m0, m0, 4096\n" \
  "v_add_u32 v2, 221184, v2\n" \
  "global_load_lds_dwordx4 v2, s[54:55]\n" \
  "s_add_u32 m0, m0, 4096\n" \
  "v_add_u32 v2, 221184, v2\n" \
  "global_load_lds_dwordx4 v2, s[54:55]\n" \
  "s_add_u32 m0, m0, 4096\n" \
  "v_add_u32 v2, 221184, v2\n" \
  "global_load_lds_dwordx4 v2, s[54:55]\n" \
  "s_add_u32 m0, m0, 4096\n" \
  "v_add_u32 v2, 221184, v2\n" \
  "global_load_lds_dwordx4 v2, s[54:55]\n" \
  "s_add_u32 m0, m0, 4096\n" \
  "v_add_u32 v2, 221184, v2\n" \
  "global_load_lds_dwordx4 v2, s[54:55]\n" \
  "s_barrier\n" \
  "s_waitcnt vmcnt(0)\n" \
  GSTAMP5 \
  "s_barrier\n" \
  "s_branch Ew0%=\n" \
  "Em1%=:\n" \
  "s_waitcnt lgkmcnt(0)\n" \
  "s_barrier\n" \
  "s_barrier\n" \
  "s_barrier\n" \
  "s_barrier\n" \
  "s_branch Ew0%=\n" \
  "Em2%=:\n" \
  "s_waitcnt lgkmcnt(0)\n" \
  "s_sleep " STR(WDLY) "\n" \
  "s_cmpk_lt_u32 %[b], 248\n" \
  "s_cbranch_scc1 Ew0%=\n" \
  "s_sleep " STR(WSLOW) "\n" \
  "Ew0%=:\n" \
  "s_setprio 0\n" \
  "s_load_dwordx4 s[52:55], %[k], 0x40\n" \
  "s_load_dwordx2 s[48:49], %[k], 0x50\n" \
  "s_load_dwordx2 s[84:85], %[k], 0x68\n" \
  "s_waitcnt lgkmcnt(0)\n" \
  "s_barrier\n" \
  "s_and_b32 s56, %[b], 7\n" \
  "s_bfe_u32 s57, %[b], 0x30003\n" \
  "s_lshr_b32 s58, s52, 7\n" \
  "s_add_u32 s58, s58, s56\n" \
  "s_sub_u32 s58, s57, s58\n" \
  "s_add_u32 s58, s58, 4\n" \
  "s_and_b32 s58, s58, 7\n" \
  "s_mul_i32 s59, s56, 9\n" \
  "s_add_u32 s58, s58, s59\n" \
  "s_lshl_b32 s58, s58, 6\n" \
  "s_add_u32 s59, s59, 8\n" \
  "s_lshl_b32 s59, s59, 6\n" \
  "s_lshl3_add_u32 s59, s57, s59\n" \
  "s_lshl3_add_u32 s57, s56, s57\n" \
  "v_mov_b32 v8, 158208\n" \
  "ds_read_b32 v8, v8\n" \
  "s_waitcnt lgkmcnt(0)\n" \
  "v_readfirstlane_b32 s44, v8\n" \
  "s_cmp_gt_u32 %[w], 8\n" \
  "s_cbranch_scc1 Ewd%=\n" \
  "v_mov_b32 v2, s52\n" \
  "v_mov_b32 v3, s53\n" \
  "v_mov_b32 v4, s54\n" \
  "v_mov_b32 v5, s55\n" \
  "s_mov_b32 s46, 294912\n" \
  "s_mov_b32 s47, 0\n" \
  "v_bfe_u32 v7, %[t], 2, 2\n" \
  "s_cmp_eq_u32 %[w], 8\n" \
  "s_cbranch_scc1 Ewx%=\n" \
  "s_mov_b32 vcc_lo, 0xff00ff00\n" \
  "s_mov_b32 vcc_hi, 0xff00ff00\n" \
  "v_cndmask_b32 v2, v2, v4, vcc\n" \
  "v_cndmask_b32 v3, v3, v5, vcc\n" \
  "v_lshrrev_b32 v4, 4, %[t]\n" \
  "v_mul_u32_u24 v4, 9216, v4\n" \
  "s_lshl_b32 s51, %[w], 1\n" \
  "s_and_b32 s51, s51, 4\n" \
  "v_or_b32 v5, s51, v11\n" \
  "v_xor_b32 v5, v5, %[t]\n" \
  "v_and_b32 v5, 7, v5\n" \
  "v_lshl_add_u32 v4, v5, 4, v4\n" \
  "s_lshl_b32 s51, s58, 1\n" \
  "v_add_u32 v4, s51, v4\n" \
  "v_mov_b32 v5, 0\n" \
  "v_lshl_add_u64 v[2:3], v[2:3], 0, v[4:5]\n" \
  "s_lshl_b32 m0, %[w], 10\n" \
  "s_nop 0\n" \
  "global_load_lds_dwordx4 v[2:3], off" WNT "\n" \
  "s_add_u32 m0, m0, 8192\n" \
  "v_lshl_add_u64 v[2:3], v[2:3], 0, s[46:47]\n" \
  "global_load_lds_dwordx4 v[2:3], off" WNT "\n" \
  "s_add_u32 m0, m0, 8192\n" \
  "v_lshl_add_u64 v[2:3], v[2:3], 0, s[46:47]\n" \
  "global_load_lds_dwordx4 v[2:3], off" WNT "\n" \
  "s_add_u32 m0, m0, 8192\n" \
  "v_lshl_add_u64 v[2:3], v[2:3], 0, s[46:47]\n" \
  "global_load_lds_dwordx4 v[2:3], off" WNT "\n" \
  "s_add_u32 m0, m0, 8192\n" \
  "v_lshl_add_u64 v[2:3], v[2:3], 0, s[46:47]\n" \
  "global_load_lds_dwordx4 v[2:3], off" WNT "\n" \
  "s_add_u32 m0, m0, 8192\n" \
  "v_lshl_add_u64 v[2:3], v[2:3], 0, s[46:47]\n" \
  "global_load_lds_dwordx4 v[2:3], off" WNT "\n" \
  "s_add_u32 m0, m0, 8192\n" \
  "v_lshl_add_u64 v[2:3], v[2:3], 0, s[46:47]\n" \
  "Ew1%=:\n" \
  "global_load_lds_dwordx4 v[2:3], off" WNT "\n" \
  "s_add_u32 m0, m0, 8192\n" \
  "v_lshl_add_u64 v[2:3], v[2:3], 0, s[46:47]\n" \
  "global_load_lds_dwordx4 v[2:3], off" WNT "\n" \
  "s_add_u32 m0, m0, 8192\n" \
  "v_lshl_add_u64 v[2:3], v[2:3], 0, s[46:47]\n" \
  "global_load_lds_dwordx4 v[2:3], off" WNT "\n" \
  "s_add_u32 m0, m0, 8192\n" \
  "v_lshl_add_u64 v[2:3], v[2:3], 0, s[46:47]\n" \
  "s_barrier\n" \
  "global_load_lds_dwordx4 v[2:3], off" WNT "\n" \
  "s_add_u32 m0, m0, 8192\n" \
  "v_lshl_add_u64 v[2:3], v[2:3], 0, s[46:47]\n" \
  "global_load_lds_dwordx4 v[2:3], off" WNT "\n" \
  "s_add_u32 m0, m0, 8192\n" \
  "v_lshl_add_u64 v[2:3], v[2:3], 0, s[46:47]\n" \
  "global_load_lds_dwordx4 v[2:3], off" WNT "\n" \
  "s_add_u32 m0, m0, 8192\n" \
  "v_lshl_add_u64 v[2:3], v[2:3], 0, s[46:47]\n" \
  "global_load_lds_dwordx4 v[2:3], off" WNT "\n" \
  "s_add_u32 m0, m0, 8192\n" \
  "v_lshl_add_u64 v[2:3], v[2:3], 0, s[46:47]\n" \
  "global_load_lds_dwordx4 v[2:3], off" WNT "\n" \
  "s_add_u32 m0, m0, 8192\n" \
  "v_lshl_add_u64 v[2:3], v[2:3], 0, s[46:47]\n" \
  "s_waitcnt vmcnt(5)\n" \
  "s_barrier\n" \
  "global_load_lds_dwordx4 v[2:3], off" WNT "\n" \
  "s_add_u32 m0, m0, 8192\n" \
  "v_lshl_add_u64 v[2:3], v[2:3], 0, s[46:47]\n" \
  "global_load_lds_dwordx4 v[2:3], off" WNT "\n" \
  "s_add_u32 m0, m0, 8192\n" \
  "v_lshl_add_u64 v[2:3], v[2:3], 0, s[46:47]\n" \
  "global_load_lds_dwordx4 v[2:3], off" WNT "\n" \
  "s_add_u32 m0, m0, 8192\n" \
  "v_lshl_add_u64 v[2:3], v[2:3], 0, s[46:47]\n" \
  "global_load_lds_dwordx4 v[2:3], off" WNT "\n" \
  "v_lshl_add_u32 v1, v11, 3, v7\n" \
  "v_lshlrev_b32 v1, 8, v1\n" \
  "v_and_b32 v4, 2, %[t]\n" \
  "v_lshl_add_u32 v1, v4, 6, v1\n" \
  "v_and_b32 v4, 1, v11\n" \
  "v_lshl_or_b32 v4, v4, 2, v7\n" \
  "v_xor_b32 v4, %[w], v4\n" \
  "v_lshl_add_u32 v1, v4, 4, v1\n" \
  "v_and_b32 v4, 1, %[t]\n" \
  "v_lshl_add_u32 v1, v4, 3, v1\n" \
  "ds_read_b64_tr_b16 v[40:41], v1 offset:0\n" \
  "ds_read_b64_tr_b16 v[42:43], v1 offset:1024\n" \
  "ds_read_b64_tr_b16 v[44:45], v1 offset:8192\n" \
  "ds_read_b64_tr_b16 v[46:47], v1 offset:9216\n" \
  "ds_read_b64_tr_b16 v[48:49], v1 offset:16384\n" \
  "ds_read_b64_tr_b16 v[50:51], v1 offset:17408\n" \
  "ds_read_b64_tr_b16 v[52:53], v1 offset:24576\n" \
  "ds_read_b64_tr_b16 v[54:55], v1 offset:25600\n" \
  "ds_read_b64_tr_b16 v[56:57], v1 offset:32768\n" \
  "ds_read_b64_tr_b16 v[58:59], v1 offset:33792\n" \
  "ds_read_b64_tr_b16 v[60:61], v1 offset:40960\n" \
  "ds_read_b64_tr_b16 v[62:63], v1 offset:41984\n" \
  "ds_read_b64_tr_b16 v[64:65], v1 offset:49152\n" \
  "ds_read_b64_tr_b16 v[66:67], v1 offset:50176\n" \
  "ds_read_b64_tr_b16 v[68:69], v1 offset:57344\n" \
  "ds_read_b64_tr_b16 v[70:71], v1 offset:58368\n" \
  "v_add_u32 v1, 0x10000, v1\n" \
  "ds_read_b64_tr_b16 v[72:73], v1\n" \
  "ds_read_b64_tr_b16 v[74:75], v1 offset:1024\n" \
  "v_add_u32 v1, 0x2000, v1\n" \
  "s_waitcnt vmcnt(0) lgkmcnt(0)\n" \
  GSTAMP6 \
  "s_barrier\n" \
  "ds_read_b64_tr_b16 v[76:77], v1 offset:0\n" \
  "ds_read_b64_tr_b16 v[78:79], v1 offset:1024\n" \
  "ds_read_b64_tr_b16 v[80:81], v1 offset:8192\n" \
  "ds_read_b64_tr_b16 v[82:83], v1 offset:9216\n" \
  "ds_read_b64_tr_b16 v[84:85], v1 offset:16384\n" \
  "ds_read_b64_tr_b16 v[86:87], v1 offset:17408\n" \
  "ds_read_b64_tr_b16 v[88:89], v1 offset:24576\n" \
  "ds_read_b64_tr_b16 v[90:91], v1 offset:25600\n" \
  "ds_read_b64_tr_b16 v[92:93], v1 offset:32768\n" \
  "ds_read_b64_tr_b16 v[94:95], v1 offset:33792\n" \
  "ds_read_b64_tr_b16 v[96:97], v1 offset:40960\n" \
  "ds_read_b64_tr_b16 v[98:99], v1 offset:41984\n" \
  "ds_read_b64_tr_b16 v[100:101], v1 offset:49152\n" \
  "ds_read_b64_tr_b16 v[102:103], v1 offset:50176\n" \
  "ds_read_b64_tr_b16 v[104:105], v1 offset:57344\n" \
  "ds_read_b64_tr_b16 v[106:107], v1 offset:58368\n" \
  "v_add_u32 v1, 0x10000, v1\n" \
  "ds_read_b64_tr_b16 v[108:109], v1\n" \
  "ds_read_b64_tr_b16 v[110:111], v1 offset:1024\n" \
  "v_add_u32 v1, 0x2000, v1\n" \
  "s_waitcnt lgkmcnt(0)\n" \
  "s_barrier\n" \
  GSTAMP7 \
  "s_branch Ek0%=\n" \
  "Ewx%=:\n" \
  "s_mov_b32 vcc_lo, 0xaaaaaaaa\n" \
  "s_mov_b32 vcc_hi, 0xaaaaaaaa\n" \
  "v_cndmask_b32 v2, v2, v4, vcc\n" \
  "v_cndmask_b32 v3, v3, v5, vcc\n" \
  "v_lshrrev_b32 v4, 1, v12\n" \
  "v_mul_u32_u24 v4, 9216, v4\n" \
  "s_lshl_b32 s51, s59, 1\n" \
  "v_add_u32 v4, s51, v4\n" \
  "v_mov_b32 v5, 0\n" \
  "v_lshl_add_u64 v[2:3], v[2:3], 0, v[4:5]\n" \
  "s_mov_b32 m0, 46080\n" \
  "v_lshlrev_b32 v1, 3, v10\n" \
  "v_lshl_add_u32 v1, v11, 8, v1\n" \
  "s_barrier\n" \
  "s_barrier\n" \
  "s_barrier\n" \
  "s_waitcnt vmcnt(" STR(CTH) ")\n" \
  "global_load_lds_dwordx4 v[2:3], off\n" \
  "s_add_u32 m0, m0, 1024\n" \
  "v_lshl_add_u64 v[2:3], v[2:3], 0, s[46:47]\n" \
  "s_waitcnt vmcnt(" STR(CTH) ")\n" \
  "global_load_lds_dwordx4 v[2:3], off\n" \
  "s_add_u32 m0, m0, 1024\n" \
  "v_lshl_add_u64 v[2:3], v[2:3], 0, s[46:47]\n" \
  "s_waitcnt vmcnt(" STR(CTH) ")\n" \
  "global_load_lds_dwordx4 v[2:3], off\n" \
  "s_add_u32 m0, m0, 1024\n" \
  "v_lshl_add_u64 v[2:3], v[2:3], 0, s[46:47]\n" \
  "s_waitcnt vmcnt(" STR(CTH) ")\n" \
  "global_load_lds_dwordx4 v[2:3], off\n" \
  "s_add_u32 m0, m0, 1024\n" \
  "v_lshl_add_u64 v[2:3], v[2:3], 0, s[46:47]\n" \
  "s_waitcnt vmcnt(" STR(CTH) ")\n" \
  "global_load_lds_dwordx4 v[2:3], off\n" \
  "s_add_u32 m0, m0, 1024\n" \
  "v_lshl_add_u64 v[2:3], v[2:3], 0, s[46:47]\n" \
  "s_waitcnt vmcnt(" STR(CTH) ")\n" \
  "global_load_lds_dwordx4 v[2:3], off\n" \
  "s_add_u32 m0, m0, 1024\n" \
  "v_lshl_add_u64 v[2:3], v[2:3], 0, s[46:47]\n" \
  "s_waitcnt vmcnt(" STR(CTH) ")\n" \
  "global_load_lds_dwordx4 v[2:3], off\n" \
  "s_add_u32 m0, m0, 1024\n" \
  "v_lshl_add_u64 v[2:3], v[2:3], 0, s[46:47]\n" \
  "s_waitcnt vmcnt(" STR(CTH) ")\n" \
  "global_load_lds_dwordx4 v[2:3], off\n" \
  "s_add_u32 m0, m0, 1024\n" \
  "v_lshl_add_u64 v[2:3], v[2:3], 0, s[46:47]\n" \
  "s_waitcnt vmcnt(" STR(CTH) ")\n" \
  "global_load_lds_dwordx4 v[2:3], off\n" \
  "s_add_u32 m0, m0, 1024\n" \
  "v_lshl_add_u64 v[2:3], v[2:3], 0, s[46:47]\n" \
  "s_barrier\n" \
  "s_waitcnt vmcnt(" STR(CTH) ")\n" \
  "global_load_lds_dwordx4 v[2:3], off\n" \
  "s_add_u32 m0, m0, 1024\n" \
  "v_lshl_add_u64 v[2:3], v[2:3], 0, s[46:47]\n" \
  "s_waitcnt vmcnt(" STR(CTH) ")\n" \
  "global_load_lds_dwordx4 v[2:3], off\n" \
  "s_add_u32 m0, m0, 1024\n" \
  "v_lshl_add_u64 v[2:3], v[2:3], 0, s[46:47]\n" \
  "s_waitcnt vmcnt(" STR(CTH) ")\n" \
  "global_load_lds_dwordx4 v[2:3], off\n" \
  "s_add_u32 m0, m0, 1024\n" \
  "v_lshl_add_u64 v[2:3], v[2:3], 0, s[46:47]\n" \
  "s_waitcnt vmcnt(" STR(CTH) ")\n" \
  "global_load_lds_dwordx4 v[2:3], off\n" \
  "s_add_u32 m0, m0, 1024\n" \
  "v_lshl_add_u64 v[2:3], v[2:3], 0, s[46:47]\n" \
  "s_waitcnt vmcnt(" STR(CTH) ")\n" \
  "global_load_lds_dwordx4 v[2:3], off\n" \
  "s_add_u32 m0, m0, 1024\n" \
  "v_lshl_add_u64 v[2:3], v[2:3], 0, s[46:47]\n" \
  "s_waitcnt vmcnt(" STR(CTH) ")\n" \
  "global_load_lds_dwordx4 v[2:3], off\n" \
  "s_add_u32 m0, m0, 1024\n" \
  "v_lshl_add_u64 v[2:3], v[2:3], 0, s[46:47]\n" \
  "s_waitcnt vmcnt(" STR(CTH) ")\n" \
  "global_load_lds_dwordx4 v[2:3], off\n" \
  "s_add_u32 m0, m0, 1024\n" \
  "v_lshl_add_u64 v[2:3], v[2:3], 0, s[46:47]\n" \
  "s_waitcnt vmcnt(" STR(CTH) ")\n" \
  "global_load_lds_dwordx4 v[2:3], off\n" \
  "s_add_u32 m0, m0, 1024\n" \
  "v_lshl_add_u64 v[2:3], v[2:3], 0, s[46:47]\n" \
  "s_waitcnt vmcnt(" STR(CTH) ")\n" \
  "global_load_lds_dwordx4 v[2:3], off\n" \
  "s_add_u32 m0, m0, 1024\n" \
  "v_lshl_add_u64 v[2:3], v[2:3], 0, s[46:47]\n" \
  GSTAMP6 \
  "s_waitcnt vmcnt(17)\n" \
  "ds_read_b64_tr_b16 v[40:41], v1 offset:46080\n" \
  "ds_read_b64_tr_b16 v[42:43], v1 offset:46208\n" \
  "s_waitcnt vmcnt(16)\n" \
  "ds_read_b64_tr_b16 v[44:45], v1 offset:47104\n" \
  "ds_read_b64_tr_b16 v[46:47], v1 offset:47232\n" \
  "s_waitcnt vmcnt(15)\n" \
  "ds_read_b64_tr_b16 v[48:49], v1 offset:48128\n" \
  "ds_read_b64_tr_b16 v[50:51], v1 offset:48256\n" \
  "s_waitcnt vmcnt(14)\n" \
  "ds_read_b64_tr_b16 v[52:53], v1 offset:49152\n" \
  "ds_read_b64_tr_b16 v[54:55], v1 offset:49280\n" \
  "s_waitcnt vmcnt(13)\n" \
  "ds_read_b64_tr_b16 v[56:57], v1 offset:50176\n" \
  "ds_read_b64_tr_b16 v[58:59], v1 offset:50304\n" \
  "s_waitcnt vmcnt(12)\n" \
  "ds_read_b64_tr_b16 v[60:61], v1 offset:51200\n" \
  "ds_read_b64_tr_b16 v[62:63], v1 offset:51328\n" \
  "s_waitcnt vmcnt(11)\n" \
  "ds_read_b64_tr_b16 v[64:65], v1 offset:52224\n" \
  "ds_read_b64_tr_b16 v[66:67], v1 offset:52352\n" \
  "s_waitcnt vmcnt(10)\n" \
  "ds_read_b64_tr_b16 v[68:69], v1 offset:53248\n" \
  "ds_read_b64_tr_b16 v[70:71], v1 offset:53376\n" \
  "s_waitcnt vmcnt(9)\n" \
  "ds_read_b64_tr_b16 v[72:73], v1 offset:54272\n" \
  "ds_read_b64_tr_b16 v[74:75], v1 offset:54400\n" \
  "s_waitcnt vmcnt(8)\n" \
  "ds_read_b64_tr_b16 v[76:77], v1 offset:55296\n" \
  "ds_read_b64_tr_b16 v[78:79], v1 offset:55424\n" \
  "s_waitcnt vmcnt(7)\n" \
  "ds_read_b64_tr_b16 v[80:81], v1 offset:56320\n" \
  "ds_read_b64_tr_b16 v[82:83], v1 offset:56448\n" \
  "s_waitcnt vmcnt(6)\n" \
  "ds_read_b64_tr_b16 v[84:85], v1 offset:57344\n" \
  "ds_read_b64_tr_b16 v[86:87], v1 offset:57472\n" \
  "s_waitcnt vmcnt(5)\n" \
  "ds_read_b64_tr_b16 v[88:89], v1 offset:58368\n" \
  "ds_read_b64_tr_b16 v[90:91], v1 offset:58496\n" \
  "s_waitcnt vmcnt(4)\n" \
  "ds_read_b64_tr_b16 v[92:93], v1 offset:59392\n" \
  "ds_read_b64_tr_b16 v[94:95], v1 offset:59520\n" \
  "s_waitcnt vmcnt(3)\n" \
  "ds_read_b64_tr_b16 v[96:97], v1 offset:60416\n" \
  "ds_read_b64_tr_b16 v[98:99], v1 offset:60544\n" \
  "s_waitcnt vmcnt(2)\n" \
  "ds_read_b64_tr_b16 v[100:101], v1 offset:61440\n" \
  "ds_read_b64_tr_b16 v[102:103], v1 offset:61568\n" \
  "s_waitcnt vmcnt(1)\n" \
  "ds_read_b64_tr_b16 v[104:105], v1 offset:62464\n" \
  "ds_read_b64_tr_b16 v[106:107], v1 offset:62592\n" \
  "s_waitcnt vmcnt(0)\n" \
  "ds_read_b64_tr_b16 v[108:109], v1 offset:63488\n" \
  "ds_read_b64_tr_b16 v[110:111], v1 offset:63616\n" \
  GSTAMP7 \
  "v_mul_u32_u24 v1, 1184, v10\n" \
  "v_lshl_add_u32 v1, v11, 4, v1\n" \
  "v_add_u32 v1, 120832, v1\n" \
  "v_lshrrev_b32 v2, 3, v12\n" \
  "v_mul_u32_u24 v2, 320, v2\n" \
  "v_and_b32 v4, 7, %[t]\n" \
  "v_lshl_add_u32 v2, v4, 1, v2\n" \
  "s_lshl4_add_u32 s46, %[w], 141312\n" \
  "v_add_u32 v2, s46, v2\n" \
  "s_barrier\n" \
  "s_barrier\n" \
  "s_barrier\n" \
  "s_branch Eg0%=\n" \
  "Ewd%=:\n" \
  "s_cmp_eq_u32 %[w], 15\n" \
  "s_cbranch_scc1 Ewz%=\n" \
  "s_sub_u32 s56, %[w], 9\n" \
  "s_mul_i32 s51, s56, 192\n" \
  "s_mul_i32 s46, s58, 1152\n" \
  "s_add_u32 s51, s51, s46\n" \
  "s_sub_u32 s47, s59, s58\n" \
  "s_mul_i32 s47, s47, 1152\n" \
  "s_sub_u32 s47, s47, 55296\n" \
  "v_add_u32 v4, 0, v12\n" \
  "v_mul_u32_u24 v5, 171, v4\n" \
  "v_lshrrev_b32 v5, 11, v5\n" \
  "v_lshrrev_b32 v6, 2, v5\n" \
  "v_and_b32 v6, 2, v6\n" \
  "v_xor_b32 v4, v4, v6\n" \
  "v_mul_u32_u24 v5, 960, v5\n" \
  "v_lshl_add_u32 v4, v4, 4, v5\n" \
  "v_add_u32 v13, s51, v4\n" \
  "v_add_u32 v4, 64, v12\n" \
  "v_mul_u32_u24 v5, 171, v4\n" \
  "v_lshrrev_b32 v5, 11, v5\n" \
  "v_lshrrev_b32 v6, 2, v5\n" \
  "v_and_b32 v6, 2, v6\n" \
  "v_xor_b32 v4, v4, v6\n" \
  "v_mul_u32_u24 v5, 960, v5\n" \
  "v_lshl_add_u32 v4, v4, 4, v5\n" \
  "v_add_u32 v14, s51, v4\n" \
  "v_add_u32 v4, 128, v12\n" \
  "v_mul_u32_u24 v5, 171, v4\n" \
  "v_lshrrev_b32 v5, 11, v5\n" \
  "v_lshrrev_b32 v6, 2, v5\n" \
  "v_and_b32 v6, 2, v6\n" \
  "v_xor_b32 v4, v4, v6\n" \
  "v_mul_u32_u24 v5, 960, v5\n" \
  "v_lshl_add_u32 v4, v4, 4, v5\n" \
  "v_add_u32 v15, s51, v4\n" \
  "s_mul_i32 s51, s56, 15360\n" \
  "s_cmp_ge_u32 s56, 3\n" \
  "s_cselect_b32 s46, 27648, 0\n" \
  "s_add_u32 s51, s51, s46\n" \
  "s_mov_b32 m0, s51\n" \
  "v_and_b32 v4, 1, v11\n" \
  "v_bfe_u32 v7, %[t], 2, 2\n" \
  "v_mul_u32_u24 v7, 192, v7\n" \
  "v_lshrrev_b32 v5, 1, v11\n" \
  "v_and_b32 v6, 3, %[t]\n" \
  "v_lshl_add_u32 v7, v6, 3, v7\n" \
  "v_add_u32 v7, s51, v7\n" \
  "v_lshl_add_u32 v2, v5, 5, v7\n" \
  "v_xor_b32 v5, v5, v4\n" \
  "v_lshl_add_u32 v1, v5, 5, v7\n" \
  "v_mul_u32_u24 v5, 1536, v4\n" \
  "v_add_u32 v1, v1, v5\n" \
  "s_barrier\n" \
  "s_barrier\n" \
  "s_barrier\n" \
  "s_barrier\n" \
  "s_waitcnt vmcnt(" STR(DTH) ")\n" \
  "global_load_lds_dwordx4 v13, s[48:49]" DNTF "\n" \
  "s_add_u32 m0, m0, 1024\n" \
  "v_add_u32 v13, 18432, v13\n" \
  "s_waitcnt vmcnt(" STR(DTH) ")\n" \
  "global_load_lds_dwordx4 v14, s[48:49]" DNTF "\n" \
  "s_add_u32 m0, m0, 1024\n" \
  "v_add_u32 v14, 18432, v14\n" \
  "s_waitcnt vmcnt(" STR(DTH) ")\n" \
  "global_load_lds_dwordx4 v15, s[48:49]" DNTF "\n" \
  "s_add_u32 m0, m0, 1024\n" \
  "v_add_u32 v15, 18432, v15\n" \
  "s_waitcnt vmcnt(" STR(DTH) ")\n" \
  "global_load_lds_dwordx4 v13, s[48:49]" DNTF "\n" \
  "s_add_u32 m0, m0, 1024\n" \
  "v_add_u32 v13, 18432, v13\n" \
  "s_waitcnt vmcnt(" STR(DTH) ")\n" \
  "global_load_lds_dwordx4 v14, s[48:49]" DNTF "\n" \
  "s_add_u32 m0, m0, 1024\n" \
  "v_add_u32 v14, 18432, v14\n" \
  "s_waitcnt vmcnt(" STR(DTH) ")\n" \
  "global_load_lds_dwordx4 v15, s[48:49]" DNTF "\n" \
  "s_add_u32 m0, m0, 1024\n" \
  "v_add_u32 v15, 18432, v15\n" \
  "s_waitcnt vmcnt(" STR(DTH) ")\n" \
  "global_load_lds_dwordx4 v13, s[48:49]" DNTF "\n" \
  "s_add_u32 m0, m0, 1024\n" \
  "v_add_u32 v13, 18432, v13\n" \
  "s_waitcnt vmcnt(" STR(DTH) ")\n" \
  "global_load_lds_dwordx4 v14, s[48:49]" DNTF "\n" \
  "s_add_u32 m0, m0, 1024\n" \
  "v_add_u32 v14, 18432, v14\n" \
  "s_waitcnt vmcnt(" STR(DTH) ")\n" \
  "global_load_lds_dwordx4 v15, s[48:49]" DNTF "\n" \
  "s_add_u32 m0, m0, 1024\n" \
  "v_add_u32 v15, 18432, v15\n" \
  "s_waitcnt vmcnt(" STR(DTH) ")\n" \
  "global_load_lds_dwordx4 v13, s[48:49]" DNTF "\n" \
  "s_add_u32 m0, m0, 1024\n" \
  "v_add_u32 v13, s47, v13\n" \
  "s_waitcnt vmcnt(" STR(DTH) ")\n" \
  "global_load_lds_dwordx4 v14, s[48:49]" DNTF "\n" \
  "s_add_u32 m0, m0, 1024\n" \
  "v_add_u32 v14, s47, v14\n" \
  "s_waitcnt vmcnt(" STR(DTH) ")\n" \
  "global_load_lds_dwordx4 v15, s[48:49]" DNTF "\n" \
  "s_add_u32 m0, m0, 1024\n" \
  "v_add_u32 v15, s47, v15\n" \
  "s_waitcnt vmcnt(" STR(DTH) ")\n" \
  "global_load_lds_dwordx4 v13, s[48:49]" DNTF "\n" \
  "s_add_u32 m0, m0, 1024\n" \
  "v_add_u32 v13, s47, v13\n" \
  "s_mov_b32 exec_hi, 0\n" \
  "s_waitcnt vmcnt(" STR(DTH) ")\n" \
  "global_load_lds_dwordx4 v14, s[48:49]" DNTF "\n" \
  "s_add_u32 m0, m0, 1024\n" \
  "v_add_u32 v14, s47, v14\n" \
  "s_mov_b64 exec, -1\n" \
  GSTAMP6 \
  "s_barrier\n" \
  "s_barrier\n" \
  "s_barrier\n" \
  "s_waitcnt vmcnt(11)\n" \
  "ds_read_b64_tr_b16 v[40:41], v1 offset:0\n" \
  "ds_read_b64_tr_b16 v[42:43], v1 offset:768\n" \
  "ds_read_b64_tr_b16 v[44:45], v1 offset:64\n" \
  "ds_read_b64_tr_b16 v[46:47], v1 offset:832\n" \
  "ds_read_b64_tr_b16 v[48:49], v1 offset:128\n" \
  "ds_read_b64_tr_b16 v[50:51], v1 offset:896\n" \
  "s_waitcnt vmcnt(8)\n" \
  "ds_read_b64_tr_b16 v[52:53], v1 offset:3072\n" \
  "ds_read_b64_tr_b16 v[54:55], v1 offset:3840\n" \
  "ds_read_b64_tr_b16 v[56:57], v1 offset:3136\n" \
  "ds_read_b64_tr_b16 v[58:59], v1 offset:3904\n" \
  "ds_read_b64_tr_b16 v[60:61], v1 offset:3200\n" \
  "ds_read_b64_tr_b16 v[62:63], v1 offset:3968\n" \
  "s_waitcnt vmcnt(5)\n" \
  "ds_read_b64_tr_b16 v[64:65], v1 offset:6144\n" \
  "ds_read_b64_tr_b16 v[66:67], v1 offset:6912\n" \
  "ds_read_b64_tr_b16 v[68:69], v1 offset:6208\n" \
  "ds_read_b64_tr_b16 v[70:71], v1 offset:6976\n" \
  "ds_read_b64_tr_b16 v[72:73], v1 offset:6272\n" \
  "ds_read_b64_tr_b16 v[74:75], v1 offset:7040\n" \
  "s_waitcnt vmcnt(2)\n" \
  "ds_read_b64_tr_b16 v[76:77], v1 offset:9216\n" \
  "ds_read_b64_tr_b16 v[78:79], v1 offset:9984\n" \
  "ds_read_b64_tr_b16 v[80:81], v1 offset:9280\n" \
  "ds_read_b64_tr_b16 v[82:83], v1 offset:10048\n" \
  "ds_read_b64_tr_b16 v[84:85], v1 offset:9344\n" \
  "ds_read_b64_tr_b16 v[86:87], v1 offset:10112\n" \
  "s_waitcnt vmcnt(0)\n" \
  "ds_read_b64_tr_b16 v[88:89], v2 offset:12288\n" \
  "ds_read_b64_tr_b16 v[90:91], v2 offset:13056\n" \
  "ds_read_b64_tr_b16 v[92:93], v2 offset:12352\n" \
  "ds_read_b64_tr_b16 v[94:95], v2 offset:13120\n" \
  "ds_read_b64_tr_b16 v[96:97], v2 offset:12416\n" \
  "ds_read_b64_tr_b16 v[98:99], v2 offset:13184\n" \
  "s_lshl_b32 s51, s56, 5\n" \
  "v_lshrrev_b32 v5, 1, v11\n" \
  "v_lshl_add_u32 v5, v5, 4, v10\n" \
  "v_add_u32 v5, s51, v5\n" \
  "v_mul_u32_u24 v6, 171, v5\n" \
  "v_lshrrev_b32 v6, 11, v6\n" \
  "v_mul_u32_u24 v6, 12096, v6\n" \
  "v_lshl_add_u32 v6, v5, 4, v6\n" \
  "v_mul_u32_u24 v4, 393216, v4\n" \
  "s_mul_i32 s51, s57, 192\n" \
  "v_add3_u32 v38, v6, v4, s51\n" \
  "s_mov_b32 s86, 6455296\n" \
  "s_mov_b32 s87, 6651904\n" \
  "v_mov_b32 v31, s44\n" \
  "v_mov_b32 v35, s44\n" \
  "v_mov_b32 v3, 159488\n" \
  GSTAMP7 \
  "s_barrier\n" \
  "s_branch Ed0%=\n" \
  "Ewz%=:\n" \
  "v_mov_b32 v4, 147456\n" \
  "v_lshl_add_u32 v4, v12, 4, v4\n" \
  "v_mov_b64 v[16:17], 0\n" \
  "v_mov_b64 v[18:19], 0\n" \
  "ds_write_b128 v4, v[16:19] offset:0\n" \
  "ds_write_b128 v4, v[16:19] offset:1024\n" \
  "ds_write_b128 v4, v[16:19] offset:2048\n" \
  "ds_write_b128 v4, v[16:19] offset:3072\n" \
  "ds_write_b128 v4, v[16:19] offset:4096\n" \
  "ds_write_b128 v4, v[16:19] offset:5120\n" \
  "ds_write_b128 v4, v[16:19] offset:6144\n" \
  "ds_write_b128 v4, v[16:19] offset:7168\n" \
  "ds_write_b128 v4, v[16:19] offset:8192\n" \
  "s_barrier\n" \
  "s_barrier\n" \
  "s_barrier\n" \
  "s_barrier\n" \
  "v_add_u32 v4, -6144, v4\n" \
  "ds_write_b128 v4, v[16:19] offset:0\n" \
  "ds_write_b128 v4, v[16:19] offset:1024\n" \
  "ds_write_b128 v4, v[16:19] offset:2048\n" \
  "v_mov_b32 v3, 159488\n" \
  "v_mov_b32 v13, 160800\n" \
  "s_waitcnt lgkmcnt(0)\n" \
  "s_barrier\n" \
  "s_barrier\n" \
  "s_barrier\n" \
  "s_branch Eh1%=\n" \
  "Ek0%=:\n" \
  "v_lshlrev_b32 v1, 4, %[t]\n" \
  "s_and_b32 s51, %[b], 7\n" \
  "s_mul_i32 s51, s51, 16512\n" \
  "s_add_u32 s51, s51, 10403840\n" \
  "s_add_u32 s52, s51, 8192\n" \
  "v_and_b32 v2, 7, %[t]\n" \
  "v_mov_b32 v3, 159488\n" \
  "v_lshl_add_u32 v4, v2, 2, v3\n" \
  "v_and_b32 v5, 3, %[t]\n" \
  "v_lshl_add_u32 v5, v5, 4, v3\n" \
  "v_mov_b32 v6, 1\n" \
  "v_mov_b32 v7, 147456\n" \
  "v_lshrrev_b32 v8, 3, %[t]\n" \
  "v_lshl_add_u32 v8, v8, 2, v7\n" \
  "v_lshl_add_u32 v9, v2, 4, v7\n" \
  "s_lshl_b32 s56, %[w], 3\n" \
  "v_mov_b32 v13, 160800\n" \
  "v_lshrrev_b32 v14, 5, %[t]\n" \
  "v_and_b32 v15, 31, %[t]\n" \
  "v_mul_u32_u24 v36, 1184, v14\n" \
  "v_mad_u32_u24 v36, v15, 12, v36\n" \
  "v_add_u32 v36, 120832, v36\n" \
  "v_lshl_add_u32 v14, v14, 2, v13\n" \
  "v_lshlrev_b32 v15, 4, v15\n" \
  "v_add_u32 v15, 6453760, v15\n" \
  "s_mov_b32 s45, -1536\n" \
  "s_movk_i32 s97, 128\n" \
  "Ekt%=:\n" \
  "s_memtime s[46:47]\n" \
  "s_waitcnt lgkmcnt(0)\n" \
  "s_sub_u32 s46, s46, s92\n" \
  "s_cmp_gt_u32 s46, " STR(KT) "\n" \
  "s_cbranch_scc1 Eku%=\n" \
  "s_sleep 2\n" \
  "s_sub_u32 s97, s97, 1\n" \
  "s_cbranch_scc0 Ekt%=\n" \
  "Eku%=:\n" \
  "s_setprio " STR(KPR) "\n" \
  "s_movk_i32 s97, 0x7fff\n" \
  "Ekar%=:\n" \
  "buffer_load_dwordx4 v[20:23], v1, s[40:43], s51 offen sc1\n" \
  "s_waitcnt vmcnt(0)\n" \
  "v_bitop3_b32 v28, v21, v23, s44 bitop3:0x7e\n" \
  "v_cmp_ne_u32 vcc, 0, v28\n" \
  "s_cbranch_vccz Ekax%=\n" \
  "s_sleep " STR(KSL0) "\n" \
  "s_sub_u32 s97, s97, 1\n" \
  "s_cbranch_scc0 Ekar%=\n" \
  "Ekax%=:\n" \
  GSTAMP3 \
  ".if " STR(KD2) "\n" \
  "s_sleep " STR(KD2) "\n" \
  ".endif\n" \
  "buffer_load_dwordx4 v[24:27], v1, s[40:43], s52 offen sc1\n" \
  "ds_max_u32 v4, v20 offset:96\n" \
  "ds_max_u32 v4, v22 offset:128\n" \
  "s_waitcnt vmcnt(0)\n" \
  "v_bitop3_b32 v28, v25, v27, s44 bitop3:0x7e\n" \
  "v_cmp_ne_u32 vcc, 0, v28\n" \
  "s_cbranch_vccz Ekbx%=\n" \
  "s_movk_i32 s97, 0x7fff\n" \
  "Ekbr%=:\n" \
  "buffer_load_dwordx4 v[24:27], v1, s[40:43], s52 offen sc1\n" \
  GSTAMPK \
  "s_waitcnt vmcnt(0)\n" \
  "v_bitop3_b32 v28, v25, v27, s44 bitop3:0x7e\n" \
  "v_cmp_ne_u32 vcc, 0, v28\n" \
  "s_cbranch_vccz Ekbx%=\n" \
  "s_sleep " STR(KSLP) "\n" \
  "s_sub_u32 s97, s97, 1\n" \
  "s_cbranch_scc0 Ekbr%=\n" \
  "Ekbx%=:\n" \
  GSTAMP8 \
  "ds_max_u32 v4, v24 offset:96\n" \
  "ds_max_u32 v4, v26 offset:128\n" \
  "s_waitcnt lgkmcnt(0)\n" \
  "s_barrier\n" \
  GSTAMP2 \
  "ds_read_b128 v[28:31], v5 offset:96\n" \
  "s_waitcnt lgkmcnt(0)\n" \
  "v_min3_u32 v28, v28, v29, v30\n" \
  "v_min_u32 v28, v28, v31\n" \
  "s_nop 1\n" \
  "v_min_u32_dpp v28, v28, v28 quad_perm:[1,0,3,2] row_mask:0xf bank_mask:0xf\n" \
  "s_nop 1\n" \
  "v_min_u32_dpp v28, v28, v28 quad_perm:[2,3,0,1] row_mask:0xf bank_mask:0xf\n" \
  "v_cmp_ge_u32 s[88:89], v20, v28\n" \
  "v_cmp_ge_u32 s[90:91], v22, v28\n" \
  "v_cmp_ge_u32 s[94:95], v24, v28\n" \
  "v_cmp_ge_u32 s[96:97], v26, v28\n" \
  "s_mov_b64 exec, s[88:89]\n" \
  "ds_add_rtn_u32 v21, v3, v6 offset:24\n" \
  "s_mov_b64 exec, s[90:91]\n" \
  "ds_add_rtn_u32 v23, v3, v6 offset:24\n" \
  "s_mov_b64 exec, s[94:95]\n" \
  "ds_add_rtn_u32 v25, v3, v6 offset:24\n" \
  "s_mov_b64 exec, s[96:97]\n" \
  "ds_add_rtn_u32 v27, v3, v6 offset:24\n" \
  "s_waitcnt lgkmcnt(0)\n" \
  "v_lshl_add_u32 v27, v27, 2, v7\n" \
  "ds_write_b32 v27, v26\n" \
  "s_mov_b64 exec, s[94:95]\n" \
  "v_lshl_add_u32 v25, v25, 2, v7\n" \
  "ds_write_b32 v25, v24\n" \
  "s_mov_b64 exec, s[90:91]\n" \
  "v_lshl_add_u32 v23, v23, 2, v7\n" \
  "ds_write_b32 v23, v22\n" \
  "s_mov_b64 exec, s[88:89]\n" \
  "v_lshl_add_u32 v21, v21, 2, v7\n" \
  "ds_write_b32 v21, v20\n" \
  "s_mov_b64 exec, -1\n" \
  "s_waitcnt lgkmcnt(0)\n" \
  "s_barrier\n" \
  GSTAMP4 \
  "ds_read_b32 v18, v3 offset:24\n" \
  "Ec1%=:\n" \
  "ds_read_b32 v17, v8\n" \
  "ds_read_b128 v[20:23], v9\n" \
  "ds_read_b128 v[24:27], v9 offset:128\n" \
  "v_mov_b32 v16, 0\n" \
  "s_waitcnt lgkmcnt(3)\n" \
  "v_readfirstlane_b32 s46, v18\n" \
  "s_cmp_ge_u32 s56, s46\n" \
  "s_cbranch_scc1 Ec4%=\n" \
  "s_mov_b32 s47, 64\n" \
  "v_mov_b32 v19, v9\n" \
  "Ec2%=:\n" \
  "s_waitcnt lgkmcnt(0)\n" \
  "v_cmp_gt_u32 s[88:89], v20, v17\n" \
  "v_cmp_gt_u32 s[90:91], v21, v17\n" \
  "v_cmp_gt_u32 s[94:95], v22, v17\n" \
  "v_cmp_gt_u32 s[96:97], v23, v17\n" \
  "v_addc_co_u32 v16, vcc, 0, v16, s[88:89]\n" \
  "v_addc_co_u32 v16, vcc, 0, v16, s[90:91]\n" \
  "v_addc_co_u32 v16, vcc, 0, v16, s[94:95]\n" \
  "v_addc_co_u32 v16, vcc, 0, v16, s[96:97]\n" \
  "v_cmp_gt_u32 s[88:89], v24, v17\n" \
  "v_cmp_gt_u32 s[90:91], v25, v17\n" \
  "v_cmp_gt_u32 s[94:95], v26, v17\n" \
  "v_cmp_gt_u32 s[96:97], v27, v17\n" \
  "v_addc_co_u32 v16, vcc, 0, v16, s[88:89]\n" \
  "v_addc_co_u32 v16, vcc, 0, v16, s[90:91]\n" \
  "v_addc_co_u32 v16, vcc, 0, v16, s[94:95]\n" \
  "v_addc_co_u32 v16, vcc, 0, v16, s[96:97]\n" \
  "s_cmp_ge_u32 s47, s46\n" \
  "s_cbranch_scc1 Ec3%=\n" \
  "s_add_u32 s47, s47, 64\n" \
  "v_add_u32 v19, 256, v19\n" \
  "ds_read_b128 v[20:23], v19\n" \
  "ds_read_b128 v[24:27], v19 offset:128\n" \
  "s_branch Ec2%=\n" \
  "Ec3%=:\n" \
  "v_add_u32_dpp v16, v16, v16 quad_perm:[1,0,3,2] row_mask:0xf bank_mask:0xf\n" \
  "s_nop 1\n" \
  "v_add_u32_dpp v16, v16, v16 quad_perm:[2,3,0,1] row_mask:0xf bank_mask:0xf\n" \
  "s_addk_i32 s56, 64\n" \
  "s_cmp_lt_u32 s56, s46\n" \
  "v_add_u32_dpp v16, v16, v16 row_half_mirror row_mask:0xf bank_mask:0xf\n" \
  "v_min_u32 v16, 16, v16\n" \
  "v_lshl_add_u32 v16, v16, 2, v13\n" \
  "ds_write_b32 v16, v17\n" \
  "s_cbranch_scc0 Ec4%=\n" \
  "v_add_u32 v8, 256, v8\n" \
  "s_branch Ec1%=\n" \
  "Ec4%=:\n" \
  "s_waitcnt lgkmcnt(0)\n" \
  "s_barrier\n" \
  GSTAMP9 \
  "ds_read_b32 v16, v14\n" \
  "s_waitcnt lgkmcnt(0)\n" \
  "v_and_b32 v16, 0x7ff, v16\n" \
  "v_mad_i32_i24 v15, v16, s45, v15\n" \
  "s_movk_i32 s97, 0x7fff\n" \
  "Ehr%=:\n" \
  "buffer_load_dwordx4 v[20:23], v15, s[40:43], 0 offen offset:0 sc1\n" \
  "buffer_load_dwordx4 v[24:27], v15, s[40:43], 0 offen offset:512 sc1\n" \
  "buffer_load_dwordx4 v[28:31], v15, s[40:43], 0 offen offset:1024 sc1\n" \
  "v_mul_u32_u24 v1, 1184, v10\n" \
  "v_lshl_add_u32 v1, v11, 4, v1\n" \
  "v_add_u32 v1, 120832, v1\n" \
  "v_lshrrev_b32 v2, 3, v12\n" \
  "v_mul_u32_u24 v2, 320, v2\n" \
  "v_and_b32 v4, 7, %[t]\n" \
  "v_lshl_add_u32 v2, v4, 1, v2\n" \
  "s_lshl4_add_u32 s46, %[w], 141312\n" \
  "v_add_u32 v2, s46, v2\n" \
  "s_waitcnt vmcnt(0)\n" \
  "v_bitop3_b32 v16, v23, v27, s44 bitop3:0x7e\n" \
  "v_xor_b32 v31, s44, v31\n" \
  "v_or_b32 v16, v16, v31\n" \
  "v_cmp_ne_u32 vcc, 0, v16\n" \
  "s_cbranch_vccz Ehx%=\n" \
  "s_sub_u32 s97, s97, 1\n" \
  "s_cbranch_scc0 Ehr%=\n" \
  "Ehx%=:\n" \
  GSTAMP10 \
  "ds_write2_b32 v36, v20, v24 offset0:0 offset1:96\n" \
  "ds_write2_b32 v36, v21, v25 offset0:1 offset1:97\n" \
  "ds_write2_b32 v36, v22, v26 offset0:2 offset1:98\n" \
  "ds_write2_b32 v36, v28, v29 offset0:192 offset1:193\n" \
  "ds_write_b32 v36, v30 offset:776\n" \
  "Eg0%=:\n" \
  "s_waitcnt lgkmcnt(0)\n" \
  "s_barrier\n" \
  GSTAMP5 \
  "ds_read_b128 v[112:115], v1 offset:0\n" \
  "ds_read_b128 v[116:119], v1 offset:64\n" \
  "ds_read_b128 v[120:123], v1 offset:128\n" \
  "ds_read_b128 v[124:127], v1 offset:192\n" \
  "ds_read_b128 v[20:23], v1 offset:256\n" \
  "ds_read_b128 v[24:27], v1 offset:320\n" \
  "ds_read_b128 v[28:31], v1 offset:384\n" \
  "ds_read_b128 v[32:35], v1 offset:448\n" \
  "s_waitcnt lgkmcnt(7)\n" \
  "v_mfma_f32_16x16x32_bf16 v[16:19], v[112:115], v[40:43], 0\n" \
  "ds_read_b128 v[112:115], v1 offset:512\n" \
  "s_waitcnt lgkmcnt(7)\n" \
  "v_mfma_f32_16x16x32_bf16 v[16:19], v[116:119], v[44:47], v[16:19]\n" \
  "ds_read_b128 v[116:119], v1 offset:576\n" \
  "s_waitcnt lgkmcnt(7)\n" \
  "v_mfma_f32_16x16x32_bf16 v[16:19], v[120:123], v[48:51], v[16:19]\n" \
  "ds_read_b128 v[120:123], v1 offset:640\n" \
  "s_waitcnt lgkmcnt(7)\n" \
  "v_mfma_f32_16x16x32_bf16 v[16:19], v[124:127], v[52:55], v[16:19]\n" \
  "ds_read_b128 v[124:127], v1 offset:704\n" \
  "s_waitcnt lgkmcnt(7)\n" \
  "v_mfma_f32_16x16x32_bf16 v[16:19], v[20:23], v[56:59], v[16:19]\n" \
  "ds_read_b128 v[20:23], v1 offset:768\n" \
  "s_waitcnt lgkmcnt(7)\n" \
  "v_mfma_f32_16x16x32_bf16 v[16:19], v[24:27], v[60:63], v[16:19]\n" \
  "ds_read_b128 v[24:27], v1 offset:832\n" \
  "s_waitcnt lgkmcnt(7)\n" \
  "v_mfma_f32_16x16x32_bf16 v[16:19], v[28:31], v[64:67], v[16:19]\n" \
  "ds_read_b128 v[28:31], v1 offset:896\n" \
  "s_waitcnt lgkmcnt(7)\n" \
  "v_mfma_f32_16x16x32_bf16 v[16:19], v[32:35], v[68:71], v[16:19]\n" \
  "ds_read_b128 v[32:35], v1 offset:960\n" \
  "s_waitcnt lgkmcnt(7)\n" \
  "v_mfma_f32_16x16x32_bf16 v[16:19], v[112:115], v[72:75], v[16:19]\n" \
  "ds_read_b128 v[112:115], v1 offset:1024\n" \
  "s_waitcnt lgkmcnt(7)\n" \
  "v_mfma_f32_16x16x32_bf16 v[16:19], v[116:119], v[76:79], v[16:19]\n" \
  "ds_read_b128 v[116:119], v1 offset:1088\n" \
  "s_waitcnt lgkmcnt(7)\n" \
  "v_mfma_f32_16x16x32_bf16 v[16:19], v[120:123], v[80:83], v[16:19]\n" \
  "s_waitcnt lgkmcnt(6)\n" \
  "v_mfma_f32_16x16x32_bf16 v[16:19], v[124:127], v[84:87], v[16:19]\n" \
  "s_waitcnt lgkmcnt(5)\n" \
  "v_mfma_f32_16x16x32_bf16 v[16:19], v[20:23], v[88:91], v[16:19]\n" \
  "s_waitcnt lgkmcnt(4)\n" \
  "v_mfma_f32_16x16x32_bf16 v[16:19], v[24:27], v[92:95], v[16:19]\n" \
  "s_waitcnt lgkmcnt(3)\n" \
  "v_mfma_f32_16x16x32_bf16 v[16:19], v[28:31], v[96:99], v[16:19]\n" \
  "s_waitcnt lgkmcnt(2)\n" \
  "v_mfma_f32_16x16x32_bf16 v[16:19], v[32:35], v[100:103], v[16:19]\n" \
  "s_waitcnt lgkmcnt(1)\n" \
  "v_mfma_f32_16x16x32_bf16 v[16:19], v[112:115], v[104:107], v[16:19]\n" \
  "s_waitcnt lgkmcnt(0)\n" \
  "v_mfma_f32_16x16x32_bf16 v[16:19], v[116:119], v[108:111], v[16:19]\n" \
  "s_nop 7\n" \
  "v_mov_b64 v[20:21], v[16:17]\n" \
  "v_mov_b32_dpp v16, v18 row_ror:8 row_mask:0xf bank_mask:0xc\n" \
  "v_mov_b32_dpp v17, v19 row_ror:8 row_mask:0xf bank_mask:0xc\n" \
  "v_mov_b32_dpp v18, v20 row_ror:8 row_mask:0xf bank_mask:0x3\n" \
  "v_mov_b32_dpp v19, v21 row_ror:8 row_mask:0xf bank_mask:0x3\n" \
  "v_cvt_pk_bf16_f32 v16, 0, v16\n" \
  "v_cvt_pk_bf16_f32 v17, 0, v17\n" \
  "v_mul_f32 v20, 0xbfb8aa3b, v16\n" \
  "v_mul_f32 v21, 0xbfb8aa3b, v17\n" \
  "v_exp_f32 v20, v20\n" \
  "v_exp_f32 v21, v21\n" \
  "v_cvt_pk_bf16_f32 v18, 0, v18\n" \
  "v_pk_add_f32 v[20:21], v[20:21], 1.0 op_sel_hi:[1,0]\n" \
  "v_rcp_f32 v20, v20\n" \
  "v_rcp_f32 v21, v21\n" \
  "v_cvt_pk_bf16_f32 v19, 0, v19\n" \
  "v_pk_mul_f32 v[16:17], v[16:17], v[20:21]\n" \
  "v_cvt_pk_bf16_f32 v16, 0, v16\n" \
  "v_cvt_pk_bf16_f32 v17, 0, v17\n" \
  "v_pk_mul_f32 v[16:17], v[16:17], v[18:19]\n" \
  "v_cvt_pk_bf16_f32 v16, v16, v17\n" \
  "ds_write_b16 v2, v16\n" \
  "ds_write_b16_d16_hi v2, v16 offset:160\n" \
  GSTAMP11 \
  "s_setprio 0\n" \
  "s_waitcnt lgkmcnt(0)\n" \
  "s_barrier\n" \
  "s_cmp_gt_u32 %[w], 2\n" \
  "s_cbranch_scc1 Ez%=\n" \
  "s_lshr_b32 s46, s57, 4\n" \
  "v_bfe_u32 v1, %[t], 3, 1\n" \
  "v_lshl_add_u32 v1, v1, 3, v3\n" \
  "s_lshl_b32 s46, s46, 4\n" \
  "v_add_u32 v1, s46, v1\n" \
  "ds_read_b64 v[4:5], v1 offset:32\n" \
  "s_and_b32 s46, s57, 15\n" \
  "s_mul_i32 s46, s46, 12\n" \
  "s_lshl2_add_u32 s46, %[w], s46\n" \
  "v_and_b32 v2, 3, %[t]\n" \
  "v_add_u32 v6, s46, v2\n" \
  "v_lshrrev_b32 v7, 5, v6\n" \
  "v_lshl_add_u32 v6, v7, 6, v6\n" \
  "v_min_u32 v7, 2, v11\n" \
  "v_lshl_add_u32 v6, v7, 5, v6\n" \
  "v_mul_u32_u24 v7, 683, v6\n" \
  "v_lshrrev_b32 v7, 11, v7\n" \
  "v_mul_u32_u24 v8, 3, v7\n" \
  "v_sub_u32 v8, v6, v8\n" \
  "v_and_b32 v9, 1, v7\n" \
  "v_sub_u32 v8, v8, v9\n" \
  "v_min_u32 v8, 2, v8\n" \
  "v_mul_u32_u24 v7, 16512, v7\n" \
  "v_lshlrev_b32 v9, 3, v6\n" \
  "s_waitcnt lgkmcnt(0)\n" \
  "v_not_b32 v13, v5\n" \
  "v_and_b32 v13, 0x7ff, v13\n" \
  "v_lshrrev_b32 v14, 1, v13\n" \
  "v_lshl_add_u32 v7, v14, 4, v7\n" \
  "v_lshrrev_b32 v14, 4, v13\n" \
  "v_lshlrev_b32 v14, 2, v14\n" \
  "v_mul_u32_u24 v4, 1184, v4\n" \
  "v_lshl_add_u32 v4, v6, 1, v4\n" \
  "v_add_u32 v4, 101888, v4\n" \
  "v_mov_b32 v15, 0\n" \
  "ds_read_u16_d16_hi v15, v4\n" \
  "s_mov_b32 s90, 139264\n" \
  "s_mov_b32 s91, 136192\n" \
  "s_mov_b32 s86, 27136\n" \
  "s_mov_b32 s87, 54784\n" \
  "s_mov_b32 s88, 82432\n" \
  "buffer_load_dwordx4 v[16:19], v7, s[40:43], s90 offen sc1\n" \
  "buffer_load_dword v20, v14, s[40:43], s91 offen sc1\n" \
  "buffer_load_dwordx2 v[22:23], v9, s[40:43], s86 offen sc1\n" \
  "buffer_load_dwordx2 v[24:25], v9, s[40:43], s87 offen sc1\n" \
  "buffer_load_dwordx2 v[26:27], v9, s[40:43], s88 offen sc1\n" \
  "v_lshrrev_b32 v28, 2, v12\n" \
  "v_mul_u32_u24 v28, 192, v28\n" \
  "v_lshl_add_u32 v28, v2, 4, v28\n" \
  "s_mul_i32 s46, s57, 12288\n" \
  "s_lshl_b32 s47, %[w], 6\n" \
  "s_add_u32 s46, s46, s47\n" \
  "s_add_u32 s46, s46, 6455296\n" \
  "s_add_u32 s47, s46, 6144\n" \
  ".if " STR(FSLP) "\n" \
  "s_sleep " STR(FSLP) "\n" \
  ".endif\n" \
  GSTAMP12 \
  "buffer_load_dwordx4 v[40:43], v28, s[40:43], s46 offen sc1\n" \
  "buffer_load_dwordx4 v[44:47], v28, s[40:43], s46 offen offset:3072 sc1\n" \
  "buffer_load_dwordx4 v[48:51], v28, s[40:43], s47 offen sc1\n" \
  "buffer_load_dwordx4 v[52:55], v28, s[40:43], s47 offen offset:3072 sc1\n" \
  "s_waitcnt vmcnt(4)\n" \
  "v_bitop3_b32 v21, v19, v20, s44 bitop3:0x7e\n" \
  "v_bitop3_b32 v30, v23, v25, s44 bitop3:0x7e\n" \
  "v_xor_b32 v29, s44, v27\n" \
  "v_or3_b32 v21, v21, v30, v29\n" \
  "v_cmp_ne_u32 vcc, 0, v21\n" \
  "s_cbranch_vccz Efx%=\n" \
  "s_movk_i32 s97, 0x7fff\n" \
  "Efr%=:\n" \
  "buffer_load_dwordx4 v[16:19], v7, s[40:43], s90 offen sc1\n" \
  "buffer_load_dword v20, v14, s[40:43], s91 offen sc1\n" \
  "buffer_load_dwordx2 v[22:23], v9, s[40:43], s86 offen sc1\n" \
  "buffer_load_dwordx2 v[24:25], v9, s[40:43], s87 offen sc1\n" \
  "buffer_load_dwordx2 v[26:27], v9, s[40:43], s88 offen sc1\n" \
  "s_waitcnt vmcnt(0)\n" \
  "v_bitop3_b32 v21, v19, v20, s44 bitop3:0x7e\n" \
  "v_bitop3_b32 v30, v23, v25, s44 bitop3:0x7e\n" \
  "v_xor_b32 v29, s44, v27\n" \
  "v_or3_b32 v21, v21, v30, v29\n" \
  "v_cmp_ne_u32 vcc, 0, v21\n" \
  "s_cbranch_vccz Efx%=\n" \
  "s_sub_u32 s97, s97, 1\n" \
  "s_cbranch_scc0 Efr%=\n" \
  "Efx%=:\n" \
  "v_add_f32 v22, v22, v24\n" \
  "v_add_f32 v22, v22, v26\n" \
  "v_cvt_pk_bf16_f32 v22, 0, v22\n" \
  "v_and_b32 v20, 1, v13\n" \
  "v_cmp_eq_u32 vcc, 1, v20\n" \
  "v_cmp_eq_u32 s[94:95], 2, v8\n" \
  "v_mul_u32_u24 v13, 1152, v13\n" \
  "v_cndmask_b32 v16, v16, v17, vcc\n" \
  "v_cndmask_b32 v8, v8, v20, s[94:95]\n" \
  "v_cndmask_b32 v16, v16, v18, s[94:95]\n" \
  "v_lshlrev_b32 v8, 4, v8\n" \
  "v_lshrrev_b32 v16, v8, v16\n" \
  "v_lshlrev_b32 v16, 16, v16\n" \
  "v_lshl_add_u32 v13, v6, 1, v13\n" \
  "v_ashrrev_i32 v17, 31, v5\n" \
  "v_not_b32 v17, v17\n" \
  "v_or_b32 v17, 0x80000000, v17\n" \
  "v_xor_b32 v5, v5, v17\n" \
  "v_and_b32 v5, 0xfffff800, v5\n" \
  "v_cvt_pk_bf16_f32 v5, 0, v5\n" \
  "v_mul_f32 v5, 0xbfb8aa3b, v5\n" \
  "v_exp_f32 v5, v5\n" \
  "v_and_b32 v17, 2, v10\n" \
  "v_add_f32 v5, 1.0, v5\n" \
  "v_rcp_f32 v5, v5\n" \
  "v_cmp_eq_u32 vcc, 0, v17\n" \
  "v_cvt_pk_bf16_f32 v5, 0, v5\n" \
  "v_and_b32 v17, 1, v10\n" \
  "v_lshlrev_b32 v17, 4, v17\n" \
  "v_mov_b32 v18, 0x3f80\n" \
  "v_lshlrev_b32 v17, v17, v18\n" \
  "v_cndmask_b32 v17, 0, v17, vcc\n" \
  "v_lshrrev_b32 v18, 2, v10\n" \
  "v_cmp_eq_u32 vcc, 0, v18\n" \
  "s_nop 1\n" \
  "v_cndmask_b32 v32, 0, v17, vcc\n" \
  "v_cmp_eq_u32 vcc, 1, v18\n" \
  "s_nop 1\n" \
  "v_cndmask_b32 v33, 0, v17, vcc\n" \
  "v_cmp_eq_u32 vcc, 2, v18\n" \
  "s_nop 1\n" \
  "v_cndmask_b32 v34, 0, v17, vcc\n" \
  "v_mov_b32 v35, 0\n" \
  "s_waitcnt vmcnt(0) lgkmcnt(0)\n" \
  "v_xor_b32 v43, s44, v43\n" \
  "v_xor_b32 v47, s44, v47\n" \
  "v_xor_b32 v51, s44, v51\n" \
  "v_xor_b32 v55, s44, v55\n" \
  "v_or3_b32 v17, v43, v47, v51\n" \
  "v_or_b32 v17, v17, v55\n" \
  "v_cmp_ne_u32 vcc, 0, v17\n" \
  "s_cbranch_vccnz Eps%=\n" \
  "Epb%=:\n" \
  GSTAMP13 \
  "v_mfma_f32_16x16x32_bf16 v[24:27], v[32:35], v[40:43], 0\n" \
  "v_mfma_f32_16x16x32_bf16 v[24:27], v[32:35], v[44:47], v[24:27]\n" \
  "v_mfma_f32_16x16x32_bf16 v[24:27], v[32:35], v[48:51], v[24:27]\n" \
  "v_mfma_f32_16x16x32_bf16 v[24:27], v[32:35], v[52:55], v[24:27]\n" \
  "s_mov_b32 s46, 0x0f0f0f0f\n" \
  "s_mov_b32 s47, 0x0f0f\n" \
  "s_nop 5\n" \
  "v_add_f32_dpp v17, v24, v24 row_ror:8 row_mask:0xf bank_mask:0x3\n" \
  "v_add_f32_dpp v17, v25, v25 row_ror:8 row_mask:0xf bank_mask:0xc\n" \
  "s_nop 1\n" \
  "v_add_f32_dpp v17, v17, v17 row_shl:4 row_mask:0xf bank_mask:0xf bound_ctrl:1\n" \
  "v_cvt_pk_bf16_f32 v17, 0, v17\n" \
  "v_mul_f32 v17, v17, v5\n" \
  "v_cvt_pk_bf16_f32 v17, 0, v17\n" \
  "v_add_f32 v17, v17, v15\n" \
  "v_cvt_pk_bf16_f32 v17, 0, v17\n" \
  "v_mul_f32 v17, v17, v22\n" \
  "v_cvt_pk_bf16_f32 v17, 0, v17\n" \
  "v_add_f32 v17, v17, v16\n" \
  "v_cvt_pk_bf16_f32 v17, 0, v17\n" \
  "s_mov_b64 exec, s[46:47]\n" \
  "global_store_short_d16_hi v13, v17, s[84:85]\n" \
  "s_or_b32 s46, s57, %[w]\n" \
  "s_cbranch_scc1 Ez%=\n" \
  "s_mov_b64 exec, 1\n" \
  "v_mov_b32 v17, s44\n" \
  "buffer_store_dword v17, off, s[40:43], 0 sc1\n" \
  "s_branch Ez%=\n" \
  "Eps%=:\n" \
  "s_movk_i32 s97, 0x7fff\n" \
  "Epr%=:\n" \
  GSTAMPC \
  "s_mov_b64 exec, vcc\n" \
  "buffer_load_dwordx4 v[40:43], v28, s[40:43], s46 offen sc1\n" \
  "buffer_load_dwordx4 v[44:47], v28, s[40:43], s46 offen offset:3072 sc1\n" \
  "buffer_load_dwordx4 v[48:51], v28, s[40:43], s47 offen sc1\n" \
  "buffer_load_dwordx4 v[52:55], v28, s[40:43], s47 offen offset:3072 sc1\n" \
  "s_waitcnt vmcnt(0)\n" \
  "v_xor_b32 v43, s44, v43\n" \
  "v_xor_b32 v47, s44, v47\n" \
  "v_xor_b32 v51, s44, v51\n" \
  "v_xor_b32 v55, s44, v55\n" \
  "v_or3_b32 v17, v43, v47, v51\n" \
  "v_or_b32 v17, v17, v55\n" \
  "v_cmp_ne_u32 vcc, 0, v17\n" \
  "s_mov_b64 exec, -1\n" \
  "s_cbranch_vccz Epb%=\n" \
  "s_sub_u32 s97, s97, 1\n" \
  "s_cbranch_scc0 Epr%=\n" \
  "s_branch Epb%=\n" \
  "Eh1%=:\n" \
  "s_setprio 3\n" \
  "v_lshl_add_u32 v1, v10, 2, v13\n" \
  "ds_read_b32 v16, v1\n" \
  "s_mov_b32 s46, 103424\n" \
  "v_lshl_add_u32 v4, v10, 2, v3\n" \
  "v_lshl_add_u32 v5, v11, 4, v3\n" \
  "v_add_u32 v18, 16, v10\n" \
  "s_waitcnt lgkmcnt(0)\n" \
  "v_not_b32 v16, v16\n" \
  "v_and_b32 v16, 0x7ff, v16\n" \
  "v_lshlrev_b32 v1, 3, v16\n" \
  "buffer_load_dwordx2 v[16:17], v1, s[40:43], s46 offen sc1\n" \
  "s_barrier\n" \
  "s_waitcnt vmcnt(0)\n" \
  "v_cmp_ne_u32 vcc, s44, v17\n" \
  "s_cbranch_vccz Eqx%=\n" \
  "s_movk_i32 s97, 0x7fff\n" \
  "Eqr%=:\n" \
  "buffer_load_dwordx2 v[16:17], v1, s[40:43], s46 offen sc1\n" \
  "s_waitcnt vmcnt(0)\n" \
  "v_cmp_ne_u32 vcc, s44, v17\n" \
  "s_cbranch_vccz Eqx%=\n" \
  "s_sub_u32 s97, s97, 1\n" \
  "s_cbranch_scc0 Eqr%=\n" \
  "Eqx%=:\n" \
  "ds_write_b32 v4, v16 offset:96\n" \
  "ds_read_b128 v[20:23], v5 offset:96\n" \
  "v_mov_b32 v17, v16\n" \
  "v_mov_b32 v19, v16\n" \
  "v_mov_b32 v16, 0\n" \
  "s_waitcnt lgkmcnt(0)\n" \
  "v_cmp_gt_u32 s[88:89], v20, v17\n" \
  "v_cmp_gt_u32 s[90:91], v21, v17\n" \
  "v_cmp_gt_u32 s[94:95], v22, v17\n" \
  "v_cmp_gt_u32 s[96:97], v23, v17\n" \
  "v_addc_co_u32 v16, vcc, 0, v16, s[88:89]\n" \
  "v_addc_co_u32 v16, vcc, 0, v16, s[90:91]\n" \
  "v_addc_co_u32 v16, vcc, 0, v16, s[94:95]\n" \
  "v_addc_co_u32 v16, vcc, 0, v16, s[96:97]\n" \
  "v_mov_b32 v17, v16\n" \
  "s_nop 1\n" \
  "v_permlane16_swap_b32 v16, v17\n" \
  "v_add_u32 v16, v16, v17\n" \
  "v_mov_b32 v17, v16\n" \
  "s_nop 1\n" \
  "v_permlane32_swap_b32 v16, v17\n" \
  "v_add_u32 v16, v16, v17\n" \
  "v_cmp_gt_u32 vcc, 8, v16\n" \
  "v_lshl_add_u32 v16, v16, 3, v3\n" \
  "s_mov_b64 exec, vcc\n" \
  "ds_write_b64 v16, v[18:19] offset:32\n" \
  "s_mov_b64 exec, -1\n" \
  GSTAMP11 \
  "s_waitcnt lgkmcnt(0)\n" \
  "s_barrier\n" \
  "s_branch Ez%=\n" \
  "Ed0%=:\n" \
  "v_and_b32 v2, 7, %[t]\n" \
  "v_lshl_add_u32 v2, v2, 3, v3\n" \
  "s_mov_b32 s97, 0x1fffff\n" \
  "Ed1%=:\n" \
  "ds_read_b32 v1, v2 offset:32\n" \
  "s_waitcnt lgkmcnt(0)\n" \
  "v_cmp_eq_u32 vcc, 0, v1\n" \
  "s_cbranch_vccz Ed2%=\n" \
  "s_sleep 1\n" \
  "s_sub_u32 s97, s97, 1\n" \
  "s_cbranch_scc0 Ed1%=\n" \
  "Ed2%=:\n" \
  "s_mov_b32 vcc_lo, 0x00ff00ff\n" \
  "s_mov_b32 vcc_hi, 0xff00ff00\n" \
  "v_cndmask_b32 v1, 32, v1, vcc\n" \
  "v_mul_u32_u24 v1, 160, v1\n" \
  "v_and_b32 v2, 16, %[t]\n" \
  "v_add_u32 v2, 138752, v2\n" \
  "v_add_u32 v1, v1, v2\n" \
  "s_setprio 3\n" \
  GSTAMP11 \
  "s_barrier\n" \
  "ds_read_b128 v[100:103], v1 offset:0\n" \
  "ds_read_b128 v[104:107], v1 offset:32\n" \
  "ds_read_b128 v[108:111], v1 offset:64\n" \
  "ds_read_b128 v[112:115], v1 offset:96\n" \
  "ds_read_b128 v[116:119], v1 offset:128\n" \
  "s_waitcnt lgkmcnt(4)\n" \
  "v_mfma_f32_16x16x32_bf16 v[16:19], v[100:103], v[40:43], 0\n" \
  "v_mfma_f32_16x16x32_bf16 v[20:23], v[100:103], v[44:47], 0\n" \
  "v_mfma_f32_16x16x32_bf16 v[24:27], v[100:103], v[48:51], 0\n" \
  "s_waitcnt lgkmcnt(3)\n" \
  "v_mfma_f32_16x16x32_bf16 v[16:19], v[104:107], v[52:55], v[16:19]\n" \
  "v_mfma_f32_16x16x32_bf16 v[20:23], v[104:107], v[56:59], v[20:23]\n" \
  "v_mfma_f32_16x16x32_bf16 v[24:27], v[104:107], v[60:63], v[24:27]\n" \
  "s_waitcnt lgkmcnt(2)\n" \
  "v_mfma_f32_16x16x32_bf16 v[16:19], v[108:111], v[64:67], v[16:19]\n" \
  "v_mfma_f32_16x16x32_bf16 v[20:23], v[108:111], v[68:71], v[20:23]\n" \
  "v_mfma_f32_16x16x32_bf16 v[24:27], v[108:111], v[72:75], v[24:27]\n" \
  "s_waitcnt lgkmcnt(1)\n" \
  "v_mfma_f32_16x16x32_bf16 v[16:19], v[112:115], v[76:79], v[16:19]\n" \
  "v_mfma_f32_16x16x32_bf16 v[20:23], v[112:115], v[80:83], v[20:23]\n" \
  "v_mfma_f32_16x16x32_bf16 v[24:27], v[112:115], v[84:87], v[24:27]\n" \
  "s_waitcnt lgkmcnt(0)\n" \
  "v_mfma_f32_16x16x32_bf16 v[16:19], v[116:119], v[88:91], v[16:19]\n" \
  "v_mfma_f32_16x16x32_bf16 v[20:23], v[116:119], v[92:95], v[20:23]\n" \
  "v_mfma_f32_16x16x32_bf16 v[24:27], v[116:119], v[96:99], v[24:27]\n" \
  "s_nop 5\n" \
  "v_cvt_pk_bf16_f32 v28, v16, v17\n" \
  "v_cvt_pk_bf16_f32 v32, v18, v19\n" \
  "v_cvt_pk_bf16_f32 v29, v20, v21\n" \
  "v_cvt_pk_bf16_f32 v33, v22, v23\n" \
  "v_cvt_pk_bf16_f32 v30, v24, v25\n" \
  "v_cvt_pk_bf16_f32 v34, v26, v27\n" \
  "buffer_store_dwordx4 v[28:31], v38, s[40:43], s86 offen sc1\n" \
  "buffer_store_dwordx4 v[32:35], v38, s[40:43], s87 offen sc1\n" \
  GSTAMP12 \
  "Ez%=:\n" \
  "s_mov_b64 exec, -1\n"

#define V4(a) "v" #a "0", "v" #a "1", "v" #a "2", "v" #a "3", "v" #a "4", "v" #a "5", "v" #a "6", "v" #a "7", "v" #a "8", "v" #a "9"
#define S4(a) "s" #a "0", "s" #a "1", "s" #a "2", "s" #a "3", "s" #a "4", "s" #a "5", "s" #a "6", "s" #a "7", "s" #a "8", "s" #a "9"
#define WAVE_CLOB "v1", "v2", "v3", "v4", "v5", "v6", "v7", "v8", "v9", V4(1), V4(2), V4(3), V4(4), V4(5), V4(6), V4(7), V4(8), V4(9), V4(10), V4(11), \
                  "v120", "v121", "v122", "v123", "v124", "v125", "v126", "v127", S4(4), S4(5), "s84", "s85", "s86", "s87", "s88", "s89", "s90", "s91", "s92", "s93", "s94", "s95", "s96", "s97", "s98", "s99", "vcc", "scc", "memory"

__global__ void __launch_bounds__(1024) __attribute__((aligned(KALIGN))) mega(const u16*, const u16*, const u16*, const u16*, const u16*, const u16*, const u16*, const u16*, const u16*, const u16*, const u16*,
                                             const u16*, const u16*, u16*, char* ws) {
  TS(0);
  int tid = threadIdx.x, blk = blockIdx.x, wave;
  const auto ka = __builtin_amdgcn_kernarg_segment_ptr();
  asm volatile("s_load_dword s99, %[k], 0x0\n\ts_load_dword s99, %[k], 0x40\n\tv_readfirstlane_b32 %[w], %[t]\n\ts_lshr_b32 %[w], %[w], 6\n\ts_cmpk_lt_u32 %[b], 192\n\ts_cbranch_scc1 Lcc%=\n" EW_HEAD "s_branch Lcm%=\n"
               "Lcc%=:\n.if " STR(ROT) "\nv_add_u32 v0, 768, v0\nv_and_b32 v0, 1023, v0\ns_add_u32 %[w], %[w], 12\ns_and_b32 %[w], %[w], 15\n.endif\n\ts_cmp_lt_u32 %[w], 12\n\ts_cbranch_scc1 Lcg%=\n" SW_HEAD "s_branch Lcs%=\nLcg%=:\n" GW_HEAD GW_BODY "s_branch Lcl%=\nLcs%=:\n" SW_BODY
               "Lcl%=:\n\ts_cmpk_ge_u32 %[b], 128\n\ts_cbranch_scc1 Lce%=\n" LW_ASM "s_branch Lce%=\nLcm%=:\n" EW_BODY
               "Lce%=:\n\ts_waitcnt lgkmcnt(0)"
               : [w] "=&s"(wave) STAMP_IO : [t] "{v0}"(tid), [b] "s"(blk), [k] "s"(ka) : WAVE_CLOB);
  if (blk >= 192) TS(15);
}

extern "C" void kernel_launch(void* const* d_in, const int* in_sizes, int n_in, void* d_out, int out_size, void* d_ws, size_t ws_size, hipStream_t stream) {
  (void)hipFuncSetAttribute((const void*)mega, hipFuncAttributeMaxDynamicSharedMemorySize, LDS_BYTES);
  mega<<<256, 1024, LDS_BYTES, stream>>>((const u16*)d_in[0], (const u16*)d_in[1], (const u16*)d_in[2], (const u16*)d_in[3], (const u16*)d_in[4],
                                         (const u16*)d_in[5], (const u16*)d_in[6], (const u16*)d_in[8], (const u16*)d_in[9], (const u16*)d_in[10],
                                         (const u16*)d_in[11], (const u16*)d_in[12], (const u16*)d_in[13], (u16*)d_out, (char*)d_ws);
}
```

```cpp
#include <hip/hip_runtime.h>
#include <stdint.h>

typedef unsigned short u16;
typedef unsigned int u32;
typedef unsigned long long u64;

#define WNT " nt"
#define DNTF ""
#define PRN 3
#define PRF 2
#define PRG 2
#define PRH 3
#define PRE_ 1
#define GPRIO 1
#define XDLY 0
#define X1T 4
#define X1SLP 15
#define PSLP 0
#define KSLP 1
#define FSLP 19
#define OFLAG " sc1"
#define WARM 0
#define PAD(k) do { if (WARM) asm volatile("Lpad" #k ":\n\ts_cbranch_execz Lwret"); } while (0)
#define PADS(k) ""
#define STR_(x) #x
#define STR(x) STR_(x)
#define PB(k) "s_branch Lpad" #k "\n\t"
#define PN "s_branch Lwret\n\t"
#define NPADC 24
#define PADTAB PB(0) PB(1) PB(2) PB(3) PB(4) PB(5) PB(6) PB(7) PB(8) PB(9) PB(10) PB(11) PB(12) PB(13) PB(14) PB(15) \
               PB(16) PB(17) PB(18) PB(19) PB(20) PB(21) PB(22) PB(23) PN PN PN PN PN PN PN PN \
               PB(32) PB(33) PB(34) PB(35) PB(36) PB(37) PB(38) PB(39) PB(40) PB(41) PB(42) PB(43) PB(44) PB(45) PB(46) PB(47)
#define CODE_LINES 218
#define OLATE 1
#define MSLP 1
#define GSLP 10
#define XT 200
#define JDLY 30
#define DT 9600
#define PFG 0x40000
#define PFT ""
#define PFD 4
#define P1D 8
#define GUW 8
#define GAE 2
#define KBAR 1
#define NOC 0
#define GED 6
#define WSLOW 0
#define YT 0
#define TLBW 0
#define NOPRM 0
#define B0D 2
#define KT 15000
#define QSLP 20
#define P2D 6
#define KALIGN 4096
#define ROT 0
#define GEAR 2
#define SDLY 1
#define PRK 0
#define PRP 1
#define PWT 0
#define DTH 2
#define ASLP 0
#define B0T 2600
#define B0L 3300
#define OSLP 24
#define PW2 1
#define CT 9000
#define D2D 12
#define KPR 2
#define CTH 5
#define KSL0 2
#define KD2 0
#define KR 170
#define KD 120
#define YEND 0x30000000
#define PRQ 1
#define P2E 0
#define XD2 0
#define WD2 0
#define WDLY 16
#define PFM 0
#define PFW 1

constexpr int LDS_BYTES = 163840;

#define TS(i) do {} while (0)
#define CNT(i) do {} while (0)

#define STA(i, r) ""
#define STAMP_IO
#define LSTAMP_IO
#define GSTAMP1 STA(1, "s[60:61]")
#define SSTAMP1 GSTAMP1
#define GSTAMP2 STA(2, "s[62:63]")
#define SSTAMP2 GSTAMP2
#define GSTAMP3 STA(3, "s[64:65]")
#define SSTAMP3 GSTAMP3
#define GSTAMP4 STA(4, "s[66:67]")
#define SSTAMP4 GSTAMP4
#define GSTAMP5 STA(5, "s[68:69]")
#define SSTAMP5 GSTAMP5
#define GSTAMP6 STA(6, "s[70:71]")
#define SSTAMP6 GSTAMP6
#define GSTAMP7 STA(7, "s[72:73]")
#define SSTAMP7 GSTAMP7
#define GSTAMP8 STA(8, "s[74:75]")
#define SSTAMP8 GSTAMP8
#define GSTAMP9 STA(9, "s[76:77]")
#define SSTAMP9 GSTAMP9
#define GSTAMP10 STA(10, "s[78:79]")
#define SSTAMP10 GSTAMP10
#define GSTAMP11 STA(11, "s[80:81]")
#define SSTAMP11 GSTAMP11
#define STC(r) ""
#define SSTAMPCm STC("s60")
#define SSTAMPCu0 STC("s62")
#define SSTAMPCu1 STC("s62")
#define SSTAMPCu2 STC("s66")
#define SSTAMPCu3 STC("s66")
#define SSTAMPCa STC("s64")
#define LSTAMPC STC("s38")
#define LSTAMPD STC("s36")
#define LSTAMP8 GSTAMP8
#define LSTAMP9 GSTAMP9
#define LSTAMP10 GSTAMP10
#define LSTAMP11 GSTAMP11
#define LSTAMP13 STA(13, "s[36:37]")
#define LSTAMP14 STA(14, "s[38:39]")
#define GSTAMP12 SSTAMPE1
#define GSTAMP13 LSTAMP13
#define GSTAMPC LSTAMPC
#define GSTAMPK STC("s60")
#define SSTAMPE0 GSTAMP6
#define SSTAMPE1 STA(12, "s[82:83]")
#define SSTAMPE2 ""
#define GW_HEAD \
  "s_load_dwordx8 s[40:47], %[k], 0x8\n" \
  "s_load_dwordx2 s[48:49], %[k], 0x28\n" \
  "s_setprio 1\n" \
  "s_lshr_b32 s50, %[w], 2\n" \
  "s_and_b32 s54, %[b], 1\n" \
  "s_add_u32 s54, s54, s50\n" \
  "s_cmp_eq_u32 s54, 3\n" \
  "s_cselect_b32 s54, 0, s54\n" \
  "s_mul_i32 s53, %[b], 3\n" \
  "s_add_u32 s53, s53, s54\n" \
  "s_add_u32 s52, %[w], s50\n" \
  "s_lshl_b32 s55, s52, 7\n" \
  "s_and_b32 s55, s55, 128\n" \
  "s_lshl_b32 s54, s53, 8\n" \
  "s_add_u32 s54, s54, s55\n" \
  "v_and_b32 v9, 63, %[t]\n" \
  "v_lshl_add_u32 v17, v9, 1, s54\n" \
  "v_mov_b64 v[108:109], 0\n" \
  "v_mov_b64 v[110:111], 0\n" \
  "s_mul_i32 s57, s50, 32096\n" \
  "s_lshl1_add_u32 m0, s55, s57\n" \
  "s_lshl_b32 s84, s53, 1\n" \
  "s_and_b32 s84, s84, -4\n" \
  "s_cmp_ge_u32 s50, " STR(GEAR) "\n" \
  "s_cbranch_scc1 Lnc%=\n" \
  "s_setprio 3\n" \
  "s_bitcmp1_b32 s52, 1\n" \
  GSTAMP1 \
  "s_waitcnt lgkmcnt(0)\n" \
  "global_load_short_d16_hi v108, v17, s[40:41]\n" \
  "global_load_short_d16_hi v109, v17, s[42:43]\n" \
  "s_load_dword s98, s[48:49], s84\n" \
  "s_cbranch_scc1 Lnd%=\n" \
  "global_load_short_d16_hi v110, v17, s[44:45]\n" \
  "global_load_short_d16_hi v111, v17, s[46:47]\n" \
  "Lnd%=:\n" \
  "s_setprio 1\n" \
  "Lnc%=:\n" \
  "s_and_b32 s51, %[w], 3\n" \
  "s_lshl_b32 s58, s50, 13\n" \
  "s_add_u32 s58, s58, 96288\n" \
  "v_and_b32 v1, 15, %[t]\n" \
  "v_and_b32 v2, 48, %[t]\n" \
  "v_mov_b64 v[12:13], 0\n" \
  "v_mov_b64 v[14:15], 0\n" \
  "v_and_b32 v5, 255, %[t]\n" \
  "v_lshl_add_u32 v5, v5, 4, s58\n" \
  "ds_write_b128 v5, v[12:15] offset:4096\n" \
  "v_min_u32 v4, 1, v9\n" \
  "v_mul_u32_u24 v4, 40, v4\n" \
  "v_add_u32 v4, v4, v9\n" \
  "v_mov_b32 v5, 159488\n" \
  "v_lshl_add_u32 v4, v4, 2, v5\n" \
  "s_movk_i32 s54, 544\n" \
  "v_mad_u32_u24 v5, v1, s54, v2\n" \
  "s_and_b32 s54, s51, 1\n" \
  "s_mul_i32 s55, s54, 8704\n" \
  "s_add_u32 s55, s55, 14688\n" \
  "s_cmp_lt_u32 s51, 2\n" \
  "s_cselect_b32 s56, 0, s55\n" \
  "s_cselect_b32 s55, s55, 8704\n" \
  "s_cselect_b32 s85, 16, 64\n" \
  "s_cselect_b32 s86, 512, 16\n" \
  "s_cselect_b32 s87, 8, 5\n" \
  "s_cselect_b32 s88, -2912, 0\n" \
  "v_add_u32 v5, s57, v5\n" \
  "v_add_u32 v6, s55, v5\n" \
  "v_add_u32 v5, s56, v5\n" \
  "s_lshl_b32 s54, s54, s87\n" \
  "s_add_u32 s54, s54, s88\n" \
  "s_add_u32 s54, s54, s58\n" \
  "v_lshrrev_b32 v7, 1, v2\n" \
  "v_and_b32 v7, 8, v7\n" \
  "v_mad_u32_u24 v7, v1, s85, v7\n" \
  "v_lshrrev_b32 v9, 5, v9\n" \
  "v_mad_u32_u24 v7, v9, s86, v7\n" \
  "v_add_u32 v7, s54, v7\n" \
  "s_lshl_b32 s59, s50, 3\n" \
  "s_lshl_b32 s59, 1, s59\n" \
  "v_mov_b32 v16, s59\n" \
  "s_not_b32 s53, s53\n" \
  "s_lshl_b32 s53, s53, 4\n" \
  "s_cmp_eq_u32 s50, 0\n" \
  "s_cbranch_scc0 Lp1%=\n" \
  "s_setprio 2\n" \
  "Lp1%=:\n" \
  "s_bitcmp1_b32 s52, 1\n" \
  "s_cbranch_scc1 Lgb%=\n" \
  "s_barrier\n"

#define GW_BODY \
  GSTAMP2 \
  "s_cmp_lt_u32 s50, " STR(GEAR) "\n" \
  "s_cbranch_scc1 Lne1%=\n" \
  "s_waitcnt lgkmcnt(0)\n" \
  "global_load_short_d16_hi v108, v17, s[40:41]\n" \
  "global_load_short_d16_hi v109, v17, s[42:43]\n" \
  "s_load_dword s98, s[48:49], s84\n" \
  "global_load_short_d16_hi v110, v17, s[44:45]\n" \
  "global_load_short_d16_hi v111, v17, s[46:47]\n" \
  "Lne1%=:\n" \
  "v_lshrrev_b32 v3, 1, v2\n" \
  "v_add3_u32 v3, v3, v1, 1\n" \
  "v_and_b32 v8, 1, v3\n" \
  "v_mul_u32_u24 v8, 1470, v8\n" \
  "v_lshl_add_u32 v3, v3, 1, v8\n" \
  "s_add_u32 s55, s58, 4544\n" \
  "v_add_u32 v3, s55, v3\n" \
  "v_lshlrev_b32 v8, 5, v1\n" \
  "v_sub_u32 v10, v2, v8\n" \
  "s_add_u32 s55, s58, 480\n" \
  "v_add_u32 v10, s55, v10\n" \
  "v_mul_u32_u24 v11, 20, v2\n" \
  "v_lshl_add_u32 v11, v1, 4, v11\n" \
  "v_add_u32 v11, s57, v11\n" \
  "v_lshlrev_b32 v8, 2, v1\n" \
  "v_sub_u32 v8, v8, v2\n" \
  "s_add_u32 s55, s58, 7540\n" \
  "v_add_u32 v8, s55, v8\n" \
  "s_bitcmp0_b32 s51, 0\n" \
  "s_cbranch_scc1 Lo1%=\n" \
  "s_lshr_b32 s55, s51, 1\n" \
  "s_mul_i32 s55, s55, 1470\n" \
  "s_add_u32 s55, s55, s58\n" \
  "v_lshrrev_b32 v19, 3, v2\n" \
  "v_mul_u32_u24 v19, 29, v19\n" \
  "v_lshl_add_u32 v19, v1, 1, v19\n" \
  "v_add_u32 v19, s55, v19\n" \
  "v_mov_b32 v20, 0x3c00\n" \
  "s_mov_b32 exec_lo, 0x001f0007\n" \
  "s_mov_b32 exec_hi, 0x01ff007f\n" \
  "ds_write_b16 v19, v20 offset:5798\n" \
  "s_mov_b64 exec, -1\n" \
  "Lo1%=:\n" \
  "s_lshl_b32 s54, s59, 2\n" \
  "v_mov_b32 v9, s57\n" \
  "s_sub_u32 s55, s51, s50\n" \
  "s_cmp_eq_u32 s51, 3\n" \
  "s_cselect_b32 s55, 3, s55\n" \
  "s_waitcnt vmcnt(0) lgkmcnt(0)\n" \
  "s_lshl_b32 s98, s98, s53\n" \
  "s_and_b32 s98, s98, 0xffff0000\n" \
  "v_mul_f32 v108, 0x3fb8aa3b, v108\n" \
  "v_mov_b32 v112, s98\n" \
  "v_mul_f32 v112, 0x3fb8aa3b, v112\n" \
  "v_exp_f32 v108, v108\n" \
  "v_exp_f32 v112, v112\n" \
  "v_max_f32 v122, |v110|, |v111|\n" \
  "v_cvt_pk_bf16_f32 v108, 0, v108\n" \
  "v_cvt_pk_bf16_f32 v112, 0, v112\n" \
  "v_max_u32_dpp v122, v122, v122 quad_perm:[1,0,3,2] row_mask:0xf bank_mask:0xf\n" \
  "v_pk_mul_f32 v[108:109], v[108:109], v[112:113] op_sel_hi:[1,0]\n" \
  "v_mul_f32 v115, 0x3e22f983, v109\n" \
  "v_max_u32_dpp v122, v122, v122 quad_perm:[2,3,0,1] row_mask:0xf bank_mask:0xf\n" \
  "v_mul_f32 v117, 0x4222f983, v109\n" \
  "v_mul_f32 v114, 0xbfb8aa3b, v108\n" \
  "v_max_u32_dpp v122, v122, v122 row_half_mirror row_mask:0xf bank_mask:0xf\n" \
  "v_mul_f32 v116, 0xc3b8aa3b, v108\n" \
  "v_fract_f32 v115, v115\n" \
  "v_max_u32_dpp v122, v122, v122 row_mirror row_mask:0xf bank_mask:0xf\n" \
  "v_fract_f32 v117, v117\n" \
  "v_exp_f32 v114, v114\n" \
  "v_max_u32_dpp v122, v122, v122 row_bcast:15 row_mask:0xa bank_mask:0xf\n" \
  "v_cos_f32 v118, v115\n" \
  "v_sin_f32 v119, v115\n" \
  "v_max_u32_dpp v122, v122, v122 row_bcast:31 row_mask:0xc bank_mask:0xf\n" \
  "v_exp_f32 v116, v116\n" \
  "v_cos_f32 v120, v117\n" \
  "v_readlane_b32 s94, v122, 63\n" \
  "v_sin_f32 v121, v117\n" \
  "v_pk_mul_f32 v[118:119], v[118:119], v[114:115] op_sel_hi:[1,0]\n" \
  "s_lshr_b32 s94, s94, 23\n" \
  "v_pk_mul_f32 v[120:121], v[120:121], v[116:117] op_sel_hi:[1,0]\n" \
  "s_max_u32 s94, s94, 26\n" \
  "s_min_u32 s94, s94, 226\n" \
  "v_mov_b32 v113, s94\n" \
  "s_sub_u32 s94, 266, s94\n" \
  "s_lshl_b32 s94, s94, 23\n" \
  "ds_write_addtid_b32 v113 offset:8704\n" \
  "ds_write_addtid_b32 v112 offset:10336\n" \
  "v_pk_mul_f32 v[110:111], v[110:111], s[94:95] op_sel_hi:[1,0]\n" \
  "v_pk_mul_f32 v[104:105], v[110:111], v[118:119] op_sel:[1,1] op_sel_hi:[1,0] neg_lo:[0,1]\n" \
  "v_pk_mul_f32 v[106:107], v[110:111], v[120:121] op_sel:[1,1] op_sel_hi:[1,0] neg_lo:[0,1]\n" \
  "v_cvt_pk_f16_f32 v112, v110, v111\n" \
  "v_pk_fma_f32 v[114:115], v[110:111], v[118:119], v[104:105] op_sel_hi:[0,1,1]\n" \
  "v_pk_fma_f32 v[116:117], v[110:111], v[120:121], v[106:107] op_sel_hi:[0,1,1]\n" \
  "ds_write_addtid_b32 v112 offset:0\n" \
  "v_pk_mul_f32 v[104:105], v[114:115], v[118:119] op_sel:[1,1] op_sel_hi:[1,0] neg_lo:[0,1]\n" \
  "v_cvt_pk_f16_f32 v112, v114, v115\n" \
  "v_cvt_pk_f16_f32 v103, v116, v117\n" \
  "v_pk_fma_f32 v[114:115], v[114:115], v[118:119], v[104:105] op_sel_hi:[0,1,1]\n" \
  "ds_write_addtid_b32 v112 offset:544\n" \
  "ds_write_addtid_b32 v103 offset:4352\n" \
  "ds_write_addtid_b32 v103 offset:4896\n" \
  "v_pk_mul_f32 v[104:105], v[114:115], v[118:119] op_sel:[1,1] op_sel_hi:[1,0] neg_lo:[0,1]\n" \
  "v_pk_mul_f32 v[106:107], v[114:115], v[120:121] op_sel:[1,1] op_sel_hi:[1,0] neg_lo:[0,1]\n" \
  "v_cvt_pk_f16_f32 v112, v114, v115\n" \
  "v_pk_fma_f32 v[122:123], v[114:115], v[118:119], v[104:105] op_sel_hi:[0,1,1]\n" \
  "v_pk_fma_f32 v[116:117], v[114:115], v[120:121], v[106:107] op_sel_hi:[0,1,1]\n" \
  "ds_write_addtid_b32 v112 offset:1088\n" \
  "v_pk_mul_f32 v[104:105], v[122:123], v[118:119] op_sel:[1,1] op_sel_hi:[1,0] neg_lo:[0,1]\n" \
  "v_cvt_pk_f16_f32 v112, v122, v123\n" \
  "v_cvt_pk_f16_f32 v103, v116, v117\n" \
  "v_pk_fma_f32 v[114:115], v[122:123], v[118:119], v[104:105] op_sel_hi:[0,1,1]\n" \
  "ds_write_addtid_b32 v112 offset:1632\n" \
  "ds_write_addtid_b32 v103 offset:5440\n" \
  "v_pk_mul_f32 v[104:105], v[114:115], v[118:119] op_sel:[1,1] op_sel_hi:[1,0] neg_lo:[0,1]\n" \
  "v_pk_mul_f32 v[106:107], v[114:115], v[120:121] op_sel:[1,1] op_sel_hi:[1,0] neg_lo:[0,1]\n" \
  "v_cvt_pk_f16_f32 v112, v114, v115\n" \
  "v_pk_fma_f32 v[122:123], v[114:115], v[118:119], v[104:105] op_sel_hi:[0,1,1]\n" \
  "v_pk_fma_f32 v[116:117], v[114:115], v[120:121], v[106:107] op_sel_hi:[0,1,1]\n" \
  "ds_write_addtid_b32 v112 offset:2176\n" \
  "v_pk_mul_f32 v[104:105], v[122:123], v[118:119] op_sel:[1,1] op_sel_hi:[1,0] neg_lo:[0,1]\n" \
  "v_pk_mul_f32 v[106:107], v[116:117], v[120:121] op_sel:[1,1] op_sel_hi:[1,0] neg_lo:[0,1]\n" \
  "v_cvt_pk_f16_f32 v112, v122, v123\n" \
  "v_cvt_pk_f16_f32 v103, v116, v117\n" \
  "v_pk_fma_f32 v[114:115], v[122:123], v[118:119], v[104:105] op_sel_hi:[0,1,1]\n" \
  "v_pk_fma_f32 v[116:117], v[116:117], v[120:121], v[106:107] op_sel_hi:[0,1,1]\n" \
  "ds_write_addtid_b32 v112 offset:2720\n" \
  "ds_write_addtid_b32 v103 offset:5984\n" \
  "ds_write_addtid_b32 v103 offset:6528\n" \
  "ds_write_addtid_b32 v103 offset:7072\n" \
  "v_pk_mul_f32 v[104:105], v[114:115], v[118:119] op_sel:[1,1] op_sel_hi:[1,0] neg_lo:[0,1]\n" \
  "v_pk_mul_f32 v[106:107], v[116:117], v[120:121] op_sel:[1,1] op_sel_hi:[1,0] neg_lo:[0,1]\n" \
  "v_cvt_pk_f16_f32 v112, v114, v115\n" \
  "v_cvt_pk_f16_f32 v103, v116, v117\n" \
  "v_pk_fma_f32 v[122:123], v[114:115], v[118:119], v[104:105] op_sel_hi:[0,1,1]\n" \
  "v_pk_fma_f32 v[116:117], v[116:117], v[120:121], v[106:107] op_sel_hi:[0,1,1]\n" \
  "ds_write_addtid_b32 v112 offset:3264\n" \
  "ds_write_addtid_b32 v103 offset:9248\n" \
  "v_pk_mul_f32 v[104:105], v[122:123], v[118:119] op_sel:[1,1] op_sel_hi:[1,0] neg_lo:[0,1]\n" \
  "v_pk_mul_f32 v[106:107], v[114:115], v[120:121] op_sel:[1,1] op_sel_hi:[1,0] neg_lo:[0,1]\n" \
  "v_cvt_pk_f16_f32 v112, v122, v123\n" \
  "v_cvt_pk_f16_f32 v103, v116, v117\n" \
  "v_pk_fma_f32 v[122:123], v[122:123], v[118:119], v[104:105] op_sel_hi:[0,1,1]\n" \
  "v_pk_fma_f32 v[116:117], v[114:115], v[120:121], v[106:107] op_sel_hi:[0,1,1]\n" \
  "ds_write_addtid_b32 v112 offset:3808\n" \
  "ds_write_addtid_b32 v103 offset:9792\n" \
  "v_pk_mul_f32 v[104:105], v[122:123], v[120:121] op_sel:[1,1] op_sel_hi:[1,0] neg_lo:[0,1]\n" \
  "v_pk_mul_f32 v[106:107], v[120:121], v[120:121] op_sel:[1,1] op_sel_hi:[1,0] neg_lo:[0,1]\n" \
  "v_cvt_pk_f16_f32 v112, v116, v117\n" \
  "v_pk_fma_f32 v[114:115], v[122:123], v[120:121], v[104:105] op_sel_hi:[0,1,1]\n" \
  "v_pk_fma_f32 v[116:117], v[120:121], v[120:121], v[106:107] op_sel_hi:[0,1,1]\n" \
  "ds_write_addtid_b32 v112 offset:7616\n" \
  "v_pk_mul_f32 v[104:105], v[122:123], v[116:117] op_sel:[1,1] op_sel_hi:[1,0] neg_lo:[0,1]\n" \
  "v_pk_mul_f32 v[106:107], v[114:115], v[116:117] op_sel:[1,1] op_sel_hi:[1,0] neg_lo:[0,1]\n" \
  "v_cvt_pk_f16_f32 v112, v114, v115\n" \
  "v_pk_fma_f32 v[122:123], v[122:123], v[116:117], v[104:105] op_sel_hi:[0,1,1]\n" \
  "v_pk_fma_f32 v[114:115], v[114:115], v[116:117], v[106:107] op_sel_hi:[0,1,1]\n" \
  "ds_write_addtid_b32 v112 offset:8160\n" \
  "v_pk_mul_f32 v[104:105], v[122:123], v[116:117] op_sel:[1,1] op_sel_hi:[1,0] neg_lo:[0,1]\n" \
  "v_pk_mul_f32 v[106:107], v[114:115], v[116:117] op_sel:[1,1] op_sel_hi:[1,0] neg_lo:[0,1]\n" \
  "v_cvt_pk_f16_f32 v112, v122, v123\n" \
  "v_cvt_pk_f16_f32 v103, v114, v115\n" \
  "v_pk_fma_f32 v[122:123], v[122:123], v[116:117], v[104:105] op_sel_hi:[0,1,1]\n" \
  "v_pk_fma_f32 v[114:115], v[114:115], v[116:117], v[106:107] op_sel_hi:[0,1,1]\n" \
  "ds_write_addtid_b32 v112 offset:10880\n" \
  "ds_write_addtid_b32 v103 offset:11424\n" \
  "v_pk_mul_f32 v[104:105], v[122:123], v[116:117] op_sel:[1,1] op_sel_hi:[1,0] neg_lo:[0,1]\n" \
  "v_pk_mul_f32 v[106:107], v[114:115], v[116:117] op_sel:[1,1] op_sel_hi:[1,0] neg_lo:[0,1]\n" \
  "v_cvt_pk_f16_f32 v112, v122, v123\n" \
  "v_cvt_pk_f16_f32 v103, v114, v115\n" \
  "v_pk_fma_f32 v[122:123], v[122:123], v[116:117], v[104:105] op_sel_hi:[0,1,1]\n" \
  "v_pk_fma_f32 v[114:115], v[114:115], v[116:117], v[106:107] op_sel_hi:[0,1,1]\n" \
  "ds_write_addtid_b32 v112 offset:12512\n" \
  "ds_write_addtid_b32 v103 offset:13056\n" \
  "v_cvt_pk_f16_f32 v112, v122, v123\n" \
  "v_cvt_pk_f16_f32 v103, v114, v115\n" \
  "ds_write_addtid_b32 v112 offset:13600\n" \
  "ds_write_addtid_b32 v103 offset:14144\n" \
  "s_branch Lgk%=\n" \
  "Lgb%=:\n" \
  "s_barrier\n" \
  GSTAMP2 \
  "s_cmp_lt_u32 s50, " STR(GEAR) "\n" \
  "s_cbranch_scc1 Lne8%=\n" \
  "s_waitcnt lgkmcnt(0)\n" \
  "global_load_short_d16_hi v108, v17, s[40:41]\n" \
  "global_load_short_d16_hi v109, v17, s[42:43]\n" \
  "s_load_dword s98, s[48:49], s84\n" \
  "Lne8%=:\n" \
  "v_lshrrev_b32 v3, 1, v2\n" \
  "v_add3_u32 v3, v3, v1, 1\n" \
  "v_and_b32 v8, 1, v3\n" \
  "v_mul_u32_u24 v8, 1470, v8\n" \
  "v_lshl_add_u32 v3, v3, 1, v8\n" \
  "s_add_u32 s55, s58, 4544\n" \
  "v_add_u32 v3, s55, v3\n" \
  "v_lshlrev_b32 v8, 5, v1\n" \
  "v_sub_u32 v10, v2, v8\n" \
  "s_add_u32 s55, s58, 480\n" \
  "v_add_u32 v10, s55, v10\n" \
  "v_mul_u32_u24 v11, 20, v2\n" \
  "v_lshl_add_u32 v11, v1, 4, v11\n" \
  "v_add_u32 v11, s57, v11\n" \
  "v_lshlrev_b32 v8, 2, v1\n" \
  "v_sub_u32 v8, v8, v2\n" \
  "s_add_u32 s55, s58, 7540\n" \
  "v_add_u32 v8, s55, v8\n" \
  "s_bitcmp0_b32 s51, 0\n" \
  "s_cbranch_scc1 Lo8%=\n" \
  "s_lshr_b32 s55, s51, 1\n" \
  "s_mul_i32 s55, s55, 1470\n" \
  "s_add_u32 s55, s55, s58\n" \
  "v_lshrrev_b32 v19, 3, v2\n" \
  "v_mul_u32_u24 v19, 29, v19\n" \
  "v_lshl_add_u32 v19, v1, 1, v19\n" \
  "v_add_u32 v19, s55, v19\n" \
  "v_mov_b32 v20, 0x3c00\n" \
  "s_mov_b32 exec_lo, 0x001f0007\n" \
  "s_mov_b32 exec_hi, 0x01ff007f\n" \
  "ds_write_b16 v19, v20 offset:5798\n" \
  "s_mov_b64 exec, -1\n" \
  "Lo8%=:\n" \
  "s_lshl_b32 s54, s59, 2\n" \
  "v_mov_b32 v9, s57\n" \
  "s_sub_u32 s55, s51, s50\n" \
  "s_cmp_eq_u32 s51, 3\n" \
  "s_cselect_b32 s55, 3, s55\n" \
  "s_waitcnt vmcnt(0) lgkmcnt(0)\n" \
  "s_lshl_b32 s98, s98, s53\n" \
  "s_and_b32 s98, s98, 0xffff0000\n" \
  "v_mul_f32 v108, 0x3fb8aa3b, v108\n" \
  "v_mov_b32 v112, s98\n" \
  "v_mul_f32 v112, 0x3fb8aa3b, v112\n" \
  "v_exp_f32 v108, v108\n" \
  "v_exp_f32 v112, v112\n" \
  "v_mov_b32 v103, 0x3c00\n" \
  "v_cvt_pk_bf16_f32 v108, 0, v108\n" \
  "v_cvt_pk_bf16_f32 v112, 0, v112\n" \
  "v_pk_mul_f32 v[108:109], v[108:109], v[112:113] op_sel_hi:[1,0]\n" \
  "v_mul_f32 v115, 0xbfa2f983, v109\n" \
  "v_mul_f32 v117, 0xc1a2f983, v109\n" \
  "v_mul_f32 v114, 0xc138aa3b, v108\n" \
  "v_mul_f32 v116, 0xc338aa3b, v108\n" \
  "v_fract_f32 v115, v115\n" \
  "v_fract_f32 v117, v117\n" \
  "v_exp_f32 v114, v114\n" \
  "v_cos_f32 v118, v115\n" \
  "v_sin_f32 v119, v115\n" \
  "v_exp_f32 v116, v116\n" \
  "v_cos_f32 v120, v117\n" \
  "v_sin_f32 v121, v117\n" \
  "v_pk_mul_f32 v[118:119], v[118:119], v[114:115] op_sel_hi:[1,0]\n" \
  "v_pk_mul_f32 v[120:121], v[120:121], v[116:117] op_sel_hi:[1,0]\n" \
  "ds_write_addtid_b32 v103 offset:14688\n" \
  "v_pk_mul_f32 v[104:105], v[118:119], v[118:119] op_sel:[1,1] op_sel_hi:[1,0] neg_lo:[0,1]\n" \
  "v_pk_mul_f32 v[106:107], v[120:121], v[118:119] op_sel:[1,1] op_sel_hi:[1,0] neg_lo:[0,1]\n" \
  "v_cvt_pk_f16_f32 v112, v118, v119\n" \
  "v_cvt_pk_f16_f32 v103, v120, v121\n" \
  "v_pk_fma_f32 v[110:111], v[118:119], v[118:119], v[104:105] op_sel_hi:[0,1,1]\n" \
  "v_pk_fma_f32 v[122:123], v[120:121], v[118:119], v[106:107] op_sel_hi:[0,1,1]\n" \
  "ds_write_addtid_b32 v112 offset:15232\n" \
  "ds_write_addtid_b32 v103 offset:23392\n" \
  "v_pk_mul_f32 v[104:105], v[110:111], v[118:119] op_sel:[1,1] op_sel_hi:[1,0] neg_lo:[0,1]\n" \
  "v_pk_mul_f32 v[106:107], v[122:123], v[118:119] op_sel:[1,1] op_sel_hi:[1,0] neg_lo:[0,1]\n" \
  "v_cvt_pk_f16_f32 v112, v110, v111\n" \
  "v_cvt_pk_f16_f32 v103, v122, v123\n" \
  "v_pk_fma_f32 v[110:111], v[110:111], v[118:119], v[104:105] op_sel_hi:[0,1,1]\n" \
  "v_pk_fma_f32 v[122:123], v[122:123], v[118:119], v[106:107] op_sel_hi:[0,1,1]\n" \
  "ds_write_addtid_b32 v112 offset:15776\n" \
  "ds_write_addtid_b32 v103 offset:23936\n" \
  "v_pk_mul_f32 v[104:105], v[110:111], v[118:119] op_sel:[1,1] op_sel_hi:[1,0] neg_lo:[0,1]\n" \
  "v_pk_mul_f32 v[106:107], v[122:123], v[118:119] op_sel:[1,1] op_sel_hi:[1,0] neg_lo:[0,1]\n" \
  "v_cvt_pk_f16_f32 v112, v110, v111\n" \
  "v_cvt_pk_f16_f32 v103, v122, v123\n" \
  "v_pk_fma_f32 v[110:111], v[110:111], v[118:119], v[104:105] op_sel_hi:[0,1,1]\n" \
  "v_pk_fma_f32 v[122:123], v[122:123], v[118:119], v[106:107] op_sel_hi:[0,1,1]\n" \
  "ds_write_addtid_b32 v112 offset:16320\n" \
  "ds_write_addtid_b32 v103 offset:24480\n" \
  "v_pk_mul_f32 v[104:105], v[110:111], v[118:119] op_sel:[1,1] op_sel_hi:[1,0] neg_lo:[0,1]\n" \
  "v_pk_mul_f32 v[106:107], v[122:123], v[118:119] op_sel:[1,1] op_sel_hi:[1,0] neg_lo:[0,1]\n" \
  "v_cvt_pk_f16_f32 v112, v110, v111\n" \
  "v_cvt_pk_f16_f32 v103, v122, v123\n" \
  "v_pk_fma_f32 v[110:111], v[110:111], v[118:119], v[104:105] op_sel_hi:[0,1,1]\n" \
  "v_pk_fma_f32 v[122:123], v[122:123], v[118:119], v[106:107] op_sel_hi:[0,1,1]\n" \
  "ds_write_addtid_b32 v112 offset:16864\n" \
  "ds_write_addtid_b32 v103 offset:25024\n" \
  "v_pk_mul_f32 v[104:105], v[110:111], v[118:119] op_sel:[1,1] op_sel_hi:[1,0] neg_lo:[0,1]\n" \
  "v_pk_mul_f32 v[106:107], v[122:123], v[118:119] op_sel:[1,1] op_sel_hi:[1,0] neg_lo:[0,1]\n" \
  "v_cvt_pk_f16_f32 v112, v110, v111\n" \
  "v_cvt_pk_f16_f32 v103, v122, v123\n" \
  "v_pk_fma_f32 v[110:111], v[110:111], v[118:119], v[104:105] op_sel_hi:[0,1,1]\n" \
  "v_pk_fma_f32 v[122:123], v[122:123], v[118:119], v[106:107] op_sel_hi:[0,1,1]\n" \
  "ds_write_addtid_b32 v112 offset:17408\n" \
  "ds_write_addtid_b32 v103 offset:25568\n" \
  "v_pk_mul_f32 v[104:105], v[110:111], v[118:119] op_sel:[1,1] op_sel_hi:[1,0] neg_lo:[0,1]\n" \
  "v_pk_mul_f32 v[106:107], v[122:123], v[118:119] op_sel:[1,1] op_sel_hi:[1,0] neg_lo:[0,1]\n" \
  "v_cvt_pk_f16_f32 v112, v110, v111\n" \
  "v_cvt_pk_f16_f32 v103, v122, v123\n" \
  "v_pk_fma_f32 v[110:111], v[110:111], v[118:119], v[104:105] op_sel_hi:[0,1,1]\n" \
  "v_pk_fma_f32 v[122:123], v[122:123], v[118:119], v[106:107] op_sel_hi:[0,1,1]\n" \
  "ds_write_addtid_b32 v112 offset:17952\n" \
  "ds_write_addtid_b32 v103 offset:26112\n" \
  "v_pk_mul_f32 v[104:105], v[110:111], v[118:119] op_sel:[1,1] op_sel_hi:[1,0] neg_lo:[0,1]\n" \
  "v_pk_mul_f32 v[106:107], v[122:123], v[118:119] op_sel:[1,1] op_sel_hi:[1,0] neg_lo:[0,1]\n" \
  "v_cvt_pk_f16_f32 v112, v110, v111\n" \
  "v_cvt_pk_f16_f32 v103, v122, v123\n" \
  "v_pk_fma_f32 v[110:111], v[110:111], v[118:119], v[104:105] op_sel_hi:[0,1,1]\n" \
  "v_pk_fma_f32 v[122:123], v[122:123], v[118:119], v[106:107] op_sel_hi:[0,1,1]\n" \
  "ds_write_addtid_b32 v112 offset:18496\n" \
  "ds_write_addtid_b32 v103 offset:26656\n" \
  "v_pk_mul_f32 v[104:105], v[110:111], v[118:119] op_sel:[1,1] op_sel_hi:[1,0] neg_lo:[0,1]\n" \
  "v_pk_mul_f32 v[106:107], v[122:123], v[118:119] op_sel:[1,1] op_sel_hi:[1,0] neg_lo:[0,1]\n" \
  "v_cvt_pk_f16_f32 v112, v110, v111\n" \
  "v_cvt_pk_f16_f32 v103, v122, v123\n" \
  "v_pk_fma_f32 v[110:111], v[110:111], v[118:119], v[104:105] op_sel_hi:[0,1,1]\n" \
  "v_pk_fma_f32 v[122:123], v[122:123], v[118:119], v[106:107] op_sel_hi:[0,1,1]\n" \
  "ds_write_addtid_b32 v112 offset:19040\n" \
  "ds_write_addtid_b32 v103 offset:27200\n" \
  "v_pk_mul_f32 v[104:105], v[110:111], v[118:119] op_sel:[1,1] op_sel_hi:[1,0] neg_lo:[0,1]\n" \
  "v_pk_mul_f32 v[106:107], v[122:123], v[118:119] op_sel:[1,1] op_sel_hi:[1,0] neg_lo:[0,1]\n" \
  "v_cvt_pk_f16_f32 v112, v110, v111\n" \
  "v_cvt_pk_f16_f32 v103, v122, v123\n" \
  "v_pk_fma_f32 v[110:111], v[110:111], v[118:119], v[104:105] op_sel_hi:[0,1,1]\n" \
  "v_pk_fma_f32 v[122:123], v[122:123], v[118:119], v[106:107] op_sel_hi:[0,1,1]\n" \
  "ds_write_addtid_b32 v112 offset:19584\n" \
  "ds_write_addtid_b32 v103 offset:27744\n" \
  "v_pk_mul_f32 v[104:105], v[110:111], v[118:119] op_sel:[1,1] op_sel_hi:[1,0] neg_lo:[0,1]\n" \
  "v_pk_mul_f32 v[106:107], v[122:123], v[118:119] op_sel:[1,1] op_sel_hi:[1,0] neg_lo:[0,1]\n" \
  "v_cvt_pk_f16_f32 v112, v110, v111\n" \
  "v_cvt_pk_f16_f32 v103, v122, v123\n" \
  "v_pk_fma_f32 v[110:111], v[110:111], v[118:119], v[104:105] op_sel_hi:[0,1,1]\n" \
  "v_pk_fma_f32 v[122:123], v[122:123], v[118:119], v[106:107] op_sel_hi:[0,1,1]\n" \
  "ds_write_addtid_b32 v112 offset:20128\n" \
  "ds_write_addtid_b32 v103 offset:28288\n" \
  "v_pk_mul_f32 v[104:105], v[110:111], v[118:119] op_sel:[1,1] op_sel_hi:[1,0] neg_lo:[0,1]\n" \
  "v_pk_mul_f32 v[106:107], v[122:123], v[118:119] op_sel:[1,1] op_sel_hi:[1,0] neg_lo:[0,1]\n" \
  "v_cvt_pk_f16_f32 v112, v110, v111\n" \
  "v_cvt_pk_f16_f32 v103, v122, v123\n" \
  "v_pk_fma_f32 v[110:111], v[110:111], v[118:119], v[104:105] op_sel_hi:[0,1,1]\n" \
  "v_pk_fma_f32 v[122:123], v[122:123], v[118:119], v[106:107] op_sel_hi:[0,1,1]\n" \
  "ds_write_addtid_b32 v112 offset:20672\n" \
  "ds_write_addtid_b32 v103 offset:28832\n" \
  "v_pk_mul_f32 v[104:105], v[110:111], v[118:119] op_sel:[1,1] op_sel_hi:[1,0] neg_lo:[0,1]\n" \
  "v_pk_mul_f32 v[106:107], v[122:123], v[118:119] op_sel:[1,1] op_sel_hi:[1,0] neg_lo:[0,1]\n" \
  "v_cvt_pk_f16_f32 v112, v110, v111\n" \
  "v_cvt_pk_f16_f32 v103, v122, v123\n" \
  "v_pk_fma_f32 v[110:111], v[110:111], v[118:119], v[104:105] op_sel_hi:[0,1,1]\n" \
  "v_pk_fma_f32 v[122:123], v[122:123], v[118:119], v[106:107] op_sel_hi:[0,1,1]\n" \
  "ds_write_addtid_b32 v112 offset:21216\n" \
  "ds_write_addtid_b32 v103 offset:29376\n" \
  "v_pk_mul_f32 v[104:105], v[110:111], v[118:119] op_sel:[1,1] op_sel_hi:[1,0] neg_lo:[0,1]\n" \
  "v_pk_mul_f32 v[106:107], v[122:123], v[118:119] op_sel:[1,1] op_sel_hi:[1,0] neg_lo:[0,1]\n" \
  "v_cvt_pk_f16_f32 v112, v110, v111\n" \
  "v_cvt_pk_f16_f32 v103, v122, v123\n" \
  "v_pk_fma_f32 v[110:111], v[110:111], v[118:119], v[104:105] op_sel_hi:[0,1,1]\n" \
  "v_pk_fma_f32 v[122:123], v[122:123], v[118:119], v[106:107] op_sel_hi:[0,1,1]\n" \
  "ds_write_addtid_b32 v112 offset:21760\n" \
  "ds_write_addtid_b32 v103 offset:29920\n" \
  "v_pk_mul_f32 v[104:105], v[110:111], v[118:119] op_sel:[1,1] op_sel_hi:[1,0] neg_lo:[0,1]\n" \
  "v_pk_mul_f32 v[106:107], v[122:123], v[118:119] op_sel:[1,1] op_sel_hi:[1,0] neg_lo:[0,1]\n" \
  "v_cvt_pk_f16_f32 v112, v110, v111\n" \
  "v_cvt_pk_f16_f32 v103, v122, v123\n" \
  "v_pk_fma_f32 v[110:111], v[110:111], v[118:119], v[104:105] op_sel_hi:[0,1,1]\n" \
  "v_pk_fma_f32 v[122:123], v[122:123], v[118:119], v[106:107] op_sel_hi:[0,1,1]\n" \
  "ds_write_addtid_b32 v112 offset:22304\n" \
  "ds_write_addtid_b32 v103 offset:30464\n" \
  "v_pk_mul_f32 v[104:105], v[122:123], v[118:119] op_sel:[1,1] op_sel_hi:[1,0] neg_lo:[0,1]\n" \
  "v_cvt_pk_f16_f32 v112, v110, v111\n" \
  "v_cvt_pk_f16_f32 v103, v122, v123\n" \
  "v_pk_fma_f32 v[122:123], v[122:123], v[118:119], v[104:105] op_sel_hi:[0,1,1]\n" \
  "ds_write_addtid_b32 v112 offset:22848\n" \
  "ds_write_addtid_b32 v103 offset:31008\n" \
  "v_cvt_pk_f16_f32 v112, v122, v123\n" \
  "ds_write_addtid_b32 v112 offset:31552\n" \
  "Lgk%=:\n" \
  "ds_add_u32 v4, v16 offset:8\n" \
  GSTAMP3 \
  "s_mov_b32 s97, 0x1fffff\n" \
  "Lkp%=:\n" \
  "ds_read_b32 v17, v4 offset:8\n" \
  "s_waitcnt lgkmcnt(0)\n" \
  "v_readfirstlane_b32 s96, v17\n" \
  "s_and_b32 s96, s96, s54\n" \
  "s_cmp_eq_u32 s96, s54\n" \
  "s_cbranch_scc1 Lkp%=x\n" \
  "s_sleep 1\n" \
  "s_sub_u32 s97, s97, 1\n" \
  "s_cbranch_scc0 Lkp%=\n" \
  "Lkp%=x:\n" \
  GSTAMP4 \
  "ds_read2st64_b32 v[18:19], v9 offset0:34 offset1:35\n" \
  "ds_read_b128 v[24:27], v5 offset:0\n" \
  "ds_read_b128 v[28:31], v6 offset:0\n" \
  "ds_read_b128 v[56:59], v5 offset:256\n" \
  "ds_read_b128 v[60:63], v6 offset:256\n" \
  "ds_read_b128 v[32:35], v5 offset:64\n" \
  "ds_read_b128 v[36:39], v6 offset:64\n" \
  "ds_read_b128 v[64:67], v5 offset:320\n" \
  "ds_read_b128 v[68:71], v6 offset:320\n" \
  "ds_read_b128 v[40:43], v5 offset:128\n" \
  "ds_read_b128 v[44:47], v6 offset:128\n" \
  "ds_read_b128 v[72:75], v5 offset:384\n" \
  "ds_read_b128 v[76:79], v6 offset:384\n" \
  "ds_read_b128 v[48:51], v5 offset:192\n" \
  "ds_read_b128 v[52:55], v6 offset:192\n" \
  "ds_read_b128 v[80:83], v5 offset:448\n" \
  "ds_read_b128 v[84:87], v6 offset:448\n" \
  "s_waitcnt lgkmcnt(14)\n" \
  "v_max_u32 v17, v18, v19\n" \
  "v_sub_u32 v20, 121, v17\n" \
  "s_waitcnt lgkmcnt(14)\n" \
  "v_mfma_f32_16x16x32_f16 v[88:91], v[24:27], v[28:31], 0\n" \
  "v_add_u32 v18, v18, v20\n" \
  "v_add_u32 v19, v19, v20\n" \
  "s_waitcnt lgkmcnt(12)\n" \
  "v_mfma_f32_16x16x32_f16 v[92:95], v[56:59], v[60:63], 0\n" \
  "v_max_i32 v18, 0, v18\n" \
  "v_max_i32 v19, 0, v19\n" \
  "s_waitcnt lgkmcnt(10)\n" \
  "v_mfma_f32_16x16x32_f16 v[88:91], v[32:35], v[36:39], v[88:91]\n" \
  "v_lshlrev_b32 v18, 23, v18\n" \
  "v_lshlrev_b32 v19, 23, v19\n" \
  "s_waitcnt lgkmcnt(8)\n" \
  "v_mfma_f32_16x16x32_f16 v[92:95], v[64:67], v[68:71], v[92:95]\n" \
  "s_waitcnt lgkmcnt(6)\n" \
  "v_mfma_f32_16x16x32_f16 v[88:91], v[40:43], v[44:47], v[88:91]\n" \
  "s_waitcnt lgkmcnt(4)\n" \
  "v_mfma_f32_16x16x32_f16 v[92:95], v[72:75], v[76:79], v[92:95]\n" \
  "s_waitcnt lgkmcnt(2)\n" \
  "v_mfma_f32_16x16x32_f16 v[88:91], v[48:51], v[52:55], v[88:91]\n" \
  "s_waitcnt lgkmcnt(0)\n" \
  "v_mfma_f32_16x16x32_f16 v[92:95], v[80:83], v[84:87], v[92:95]\n" \
  "s_cmp_lt_u32 s51, 2\n" \
  "v_mov_b32 v21, 1\n" \
  "s_nop 3\n" \
  "v_pk_mul_f32 v[88:89], v[88:89], v[18:19] op_sel_hi:[1,0]\n" \
  "v_pk_mul_f32 v[90:91], v[90:91], v[18:19] op_sel_hi:[1,0]\n" \
  "v_pk_fma_f32 v[88:89], v[92:93], v[18:19], v[88:89] op_sel:[0,1,0]\n" \
  "v_pk_fma_f32 v[90:91], v[94:95], v[18:19], v[90:91] op_sel:[0,1,0]\n" \
  "v_cvt_pk_f16_f32 v88, v88, v89\n" \
  "v_cvt_pk_f16_f32 v89, v90, v91\n" \
  "s_cbranch_scc0 Lkf%=\n" \
  "ds_write_b64 v7, v[88:89] offset:7488\n" \
  "v_alignbit_b32 v90, v89, v88, 16\n" \
  "ds_write_b16 v7, v88 offset:8958\n" \
  "ds_write_b16_d16_hi v7, v89 offset:8964\n" \
  "ds_write_b32 v7, v90 offset:8960\n" \
  "s_cmp_eq_u32 s51, 0\n" \
  "s_cbranch_scc1 Lkd%=\n" \
  "s_mov_b32 exec_lo, 0\n" \
  "s_brev_b32 exec_hi, 1\n" \
  "ds_write_b16_d16_hi v7, v89 offset:7496\n" \
  "ds_write_b16_d16_hi v7, v89 offset:7498\n" \
  "ds_write_b16_d16_hi v7, v89 offset:7500\n" \
  "ds_write_b16_d16_hi v7, v89 offset:8966\n" \
  "ds_write_b16_d16_hi v7, v89 offset:8968\n" \
  "ds_write_b16_d16_hi v7, v89 offset:8970\n" \
  "s_branch Lke%=\n" \
  "Lkf%=:\n" \
  "s_mov_b32 exec_lo, 0x07b607b6\n" \
  "s_mov_b32 exec_hi, 0x07b607b6\n" \
  "ds_write_b64 v7, v[88:89] offset:7488\n" \
  "s_cmp_eq_u32 s51, 2\n" \
  "s_cbranch_scc1 Lk2%=\n" \
  "s_mov_b32 exec_lo, 0\n" \
  "s_mov_b32 exec_hi, 0x200000\n" \
  "ds_write_b16_d16_hi v7, v89 offset:5358\n" \
  "ds_write_b16_d16_hi v7, v89 offset:5360\n" \
  "ds_write_b16_d16_hi v7, v89 offset:6828\n" \
  "ds_write_b16_d16_hi v7, v89 offset:6830\n" \
  "s_branch Lke%=\n" \
  "Lk2%=:\n" \
  "s_mov_b64 exec, -1\n" \
  "ds_add_u32 v4, v16 offset:12\n" \
  "ds_read_b32 v18, v9 offset:10336\n" \
  "v_add_u32 v17, -5, v17\n" \
  "v_lshlrev_b32 v17, 23, v17\n" \
  "s_lshl2_add_u32 s56, s50, 158224\n" \
  "v_mov_b32 v19, s56\n" \
  "s_waitcnt lgkmcnt(0)\n" \
  "v_mul_f32 v17, v17, v18\n" \
  "ds_write_b32 v19, v17\n" \
  "s_branch Lkg%=\n" \
  "Lke%=:\n" \
  "s_mov_b64 exec, -1\n" \
  "Lkd%=:\n" \
  "ds_add_u32 v4, v16 offset:12\n" \
  "Lkg%=:\n" \
  "ds_add_u32 v4, v21 offset:4\n" \
  GSTAMP5 \
  "s_setprio " STR(PRK) "\n" \
  "s_cmp_eq_u32 s55, 2\n" \
  "s_cbranch_scc1 Lr4%=\n" \
  "s_cmp_eq_u32 s55, -1\n" \
  "s_cbranch_scc1 Lr4%=\n" \
  "s_mov_b32 s97, 0x1fffff\n" \
  "Lkq%=:\n" \
  "ds_read_b32 v17, v4 offset:12\n" \
  "s_waitcnt lgkmcnt(0)\n" \
  "v_readfirstlane_b32 s96, v17\n" \
  "s_and_b32 s96, s96, s54\n" \
  "s_cmp_eq_u32 s96, s54\n" \
  "s_cbranch_scc1 Lkq%=x\n" \
  "s_sleep 1\n" \
  "s_sub_u32 s97, s97, 1\n" \
  "s_cbranch_scc0 Lkq%=\n" \
  "Lkq%=x:\n" \
  GSTAMP6 \
  "s_cmp_eq_u32 s55, 3\n" \
  "s_cbranch_scc1 Lr1%=\n" \
  "s_cmp_lg_u32 s55, 0\n" \
  "s_cbranch_scc1 Lr2%=\n" \
  "ds_read2_b32 v[60:61], v3 offset0:0 offset1:1\n" \
  "ds_read2_b32 v[62:63], v3 offset0:2 offset1:3\n" \
  "ds_read2_b32 v[92:93], v3 offset0:128 offset1:129\n" \
  "ds_read2_b32 v[94:95], v3 offset0:130 offset1:131\n" \
  "ds_read2_b32 v[64:65], v3 offset0:16 offset1:17\n" \
  "ds_read2_b32 v[66:67], v3 offset0:18 offset1:19\n" \
  "ds_read2_b32 v[96:97], v3 offset0:144 offset1:145\n" \
  "ds_read2_b32 v[98:99], v3 offset0:146 offset1:147\n" \
  "ds_read2_b32 v[68:69], v3 offset0:32 offset1:33\n" \
  "ds_read2_b32 v[70:71], v3 offset0:34 offset1:35\n" \
  "ds_read2_b32 v[100:101], v3 offset0:160 offset1:161\n" \
  "ds_read2_b32 v[102:103], v3 offset0:162 offset1:163\n" \
  "ds_read2_b32 v[72:73], v3 offset0:48 offset1:49\n" \
  "ds_read2_b32 v[74:75], v3 offset0:50 offset1:51\n" \
  "ds_read2_b32 v[104:105], v3 offset0:176 offset1:177\n" \
  "ds_read2_b32 v[106:107], v3 offset0:178 offset1:179\n" \
  "ds_read2_b32 v[76:77], v3 offset0:64 offset1:65\n" \
  "ds_read2_b32 v[78:79], v3 offset0:66 offset1:67\n" \
  "ds_read2_b32 v[108:109], v3 offset0:192 offset1:193\n" \
  "ds_read2_b32 v[110:111], v3 offset0:194 offset1:195\n" \
  "ds_read2_b32 v[80:81], v3 offset0:80 offset1:81\n" \
  "ds_read2_b32 v[82:83], v3 offset0:82 offset1:83\n" \
  "ds_read2_b32 v[112:113], v3 offset0:208 offset1:209\n" \
  "ds_read2_b32 v[114:115], v3 offset0:210 offset1:211\n" \
  "ds_read2_b32 v[84:85], v3 offset0:96 offset1:97\n" \
  "ds_read2_b32 v[86:87], v3 offset0:98 offset1:99\n" \
  "ds_read2_b32 v[116:117], v3 offset0:224 offset1:225\n" \
  "ds_read2_b32 v[118:119], v3 offset0:226 offset1:227\n" \
  "ds_read2_b32 v[88:89], v3 offset0:112 offset1:113\n" \
  "ds_read2_b32 v[90:91], v3 offset0:114 offset1:115\n" \
  "ds_read2_b32 v[120:121], v3 offset0:240 offset1:241\n" \
  "ds_read2_b32 v[122:123], v3 offset0:242 offset1:243\n" \
  "v_add_u32 v9, 1024, v3\n" \
  "ds_read2_b32 v[124:125], v9 offset0:0 offset1:1\n" \
  "ds_read2_b32 v[126:127], v9 offset0:2 offset1:3\n" \
  "s_mov_b32 s97, 0x1fffff\n" \
  "Lnp%=:\n" \
  "ds_read_b32 v8, v4 offset:4\n" \
  "s_waitcnt lgkmcnt(0)\n" \
  "v_readfirstlane_b32 s96, v8\n" \
  "s_and_b32 s96, s96, 0xff\n" \
  "s_cmp_eq_u32 s96, 16\n" \
  "s_cbranch_scc1 Lnp%=x\n" \
  "s_sleep 1\n" \
  "s_sub_u32 s97, s97, 1\n" \
  "s_cbranch_scc0 Lnp%=\n" \
  "Lnp%=x:\n" \
  "s_setprio " STR(PRN) "\n" \
  "ds_read_b128 v[28:31], v10 offset:3584\n" \
  "ds_read_b128 v[32:35], v10 offset:3648\n" \
  "ds_read_b128 v[36:39], v10 offset:3712\n" \
  "ds_read_b128 v[40:43], v10 offset:3776\n" \
  "ds_read_b128 v[44:47], v10 offset:3840\n" \
  "ds_read_b128 v[48:51], v10 offset:3904\n" \
  "ds_read_b128 v[52:55], v10 offset:3968\n" \
  "ds_read_b128 v[56:59], v10 offset:4032\n" \
  "s_waitcnt lgkmcnt(6)\n" \
  "v_mfma_f32_16x16x32_f16 v[12:15], v[60:63], v[28:31], 0\n" \
  "v_mfma_f32_16x16x32_f16 v[16:19], v[92:95], v[28:31], 0\n" \
  "v_mfma_f32_16x16x32_f16 v[20:23], v[124:127], v[28:31], 0\n" \
  "ds_read_b128 v[28:31], v10 offset:3072\n" \
  "v_mfma_f32_16x16x32_f16 v[12:15], v[64:67], v[32:35], v[12:15]\n" \
  "v_mfma_f32_16x16x32_f16 v[16:19], v[96:99], v[32:35], v[16:19]\n" \
  "ds_read_b128 v[32:35], v10 offset:3136\n" \
  "s_waitcnt lgkmcnt(6)\n" \
  "v_mfma_f32_16x16x32_f16 v[12:15], v[68:71], v[36:39], v[12:15]\n" \
  "v_mfma_f32_16x16x32_f16 v[16:19], v[100:103], v[36:39], v[16:19]\n" \
  "ds_read_b128 v[36:39], v10 offset:3200\n" \
  "v_mfma_f32_16x16x32_f16 v[12:15], v[72:75], v[40:43], v[12:15]\n" \
  "v_mfma_f32_16x16x32_f16 v[16:19], v[104:107], v[40:43], v[16:19]\n" \
  "ds_read_b128 v[40:43], v10 offset:3264\n" \
  "s_waitcnt lgkmcnt(6)\n" \
  "v_mfma_f32_16x16x32_f16 v[12:15], v[76:79], v[44:47], v[12:15]\n" \
  "v_mfma_f32_16x16x32_f16 v[16:19], v[108:111], v[44:47], v[16:19]\n" \
  "ds_read_b128 v[44:47], v10 offset:3328\n" \
  "v_mfma_f32_16x16x32_f16 v[12:15], v[80:83], v[48:51], v[12:15]\n" \
  "v_mfma_f32_16x16x32_f16 v[16:19], v[112:115], v[48:51], v[16:19]\n" \
  "ds_read_b128 v[48:51], v10 offset:3392\n" \
  "s_waitcnt lgkmcnt(6)\n" \
  "v_mfma_f32_16x16x32_f16 v[12:15], v[84:87], v[52:55], v[12:15]\n" \
  "v_mfma_f32_16x16x32_f16 v[16:19], v[116:119], v[52:55], v[16:19]\n" \
  "ds_read_b128 v[52:55], v10 offset:3456\n" \
  "v_mfma_f32_16x16x32_f16 v[12:15], v[88:91], v[56:59], v[12:15]\n" \
  "v_mfma_f32_16x16x32_f16 v[16:19], v[120:123], v[56:59], v[16:19]\n" \
  "ds_read_b128 v[56:59], v10 offset:3520\n" \
  "s_waitcnt lgkmcnt(6)\n" \
  "v_mfma_f32_16x16x32_f16 v[16:19], v[60:63], v[28:31], v[16:19]\n" \
  "v_mfma_f32_16x16x32_f16 v[20:23], v[92:95], v[28:31], v[20:23]\n" \
  "v_mfma_f32_16x16x32_f16 v[24:27], v[124:127], v[28:31], 0\n" \
  "ds_read_b128 v[28:31], v10 offset:2560\n" \
  "v_mfma_f32_16x16x32_f16 v[16:19], v[64:67], v[32:35], v[16:19]\n" \
  "v_mfma_f32_16x16x32_f16 v[20:23], v[96:99], v[32:35], v[20:23]\n" \
  "ds_read_b128 v[32:35], v10 offset:2624\n" \
  "s_waitcnt lgkmcnt(6)\n" \
  "v_mfma_f32_16x16x32_f16 v[16:19], v[68:71], v[36:39], v[16:19]\n" \
  "v_mov_b32 v9, 0x1\n" \
  "ds_write_b128 v11, v[12:15] offset:0\n" \
  "ds_add_u32 v4, v9 offset:0\n" \
  "v_mfma_f32_16x16x32_f16 v[20:23], v[100:103], v[36:39], v[20:23]\n" \
  "ds_read_b128 v[36:39], v10 offset:2688\n" \
  "v_mfma_f32_16x16x32_f16 v[16:19], v[72:75], v[40:43], v[16:19]\n" \
  "v_mfma_f32_16x16x32_f16 v[20:23], v[104:107], v[40:43], v[20:23]\n" \
  "ds_read_b128 v[40:43], v10 offset:2752\n" \
  "s_waitcnt lgkmcnt(8)\n" \
  "v_mfma_f32_16x16x32_f16 v[16:19], v[76:79], v[44:47], v[16:19]\n" \
  "v_mfma_f32_16x16x32_f16 v[20:23], v[108:111], v[44:47], v[20:23]\n" \
  "ds_read_b128 v[44:47], v10 offset:2816\n" \
  "v_mfma_f32_16x16x32_f16 v[16:19], v[80:83], v[48:51], v[16:19]\n" \
  "v_mfma_f32_16x16x32_f16 v[20:23], v[112:115], v[48:51], v[20:23]\n" \
  "ds_read_b128 v[48:51], v10 offset:2880\n" \
  "s_waitcnt lgkmcnt(8)\n" \
  "v_mfma_f32_16x16x32_f16 v[16:19], v[84:87], v[52:55], v[16:19]\n" \
  "v_mfma_f32_16x16x32_f16 v[20:23], v[116:119], v[52:55], v[20:23]\n" \
  "ds_read_b128 v[52:55], v10 offset:2944\n" \
  "v_mfma_f32_16x16x32_f16 v[16:19], v[88:91], v[56:59], v[16:19]\n" \
  "v_mfma_f32_16x16x32_f16 v[20:23], v[120:123], v[56:59], v[20:23]\n" \
  "ds_read_b128 v[56:59], v10 offset:3008\n" \
  "s_waitcnt lgkmcnt(8)\n" \
  "v_mfma_f32_16x16x32_f16 v[20:23], v[60:63], v[28:31], v[20:23]\n" \
  "v_mfma_f32_16x16x32_f16 v[24:27], v[92:95], v[28:31], v[24:27]\n" \
  "ds_read_b128 v[28:31], v10 offset:2048\n" \
  "v_mfma_f32_16x16x32_f16 v[20:23], v[64:67], v[32:35], v[20:23]\n" \
  "v_mfma_f32_16x16x32_f16 v[24:27], v[96:99], v[32:35], v[24:27]\n" \
  "ds_read_b128 v[32:35], v10 offset:2112\n" \
  "s_waitcnt lgkmcnt(6)\n" \
  "v_mfma_f32_16x16x32_f16 v[20:23], v[68:71], v[36:39], v[20:23]\n" \
  "v_mov_b32 v9, 0x10\n" \
  "ds_write_b128 v11, v[16:19] offset:1280\n" \
  "ds_add_u32 v4, v9 offset:0\n" \
  "v_mfma_f32_16x16x32_f16 v[24:27], v[100:103], v[36:39], v[24:27]\n" \
  "ds_read_b128 v[36:39], v10 offset:2176\n" \
  "v_mfma_f32_16x16x32_f16 v[20:23], v[72:75], v[40:43], v[20:23]\n" \
  "v_mfma_f32_16x16x32_f16 v[24:27], v[104:107], v[40:43], v[24:27]\n" \
  "ds_read_b128 v[40:43], v10 offset:2240\n" \
  "s_waitcnt lgkmcnt(8)\n" \
  "v_mfma_f32_16x16x32_f16 v[20:23], v[76:79], v[44:47], v[20:23]\n" \
  "v_mfma_f32_16x16x32_f16 v[24:27], v[108:111], v[44:47], v[24:27]\n" \
  "ds_read_b128 v[44:47], v10 offset:2304\n" \
  "v_mfma_f32_16x16x32_f16 v[20:23], v[80:83], v[48:51], v[20:23]\n" \
  "v_mfma_f32_16x16x32_f16 v[24:27], v[112:115], v[48:51], v[24:27]\n" \
  "ds_read_b128 v[48:51], v10 offset:2368\n" \
  "s_waitcnt lgkmcnt(8)\n" \
  "v_mfma_f32_16x16x32_f16 v[20:23], v[84:87], v[52:55], v[20:23]\n" \
  "v_mfma_f32_16x16x32_f16 v[24:27], v[116:119], v[52:55], v[24:27]\n" \
  "ds_read_b128 v[52:55], v10 offset:2432\n" \
  "v_mfma_f32_16x16x32_f16 v[20:23], v[88:91], v[56:59], v[20:23]\n" \
  "v_mfma_f32_16x16x32_f16 v[24:27], v[120:123], v[56:59], v[24:27]\n" \
  "ds_read_b128 v[56:59], v10 offset:2496\n" \
  "s_waitcnt lgkmcnt(8)\n" \
  "v_mfma_f32_16x16x32_f16 v[24:27], v[60:63], v[28:31], v[24:27]\n" \
  "v_mfma_f32_16x16x32_f16 v[24:27], v[64:67], v[32:35], v[24:27]\n" \
  "s_waitcnt lgkmcnt(4)\n" \
  "v_mfma_f32_16x16x32_f16 v[24:27], v[68:71], v[36:39], v[24:27]\n" \
  "v_mov_b32 v9, 0x100\n" \
  "ds_write_b128 v11, v[20:23] offset:2560\n" \
  "ds_add_u32 v4, v9 offset:0\n" \
  "v_mfma_f32_16x16x32_f16 v[24:27], v[72:75], v[40:43], v[24:27]\n" \
  "s_waitcnt lgkmcnt(4)\n" \
  "v_mfma_f32_16x16x32_f16 v[24:27], v[76:79], v[44:47], v[24:27]\n" \
  "v_mfma_f32_16x16x32_f16 v[24:27], v[80:83], v[48:51], v[24:27]\n" \
  "s_waitcnt lgkmcnt(2)\n" \
  "v_mfma_f32_16x16x32_f16 v[24:27], v[84:87], v[52:55], v[24:27]\n" \
  "v_mfma_f32_16x16x32_f16 v[24:27], v[88:91], v[56:59], v[24:27]\n" \
  "v_mov_b32 v9, 0x1000\n" \
  "s_nop 6\n" \
  "ds_write_b128 v11, v[24:27] offset:3840\n" \
  "ds_add_u32 v4, v9 offset:0\n" \
  "s_branch Lr3%=\n" \
  "Lr1%=:\n" \
  "v_add_u32 v9, 1024, v3\n" \
  "ds_read2_b32 v[12:13], v9 offset0:48 offset1:49\n" \
  "ds_read2_b32 v[14:15], v9 offset0:50 offset1:51\n" \
  "ds_read2_b32 v[16:17], v9 offset0:44 offset1:45\n" \
  "ds_read2_b32 v[18:19], v9 offset0:46 offset1:47\n" \
  "ds_read2_b32 v[20:21], v9 offset0:61 offset1:62\n" \
  "ds_read2_b32 v[22:23], v9 offset0:63 offset1:64\n" \
  "ds_read2_b32 v[24:25], v8 offset0:0 offset1:1\n" \
  "ds_read2_b32 v[26:27], v8 offset0:2 offset1:3\n" \
  "ds_read2_b32 v[36:37], v8 offset0:48 offset1:49\n" \
  "ds_read2_b32 v[38:39], v8 offset0:50 offset1:51\n" \
  "ds_read2_b32 v[28:29], v8 offset0:16 offset1:17\n" \
  "ds_read2_b32 v[30:31], v8 offset0:18 offset1:19\n" \
  "ds_read2_b32 v[40:41], v8 offset0:64 offset1:65\n" \
  "ds_read2_b32 v[42:43], v8 offset0:66 offset1:67\n" \
  "ds_read2_b32 v[32:33], v8 offset0:32 offset1:33\n" \
  "ds_read2_b32 v[34:35], v8 offset0:34 offset1:35\n" \
  "ds_read2_b32 v[44:45], v8 offset0:80 offset1:81\n" \
  "ds_read2_b32 v[46:47], v8 offset0:82 offset1:83\n" \
  "s_mov_b32 s97, 0x1fffff\n" \
  "Lfp%=:\n" \
  "ds_read_b32 v8, v4 offset:4\n" \
  "s_waitcnt lgkmcnt(0)\n" \
  "v_readfirstlane_b32 s96, v8\n" \
  "s_and_b32 s96, s96, 0xff\n" \
  "s_cmp_eq_u32 s96, 16\n" \
  "s_cbranch_scc1 Lfp%=x\n" \
  "s_sleep 1\n" \
  "s_sub_u32 s97, s97, 1\n" \
  "s_cbranch_scc0 Lfp%=\n" \
  "Lfp%=x:\n" \
  "s_setprio " STR(PRF) "\n" \
  "ds_read_b128 v[48:51], v10 offset:3584\n" \
  "ds_read_b128 v[52:55], v10 offset:3600\n" \
  "ds_read_b128 v[56:59], v10 offset:3072\n" \
  "ds_read_b128 v[60:63], v10 offset:3088\n" \
  "s_waitcnt lgkmcnt(3)\n" \
  "v_mfma_f32_16x16x32_f16 v[64:67], v[48:51], v[12:15], 0\n" \
  "v_mfma_f32_16x16x32_f16 v[68:71], v[48:51], v[16:19], 0\n" \
  "v_mfma_f32_16x16x32_f16 v[72:75], v[48:51], v[20:23], 0\n" \
  "s_waitcnt lgkmcnt(2)\n" \
  "v_mfma_f32_16x16x32_f16 v[76:79], v[52:55], v[20:23], 0\n" \
  "ds_read_b128 v[48:51], v10 offset:2560\n" \
  "ds_read_b128 v[52:55], v10 offset:2576\n" \
  "s_nop 2\n" \
  "v_cvt_pk_f16_f32 v80, v67, v71\n" \
  "v_cvt_pk_f16_f32 v81, v66, v70\n" \
  "v_cvt_pk_f16_f32 v82, v65, v69\n" \
  "v_cvt_pk_f16_f32 v83, v64, v68\n" \
  "v_cvt_pk_f16_f32 v84, v75, v79\n" \
  "v_cvt_pk_f16_f32 v85, v74, v78\n" \
  "v_cvt_pk_f16_f32 v86, v73, v77\n" \
  "v_cvt_pk_f16_f32 v87, v72, v76\n" \
  "s_waitcnt lgkmcnt(3)\n" \
  "v_mfma_f32_16x16x32_f16 v[64:67], v[56:59], v[12:15], 0\n" \
  "v_mfma_f32_16x16x32_f16 v[68:71], v[56:59], v[16:19], 0\n" \
  "v_mfma_f32_16x16x32_f16 v[72:75], v[56:59], v[20:23], 0\n" \
  "s_waitcnt lgkmcnt(2)\n" \
  "v_mfma_f32_16x16x32_f16 v[76:79], v[60:63], v[20:23], 0\n" \
  "ds_read_b128 v[56:59], v10 offset:2048\n" \
  "ds_read_b128 v[60:63], v10 offset:2064\n" \
  "v_mfma_f32_16x16x32_f16 v[104:107], v[80:83], v[24:27], 0\n" \
  "v_mfma_f32_16x16x32_f16 v[108:111], v[80:83], v[28:31], 0\n" \
  "s_nop 0\n" \
  "v_cvt_pk_f16_f32 v88, v67, v71\n" \
  "v_cvt_pk_f16_f32 v89, v66, v70\n" \
  "v_mfma_f32_16x16x32_f16 v[104:107], v[84:87], v[36:39], v[104:107]\n" \
  "v_cvt_pk_f16_f32 v90, v65, v69\n" \
  "v_cvt_pk_f16_f32 v91, v64, v68\n" \
  "v_mfma_f32_16x16x32_f16 v[96:99], v[80:83], v[32:35], 0\n" \
  "v_cvt_pk_f16_f32 v92, v75, v79\n" \
  "v_cvt_pk_f16_f32 v93, v74, v78\n" \
  "v_mfma_f32_16x16x32_f16 v[108:111], v[84:87], v[40:43], v[108:111]\n" \
  "v_cvt_pk_f16_f32 v94, v73, v77\n" \
  "v_cvt_pk_f16_f32 v95, v72, v76\n" \
  "v_mfma_f32_16x16x32_f16 v[96:99], v[84:87], v[44:47], v[96:99]\n" \
  "s_waitcnt lgkmcnt(3)\n" \
  "v_mfma_f32_16x16x32_f16 v[64:67], v[48:51], v[12:15], 0\n" \
  "v_mfma_f32_16x16x32_f16 v[68:71], v[48:51], v[16:19], 0\n" \
  "v_mfma_f32_16x16x32_f16 v[72:75], v[48:51], v[20:23], 0\n" \
  "s_waitcnt lgkmcnt(2)\n" \
  "v_mfma_f32_16x16x32_f16 v[76:79], v[52:55], v[20:23], 0\n" \
  "s_mov_b32 s97, 0x1fffff\n" \
  "Lfq%=:\n" \
  "ds_read_b32 v8, v4 offset:4\n" \
  "s_waitcnt lgkmcnt(0)\n" \
  "v_readfirstlane_b32 s96, v8\n" \
  "s_and_b32 s96, s96, 0x400\n" \
  "s_cmp_eq_u32 s96, 0x400\n" \
  "s_cbranch_scc1 Lfq%=x\n" \
  "s_sub_u32 s97, s97, 1\n" \
  "s_cbranch_scc0 Lfq%=\n" \
  "Lfq%=x:\n" \
  "ds_read_b128 v[48:51], v10 offset:1536\n" \
  "ds_read_b128 v[52:55], v10 offset:1552\n" \
  "v_mov_b32 v9, 0x100\n" \
  "ds_write_b128 v11, v[104:107] offset:10240\n" \
  "ds_add_u32 v4, v9 offset:0\n" \
  "v_mfma_f32_16x16x32_f16 v[108:111], v[88:91], v[24:27], v[108:111]\n" \
  "v_mfma_f32_16x16x32_f16 v[96:99], v[88:91], v[28:31], v[96:99]\n" \
  "v_cvt_pk_f16_f32 v80, v67, v71\n" \
  "v_cvt_pk_f16_f32 v81, v66, v70\n" \
  "v_mfma_f32_16x16x32_f16 v[108:111], v[92:95], v[36:39], v[108:111]\n" \
  "v_cvt_pk_f16_f32 v82, v65, v69\n" \
  "v_cvt_pk_f16_f32 v83, v64, v68\n" \
  "v_mfma_f32_16x16x32_f16 v[100:103], v[88:91], v[32:35], 0\n" \
  "v_cvt_pk_f16_f32 v84, v75, v79\n" \
  "v_cvt_pk_f16_f32 v85, v74, v78\n" \
  "v_mfma_f32_16x16x32_f16 v[96:99], v[92:95], v[40:43], v[96:99]\n" \
  "v_cvt_pk_f16_f32 v86, v73, v77\n" \
  "v_cvt_pk_f16_f32 v87, v72, v76\n" \
  "v_mfma_f32_16x16x32_f16 v[100:103], v[92:95], v[44:47], v[100:103]\n" \
  "v_mfma_f32_16x16x32_f16 v[64:67], v[56:59], v[12:15], 0\n" \
  "v_mfma_f32_16x16x32_f16 v[68:71], v[56:59], v[16:19], 0\n" \
  "v_mfma_f32_16x16x32_f16 v[72:75], v[56:59], v[20:23], 0\n" \
  "v_mfma_f32_16x16x32_f16 v[76:79], v[60:63], v[20:23], 0\n" \
  "ds_read_b128 v[56:59], v10 offset:1024\n" \
  "ds_read_b128 v[60:63], v10 offset:1040\n" \
  "v_mov_b32 v9, 0x1000\n" \
  "ds_write_b128 v11, v[108:111] offset:11520\n" \
  "ds_add_u32 v4, v9 offset:0\n" \
  "v_mfma_f32_16x16x32_f16 v[96:99], v[80:83], v[24:27], v[96:99]\n" \
  "v_mfma_f32_16x16x32_f16 v[100:103], v[80:83], v[28:31], v[100:103]\n" \
  "v_cvt_pk_f16_f32 v88, v67, v71\n" \
  "v_cvt_pk_f16_f32 v89, v66, v70\n" \
  "v_mfma_f32_16x16x32_f16 v[96:99], v[84:87], v[36:39], v[96:99]\n" \
  "v_cvt_pk_f16_f32 v90, v65, v69\n" \
  "v_cvt_pk_f16_f32 v91, v64, v68\n" \
  "v_mfma_f32_16x16x32_f16 v[104:107], v[80:83], v[32:35], 0\n" \
  "v_cvt_pk_f16_f32 v92, v75, v79\n" \
  "v_cvt_pk_f16_f32 v93, v74, v78\n" \
  "v_mfma_f32_16x16x32_f16 v[100:103], v[84:87], v[40:43], v[100:103]\n" \
  "v_cvt_pk_f16_f32 v94, v73, v77\n" \
  "v_cvt_pk_f16_f32 v95, v72, v76\n" \
  "v_mfma_f32_16x16x32_f16 v[104:107], v[84:87], v[44:47], v[104:107]\n" \
  "s_waitcnt lgkmcnt(7)\n" \
  "v_mfma_f32_16x16x32_f16 v[64:67], v[48:51], v[12:15], 0\n" \
  "v_mfma_f32_16x16x32_f16 v[68:71], v[48:51], v[16:19], 0\n" \
  "v_mfma_f32_16x16x32_f16 v[72:75], v[48:51], v[20:23], 0\n" \
  "s_waitcnt lgkmcnt(6)\n" \
  "v_mfma_f32_16x16x32_f16 v[76:79], v[52:55], v[20:23], 0\n" \
  "v_mov_b32 v9, 0x10000\n" \
  "ds_write_b128 v11, v[96:99] offset:12800\n" \
  "ds_add_u32 v4, v9 offset:0\n" \
  "v_mfma_f32_16x16x32_f16 v[100:103], v[88:91], v[24:27], v[100:103]\n" \
  "v_mfma_f32_16x16x32_f16 v[104:107], v[88:91], v[28:31], v[104:107]\n" \
  "v_cvt_pk_f16_f32 v80, v67, v71\n" \
  "v_cvt_pk_f16_f32 v81, v66, v70\n" \
  "v_mfma_f32_16x16x32_f16 v[100:103], v[92:95], v[36:39], v[100:103]\n" \
  "v_cvt_pk_f16_f32 v82, v65, v69\n" \
  "v_cvt_pk_f16_f32 v83, v64, v68\n" \
  "v_mfma_f32_16x16x32_f16 v[108:111], v[88:91], v[32:35], 0\n" \
  "v_cvt_pk_f16_f32 v84, v75, v79\n" \
  "v_cvt_pk_f16_f32 v85, v74, v78\n" \
  "v_mfma_f32_16x16x32_f16 v[104:107], v[92:95], v[40:43], v[104:107]\n" \
  "v_cvt_pk_f16_f32 v86, v73, v77\n" \
  "v_cvt_pk_f16_f32 v87, v72, v76\n" \
  "v_mfma_f32_16x16x32_f16 v[108:111], v[92:95], v[44:47], v[108:111]\n" \
  "s_waitcnt lgkmcnt(5)\n" \
  "v_mfma_f32_16x16x32_f16 v[64:67], v[56:59], v[12:15], 0\n" \
  "v_mfma_f32_16x16x32_f16 v[68:71], v[56:59], v[16:19], 0\n" \
  "v_mfma_f32_16x16x32_f16 v[72:75], v[56:59], v[20:23], 0\n" \
  "s_waitcnt lgkmcnt(4)\n" \
  "v_mfma_f32_16x16x32_f16 v[76:79], v[60:63], v[20:23], 0\n" \
  "v_mov_b32 v9, 0x100000\n" \
  "ds_write_b128 v11, v[100:103] offset:14080\n" \
  "ds_add_u32 v4, v9 offset:0\n" \
  "v_mfma_f32_16x16x32_f16 v[104:107], v[80:83], v[24:27], v[104:107]\n" \
  "s_nop 0\n" \
  "v_cvt_pk_f16_f32 v88, v67, v71\n" \
  "v_cvt_pk_f16_f32 v89, v66, v70\n" \
  "v_mfma_f32_16x16x32_f16 v[108:111], v[80:83], v[28:31], v[108:111]\n" \
  "v_cvt_pk_f16_f32 v90, v65, v69\n" \
  "v_cvt_pk_f16_f32 v91, v64, v68\n" \
  "v_mfma_f32_16x16x32_f16 v[104:107], v[84:87], v[36:39], v[104:107]\n" \
  "v_cvt_pk_f16_f32 v92, v75, v79\n" \
  "v_cvt_pk_f16_f32 v93, v74, v78\n" \
  "v_mfma_f32_16x16x32_f16 v[108:111], v[84:87], v[40:43], v[108:111]\n" \
  "v_cvt_pk_f16_f32 v94, v73, v77\n" \
  "v_cvt_pk_f16_f32 v95, v72, v76\n" \
  "v_mov_b32 v9, 0x1000000\n" \
  "s_nop 1\n" \
  "ds_write_b128 v11, v[104:107] offset:15360\n" \
  "ds_add_u32 v4, v9 offset:0\n" \
  "v_mfma_f32_16x16x32_f16 v[108:111], v[88:91], v[24:27], v[108:111]\n" \
  "v_mfma_f32_16x16x32_f16 v[108:111], v[92:95], v[36:39], v[108:111]\n" \
  "v_mov_b32 v9, 0x10000000\n" \
  "s_nop 6\n" \
  "ds_write_b128 v11, v[108:111] offset:16640\n" \
  "ds_add_u32 v4, v9 offset:0\n" \
  "s_branch Lr3%=\n" \
  "Lr2%=:\n" \
  "ds_read2_b32 v[24:25], v8 offset0:96 offset1:97\n" \
  "ds_read2_b32 v[26:27], v8 offset0:98 offset1:99\n" \
  "ds_read2_b32 v[28:29], v8 offset0:112 offset1:113\n" \
  "ds_read2_b32 v[30:31], v8 offset0:114 offset1:115\n" \
  "ds_read2_b32 v[32:33], v8 offset0:128 offset1:129\n" \
  "ds_read2_b32 v[34:35], v8 offset0:130 offset1:131\n" \
  "ds_read2_b32 v[36:37], v8 offset0:144 offset1:145\n" \
  "ds_read2_b32 v[38:39], v8 offset0:146 offset1:147\n" \
  "v_add_u32 v9, 1024, v3\n" \
  "ds_read2_b32 v[124:125], v9 offset0:0 offset1:1\n" \
  "ds_read2_b32 v[126:127], v9 offset0:2 offset1:3\n" \
  "ds_read2_b32 v[20:21], v9 offset0:32 offset1:33\n" \
  "ds_read2_b32 v[22:23], v9 offset0:34 offset1:35\n" \
  "ds_read2_b32 v[12:13], v9 offset0:76 offset1:77\n" \
  "ds_read2_b32 v[14:15], v9 offset0:78 offset1:79\n" \
  "ds_read2_b32 v[16:17], v9 offset0:91 offset1:92\n" \
  "ds_read2_b32 v[18:19], v9 offset0:93 offset1:94\n" \
  "ds_read2_b32 v[40:41], v3 offset0:112 offset1:113\n" \
  "ds_read2_b32 v[42:43], v3 offset0:114 offset1:115\n" \
  "ds_read2_b32 v[44:45], v3 offset0:128 offset1:129\n" \
  "ds_read2_b32 v[46:47], v3 offset0:130 offset1:131\n" \
  "ds_read2_b32 v[48:49], v3 offset0:144 offset1:145\n" \
  "ds_read2_b32 v[50:51], v3 offset0:146 offset1:147\n" \
  "ds_read2_b32 v[52:53], v3 offset0:160 offset1:161\n" \
  "ds_read2_b32 v[54:55], v3 offset0:162 offset1:163\n" \
  "ds_read2_b32 v[56:57], v3 offset0:176 offset1:177\n" \
  "ds_read2_b32 v[58:59], v3 offset0:178 offset1:179\n" \
  "ds_read2_b32 v[60:61], v3 offset0:192 offset1:193\n" \
  "ds_read2_b32 v[62:63], v3 offset0:194 offset1:195\n" \
  "ds_read2_b32 v[64:65], v3 offset0:208 offset1:209\n" \
  "ds_read2_b32 v[66:67], v3 offset0:210 offset1:211\n" \
  "ds_read2_b32 v[68:69], v3 offset0:224 offset1:225\n" \
  "ds_read2_b32 v[70:71], v3 offset0:226 offset1:227\n" \
  "ds_read2_b32 v[72:73], v3 offset0:240 offset1:241\n" \
  "ds_read2_b32 v[74:75], v3 offset0:242 offset1:243\n" \
  "s_mov_b32 s97, 0x1fffff\n" \
  "Lgp%=:\n" \
  "ds_read_b32 v8, v4 offset:4\n" \
  "s_waitcnt lgkmcnt(0)\n" \
  "v_readfirstlane_b32 s96, v8\n" \
  "s_and_b32 s96, s96, 0xff\n" \
  "s_cmp_eq_u32 s96, 16\n" \
  "s_cbranch_scc1 Lgp%=x\n" \
  "s_sleep 1\n" \
  "s_sub_u32 s97, s97, 1\n" \
  "s_cbranch_scc0 Lgp%=\n" \
  "Lgp%=x:\n" \
  "s_setprio " STR(PRG) "\n" \
  "ds_read_b128 v[76:79], v10 offset:3584\n" \
  "ds_read_b128 v[80:83], v10 offset:3600\n" \
  "ds_read_b128 v[84:87], v10 offset:3072\n" \
  "ds_read_b128 v[88:91], v10 offset:3088\n" \
  "s_waitcnt lgkmcnt(3)\n" \
  "v_mfma_f32_16x16x32_f16 v[92:95], v[76:79], v[12:15], 0\n" \
  "s_waitcnt lgkmcnt(2)\n" \
  "v_mfma_f32_16x16x32_f16 v[96:99], v[80:83], v[16:19], 0\n" \
  "v_mfma_f32_16x16x32_f16 v[108:111], v[20:23], v[76:79], 0\n" \
  "ds_read_b128 v[76:79], v10 offset:2560\n" \
  "ds_read_b128 v[80:83], v10 offset:2576\n" \
  "s_nop 4\n" \
  "v_cvt_pk_f16_f32 v100, v95, v99\n" \
  "v_cvt_pk_f16_f32 v101, v94, v98\n" \
  "v_cvt_pk_f16_f32 v102, v93, v97\n" \
  "v_cvt_pk_f16_f32 v103, v92, v96\n" \
  "s_waitcnt lgkmcnt(3)\n" \
  "v_mfma_f32_16x16x32_f16 v[92:95], v[84:87], v[12:15], 0\n" \
  "s_waitcnt lgkmcnt(2)\n" \
  "v_mfma_f32_16x16x32_f16 v[96:99], v[88:91], v[16:19], 0\n" \
  "v_mfma_f32_16x16x32_f16 v[112:115], v[20:23], v[84:87], 0\n" \
  "ds_read_b128 v[84:87], v10 offset:2048\n" \
  "ds_read_b128 v[88:91], v10 offset:2064\n" \
  "v_mfma_f32_16x16x32_f16 v[108:111], v[100:103], v[24:27], v[108:111]\n" \
  "v_mfma_f32_16x16x32_f16 v[112:115], v[100:103], v[28:31], v[112:115]\n" \
  "v_mfma_f32_16x16x32_f16 v[116:119], v[100:103], v[32:35], 0\n" \
  "v_mfma_f32_16x16x32_f16 v[120:123], v[100:103], v[36:39], 0\n" \
  "ds_read2_b32 v[36:37], v3 offset0:96 offset1:97\n" \
  "ds_read2_b32 v[38:39], v3 offset0:98 offset1:99\n" \
  "v_cvt_pk_f16_f32 v104, v95, v99\n" \
  "v_cvt_pk_f16_f32 v105, v94, v98\n" \
  "v_cvt_pk_f16_f32 v106, v93, v97\n" \
  "v_cvt_pk_f16_f32 v107, v92, v96\n" \
  "s_waitcnt lgkmcnt(5)\n" \
  "v_mfma_f32_16x16x32_f16 v[92:95], v[76:79], v[12:15], 0\n" \
  "s_waitcnt lgkmcnt(4)\n" \
  "v_mfma_f32_16x16x32_f16 v[96:99], v[80:83], v[16:19], 0\n" \
  "v_mfma_f32_16x16x32_f16 v[116:119], v[20:23], v[76:79], v[116:119]\n" \
  "v_mfma_f32_16x16x32_f16 v[108:111], v[124:127], v[76:79], v[108:111]\n" \
  "ds_read_b128 v[76:79], v10 offset:2048\n" \
  "ds_read_b128 v[80:83], v10 offset:2112\n" \
  "v_mfma_f32_16x16x32_f16 v[112:115], v[104:107], v[24:27], v[112:115]\n" \
  "v_mfma_f32_16x16x32_f16 v[116:119], v[104:107], v[28:31], v[116:119]\n" \
  "v_mfma_f32_16x16x32_f16 v[120:123], v[104:107], v[32:35], v[120:123]\n" \
  "ds_read2_b32 v[32:33], v3 offset0:80 offset1:81\n" \
  "ds_read2_b32 v[34:35], v3 offset0:82 offset1:83\n" \
  "v_cvt_pk_f16_f32 v100, v95, v99\n" \
  "v_cvt_pk_f16_f32 v101, v94, v98\n" \
  "v_cvt_pk_f16_f32 v102, v93, v97\n" \
  "v_cvt_pk_f16_f32 v103, v92, v96\n" \
  "s_waitcnt lgkmcnt(7)\n" \
  "v_mfma_f32_16x16x32_f16 v[92:95], v[84:87], v[12:15], 0\n" \
  "s_waitcnt lgkmcnt(6)\n" \
  "v_mfma_f32_16x16x32_f16 v[96:99], v[88:91], v[16:19], 0\n" \
  "v_mfma_f32_16x16x32_f16 v[120:123], v[20:23], v[84:87], v[120:123]\n" \
  "ds_read_b128 v[84:87], v10 offset:2176\n" \
  "ds_read_b128 v[88:91], v10 offset:2240\n" \
  "ds_read2_b32 v[12:13], v3 offset0:0 offset1:1\n" \
  "ds_read2_b32 v[14:15], v3 offset0:2 offset1:3\n" \
  "ds_read2_b32 v[16:17], v3 offset0:16 offset1:17\n" \
  "ds_read2_b32 v[18:19], v3 offset0:18 offset1:19\n" \
  "ds_read2_b32 v[20:21], v3 offset0:32 offset1:33\n" \
  "ds_read2_b32 v[22:23], v3 offset0:34 offset1:35\n" \
  "v_mfma_f32_16x16x32_f16 v[116:119], v[100:103], v[24:27], v[116:119]\n" \
  "v_mfma_f32_16x16x32_f16 v[120:123], v[100:103], v[28:31], v[120:123]\n" \
  "ds_read2_b32 v[28:29], v3 offset0:64 offset1:65\n" \
  "ds_read2_b32 v[30:31], v3 offset0:66 offset1:67\n" \
  "v_cvt_pk_f16_f32 v104, v95, v99\n" \
  "v_cvt_pk_f16_f32 v105, v94, v98\n" \
  "v_cvt_pk_f16_f32 v106, v93, v97\n" \
  "v_cvt_pk_f16_f32 v107, v92, v96\n" \
  "s_mov_b32 s97, 0x1fffff\n" \
  "Lgq%=:\n" \
  "ds_read_b32 v8, v4 offset:4\n" \
  "s_waitcnt lgkmcnt(0)\n" \
  "v_readfirstlane_b32 s96, v8\n" \
  "s_and_b32 s96, s96, 0x400\n" \
  "s_cmp_eq_u32 s96, 0x400\n" \
  "s_cbranch_scc1 Lgq%=x\n" \
  "s_sub_u32 s97, s97, 1\n" \
  "s_cbranch_scc0 Lgq%=\n" \
  "Lgq%=x:\n" \
  "ds_read_b128 v[92:95], v10 offset:2304\n" \
  "ds_read_b128 v[96:99], v10 offset:2368\n" \
  "ds_read_b128 v[100:103], v10 offset:2432\n" \
  "v_mfma_f32_16x16x32_f16 v[120:123], v[104:107], v[24:27], v[120:123]\n" \
  "ds_read2_b32 v[24:25], v3 offset0:48 offset1:49\n" \
  "ds_read2_b32 v[26:27], v3 offset0:50 offset1:51\n" \
  "ds_read_b128 v[104:107], v10 offset:2496\n" \
  "v_mfma_f32_16x16x32_f16 v[108:111], v[44:47], v[76:79], v[108:111]\n" \
  "v_mfma_f32_16x16x32_f16 v[112:115], v[124:127], v[76:79], v[112:115]\n" \
  "ds_read_b128 v[76:79], v10 offset:1536\n" \
  "v_mfma_f32_16x16x32_f16 v[108:111], v[48:51], v[80:83], v[108:111]\n" \
  "ds_read_b128 v[80:83], v10 offset:1600\n" \
  "v_mfma_f32_16x16x32_f16 v[108:111], v[52:55], v[84:87], v[108:111]\n" \
  "ds_read_b128 v[84:87], v10 offset:1664\n" \
  "v_mfma_f32_16x16x32_f16 v[108:111], v[56:59], v[88:91], v[108:111]\n" \
  "ds_read_b128 v[88:91], v10 offset:1728\n" \
  "s_waitcnt lgkmcnt(8)\n" \
  "v_mfma_f32_16x16x32_f16 v[108:111], v[60:63], v[92:95], v[108:111]\n" \
  "ds_read_b128 v[92:95], v10 offset:1792\n" \
  "v_mfma_f32_16x16x32_f16 v[108:111], v[64:67], v[96:99], v[108:111]\n" \
  "ds_read_b128 v[96:99], v10 offset:1856\n" \
  "s_waitcnt lgkmcnt(6)\n" \
  "v_mfma_f32_16x16x32_f16 v[108:111], v[68:71], v[100:103], v[108:111]\n" \
  "ds_read_b128 v[100:103], v10 offset:1920\n" \
  "v_mfma_f32_16x16x32_f16 v[108:111], v[72:75], v[104:107], v[108:111]\n" \
  "ds_read_b128 v[104:107], v10 offset:1984\n" \
  "s_waitcnt lgkmcnt(6)\n" \
  "v_mfma_f32_16x16x32_f16 v[108:111], v[12:15], v[76:79], v[108:111]\n" \
  "v_mfma_f32_16x16x32_f16 v[112:115], v[44:47], v[76:79], v[112:115]\n" \
  "v_mfma_f32_16x16x32_f16 v[116:119], v[124:127], v[76:79], v[116:119]\n" \
  "ds_read_b128 v[76:79], v10 offset:1024\n" \
  "v_mfma_f32_16x16x32_f16 v[108:111], v[16:19], v[80:83], v[108:111]\n" \
  "v_mfma_f32_16x16x32_f16 v[112:115], v[48:51], v[80:83], v[112:115]\n" \
  "ds_read_b128 v[80:83], v10 offset:1088\n" \
  "s_waitcnt lgkmcnt(6)\n" \
  "v_mfma_f32_16x16x32_f16 v[108:111], v[20:23], v[84:87], v[108:111]\n" \
  "v_mfma_f32_16x16x32_f16 v[112:115], v[52:55], v[84:87], v[112:115]\n" \
  "ds_read_b128 v[84:87], v10 offset:1152\n" \
  "v_mfma_f32_16x16x32_f16 v[108:111], v[24:27], v[88:91], v[108:111]\n" \
  "v_mfma_f32_16x16x32_f16 v[112:115], v[56:59], v[88:91], v[112:115]\n" \
  "ds_read_b128 v[88:91], v10 offset:1216\n" \
  "s_waitcnt lgkmcnt(6)\n" \
  "v_mfma_f32_16x16x32_f16 v[108:111], v[28:31], v[92:95], v[108:111]\n" \
  "v_mfma_f32_16x16x32_f16 v[112:115], v[60:63], v[92:95], v[112:115]\n" \
  "ds_read_b128 v[92:95], v10 offset:1280\n" \
  "v_mfma_f32_16x16x32_f16 v[108:111], v[32:35], v[96:99], v[108:111]\n" \
  "v_mfma_f32_16x16x32_f16 v[112:115], v[64:67], v[96:99], v[112:115]\n" \
  "ds_read_b128 v[96:99], v10 offset:1344\n" \
  "s_waitcnt lgkmcnt(6)\n" \
  "v_mfma_f32_16x16x32_f16 v[108:111], v[36:39], v[100:103], v[108:111]\n" \
  "v_mfma_f32_16x16x32_f16 v[112:115], v[68:71], v[100:103], v[112:115]\n" \
  "ds_read_b128 v[100:103], v10 offset:1408\n" \
  "v_mfma_f32_16x16x32_f16 v[108:111], v[40:43], v[104:107], v[108:111]\n" \
  "v_mfma_f32_16x16x32_f16 v[112:115], v[72:75], v[104:107], v[112:115]\n" \
  "ds_read_b128 v[104:107], v10 offset:1472\n" \
  "s_waitcnt lgkmcnt(6)\n" \
  "v_mfma_f32_16x16x32_f16 v[112:115], v[12:15], v[76:79], v[112:115]\n" \
  "v_mfma_f32_16x16x32_f16 v[116:119], v[44:47], v[76:79], v[116:119]\n" \
  "v_mfma_f32_16x16x32_f16 v[120:123], v[124:127], v[76:79], v[120:123]\n" \
  "ds_read_b128 v[76:79], v10 offset:512\n" \
  "v_mfma_f32_16x16x32_f16 v[112:115], v[16:19], v[80:83], v[112:115]\n" \
  "v_mfma_f32_16x16x32_f16 v[116:119], v[48:51], v[80:83], v[116:119]\n" \
  "ds_read_b128 v[80:83], v10 offset:576\n" \
  "s_waitcnt lgkmcnt(6)\n" \
  "v_mfma_f32_16x16x32_f16 v[112:115], v[20:23], v[84:87], v[112:115]\n" \
  "v_mov_b32 v9, 0x10000\n" \
  "ds_write_b128 v11, v[108:111] offset:5120\n" \
  "ds_add_u32 v4, v9 offset:0\n" \
  "v_mfma_f32_16x16x32_f16 v[116:119], v[52:55], v[84:87], v[116:119]\n" \
  "ds_read_b128 v[84:87], v10 offset:640\n" \
  "v_mfma_f32_16x16x32_f16 v[112:115], v[24:27], v[88:91], v[112:115]\n" \
  "v_mfma_f32_16x16x32_f16 v[116:119], v[56:59], v[88:91], v[116:119]\n" \
  "ds_read_b128 v[88:91], v10 offset:704\n" \
  "s_waitcnt lgkmcnt(8)\n" \
  "v_mfma_f32_16x16x32_f16 v[112:115], v[28:31], v[92:95], v[112:115]\n" \
  "v_mfma_f32_16x16x32_f16 v[116:119], v[60:63], v[92:95], v[116:119]\n" \
  "ds_read_b128 v[92:95], v10 offset:768\n" \
  "v_mfma_f32_16x16x32_f16 v[112:115], v[32:35], v[96:99], v[112:115]\n" \
  "v_mfma_f32_16x16x32_f16 v[116:119], v[64:67], v[96:99], v[116:119]\n" \
  "ds_read_b128 v[96:99], v10 offset:832\n" \
  "s_waitcnt lgkmcnt(8)\n" \
  "v_mfma_f32_16x16x32_f16 v[112:115], v[36:39], v[100:103], v[112:115]\n" \
  "v_mfma_f32_16x16x32_f16 v[116:119], v[68:71], v[100:103], v[116:119]\n" \
  "ds_read_b128 v[100:103], v10 offset:896\n" \
  "v_mfma_f32_16x16x32_f16 v[112:115], v[40:43], v[104:107], v[112:115]\n" \
  "v_mfma_f32_16x16x32_f16 v[116:119], v[72:75], v[104:107], v[116:119]\n" \
  "ds_read_b128 v[104:107], v10 offset:960\n" \
  "s_waitcnt lgkmcnt(8)\n" \
  "v_mfma_f32_16x16x32_f16 v[116:119], v[12:15], v[76:79], v[116:119]\n" \
  "v_mfma_f32_16x16x32_f16 v[120:123], v[44:47], v[76:79], v[120:123]\n" \
  "ds_read_b128 v[76:79], v10 offset:0\n" \
  "v_mfma_f32_16x16x32_f16 v[116:119], v[16:19], v[80:83], v[116:119]\n" \
  "v_mfma_f32_16x16x32_f16 v[120:123], v[48:51], v[80:83], v[120:123]\n" \
  "ds_read_b128 v[80:83], v10 offset:64\n" \
  "s_waitcnt lgkmcnt(6)\n" \
  "v_mfma_f32_16x16x32_f16 v[116:119], v[20:23], v[84:87], v[116:119]\n" \
  "v_mov_b32 v9, 0x100000\n" \
  "ds_write_b128 v11, v[112:115] offset:6400\n" \
  "ds_add_u32 v4, v9 offset:0\n" \
  "v_mfma_f32_16x16x32_f16 v[120:123], v[52:55], v[84:87], v[120:123]\n" \
  "ds_read_b128 v[84:87], v10 offset:128\n" \
  "v_mfma_f32_16x16x32_f16 v[116:119], v[24:27], v[88:91], v[116:119]\n" \
  "v_mfma_f32_16x16x32_f16 v[120:123], v[56:59], v[88:91], v[120:123]\n" \
  "ds_read_b128 v[88:91], v10 offset:192\n" \
  "s_waitcnt lgkmcnt(8)\n" \
  "v_mfma_f32_16x16x32_f16 v[116:119], v[28:31], v[92:95], v[116:119]\n" \
  "v_mfma_f32_16x16x32_f16 v[120:123], v[60:63], v[92:95], v[120:123]\n" \
  "ds_read_b128 v[92:95], v10 offset:256\n" \
  "v_mfma_f32_16x16x32_f16 v[116:119], v[32:35], v[96:99], v[116:119]\n" \
  "v_mfma_f32_16x16x32_f16 v[120:123], v[64:67], v[96:99], v[120:123]\n" \
  "ds_read_b128 v[96:99], v10 offset:320\n" \
  "s_waitcnt lgkmcnt(8)\n" \
  "v_mfma_f32_16x16x32_f16 v[116:119], v[36:39], v[100:103], v[116:119]\n" \
  "v_mfma_f32_16x16x32_f16 v[120:123], v[68:71], v[100:103], v[120:123]\n" \
  "ds_read_b128 v[100:103], v10 offset:384\n" \
  "v_mfma_f32_16x16x32_f16 v[116:119], v[40:43], v[104:107], v[116:119]\n" \
  "v_mfma_f32_16x16x32_f16 v[120:123], v[72:75], v[104:107], v[120:123]\n" \
  "ds_read_b128 v[104:107], v10 offset:448\n" \
  "s_waitcnt lgkmcnt(8)\n" \
  "v_mfma_f32_16x16x32_f16 v[120:123], v[12:15], v[76:79], v[120:123]\n" \
  "v_mfma_f32_16x16x32_f16 v[120:123], v[16:19], v[80:83], v[120:123]\n" \
  "s_waitcnt lgkmcnt(4)\n" \
  "v_mfma_f32_16x16x32_f16 v[120:123], v[20:23], v[84:87], v[120:123]\n" \
  "v_mov_b32 v9, 0x1000000\n" \
  "ds_write_b128 v11, v[116:119] offset:7680\n" \
  "ds_add_u32 v4, v9 offset:0\n" \
  "v_mfma_f32_16x16x32_f16 v[120:123], v[24:27], v[88:91], v[120:123]\n" \
  "s_waitcnt lgkmcnt(4)\n" \
  "v_mfma_f32_16x16x32_f16 v[120:123], v[28:31], v[92:95], v[120:123]\n" \
  "v_mfma_f32_16x16x32_f16 v[120:123], v[32:35], v[96:99], v[120:123]\n" \
  "s_waitcnt lgkmcnt(2)\n" \
  "v_mfma_f32_16x16x32_f16 v[120:123], v[36:39], v[100:103], v[120:123]\n" \
  "v_mfma_f32_16x16x32_f16 v[120:123], v[40:43], v[104:107], v[120:123]\n" \
  "v_mov_b32 v9, 0x10000000\n" \
  "s_nop 6\n" \
  "ds_write_b128 v11, v[120:123] offset:8960\n" \
  "ds_add_u32 v4, v9 offset:0\n" \
  "s_branch Lr3%=\n" \
  "Lr4%=:\n" \
  "s_load_dwordx4 s[84:87], %[k], 0x40\n" \
  "s_load_dwordx2 s[88:89], %[k], 0x50\n" \
  "s_and_b32 s46, %[b], 7\n" \
  "s_lshr_b32 s47, %[b], 3\n" \
  "s_mul_i32 s47, s47, 3\n" \
  "s_add_u32 s47, s47, s50\n" \
  "v_and_b32 v9, 63, %[t]\n" \
  "s_mul_i32 s48, s46, 72\n" \
  "s_add_u32 s48, s48, s47\n" \
  "s_mul_i32 s48, s48, 72\n" \
  "v_min_u32 v5, 35, v9\n" \
  "v_add_lshl_u32 v121, v5, s48, 7\n" \
  "v_add_u32 v122, 4608, v121\n" \
  "s_mul_i32 s48, s47, 0\n" \
  "v_add_u32 v6, s48, v9\n" \
  "v_mul_u32_u24 v7, 7282, v6\n" \
  "v_lshrrev_b32 v7, 16, v7\n" \
  "v_mul_u32_u24 v7, 8064, v7\n" \
  "v_lshl_add_u32 v6, v6, 7, v7\n" \
  "v_add_u32 v6, 5308416, v6\n" \
  "v_cmp_gt_u32 vcc, 0, v9\n" \
  "v_subrev_u32 v9, 0, v9\n" \
  "v_min_u32 v9, 15, v9\n" \
  "s_mul_i32 s47, s47, 8\n" \
  "v_lshrrev_b32 v7, 1, v9\n" \
  "v_add_u32 v7, s47, v7\n" \
  "v_mul_u32_u24 v7, 9216, v7\n" \
  "v_and_b32 v9, 1, v9\n" \
  "s_cmp_lt_u32 s47, 288\n" \
  "s_cselect_b32 s48, 1, 0\n" \
  "v_or_b32 v9, s48, v9\n" \
  "s_waitcnt lgkmcnt(0)\n" \
  "s_lshr_b32 s47, s84, 7\n" \
  "s_add_u32 s47, s47, s46\n" \
  "s_sub_u32 s47, 3, s47\n" \
  "s_and_b32 s47, s47, 7\n" \
  "s_sub_u32 s48, 8, s47\n" \
  "v_mul_u32_u24 v9, s48, v9\n" \
  "v_add_lshl_u32 v9, v9, s47, 7\n" \
  "v_add_u32 v7, v7, v9\n" \
  "v_cndmask_b32 v6, v7, v6, vcc\n" \
  "s_mul_i32 s46, s46, 1152\n" \
  "v_add_u32 v120, s46, v6\n" \
  "s_setprio 3\n" \
  "s_mov_b32 s97, 0x1fffff\n" \
  "Lkr%=:\n" \
  "ds_read_b32 v17, v4 offset:4\n" \
  "s_waitcnt lgkmcnt(0)\n" \
  "v_readfirstlane_b32 s96, v17\n" \
  "s_and_b32 s96, s96, " STR(PFG) "\n" \
  "s_cmp_eq_u32 s96, " STR(PFG) "\n" \
  "s_cbranch_scc1 Lkr%=x\n" \
  "s_sleep 2\n" \
  "s_sub_u32 s97, s97, 1\n" \
  "s_cbranch_scc0 Lkr%=\n" \
  "Lkr%=x:\n" \
  "s_lshl_b64 exec, -1, 0\n" \
  "global_load_dword v126, v120, s[84:85]\n" \
  "global_load_dword v126, v120, s[86:87]\n" \
  "s_mov_b64 exec, -1\n" \
  "global_load_dword v126, v121, s[88:89]\n" \
  "global_load_dword v126, v122, s[88:89]\n" \
  "Lr3%=:\n" \
  GSTAMP7 \
  "s_setprio 0\n" \
  "Lr5%=:\n"

#define SW_HEAD \
  "s_load_dwordx2 s[46:47], %[k], 0x0\n" \
  "s_load_dwordx2 s[48:49], %[k], 0x30\n" \
  "s_load_dwordx2 s[40:41], %[k], 0x70\n" \
  "s_setprio 3\n" \
  "s_and_b32 s50, %[w], 3\n" \
  "s_mul_i32 s51, %[b], 9216\n" \
  "s_mul_i32 s52, s50, 288\n" \
  "s_add_u32 s51, s51, s52\n" \
  "v_bfe_u32 v1, %[t], 3, 3\n" \
  "v_and_b32 v2, 7, %[t]\n" \
  "v_mul_u32_u24 v3, 1152, v1\n" \
  "v_mul_u32_u24 v4, 12, v2\n" \
  "v_add3_u32 v3, v3, v4, s51\n" \
  "v_and_b32 v5, 255, %[t]\n" \
  "v_mov_b32 v6, 158464\n" \
  "v_lshl_add_u32 v6, v5, 3, v6\n" \
  "v_mov_b64 v[8:9], 0\n" \
  "ds_write_b64 v6, v[8:9]\n" \
  "s_mov_b32 s42, 10668032\n" \
  "s_mov_b32 s43, 0x20000\n" \
  "s_mul_i32 s52, %[b], 6\n" \
  "s_and_b32 s52, s52, -4\n" \
  "s_cmp_lt_u32 %[b], 128\n" \
  SSTAMP1 \
  "s_waitcnt lgkmcnt(0)\n" \
  "s_memrealtime s[58:59]\n" \
  ".if " STR(SDLY) "\n" \
  "s_sleep " STR(SDLY) "\n" \
  ".endif\n" \
  "global_load_dwordx3 v[16:18], v3, s[46:47] offset:0\n" \
  "global_load_dwordx3 v[20:22], v3, s[46:47] offset:96\n" \
  "global_load_dwordx3 v[24:26], v3, s[46:47] offset:192\n" \
  "buffer_load_dword v7, off, s[40:43], 0 sc1\n" \
  "s_load_dwordx2 s[54:55], s[48:49], s52\n" \
  "s_cbranch_scc1 Lone%=\n" \
  "v_add_u32 v3, 589824, v3\n" \
  "global_load_dwordx3 v[28:30], v3, s[46:47] offset:0\n" \
  "global_load_dwordx3 v[32:34], v3, s[46:47] offset:96\n" \
  "global_load_dwordx3 v[36:38], v3, s[46:47] offset:192\n" \
  "Lone%=:\n" \
  "s_barrier\n"

#define SW_BODY \
  "s_mul_i32 s51, s50, 3200\n" \
  "s_add_u32 s51, s51, 125088\n" \
  "v_mul_u32_u24 v3, 400, v1\n" \
  "v_lshlrev_b32 v4, 4, v2\n" \
  "v_add3_u32 v3, v3, v4, s51\n" \
  "v_and_b32 v10, 3, %[t]\n" \
  "v_mul_u32_u24 v4, 800, v10\n" \
  "v_lshl_add_u32 v4, v1, 4, v4\n" \
  "v_add_u32 v4, s51, v4\n" \
  "v_and_b32 v11, 4, %[t]\n" \
  "v_lshl_add_u32 v6, v11, 1, v4\n" \
  "v_mul_u32_u24 v12, 8421504, v11\n" \
  "v_lshrrev_b32 v13, 1, v11\n" \
  "v_add_u32 v12, v12, v13\n" \
  "v_add_u32 v12, 0x05040100, v12\n" \
  "s_lshr_b32 s51, %[b], 1\n" \
  "s_mul_i32 s51, s51, 24704\n" \
  "s_and_b32 s52, %[b], 1\n" \
  "s_lshl_b32 s52, s52, 6\n" \
  "s_add_u32 s51, s51, s52\n" \
  "s_mul_i32 s52, s50, 6144\n" \
  "s_add_u32 s51, s51, s52\n" \
  "s_add_u32 s51, s51, 7241728\n" \
  "v_bfe_u32 v13, %[t], 2, 4\n" \
  "v_lshlrev_b32 v13, 7, v13\n" \
  "v_lshl_add_u32 v13, v10, 4, v13\n" \
  "v_add_u32 v13, s51, v13\n" \
  "s_lshl4_add_u32 s51, s50, 158464\n" \
  "v_lshl_add_u32 v14, v1, 6, s51\n" \
  "v_mov_b32 v42, 1\n" \
  "s_mov_b32 s53, 3\n" \
  "s_movk_i32 s56, 0x1000\n" \
  "v_and_b32 v15, 63, %[t]\n" \
  "v_min_u32 v111, 1, v15\n" \
  "v_mul_u32_u24 v111, 40, v111\n" \
  "v_add_u32 v111, v111, v15\n" \
  "v_mov_b32 v8, 159488\n" \
  "v_lshl_add_u32 v111, v111, 2, v8\n" \
  "v_lshlrev_b32 v8, 2, v5\n" \
  "v_sub_u32 v105, 97308, v8\n" \
  "v_lshrrev_b32 v8, 3, v5\n" \
  "v_mul_u32_u24 v106, 24704, v8\n" \
  "v_lshl_add_u32 v106, v2, 4, v106\n" \
  "s_lshl_b32 s51, %[b], 7\n" \
  "s_add_u32 s51, s51, 7241728\n" \
  "v_add_u32 v106, s51, v106\n" \
  "v_lshlrev_b32 v107, 4, v5\n" \
  "s_mul_i32 s51, %[b], 16512\n" \
  "s_add_u32 s51, s51, 139264\n" \
  "v_add_u32 v117, s51, v107\n" \
  "s_and_b32 s51, %[b], 0\n" \
  "s_mul_i32 s51, s51, 16512\n" \
  "s_add_u32 s51, s51, 10535936\n" \
  "v_add_u32 v107, s51, v107\n" \
  "v_and_b32 v9, 4, v1\n" \
  "v_add_u32 v9, v9, v10\n" \
  "v_mul_u32_u24 v122, 16512, v9\n" \
  "v_and_b32 v9, 3, v1\n" \
  "v_lshl_add_u32 v122, v9, 4, v122\n" \
  "v_and_b32 v8, 15, v8\n" \
  "v_bfe_u32 v9, v5, 1, 2\n" \
  "v_mul_u32_u24 v9, 320, v9\n" \
  "v_lshl_add_u32 v115, v8, 4, v9\n" \
  "v_and_b32 v9, 1, v5\n" \
  "v_lshl_add_u32 v115, v9, 3, v115\n" \
  "s_lshr_b32 s84, s50, 1\n" \
  "s_mul_i32 s51, s84, 1280\n" \
  "v_add_u32 v115, s51, v115\n" \
  "v_add_u32 v116, 64192, v115\n" \
  "s_lshl_b32 s84, s84, 2\n" \
  "s_and_b32 s51, %[b], 1\n" \
  "v_min_u32 v8, 2, v1\n" \
  "v_add_u32 v8, s51, v8\n" \
  "v_cmp_ne_u32 vcc, 3, v8\n" \
  "v_min_u32 v9, 2, v10\n" \
  "v_mul_u32_u24 v9, 27648, v9\n" \
  "v_cndmask_b32 v8, 0, v8, vcc\n" \
  "v_lshl_add_u32 v9, v8, 3, v9\n" \
  "s_mul_i32 s52, %[b], 24\n" \
  "s_add_u32 s52, s52, 4096\n" \
  "v_add_u32 v9, s52, v9\n" \
  "v_mul_u32_u24 v108, 1152, v11\n" \
  "v_add_u32 v108, v108, v9\n" \
  "v_add_u32 v114, 9216, v9\n" \
  "v_add_lshl_u32 v109, v8, s51, 4\n" \
  "v_cmp_ne_u32 vcc, 3, v10\n" \
  "s_nop 1\n" \
  "v_cndmask_b32 v110, 0, 1.0, vcc\n" \
  "s_waitcnt vmcnt(0) lgkmcnt(0)\n" \
  SSTAMP2 \
  "v_readfirstlane_b32 s44, v7\n" \
  "Lun%=:\n" \
  "ds_write_b96 v3, v[16:18] offset:0\n" \
  "ds_write_b96 v3, v[20:22] offset:128\n" \
  "ds_write_b96 v3, v[24:26] offset:256\n" \
  "ds_read2_b32 v[44:45], v6 offset0:0 offset1:100\n" \
  "ds_read2_b32 v[56:57], v4 offset0:1 offset1:101\n" \
  "ds_read2_b32 v[48:49], v6 offset0:32 offset1:132\n" \
  "ds_read2_b32 v[58:59], v4 offset0:33 offset1:133\n" \
  "ds_read2_b32 v[52:53], v6 offset0:64 offset1:164\n" \
  "ds_read2_b32 v[60:61], v4 offset0:65 offset1:165\n" \
  "v_mov_b64 v[40:41], 0\n" \
  "s_add_u32 s45, s44, 1\n" \
  "v_dot2c_f32_bf16 v40, 0x3f803f80, v16\n" \
  "v_dot2c_f32_bf16 v41, v16, v16\n" \
  "v_dot2c_f32_bf16 v40, 0x3f803f80, v17\n" \
  "v_dot2c_f32_bf16 v41, v17, v17\n" \
  "v_dot2c_f32_bf16 v40, 0x3f803f80, v18\n" \
  "v_dot2c_f32_bf16 v41, v18, v18\n" \
  "v_dot2c_f32_bf16 v40, 0x3f803f80, v20\n" \
  "v_dot2c_f32_bf16 v41, v20, v20\n" \
  "v_dot2c_f32_bf16 v40, 0x3f803f80, v21\n" \
  "v_dot2c_f32_bf16 v41, v21, v21\n" \
  "v_dot2c_f32_bf16 v40, 0x3f803f80, v22\n" \
  "v_dot2c_f32_bf16 v41, v22, v22\n" \
  "v_dot2c_f32_bf16 v40, 0x3f803f80, v24\n" \
  "v_dot2c_f32_bf16 v41, v24, v24\n" \
  "v_dot2c_f32_bf16 v40, 0x3f803f80, v25\n" \
  "v_dot2c_f32_bf16 v41, v25, v25\n" \
  "v_dot2c_f32_bf16 v40, 0x3f803f80, v26\n" \
  "v_dot2c_f32_bf16 v41, v26, v26\n" \
  "v_mov_b32 v47, s45\n" \
  "v_mov_b32 v51, s45\n" \
  "v_mov_b32 v55, s45\n" \
  "s_waitcnt lgkmcnt(0)\n" \
  "v_perm_b32 v46, v57, v56, v12\n" \
  "v_add_f32_dpp v40, v40, v40 quad_perm:[1,0,3,2] row_mask:0xf bank_mask:0xf bound_ctrl:1\n" \
  "v_add_f32_dpp v41, v41, v41 quad_perm:[1,0,3,2] row_mask:0xf bank_mask:0xf bound_ctrl:1\n" \
  "buffer_store_dwordx4 v[44:47], v13, s[40:43], 0 offen sc1\n" \
  "v_perm_b32 v50, v59, v58, v12\n" \
  "v_add_f32_dpp v40, v40, v40 quad_perm:[2,3,0,1] row_mask:0xf bank_mask:0xf bound_ctrl:1\n" \
  "v_add_f32_dpp v41, v41, v41 quad_perm:[2,3,0,1] row_mask:0xf bank_mask:0xf bound_ctrl:1\n" \
  "buffer_store_dwordx4 v[48:51], v13, s[40:43], 0 offen offset:2048 sc1\n" \
  "v_perm_b32 v54, v61, v60, v12\n" \
  "v_add_f32_dpp v40, v40, v40 row_half_mirror row_mask:0xf bank_mask:0xf bound_ctrl:1\n" \
  "v_add_f32_dpp v41, v41, v41 row_half_mirror row_mask:0xf bank_mask:0xf bound_ctrl:1\n" \
  "buffer_store_dwordx4 v[52:55], v13, s[40:43], s56 offen sc1\n" \
  "ds_write_b96 v14, v[40:42]\n" \
  SSTAMP3 \
  "s_cmp_lt_u32 %[b], 128\n" \
  "s_cbranch_scc1 Lsn%=\n" \
  "s_cmp_eq_u32 s53, 2\n" \
  "s_cbranch_scc1 Lsn%=\n" \
  "s_mov_b32 s53, 2\n" \
  "v_add_u32 v13, 790528, v13\n" \
  "v_add_u32 v14, 512, v14\n" \
  "v_mov_b64 v[16:17], v[28:29]\n" \
  "v_mov_b32 v18, v30\n" \
  "v_mov_b64 v[20:21], v[32:33]\n" \
  "v_mov_b32 v22, v34\n" \
  "v_mov_b64 v[24:25], v[36:37]\n" \
  "v_mov_b32 v26, v38\n" \
  "s_branch Lun%=\n" \
  "Lsn%=:\n" \
  "s_cmp_lt_u32 s50, s53\n" \
  "s_cbranch_scc1 Lsd%=\n" \
  "s_sub_u32 s57, 3, s50\n" \
  "s_lshl_b32 s57, s57, 9\n" \
  "v_and_b32 v15, 31, %[t]\n" \
  "s_add_u32 s51, s57, 158464\n" \
  "v_lshl_add_u32 v15, v15, 4, s51\n" \
  "v_mov_b32 v43, 158208\n" \
  "ds_write_b32 v43, v55\n" \
  "s_movk_i32 s97, 0x7fff\n" \
  "Lsp%=:\n" \
  "ds_read_b96 v[56:58], v15\n" \
  "s_waitcnt lgkmcnt(0)\n" \
  "v_cmp_ne_u32 vcc, 1, v58\n" \
  "s_cbranch_vccz Lsq%=\n" \
  "s_sub_u32 s97, s97, 1\n" \
  "s_cbranch_scc0 Lsp%=\n" \
  "Lsq%=:\n" \
  "v_add_f32_dpp v56, v56, v56 quad_perm:[1,0,3,2] row_mask:0xf bank_mask:0xf bound_ctrl:1\n" \
  "v_add_f32_dpp v57, v57, v57 quad_perm:[1,0,3,2] row_mask:0xf bank_mask:0xf bound_ctrl:1\n" \
  "s_lshl_b32 s51, %[b], 6\n" \
  "v_add_f32_dpp v56, v56, v56 quad_perm:[2,3,0,1] row_mask:0xf bank_mask:0xf bound_ctrl:1\n" \
  "v_add_f32_dpp v57, v57, v57 quad_perm:[2,3,0,1] row_mask:0xf bank_mask:0xf bound_ctrl:1\n" \
  "v_mul_f32 v56, 0x3ae38e39, v56\n" \
  "v_mul_f32 v57, 0x3ae38e39, v57\n" \
  "v_fma_f32 v57, -v56, v56, v57\n" \
  "v_max_f32 v57, 0, v57\n" \
  "v_cvt_pk_bf16_f32 v57, 0, v57\n" \
  "v_add_f32 v57, 0x3727c5ac, v57\n" \
  "v_cvt_pk_bf16_f32 v57, 0, v57\n" \
  "v_sqrt_f32 v57, v57\n" \
  "v_mov_b32_dpp v58, v56 row_shl:4 row_mask:0xf bank_mask:0xf bound_ctrl:1\n" \
  "v_cvt_pk_bf16_f32 v57, 0, v57\n" \
  "v_rcp_f32 v60, v57\n" \
  "v_cvt_pk_bf16_f32 v62, v56, v58\n" \
  "v_mov_b32 v63, s45\n" \
  "s_lshl3_add_u32 s51, s57, s51\n" \
  "v_mov_b32_dpp v61, v60 row_shl:4 row_mask:0xf bank_mask:0xf bound_ctrl:1\n" \
  "s_add_u32 s51, s51, 10535936\n" \
  "s_mov_b32 exec_lo, 16843009\n" \
  "s_mov_b32 exec_hi, 0\n" \
  "buffer_store_dwordx4 v[60:63], v122, s[40:43], s51 offen sc1\n" \
  "s_mov_b64 exec, -1\n" \
  "Lsd%=:\n" \
  SSTAMP4 \
  "s_mov_b32 s44, s45\n" \
  "s_mov_b32 s45, s84\n" \
  "v_lshrrev_b64 v[8:9], v109, s[54:55]\n" \
  "v_lshlrev_b32 v109, 16, v8\n" \
  "s_movk_i32 s97, 128\n" \
  "Lxw%=:\n" \
  "s_memrealtime s[46:47]\n" \
  "s_waitcnt lgkmcnt(0)\n" \
  "s_sub_u32 s46, s46, s58\n" \
  "s_cmp_gt_u32 s46, " STR(XT) "\n" \
  "s_cbranch_scc1 Lxv%=\n" \
  "s_nop 7\n" \
  "s_sub_u32 s97, s97, 1\n" \
  "s_cbranch_scc0 Lxw%=\n" \
  "Lxv%=:\n" \
  "s_setprio " STR(PRH) "\n" \
  SSTAMP5 \
  "buffer_load_dwordx2 v[102:103], v108, s[40:43], 0 offen sc1\n" \
  "s_mov_b32 s94, 0\n" \
  "s_mov_b32 s95, 0\n" \
  "buffer_load_dwordx4 v[64:67], v106, s[40:43], s94 offen sc1\n" \
  "buffer_load_dwordx4 v[68:71], v107, s[40:43], s95 offen sc1\n" \
  "s_mov_b32 s94, 790528\n" \
  "s_mov_b32 s95, 4096\n" \
  "buffer_load_dwordx4 v[72:75], v106, s[40:43], s94 offen sc1\n" \
  "buffer_load_dwordx4 v[76:79], v107, s[40:43], s95 offen sc1\n" \
  "s_waitcnt vmcnt(4)\n" \
  SSTAMP6 \
  "v_cmp_ne_u32 vcc, s44, v103\n" \
  "s_cmp_lg_u64 vcc, 0\n" \
  "s_cbranch_scc1 Lms%=\n" \
  "Lmb%=:\n" \
  "v_mul_f32 v102, v102, v110\n" \
  "v_mov_b32 v104, 226\n" \
  "s_mov_b32 s94, 0x42000000\n" \
  "v_add_f32_dpp v102, v102, v102 quad_perm:[1,0,3,2] row_mask:0xf bank_mask:0xf bound_ctrl:1\n" \
  "v_mov_b32 v112, 1\n" \
  "s_nop 0\n" \
  "v_add_f32_dpp v102, v102, v102 quad_perm:[2,3,0,1] row_mask:0xf bank_mask:0xf bound_ctrl:1\n" \
  "v_cvt_pk_bf16_f32 v102, 0, v102\n" \
  "s_nop 1\n" \
  "v_mov_b32_dpp v103, v102 row_shl:4 row_mask:0xf bank_mask:0xf bound_ctrl:1\n" \
  "v_add_f32 v103, 1.0, v103\n" \
  "v_cvt_pk_bf16_f32 v103, 0, v103\n" \
  "v_fma_f32 v96, |v103|, s94, |v102|\n" \
  "v_lshrrev_b32 v96, 23, v96\n" \
  "v_med3_u32 v96, v96, 26, v104\n" \
  "v_lshlrev_b32 v96, 23, v96\n" \
  "v_sub_u32 v97, 0x84000000, v96\n" \
  "v_add_u32 v104, 0xfb000000, v96\n" \
  "v_mul_f32 v103, v103, v97\n" \
  "v_mul_f32 v102, v102, v97\n" \
  "v_mul_f32 v113, v109, v104\n" \
  "v_readlane_b32 s84, v103, 0\n" \
  "v_readlane_b32 s87, v102, 0\n" \
  "v_readlane_b32 s85, v103, 8\n" \
  "v_readlane_b32 s88, v102, 8\n" \
  "v_readlane_b32 s86, v103, 16\n" \
  "v_readlane_b32 s89, v102, 16\n" \
  "v_mov_b32 v96, s84\n" \
  "v_mov_b32 v97, s87\n" \
  "v_mov_b32 v98, s85\n" \
  "v_mov_b32 v99, s88\n" \
  "v_mov_b32 v100, s86\n" \
  "v_mov_b32 v101, s89\n" \
  "v_readlane_b32 s84, v113, 0\n" \
  "v_readlane_b32 s87, v104, 0\n" \
  "v_readlane_b32 s85, v113, 8\n" \
  "v_readlane_b32 s88, v104, 8\n" \
  "v_readlane_b32 s86, v113, 16\n" \
  "v_readlane_b32 s89, v104, 16\n" \
  "s_waitcnt vmcnt(2)\n" \
  "v_cmp_ne_u32 vcc, s44, v67\n" \
  "v_cmp_ne_u32 s[94:95], s44, v71\n" \
  "s_or_b64 vcc, vcc, s[94:95]\n" \
  "s_cbranch_scc1 Lu0s%=\n" \
  "Lu0b%=:\n" \
  "v_lshlrev_b32 v102, 16, v70\n" \
  "v_and_b32 v103, 0xffff0000, v70\n" \
  "v_lshlrev_b32 v16, 16, v64\n" \
  "v_lshlrev_b32 v17, 16, v65\n" \
  "v_and_b32 v18, 0xffff0000, v64\n" \
  "v_and_b32 v19, 0xffff0000, v65\n" \
  "v_lshlrev_b32 v20, 16, v66\n" \
  "v_and_b32 v21, 0xffff0000, v66\n" \
  "v_pk_add_f32 v[40:41], v[16:17], v[102:103] neg_lo:[0,1] neg_hi:[0,1]\n" \
  "v_pk_add_f32 v[42:43], v[18:19], v[102:103] neg_lo:[0,1] neg_hi:[0,1]\n" \
  "v_pk_add_f32 v[44:45], v[20:21], v[102:103] neg_lo:[0,1] neg_hi:[0,1]\n" \
  "v_cvt_pk_bf16_f32 v40, 0, v40\n" \
  "v_cvt_pk_bf16_f32 v41, 0, v41\n" \
  "v_cvt_pk_bf16_f32 v42, 0, v42\n" \
  "v_cvt_pk_bf16_f32 v43, 0, v43\n" \
  "v_cvt_pk_bf16_f32 v44, 0, v44\n" \
  "v_cvt_pk_bf16_f32 v45, 0, v45\n" \
  "v_pk_mul_f32 v[40:41], v[40:41], v[68:69]\n" \
  "v_pk_mul_f32 v[42:43], v[42:43], v[68:69]\n" \
  "v_pk_mul_f32 v[44:45], v[44:45], v[68:69]\n" \
  "v_cvt_pk_bf16_f32 v40, 0, v40\n" \
  "v_cvt_pk_bf16_f32 v41, 0, v41\n" \
  "v_cvt_pk_bf16_f32 v42, 0, v42\n" \
  "v_cvt_pk_bf16_f32 v43, 0, v43\n" \
  "v_cvt_pk_bf16_f32 v44, 0, v44\n" \
  "v_cvt_pk_bf16_f32 v45, 0, v45\n" \
  "v_pk_mul_f32 v[40:41], v[40:41], v[96:97] op_sel_hi:[1,0]\n" \
  "v_pk_mul_f32 v[42:43], v[42:43], v[98:99] op_sel_hi:[1,0]\n" \
  "v_pk_mul_f32 v[44:45], v[44:45], v[100:101] op_sel_hi:[1,0]\n" \
  "v_cvt_pk_bf16_f32 v40, 0, v40\n" \
  "v_cvt_pk_bf16_f32 v41, 0, v41\n" \
  "v_cvt_pk_bf16_f32 v42, 0, v42\n" \
  "v_cvt_pk_bf16_f32 v43, 0, v43\n" \
  "v_cvt_pk_bf16_f32 v44, 0, v44\n" \
  "v_cvt_pk_bf16_f32 v45, 0, v45\n" \
  "v_pk_add_f32 v[40:41], v[40:41], v[96:97] op_sel:[0,1]\n" \
  "v_pk_add_f32 v[42:43], v[42:43], v[98:99] op_sel:[0,1]\n" \
  "v_pk_add_f32 v[44:45], v[44:45], v[100:101] op_sel:[0,1]\n" \
  "v_cvt_pk_bf16_f32 v40, 0, v40\n" \
  "v_cvt_pk_bf16_f32 v41, 0, v41\n" \
  "v_cvt_pk_bf16_f32 v42, 0, v42\n" \
  "v_cvt_pk_bf16_f32 v43, 0, v43\n" \
  "v_cvt_pk_bf16_f32 v44, 0, v44\n" \
  "v_cvt_pk_bf16_f32 v45, 0, v45\n" \
  "v_cvt_pkrtz_f16_f32 v102, v41, v40\n" \
  "v_cvt_pkrtz_f16_f32 v103, v43, v42\n" \
  "v_cvt_pkrtz_f16_f32 v104, v45, v44\n" \
  "ds_write_b32 v105, v102 offset:3072\n" \
  "ds_write_b32 v105, v103 offset:11264\n" \
  "ds_write_b32 v105, v104 offset:19456\n" \
  SSTAMP7 \
  "s_mov_b32 s94, 1581056\n" \
  "s_mov_b32 s95, 8192\n" \
  "buffer_load_dwordx4 v[80:83], v106, s[40:43], s94 offen sc1\n" \
  "buffer_load_dwordx4 v[84:87], v107, s[40:43], s95 offen sc1\n" \
  "s_mov_b32 s94, 2371584\n" \
  "s_mov_b32 s95, 12288\n" \
  "buffer_load_dwordx4 v[88:91], v106, s[40:43], s94 offen sc1\n" \
  "buffer_load_dwordx4 v[92:95], v107, s[40:43], s95 offen sc1\n" \
  "buffer_load_dwordx2 v[118:119], v114, s[40:43], 0 offen sc1\n" \
  "v_mov_b32 v102, 0x10000\n" \
  "ds_add_u32 v111, v102 offset:4\n" \
  "s_waitcnt vmcnt(5)\n" \
  "v_cmp_ne_u32 vcc, s44, v75\n" \
  "v_cmp_ne_u32 s[94:95], s44, v79\n" \
  "s_or_b64 vcc, vcc, s[94:95]\n" \
  "s_cbranch_scc1 Lu1s%=\n" \
  "Lu1b%=:\n" \
  "v_lshlrev_b32 v102, 16, v78\n" \
  "v_and_b32 v103, 0xffff0000, v78\n" \
  "v_lshlrev_b32 v22, 16, v72\n" \
  "v_lshlrev_b32 v23, 16, v73\n" \
  "v_and_b32 v24, 0xffff0000, v72\n" \
  "v_and_b32 v25, 0xffff0000, v73\n" \
  "v_lshlrev_b32 v26, 16, v74\n" \
  "v_and_b32 v27, 0xffff0000, v74\n" \
  "v_pk_add_f32 v[46:47], v[22:23], v[102:103] neg_lo:[0,1] neg_hi:[0,1]\n" \
  "v_pk_add_f32 v[48:49], v[24:25], v[102:103] neg_lo:[0,1] neg_hi:[0,1]\n" \
  "v_pk_add_f32 v[50:51], v[26:27], v[102:103] neg_lo:[0,1] neg_hi:[0,1]\n" \
  "v_cvt_pk_bf16_f32 v46, 0, v46\n" \
  "v_cvt_pk_bf16_f32 v47, 0, v47\n" \
  "v_cvt_pk_bf16_f32 v48, 0, v48\n" \
  "v_cvt_pk_bf16_f32 v49, 0, v49\n" \
  "v_cvt_pk_bf16_f32 v50, 0, v50\n" \
  "v_cvt_pk_bf16_f32 v51, 0, v51\n" \
  "v_pk_mul_f32 v[46:47], v[46:47], v[76:77]\n" \
  "v_pk_mul_f32 v[48:49], v[48:49], v[76:77]\n" \
  "v_pk_mul_f32 v[50:51], v[50:51], v[76:77]\n" \
  "v_cvt_pk_bf16_f32 v46, 0, v46\n" \
  "v_cvt_pk_bf16_f32 v47, 0, v47\n" \
  "v_cvt_pk_bf16_f32 v48, 0, v48\n" \
  "v_cvt_pk_bf16_f32 v49, 0, v49\n" \
  "v_cvt_pk_bf16_f32 v50, 0, v50\n" \
  "v_cvt_pk_bf16_f32 v51, 0, v51\n" \
  "v_pk_mul_f32 v[46:47], v[46:47], v[96:97] op_sel_hi:[1,0]\n" \
  "v_pk_mul_f32 v[48:49], v[48:49], v[98:99] op_sel_hi:[1,0]\n" \
  "v_pk_mul_f32 v[50:51], v[50:51], v[100:101] op_sel_hi:[1,0]\n" \
  "v_cvt_pk_bf16_f32 v46, 0, v46\n" \
  "v_cvt_pk_bf16_f32 v47, 0, v47\n" \
  "v_cvt_pk_bf16_f32 v48, 0, v48\n" \
  "v_cvt_pk_bf16_f32 v49, 0, v49\n" \
  "v_cvt_pk_bf16_f32 v50, 0, v50\n" \
  "v_cvt_pk_bf16_f32 v51, 0, v51\n" \
  "v_pk_add_f32 v[46:47], v[46:47], v[96:97] op_sel:[0,1]\n" \
  "v_pk_add_f32 v[48:49], v[48:49], v[98:99] op_sel:[0,1]\n" \
  "v_pk_add_f32 v[50:51], v[50:51], v[100:101] op_sel:[0,1]\n" \
  "v_cvt_pk_bf16_f32 v46, 0, v46\n" \
  "v_cvt_pk_bf16_f32 v47, 0, v47\n" \
  "v_cvt_pk_bf16_f32 v48, 0, v48\n" \
  "v_cvt_pk_bf16_f32 v49, 0, v49\n" \
  "v_cvt_pk_bf16_f32 v50, 0, v50\n" \
  "v_cvt_pk_bf16_f32 v51, 0, v51\n" \
  "v_cvt_pkrtz_f16_f32 v102, v47, v46\n" \
  "v_cvt_pkrtz_f16_f32 v103, v49, v48\n" \
  "v_cvt_pkrtz_f16_f32 v104, v51, v50\n" \
  "ds_write_b32 v105, v102 offset:2048\n" \
  "ds_write_b32 v105, v103 offset:10240\n" \
  "ds_write_b32 v105, v104 offset:18432\n" \
  "ds_add_u32 v111, v112 offset:4\n" \
  "v_mov_b32 v112, 0x100\n" \
  SSTAMP8 \
  "s_waitcnt vmcnt(0)\n" \
  "v_cmp_ne_u32 vcc, s44, v83\n" \
  "v_cmp_ne_u32 s[94:95], s44, v87\n" \
  "s_or_b64 vcc, vcc, s[94:95]\n" \
  "s_cbranch_scc1 Lu2s%=\n" \
  "Lu2b%=:\n" \
  "v_cmp_ne_u32 vcc, s44, v91\n" \
  "v_cmp_ne_u32 s[94:95], s44, v95\n" \
  "s_or_b64 vcc, vcc, s[94:95]\n" \
  "s_cbranch_scc1 Lu3s%=\n" \
  "Lu3b%=:\n" \
  "v_cmp_ne_u32 vcc, s44, v119\n" \
  "s_cmp_lg_u64 vcc, 0\n" \
  "s_cbranch_scc1 Las%=\n" \
  "Lab%=:\n" \
  "v_lshlrev_b32 v102, 16, v86\n" \
  "v_and_b32 v103, 0xffff0000, v86\n" \
  "v_lshlrev_b32 v28, 16, v80\n" \
  "v_lshlrev_b32 v29, 16, v81\n" \
  "v_and_b32 v30, 0xffff0000, v80\n" \
  "v_and_b32 v31, 0xffff0000, v81\n" \
  "v_lshlrev_b32 v32, 16, v82\n" \
  "v_and_b32 v33, 0xffff0000, v82\n" \
  "v_sub_f32 v52, v28, v102\n" \
  "v_sub_f32 v53, v29, v103\n" \
  "v_sub_f32 v54, v30, v102\n" \
  "v_sub_f32 v55, v31, v103\n" \
  "v_sub_f32 v56, v32, v102\n" \
  "v_sub_f32 v57, v33, v103\n" \
  "v_cvt_pk_bf16_f32 v52, 0, v52\n" \
  "v_cvt_pk_bf16_f32 v53, 0, v53\n" \
  "v_cvt_pk_bf16_f32 v54, 0, v54\n" \
  "v_cvt_pk_bf16_f32 v55, 0, v55\n" \
  "v_cvt_pk_bf16_f32 v56, 0, v56\n" \
  "v_cvt_pk_bf16_f32 v57, 0, v57\n" \
  "v_mul_f32 v52, v52, v84\n" \
  "v_mul_f32 v53, v53, v85\n" \
  "v_mul_f32 v54, v54, v84\n" \
  "v_mul_f32 v55, v55, v85\n" \
  "v_mul_f32 v56, v56, v84\n" \
  "v_mul_f32 v57, v57, v85\n" \
  "v_cvt_pk_bf16_f32 v52, 0, v52\n" \
  "v_cvt_pk_bf16_f32 v53, 0, v53\n" \
  "v_cvt_pk_bf16_f32 v54, 0, v54\n" \
  "v_cvt_pk_bf16_f32 v55, 0, v55\n" \
  "v_cvt_pk_bf16_f32 v56, 0, v56\n" \
  "v_cvt_pk_bf16_f32 v57, 0, v57\n" \
  "v_mul_f32 v52, v52, v96\n" \
  "v_mul_f32 v53, v53, v96\n" \
  "v_mul_f32 v54, v54, v98\n" \
  "v_mul_f32 v55, v55, v98\n" \
  "v_mul_f32 v56, v56, v100\n" \
  "v_mul_f32 v57, v57, v100\n" \
  "v_cvt_pk_bf16_f32 v52, 0, v52\n" \
  "v_cvt_pk_bf16_f32 v53, 0, v53\n" \
  "v_cvt_pk_bf16_f32 v54, 0, v54\n" \
  "v_cvt_pk_bf16_f32 v55, 0, v55\n" \
  "v_cvt_pk_bf16_f32 v56, 0, v56\n" \
  "v_cvt_pk_bf16_f32 v57, 0, v57\n" \
  "v_add_f32 v52, v52, v97\n" \
  "v_add_f32 v53, v53, v97\n" \
  "v_add_f32 v54, v54, v99\n" \
  "v_add_f32 v55, v55, v99\n" \
  "v_add_f32 v56, v56, v101\n" \
  "v_add_f32 v57, v57, v101\n" \
  "v_cvt_pk_bf16_f32 v52, 0, v52\n" \
  "v_cvt_pk_bf16_f32 v53, 0, v53\n" \
  "v_cvt_pk_bf16_f32 v54, 0, v54\n" \
  "v_cvt_pk_bf16_f32 v55, 0, v55\n" \
  "v_cvt_pk_bf16_f32 v56, 0, v56\n" \
  "v_cvt_pk_bf16_f32 v57, 0, v57\n" \
  "v_cvt_pkrtz_f16_f32 v102, v53, v52\n" \
  "v_cvt_pkrtz_f16_f32 v103, v55, v54\n" \
  "v_cvt_pkrtz_f16_f32 v104, v57, v56\n" \
  "ds_write_b32 v105, v102 offset:1024\n" \
  "ds_write_b32 v105, v103 offset:9216\n" \
  "ds_write_b32 v105, v104 offset:17408\n" \
  "v_lshlrev_b32 v102, 16, v94\n" \
  "v_and_b32 v103, 0xffff0000, v94\n" \
  "v_lshlrev_b32 v34, 16, v88\n" \
  "v_lshlrev_b32 v35, 16, v89\n" \
  "v_and_b32 v36, 0xffff0000, v88\n" \
  "v_and_b32 v37, 0xffff0000, v89\n" \
  "v_lshlrev_b32 v38, 16, v90\n" \
  "v_and_b32 v39, 0xffff0000, v90\n" \
  "v_sub_f32 v58, v34, v102\n" \
  "v_sub_f32 v59, v35, v103\n" \
  "v_sub_f32 v60, v36, v102\n" \
  "v_sub_f32 v61, v37, v103\n" \
  "v_sub_f32 v62, v38, v102\n" \
  "v_sub_f32 v63, v39, v103\n" \
  "v_cvt_pk_bf16_f32 v58, 0, v58\n" \
  "v_cvt_pk_bf16_f32 v59, 0, v59\n" \
  "v_cvt_pk_bf16_f32 v60, 0, v60\n" \
  "v_cvt_pk_bf16_f32 v61, 0, v61\n" \
  "v_cvt_pk_bf16_f32 v62, 0, v62\n" \
  "v_cvt_pk_bf16_f32 v63, 0, v63\n" \
  "v_mul_f32 v58, v58, v92\n" \
  "v_mul_f32 v59, v59, v93\n" \
  "v_mul_f32 v60, v60, v92\n" \
  "v_mul_f32 v61, v61, v93\n" \
  "v_mul_f32 v62, v62, v92\n" \
  "v_mul_f32 v63, v63, v93\n" \
  "v_cvt_pk_bf16_f32 v58, 0, v58\n" \
  "v_cvt_pk_bf16_f32 v59, 0, v59\n" \
  "v_cvt_pk_bf16_f32 v60, 0, v60\n" \
  "v_cvt_pk_bf16_f32 v61, 0, v61\n" \
  "v_cvt_pk_bf16_f32 v62, 0, v62\n" \
  "v_cvt_pk_bf16_f32 v63, 0, v63\n" \
  "v_mul_f32 v58, v58, v96\n" \
  "v_mul_f32 v59, v59, v96\n" \
  "v_mul_f32 v60, v60, v98\n" \
  "v_mul_f32 v61, v61, v98\n" \
  "v_mul_f32 v62, v62, v100\n" \
  "v_mul_f32 v63, v63, v100\n" \
  "v_cvt_pk_bf16_f32 v58, 0, v58\n" \
  "v_cvt_pk_bf16_f32 v59, 0, v59\n" \
  "v_cvt_pk_bf16_f32 v60, 0, v60\n" \
  "v_cvt_pk_bf16_f32 v61, 0, v61\n" \
  "v_cvt_pk_bf16_f32 v62, 0, v62\n" \
  "v_cvt_pk_bf16_f32 v63, 0, v63\n" \
  "v_add_f32 v58, v58, v97\n" \
  "v_add_f32 v59, v59, v97\n" \
  "v_add_f32 v60, v60, v99\n" \
  "v_add_f32 v61, v61, v99\n" \
  "v_add_f32 v62, v62, v101\n" \
  "v_add_f32 v63, v63, v101\n" \
  "v_cvt_pk_bf16_f32 v58, 0, v58\n" \
  "v_cvt_pk_bf16_f32 v59, 0, v59\n" \
  "v_cvt_pk_bf16_f32 v60, 0, v60\n" \
  "v_cvt_pk_bf16_f32 v61, 0, v61\n" \
  "v_cvt_pk_bf16_f32 v62, 0, v62\n" \
  "v_cvt_pk_bf16_f32 v63, 0, v63\n" \
  "v_cvt_pkrtz_f16_f32 v102, v59, v58\n" \
  "v_cvt_pkrtz_f16_f32 v103, v61, v60\n" \
  "v_cvt_pkrtz_f16_f32 v104, v63, v62\n" \
  "ds_write_b32 v105, v102 offset:0\n" \
  "ds_write_b32 v105, v103 offset:8192\n" \
  "ds_write_b32 v105, v104 offset:16384\n" \
  "ds_add_u32 v111, v112 offset:4\n" \
  SSTAMP9 \
  "s_setprio " STR(PRE_) "\n" \
  "v_mul_f32 v118, v118, v110\n" \
  "v_mov_b32 v90, 158224\n" \
  "v_mov_b32 v85, s44\n" \
  "v_add_f32_dpp v118, v118, v118 quad_perm:[1,0,3,2] row_mask:0xf bank_mask:0xf bound_ctrl:1\n" \
  "s_nop 1\n" \
  "v_add_f32_dpp v118, v118, v118 quad_perm:[2,3,0,1] row_mask:0xf bank_mask:0xf bound_ctrl:1\n" \
  "v_cvt_pk_bf16_f32 v118, 0, v118\n" \
  "s_nop 0\n" \
  "v_readlane_b32 s94, v118, 0\n" \
  "v_readlane_b32 s95, v118, 8\n" \
  "v_readlane_b32 s96, v118, 16\n" \
  "v_mov_b32 v77, s94\n" \
  "v_mov_b32 v79, s95\n" \
  "v_mov_b32 v81, s96\n" \
  "ds_read_b32 v86, v111\n" \
  "ds_read_b64 v[64:65], v115 offset:0\n" \
  "ds_read_b64 v[66:67], v115 offset:32096\n" \
  "ds_read_b64 v[68:69], v116 offset:0\n" \
  "ds_read_b128 v[92:95], v90\n" \
  "v_mul_f32 v40, s84, v40\n" \
  "v_mul_f32 v41, s84, v41\n" \
  "v_mul_f32 v42, s85, v42\n" \
  "v_mul_f32 v43, s85, v43\n" \
  "v_mul_f32 v44, s86, v44\n" \
  "v_mul_f32 v45, s86, v45\n" \
  "v_cvt_pk_bf16_f32 v40, 0, v40\n" \
  "v_cvt_pk_bf16_f32 v41, 0, v41\n" \
  "v_cvt_pk_bf16_f32 v42, 0, v42\n" \
  "v_cvt_pk_bf16_f32 v43, 0, v43\n" \
  "v_cvt_pk_bf16_f32 v44, 0, v44\n" \
  "v_cvt_pk_bf16_f32 v45, 0, v45\n" \
  "s_waitcnt lgkmcnt(0)\n" \
  "v_readfirstlane_b32 s94, v86\n" \
  "s_lshr_b32 s94, s94, s45\n" \
  "s_bfe_u32 s94, s94, 0x40000\n" \
  "s_cmp_lg_u32 s94, 3\n" \
  "s_cbranch_scc1 Le0s%=\n" \
  "Le0b%=:\n" \
  "v_mul_f32 v76, s87, v92\n" \
  "v_mul_f32 v78, s88, v93\n" \
  "v_mul_f32 v80, s89, v94\n" \
  "v_mul_f32 v64, v64, v76\n" \
  "v_mul_f32 v65, v65, v76\n" \
  "v_mul_f32 v66, v66, v78\n" \
  "v_mul_f32 v67, v67, v78\n" \
  "v_mul_f32 v68, v68, v80\n" \
  "v_mul_f32 v69, v69, v80\n" \
  "v_cvt_pk_bf16_f32 v64, 0, v64\n" \
  "v_cvt_pk_bf16_f32 v65, 0, v65\n" \
  "v_cvt_pk_bf16_f32 v66, 0, v66\n" \
  "v_cvt_pk_bf16_f32 v67, 0, v67\n" \
  "v_cvt_pk_bf16_f32 v68, 0, v68\n" \
  "v_cvt_pk_bf16_f32 v69, 0, v69\n" \
  "v_add_f32 v64, v64, v40\n" \
  "v_add_f32 v65, v65, v41\n" \
  "v_add_f32 v66, v66, v42\n" \
  "v_add_f32 v67, v67, v43\n" \
  "v_add_f32 v68, v68, v44\n" \
  "v_add_f32 v69, v69, v45\n" \
  "v_cvt_pk_bf16_f32 v64, 0, v64\n" \
  "v_cvt_pk_bf16_f32 v65, 0, v65\n" \
  "v_cvt_pk_bf16_f32 v66, 0, v66\n" \
  "v_cvt_pk_bf16_f32 v67, 0, v67\n" \
  "v_cvt_pk_bf16_f32 v68, 0, v68\n" \
  "v_cvt_pk_bf16_f32 v69, 0, v69\n" \
  "v_mul_f32 v64, v64, v77\n" \
  "v_mul_f32 v65, v65, v77\n" \
  "v_mul_f32 v66, v66, v79\n" \
  "v_mul_f32 v67, v67, v79\n" \
  "v_mul_f32 v68, v68, v81\n" \
  "v_mul_f32 v69, v69, v81\n" \
  "v_cvt_pk_bf16_f32 v64, 0, v64\n" \
  "v_cvt_pk_bf16_f32 v65, 0, v65\n" \
  "v_cvt_pk_bf16_f32 v66, 0, v66\n" \
  "v_cvt_pk_bf16_f32 v67, 0, v67\n" \
  "v_cvt_pk_bf16_f32 v68, 0, v68\n" \
  "v_cvt_pk_bf16_f32 v69, 0, v69\n" \
  "v_add_f32 v64, v64, v16\n" \
  "v_add_f32 v65, v65, v17\n" \
  "v_add_f32 v66, v66, v18\n" \
  "v_add_f32 v67, v67, v19\n" \
  "v_add_f32 v68, v68, v20\n" \
  "v_add_f32 v69, v69, v21\n" \
  "v_cvt_pk_bf16_f32 v82, v64, v66\n" \
  "v_cvt_pk_bf16_f32 v83, v65, v67\n" \
  "v_cvt_pk_bf16_f32 v84, v68, v69\n" \
  "s_movk_i32 s94, 0\n" \
  "buffer_store_dwordx4 v[82:85], v117, s[40:43], s94 offen sc1\n" \
  "ds_read_b32 v86, v111\n" \
  "ds_read_b64 v[64:65], v115 offset:2560\n" \
  "ds_read_b64 v[70:71], v115 offset:10240\n" \
  "ds_read_b64 v[66:67], v115 offset:34656\n" \
  "ds_read_b64 v[72:73], v115 offset:42336\n" \
  "ds_read_b64 v[68:69], v116 offset:2560\n" \
  "ds_read_b64 v[74:75], v116 offset:10240\n" \
  "v_mul_f32 v46, s84, v46\n" \
  "v_mul_f32 v47, s84, v47\n" \
  "v_mul_f32 v48, s85, v48\n" \
  "v_mul_f32 v49, s85, v49\n" \
  "v_mul_f32 v50, s86, v50\n" \
  "v_mul_f32 v51, s86, v51\n" \
  "v_cvt_pk_bf16_f32 v46, 0, v46\n" \
  "v_cvt_pk_bf16_f32 v47, 0, v47\n" \
  "v_cvt_pk_bf16_f32 v48, 0, v48\n" \
  "v_cvt_pk_bf16_f32 v49, 0, v49\n" \
  "v_cvt_pk_bf16_f32 v50, 0, v50\n" \
  "v_cvt_pk_bf16_f32 v51, 0, v51\n" \
  "s_waitcnt lgkmcnt(0)\n" \
  "v_readfirstlane_b32 s94, v86\n" \
  "s_lshr_b32 s94, s94, s45\n" \
  "s_bfe_u32 s94, s94, 0x40008\n" \
  "s_cmp_lg_u32 s94, 6\n" \
  "s_cbranch_scc1 Le1s%=\n" \
  "Le1b%=:\n" \
  "v_add_f32 v64, v64, v70\n" \
  "v_add_f32 v65, v65, v71\n" \
  "v_add_f32 v66, v66, v72\n" \
  "v_add_f32 v67, v67, v73\n" \
  "v_add_f32 v68, v68, v74\n" \
  "v_add_f32 v69, v69, v75\n" \
  "v_mul_f32 v64, v64, v76\n" \
  "v_mul_f32 v65, v65, v76\n" \
  "v_mul_f32 v66, v66, v78\n" \
  "v_mul_f32 v67, v67, v78\n" \
  "v_mul_f32 v68, v68, v80\n" \
  "v_mul_f32 v69, v69, v80\n" \
  "v_cvt_pk_bf16_f32 v64, 0, v64\n" \
  "v_cvt_pk_bf16_f32 v65, 0, v65\n" \
  "v_cvt_pk_bf16_f32 v66, 0, v66\n" \
  "v_cvt_pk_bf16_f32 v67, 0, v67\n" \
  "v_cvt_pk_bf16_f32 v68, 0, v68\n" \
  "v_cvt_pk_bf16_f32 v69, 0, v69\n" \
  "v_add_f32 v64, v64, v46\n" \
  "v_add_f32 v65, v65, v47\n" \
  "v_add_f32 v66, v66, v48\n" \
  "v_add_f32 v67, v67, v49\n" \
  "v_add_f32 v68, v68, v50\n" \
  "v_add_f32 v69, v69, v51\n" \
  "v_cvt_pk_bf16_f32 v64, 0, v64\n" \
  "v_cvt_pk_bf16_f32 v65, 0, v65\n" \
  "v_cvt_pk_bf16_f32 v66, 0, v66\n" \
  "v_cvt_pk_bf16_f32 v67, 0, v67\n" \
  "v_cvt_pk_bf16_f32 v68, 0, v68\n" \
  "v_cvt_pk_bf16_f32 v69, 0, v69\n" \
  "v_mul_f32 v64, v64, v77\n" \
  "v_mul_f32 v65, v65, v77\n" \
  "v_mul_f32 v66, v66, v79\n" \
  "v_mul_f32 v67, v67, v79\n" \
  "v_mul_f32 v68, v68, v81\n" \
  "v_mul_f32 v69, v69, v81\n" \
  "v_cvt_pk_bf16_f32 v64, 0, v64\n" \
  "v_cvt_pk_bf16_f32 v65, 0, v65\n" \
  "v_cvt_pk_bf16_f32 v66, 0, v66\n" \
  "v_cvt_pk_bf16_f32 v67, 0, v67\n" \
  "v_cvt_pk_bf16_f32 v68, 0, v68\n" \
  "v_cvt_pk_bf16_f32 v69, 0, v69\n" \
  "v_add_f32 v64, v64, v22\n" \
  "v_add_f32 v65, v65, v23\n" \
  "v_add_f32 v66, v66, v24\n" \
  "v_add_f32 v67, v67, v25\n" \
  "v_add_f32 v68, v68, v26\n" \
  "v_add_f32 v69, v69, v27\n" \
  "v_cvt_pk_bf16_f32 v82, v64, v66\n" \
  "v_cvt_pk_bf16_f32 v83, v65, v67\n" \
  "v_cvt_pk_bf16_f32 v84, v68, v69\n" \
  "s_movk_i32 s94, 4096\n" \
  "buffer_store_dwordx4 v[82:85], v117, s[40:43], s94 offen sc1\n" \
  SSTAMPE1 \
  "ds_read_b32 v86, v111\n" \
  "ds_read_b64 v[64:65], v115 offset:5120\n" \
  "ds_read_b64 v[70:71], v115 offset:12800\n" \
  "ds_read_b64 v[66:67], v115 offset:37216\n" \
  "ds_read_b64 v[72:73], v115 offset:44896\n" \
  "ds_read_b64 v[68:69], v116 offset:5120\n" \
  "ds_read_b64 v[74:75], v116 offset:12800\n" \
  "v_mul_f32 v52, s84, v52\n" \
  "v_mul_f32 v53, s84, v53\n" \
  "v_mul_f32 v54, s85, v54\n" \
  "v_mul_f32 v55, s85, v55\n" \
  "v_mul_f32 v56, s86, v56\n" \
  "v_mul_f32 v57, s86, v57\n" \
  "v_cvt_pk_bf16_f32 v52, 0, v52\n" \
  "v_cvt_pk_bf16_f32 v53, 0, v53\n" \
  "v_cvt_pk_bf16_f32 v54, 0, v54\n" \
  "v_cvt_pk_bf16_f32 v55, 0, v55\n" \
  "v_cvt_pk_bf16_f32 v56, 0, v56\n" \
  "v_cvt_pk_bf16_f32 v57, 0, v57\n" \
  "s_waitcnt lgkmcnt(0)\n" \
  "v_readfirstlane_b32 s94, v86\n" \
  "s_lshr_b32 s94, s94, s45\n" \
  "s_bfe_u32 s94, s94, 0x40010\n" \
  "s_cmp_lg_u32 s94, 6\n" \
  "s_cbranch_scc1 Le2s%=\n" \
  "Le2b%=:\n" \
  "v_add_f32 v64, v64, v70\n" \
  "v_add_f32 v65, v65, v71\n" \
  "v_add_f32 v66, v66, v72\n" \
  "v_add_f32 v67, v67, v73\n" \
  "v_add_f32 v68, v68, v74\n" \
  "v_add_f32 v69, v69, v75\n" \
  "v_mul_f32 v64, v64, v76\n" \
  "v_mul_f32 v65, v65, v76\n" \
  "v_mul_f32 v66, v66, v78\n" \
  "v_mul_f32 v67, v67, v78\n" \
  "v_mul_f32 v68, v68, v80\n" \
  "v_mul_f32 v69, v69, v80\n" \
  "v_cvt_pk_bf16_f32 v64, 0, v64\n" \
  "v_cvt_pk_bf16_f32 v65, 0, v65\n" \
  "v_cvt_pk_bf16_f32 v66, 0, v66\n" \
  "v_cvt_pk_bf16_f32 v67, 0, v67\n" \
  "v_cvt_pk_bf16_f32 v68, 0, v68\n" \
  "v_cvt_pk_bf16_f32 v69, 0, v69\n" \
  "v_add_f32 v64, v64, v52\n" \
  "v_add_f32 v65, v65, v53\n" \
  "v_add_f32 v66, v66, v54\n" \
  "v_add_f32 v67, v67, v55\n" \
  "v_add_f32 v68, v68, v56\n" \
  "v_add_f32 v69, v69, v57\n" \
  "v_cvt_pk_bf16_f32 v64, 0, v64\n" \
  "v_cvt_pk_bf16_f32 v65, 0, v65\n" \
  "v_cvt_pk_bf16_f32 v66, 0, v66\n" \
  "v_cvt_pk_bf16_f32 v67, 0, v67\n" \
  "v_cvt_pk_bf16_f32 v68, 0, v68\n" \
  "v_cvt_pk_bf16_f32 v69, 0, v69\n" \
  "v_mul_f32 v64, v64, v77\n" \
  "v_mul_f32 v65, v65, v77\n" \
  "v_mul_f32 v66, v66, v79\n" \
  "v_mul_f32 v67, v67, v79\n" \
  "v_mul_f32 v68, v68, v81\n" \
  "v_mul_f32 v69, v69, v81\n" \
  "v_cvt_pk_bf16_f32 v64, 0, v64\n" \
  "v_cvt_pk_bf16_f32 v65, 0, v65\n" \
  "v_cvt_pk_bf16_f32 v66, 0, v66\n" \
  "v_cvt_pk_bf16_f32 v67, 0, v67\n" \
  "v_cvt_pk_bf16_f32 v68, 0, v68\n" \
  "v_cvt_pk_bf16_f32 v69, 0, v69\n" \
  "v_add_f32 v64, v64, v28\n" \
  "v_add_f32 v65, v65, v29\n" \
  "v_add_f32 v66, v66, v30\n" \
  "v_add_f32 v67, v67, v31\n" \
  "v_add_f32 v68, v68, v32\n" \
  "v_add_f32 v69, v69, v33\n" \
  "v_cvt_pk_bf16_f32 v82, v64, v66\n" \
  "v_cvt_pk_bf16_f32 v83, v65, v67\n" \
  "v_cvt_pk_bf16_f32 v84, v68, v69\n" \
  "s_movk_i32 s94, 8192\n" \
  "buffer_store_dwordx4 v[82:85], v117, s[40:43], s94 offen sc1\n" \
  "ds_read_b32 v86, v111\n" \
  "ds_read_b64 v[64:65], v115 offset:7680\n" \
  "ds_read_b64 v[70:71], v115 offset:15360\n" \
  "ds_read_b64 v[66:67], v115 offset:39776\n" \
  "ds_read_b64 v[72:73], v115 offset:47456\n" \
  "ds_read_b64 v[68:69], v116 offset:7680\n" \
  "ds_read_b64 v[74:75], v116 offset:15360\n" \
  "v_mul_f32 v58, s84, v58\n" \
  "v_mul_f32 v59, s84, v59\n" \
  "v_mul_f32 v60, s85, v60\n" \
  "v_mul_f32 v61, s85, v61\n" \
  "v_mul_f32 v62, s86, v62\n" \
  "v_mul_f32 v63, s86, v63\n" \
  "v_cvt_pk_bf16_f32 v58, 0, v58\n" \
  "v_cvt_pk_bf16_f32 v59, 0, v59\n" \
  "v_cvt_pk_bf16_f32 v60, 0, v60\n" \
  "v_cvt_pk_bf16_f32 v61, 0, v61\n" \
  "v_cvt_pk_bf16_f32 v62, 0, v62\n" \
  "v_cvt_pk_bf16_f32 v63, 0, v63\n" \
  "s_waitcnt lgkmcnt(0)\n" \
  "v_readfirstlane_b32 s94, v86\n" \
  "s_lshr_b32 s94, s94, s45\n" \
  "s_bfe_u32 s94, s94, 0x40018\n" \
  "s_cmp_lg_u32 s94, 6\n" \
  "s_cbranch_scc1 Le3s%=\n" \
  "Le3b%=:\n" \
  SSTAMP10 \
  "v_pk_add_f32 v[64:65], v[64:65], v[70:71]\n" \
  "v_pk_add_f32 v[66:67], v[66:67], v[72:73]\n" \
  "v_pk_add_f32 v[68:69], v[68:69], v[74:75]\n" \
  "v_pk_mul_f32 v[64:65], v[64:65], v[76:77] op_sel_hi:[1,0]\n" \
  "v_pk_mul_f32 v[66:67], v[66:67], v[78:79] op_sel_hi:[1,0]\n" \
  "v_pk_mul_f32 v[68:69], v[68:69], v[80:81] op_sel_hi:[1,0]\n" \
  "v_cvt_pk_bf16_f32 v64, 0, v64\n" \
  "v_cvt_pk_bf16_f32 v65, 0, v65\n" \
  "v_cvt_pk_bf16_f32 v66, 0, v66\n" \
  "v_cvt_pk_bf16_f32 v67, 0, v67\n" \
  "v_cvt_pk_bf16_f32 v68, 0, v68\n" \
  "v_cvt_pk_bf16_f32 v69, 0, v69\n" \
  "v_pk_add_f32 v[64:65], v[64:65], v[58:59]\n" \
  "v_pk_add_f32 v[66:67], v[66:67], v[60:61]\n" \
  "v_pk_add_f32 v[68:69], v[68:69], v[62:63]\n" \
  "v_cvt_pk_bf16_f32 v64, 0, v64\n" \
  "v_cvt_pk_bf16_f32 v65, 0, v65\n" \
  "v_cvt_pk_bf16_f32 v66, 0, v66\n" \
  "v_cvt_pk_bf16_f32 v67, 0, v67\n" \
  "v_cvt_pk_bf16_f32 v68, 0, v68\n" \
  "v_cvt_pk_bf16_f32 v69, 0, v69\n" \
  "v_pk_mul_f32 v[64:65], v[64:65], v[76:77] op_sel:[0,1]\n" \
  "v_pk_mul_f32 v[66:67], v[66:67], v[78:79] op_sel:[0,1]\n" \
  "v_pk_mul_f32 v[68:69], v[68:69], v[80:81] op_sel:[0,1]\n" \
  "v_cvt_pk_bf16_f32 v64, 0, v64\n" \
  "v_cvt_pk_bf16_f32 v65, 0, v65\n" \
  "v_cvt_pk_bf16_f32 v66, 0, v66\n" \
  "v_cvt_pk_bf16_f32 v67, 0, v67\n" \
  "v_cvt_pk_bf16_f32 v68, 0, v68\n" \
  "v_cvt_pk_bf16_f32 v69, 0, v69\n" \
  "v_pk_add_f32 v[64:65], v[64:65], v[34:35]\n" \
  "v_pk_add_f32 v[66:67], v[66:67], v[36:37]\n" \
  "v_pk_add_f32 v[68:69], v[68:69], v[38:39]\n" \
  "v_cvt_pk_bf16_f32 v82, v64, v66\n" \
  "v_cvt_pk_bf16_f32 v83, v65, v67\n" \
  "v_cvt_pk_bf16_f32 v84, v68, v69\n" \
  "s_movk_i32 s94, 12288\n" \
  "buffer_store_dwordx4 v[82:85], v117, s[40:43], s94 offen sc1\n" \
  "s_cmpk_lt_u32 %[b], 128\n" \
  "s_cbranch_scc1 Lhe%=\n" \
  "s_cmp_lg_u32 s50, 3\n" \
  "s_cbranch_scc1 Lhe%=\n" \
  "s_memrealtime s[46:47]\n" \
  "s_lshl_b32 s94, %[b], 7\n" \
  "s_add_u32 s94, s94, 70656\n" \
  "s_mov_b64 exec, 1\n" \
  "v_mov_b32 v87, s44\n" \
  "s_waitcnt lgkmcnt(0)\n" \
  "s_add_u32 s46, s46, " STR(KR) "\n" \
  "v_mov_b32 v86, s46\n" \
  "buffer_store_dwordx2 v[86:87], off, s[40:43], s94 sc1\n" \
  "s_mov_b64 exec, -1\n" \
  "s_branch Lhe%=\n" \
  "Lms%=:\n" \
  "s_mov_b32 s97, 0x7fff\n" \
  "Lmr%=:\n" \
  SSTAMPCm \
  "s_mov_b64 exec, vcc\n" \
  "buffer_load_dwordx2 v[102:103], v108, s[40:43], 0 offen sc1\n" \
  "s_mov_b64 exec, -1\n" \
  "s_waitcnt vmcnt(0)\n" \
  "v_cmp_ne_u32 vcc, s44, v103\n" \
  "s_cmp_lg_u64 vcc, 0\n" \
  "s_cbranch_scc0 Lmb%=\n" \
  "s_sub_u32 s97, s97, 1\n" \
  "s_cbranch_scc0 Lmr%=\n" \
  "s_branch Lmb%=\n" \
  "Lu0s%=:\n" \
  "s_mov_b32 s97, 0x7fff\n" \
  "Lu0r%=:\n" \
  SSTAMPCu0 \
  "s_mov_b64 exec, vcc\n" \
  "s_mov_b32 s94, 0\n" \
  "s_mov_b32 s95, 0\n" \
  "buffer_load_dwordx4 v[64:67], v106, s[40:43], s94 offen sc1\n" \
  "buffer_load_dwordx4 v[68:71], v107, s[40:43], s95 offen sc1\n" \
  "s_mov_b64 exec, -1\n" \
  "s_waitcnt vmcnt(0)\n" \
  "v_cmp_ne_u32 vcc, s44, v67\n" \
  "v_cmp_ne_u32 s[94:95], s44, v71\n" \
  "s_or_b64 vcc, vcc, s[94:95]\n" \
  "s_cbranch_scc0 Lu0b%=\n" \
  "s_sub_u32 s97, s97, 1\n" \
  "s_cbranch_scc0 Lu0r%=\n" \
  "s_branch Lu0b%=\n" \
  "Lu1s%=:\n" \
  "s_mov_b32 s97, 0x7fff\n" \
  "Lu1r%=:\n" \
  SSTAMPCu1 \
  "s_mov_b64 exec, vcc\n" \
  "s_mov_b32 s94, 790528\n" \
  "s_mov_b32 s95, 4096\n" \
  "buffer_load_dwordx4 v[72:75], v106, s[40:43], s94 offen sc1\n" \
  "buffer_load_dwordx4 v[76:79], v107, s[40:43], s95 offen sc1\n" \
  "s_mov_b64 exec, -1\n" \
  "s_waitcnt vmcnt(0)\n" \
  "v_cmp_ne_u32 vcc, s44, v75\n" \
  "v_cmp_ne_u32 s[94:95], s44, v79\n" \
  "s_or_b64 vcc, vcc, s[94:95]\n" \
  "s_cbranch_scc0 Lu1b%=\n" \
  "s_sub_u32 s97, s97, 1\n" \
  "s_cbranch_scc0 Lu1r%=\n" \
  "s_branch Lu1b%=\n" \
  "Lu2s%=:\n" \
  "s_mov_b32 s97, 0x7fff\n" \
  "Lu2r%=:\n" \
  SSTAMPCu2 \
  "s_mov_b64 exec, vcc\n" \
  "s_mov_b32 s94, 1581056\n" \
  "s_mov_b32 s95, 8192\n" \
  "buffer_load_dwordx4 v[80:83], v106, s[40:43], s94 offen sc1\n" \
  "buffer_load_dwordx4 v[84:87], v107, s[40:43], s95 offen sc1\n" \
  "s_mov_b64 exec, -1\n" \
  "s_waitcnt vmcnt(0)\n" \
  "v_cmp_ne_u32 vcc, s44, v83\n" \
  "v_cmp_ne_u32 s[94:95], s44, v87\n" \
  "s_or_b64 vcc, vcc, s[94:95]\n" \
  "s_cbranch_scc0 Lu2b%=\n" \
  "s_sub_u32 s97, s97, 1\n" \
  "s_cbranch_scc0 Lu2r%=\n" \
  "s_branch Lu2b%=\n" \
  "Lu3s%=:\n" \
  "s_mov_b32 s97, 0x7fff\n" \
  "Lu3r%=:\n" \
  SSTAMPCu3 \
  "s_mov_b64 exec, vcc\n" \
  "s_mov_b32 s94, 2371584\n" \
  "s_mov_b32 s95, 12288\n" \
  "buffer_load_dwordx4 v[88:91], v106, s[40:43], s94 offen sc1\n" \
  "buffer_load_dwordx4 v[92:95], v107, s[40:43], s95 offen sc1\n" \
  "s_mov_b64 exec, -1\n" \
  "s_waitcnt vmcnt(0)\n" \
  "v_cmp_ne_u32 vcc, s44, v91\n" \
  "v_cmp_ne_u32 s[94:95], s44, v95\n" \
  "s_or_b64 vcc, vcc, s[94:95]\n" \
  "s_cbranch_scc0 Lu3b%=\n" \
  "s_sub_u32 s97, s97, 1\n" \
  "s_cbranch_scc0 Lu3r%=\n" \
  "s_branch Lu3b%=\n" \
  "Las%=:\n" \
  "s_mov_b32 s97, 0x7fff\n" \
  "Lar%=:\n" \
  SSTAMPCa \
  "s_mov_b64 exec, vcc\n" \
  "buffer_load_dwordx2 v[118:119], v114, s[40:43], 0 offen sc1\n" \
  "s_mov_b64 exec, -1\n" \
  "s_waitcnt vmcnt(0)\n" \
  "v_cmp_ne_u32 vcc, s44, v119\n" \
  "s_cmp_lg_u64 vcc, 0\n" \
  "s_cbranch_scc0 Lab%=\n" \
  "s_sub_u32 s97, s97, 1\n" \
  "s_cbranch_scc0 Lar%=\n" \
  "s_branch Lab%=\n" \
  "Le0s%=:\n" \
  "s_mov_b32 s97, 0x1fffff\n" \
  "Le0r%=:\n" \
  "s_sleep 1\n" \
  "ds_read_b32 v86, v111\n" \
  "s_waitcnt lgkmcnt(0)\n" \
  "v_readfirstlane_b32 s94, v86\n" \
  "s_lshr_b32 s94, s94, s45\n" \
  "s_bfe_u32 s94, s94, 0x40000\n" \
  "s_cmp_lg_u32 s94, 3\n" \
  "s_cbranch_scc0 Le0x%=\n" \
  "s_sub_u32 s97, s97, 1\n" \
  "s_cbranch_scc0 Le0r%=\n" \
  "Le0x%=:\n" \
  "ds_read_b64 v[64:65], v115 offset:0\n" \
  "ds_read_b64 v[66:67], v115 offset:32096\n" \
  "ds_read_b64 v[68:69], v116 offset:0\n" \
  "ds_read_b128 v[92:95], v90\n" \
  "s_waitcnt lgkmcnt(0)\n" \
  "s_branch Le0b%=\n" \
  "Le1s%=:\n" \
  "s_mov_b32 s97, 0x1fffff\n" \
  "Le1r%=:\n" \
  "s_sleep 1\n" \
  "ds_read_b32 v86, v111\n" \
  "s_waitcnt lgkmcnt(0)\n" \
  "v_readfirstlane_b32 s94, v86\n" \
  "s_lshr_b32 s94, s94, s45\n" \
  "s_bfe_u32 s94, s94, 0x40008\n" \
  "s_cmp_lg_u32 s94, 6\n" \
  "s_cbranch_scc0 Le1x%=\n" \
  "s_sub_u32 s97, s97, 1\n" \
  "s_cbranch_scc0 Le1r%=\n" \
  "Le1x%=:\n" \
  "ds_read_b64 v[64:65], v115 offset:2560\n" \
  "ds_read_b64 v[70:71], v115 offset:10240\n" \
  "ds_read_b64 v[66:67], v115 offset:34656\n" \
  "ds_read_b64 v[72:73], v115 offset:42336\n" \
  "ds_read_b64 v[68:69], v116 offset:2560\n" \
  "ds_read_b64 v[74:75], v116 offset:10240\n" \
  "s_waitcnt lgkmcnt(0)\n" \
  "s_branch Le1b%=\n" \
  "Le2s%=:\n" \
  "s_mov_b32 s97, 0x1fffff\n" \
  "Le2r%=:\n" \
  "s_sleep 1\n" \
  "ds_read_b32 v86, v111\n" \
  "s_waitcnt lgkmcnt(0)\n" \
  "v_readfirstlane_b32 s94, v86\n" \
  "s_lshr_b32 s94, s94, s45\n" \
  "s_bfe_u32 s94, s94, 0x40010\n" \
  "s_cmp_lg_u32 s94, 6\n" \
  "s_cbranch_scc0 Le2x%=\n" \
  "s_sub_u32 s97, s97, 1\n" \
  "s_cbranch_scc0 Le2r%=\n" \
  "Le2x%=:\n" \
  "ds_read_b64 v[64:65], v115 offset:5120\n" \
  "ds_read_b64 v[70:71], v115 offset:12800\n" \
  "ds_read_b64 v[66:67], v115 offset:37216\n" \
  "ds_read_b64 v[72:73], v115 offset:44896\n" \
  "ds_read_b64 v[68:69], v116 offset:5120\n" \
  "ds_read_b64 v[74:75], v116 offset:12800\n" \
  "s_waitcnt lgkmcnt(0)\n" \
  "s_branch Le2b%=\n" \
  "Le3s%=:\n" \
  "s_mov_b32 s97, 0x1fffff\n" \
  "Le3r%=:\n" \
  "ds_read_b32 v86, v111\n" \
  "ds_read_b64 v[64:65], v115 offset:7680\n" \
  "ds_read_b64 v[70:71], v115 offset:15360\n" \
  "ds_read_b64 v[66:67], v115 offset:39776\n" \
  "ds_read_b64 v[72:73], v115 offset:47456\n" \
  "ds_read_b64 v[68:69], v116 offset:7680\n" \
  "ds_read_b64 v[74:75], v116 offset:15360\n" \
  "s_waitcnt lgkmcnt(0)\n" \
  "v_readfirstlane_b32 s94, v86\n" \
  "s_lshr_b32 s94, s94, s45\n" \
  "s_bfe_u32 s94, s94, 0x40018\n" \
  "s_cmp_lg_u32 s94, 6\n" \
  "s_cbranch_scc0 Le3b%=\n" \
  "s_sub_u32 s97, s97, 1\n" \
  "s_cbranch_scc0 Le3r%=\n" \
  "s_branch Le3b%=\n" \
  "Lhe%=:\n" \
  SSTAMP11 \
  "s_setprio 0\n"

#define LW_ASM \
  "s_load_dwordx2 s[40:41], %[k], 0x70\n" \
  "s_load_dwordx2 s[46:47], %[k], 0x38\n" \
  "s_load_dwordx2 s[48:49], %[k], 0x68\n" \
  "v_mov_b32 v1, 158208\n" \
  "ds_read_b32 v2, v1\n" \
  "s_mov_b32 s42, 10668032\n" \
  "s_mov_b32 s43, 0x20000\n" \
  "v_and_b32 v3, 7, %[t]\n" \
  "v_and_b32 v4, 63, %[t]\n" \
  "s_waitcnt lgkmcnt(0)\n" \
  "v_readfirstlane_b32 s44, v2\n" \
  "s_cmp_ge_u32 %[w], 12\n" \
  "s_cbranch_scc1 Mls%=\n" \
  "s_lshr_b32 s50, 0x214, %[w]\n" \
  "s_bitcmp0_b32 s50, 0\n" \
  "s_cbranch_scc1 Mla%=\n" \
  "s_sleep " STR(PSLP) "\n" \
  "s_lshr_b32 s50, %[w], 2\n" \
  "s_mul_i32 s51, s50, 96\n" \
  "v_add_u32 v5, s51, v4\n" \
  "v_mov_b32 v92, 0\n" \
  "s_mov_b32 exec_hi, 0xffff\n" \
  "v_lshlrev_b32 v6, 4, v5\n" \
  "v_lshlrev_b32 v7, 2, v5\n" \
  "s_waitcnt vmcnt(" STR(PW2) ")\n" \
  "s_setprio " STR(PRP) "\n" \
  "global_load_dword v90, v7, s[46:47]\n" \
  "global_load_dword v93, v7, s[46:47] offset:192\n" \
  "s_mov_b32 s51, 17920\n" \
  "buffer_load_dwordx4 v[12:15], v6, s[40:43], s51 offen sc1\n" \
  "s_mov_b32 s51, 45568\n" \
  "buffer_load_dwordx4 v[16:19], v6, s[40:43], s51 offen sc1\n" \
  "s_mov_b32 s51, 73216\n" \
  "buffer_load_dwordx4 v[20:23], v6, s[40:43], s51 offen sc1\n" \
  "s_mov_b32 s51, 22528\n" \
  "buffer_load_dwordx4 v[24:27], v6, s[40:43], s51 offen sc1\n" \
  "s_mov_b32 s51, 50176\n" \
  "buffer_load_dwordx4 v[28:31], v6, s[40:43], s51 offen sc1\n" \
  "s_mov_b32 s51, 77824\n" \
  "buffer_load_dwordx4 v[32:35], v6, s[40:43], s51 offen sc1\n" \
  "s_mov_b32 s51, 27136\n" \
  "buffer_load_dwordx4 v[36:39], v6, s[40:43], s51 offen sc1\n" \
  "s_mov_b32 s51, 54784\n" \
  "buffer_load_dwordx4 v[40:43], v6, s[40:43], s51 offen sc1\n" \
  "s_mov_b32 s51, 82432\n" \
  "buffer_load_dwordx4 v[44:47], v6, s[40:43], s51 offen sc1\n" \
  "s_mov_b32 s51, 17920\n" \
  "buffer_load_dwordx4 v[48:51], v6, s[40:43], s51 offen offset:768 sc1\n" \
  "s_mov_b32 s51, 45568\n" \
  "buffer_load_dwordx4 v[52:55], v6, s[40:43], s51 offen offset:768 sc1\n" \
  "s_mov_b32 s51, 73216\n" \
  "buffer_load_dwordx4 v[56:59], v6, s[40:43], s51 offen offset:768 sc1\n" \
  "s_mov_b32 s51, 22528\n" \
  "buffer_load_dwordx4 v[60:63], v6, s[40:43], s51 offen offset:768 sc1\n" \
  "s_mov_b32 s51, 50176\n" \
  "buffer_load_dwordx4 v[64:67], v6, s[40:43], s51 offen offset:768 sc1\n" \
  "s_mov_b32 s51, 77824\n" \
  "buffer_load_dwordx4 v[68:71], v6, s[40:43], s51 offen offset:768 sc1\n" \
  "s_mov_b32 s51, 27136\n" \
  "buffer_load_dwordx4 v[72:75], v6, s[40:43], s51 offen offset:768 sc1\n" \
  "s_mov_b32 s51, 54784\n" \
  "buffer_load_dwordx4 v[76:79], v6, s[40:43], s51 offen offset:768 sc1\n" \
  "s_mov_b32 s51, 82432\n" \
  "buffer_load_dwordx4 v[80:83], v6, s[40:43], s51 offen offset:768 sc1\n" \
  "s_mov_b64 exec, -1\n" \
  "s_setprio 0\n" \
  "Mla%=:\n" \
  "v_mov_b32 v8, 159488\n" \
  "s_mov_b32 s97, 0x1fffff\n" \
  "Mwa%=:\n" \
  "ds_read_b32 v9, v8\n" \
  "s_waitcnt lgkmcnt(0)\n" \
  "v_readfirstlane_b32 s51, v9\n" \
  "s_cmp_ge_u32 s51, " STR(YEND) "\n" \
  "s_cbranch_scc1 Mwb%=\n" \
  "s_sleep 3\n" \
  "s_sub_u32 s97, s97, 1\n" \
  "s_cbranch_scc0 Mwa%=\n" \
  "Mwb%=:\n" \
  "v_lshrrev_b32 v95, 3, %[t]\n" \
  "v_mul_u32_u24 v94, 33024, v95\n" \
  "v_lshl_add_u32 v94, v3, 4, v94\n" \
  "s_lshl_b32 s58, %[b], 7\n" \
  "s_add_u32 s58, s58, 139264\n" \
  "s_add_u32 s59, s58, 16512\n" \
  "s_lshr_b32 s51, 0x214, %[w]\n" \
  "s_bitcmp0_b32 s51, 0\n" \
  "s_cbranch_scc1 Mlb%=\n" \
  "s_setprio " STR(PRQ) "\n" \
  "s_mov_b32 exec_hi, 0xffff\n" \
  "v_mov_b32 v8, 147456\n" \
  "v_lshl_add_u32 v7, v6, 1, v8\n" \
  "s_movk_i32 s97, 0x7fff\n" \
  "s_waitcnt vmcnt(9)\n" \
  "Mpr0%=:\n" \
  "v_bitop3_b32 v13, v13, v15, s44 bitop3:0x7e\n" \
  "v_bitop3_b32 v17, v17, v19, s44 bitop3:0x7e\n" \
  "v_bitop3_b32 v21, v21, v23, s44 bitop3:0x7e\n" \
  "v_bitop3_b32 v25, v25, v27, s44 bitop3:0x7e\n" \
  "v_bitop3_b32 v29, v29, v31, s44 bitop3:0x7e\n" \
  "v_bitop3_b32 v33, v33, v35, s44 bitop3:0x7e\n" \
  "v_bitop3_b32 v37, v37, v39, s44 bitop3:0x7e\n" \
  "v_bitop3_b32 v41, v41, v43, s44 bitop3:0x7e\n" \
  "v_bitop3_b32 v45, v45, v47, s44 bitop3:0x7e\n" \
  "v_or3_b32 v13, v13, v17, v21\n" \
  "v_or3_b32 v25, v25, v29, v33\n" \
  "v_or3_b32 v37, v37, v41, v45\n" \
  "v_or3_b32 v13, v13, v25, v37\n" \
  "v_cmp_ne_u32 vcc, 0, v13\n" \
  "s_cbranch_vccnz Mps0%=\n" \
  "Mpk0%=:\n" \
  "v_add_f32 v86, v12, v16\n" \
  "v_add_f32 v87, v14, v18\n" \
  "v_add_f32 v84, v24, v28\n" \
  "v_add_f32 v85, v26, v30\n" \
  "v_add_f32 v88, v36, v40\n" \
  "v_add_f32 v89, v38, v42\n" \
  "v_add_f32 v86, v86, v20\n" \
  "v_add_f32 v87, v87, v22\n" \
  "v_add_f32 v84, v84, v32\n" \
  "v_add_f32 v85, v85, v34\n" \
  "v_add_f32 v88, v88, v44\n" \
  "v_add_f32 v89, v89, v46\n" \
  "v_cvt_pk_bf16_f32 v84, 0, v84\n" \
  "v_cvt_pk_bf16_f32 v85, 0, v85\n" \
  "v_cvt_pk_bf16_f32 v86, 0, v86\n" \
  "v_cvt_pk_bf16_f32 v87, 0, v87\n" \
  "v_cvt_pk_bf16_f32 v88, v88, 0\n" \
  "v_cvt_pk_bf16_f32 v89, 0, v89\n" \
  "v_add_f32 v84, 1.0, v84\n" \
  "v_add_f32 v85, 1.0, v85\n" \
  "v_cvt_pk_bf16_f32 v84, 0, v84\n" \
  "v_cvt_pk_bf16_f32 v85, 0, v85\n" \
  "v_lshlrev_b32 v8, 16, v90\n" \
  "v_and_b32 v9, 0xffff0000, v90\n" \
  "v_mul_f32 v8, v8, v84\n" \
  "v_mul_f32 v9, v9, v85\n" \
  "v_cvt_pk_bf16_f32 v8, 0, v8\n" \
  "v_cvt_pk_bf16_f32 v9, 0, v9\n" \
  "v_add_f32 v92, v92, v8\n" \
  "v_add_f32 v92, v92, v9\n" \
  "v_lshrrev_b32 v8, 16, v8\n" \
  "v_or_b32 v91, v8, v9\n" \
  "ds_write_b128 v7, v[84:87] offset:0\n" \
  "ds_write_b128 v7, v[88:91] offset:16\n" \
  "s_movk_i32 s97, 0x7fff\n" \
  "s_waitcnt vmcnt(0)\n" \
  "Mpr1%=:\n" \
  "v_bitop3_b32 v49, v49, v51, s44 bitop3:0x7e\n" \
  "v_bitop3_b32 v53, v53, v55, s44 bitop3:0x7e\n" \
  "v_bitop3_b32 v57, v57, v59, s44 bitop3:0x7e\n" \
  "v_bitop3_b32 v61, v61, v63, s44 bitop3:0x7e\n" \
  "v_bitop3_b32 v65, v65, v67, s44 bitop3:0x7e\n" \
  "v_bitop3_b32 v69, v69, v71, s44 bitop3:0x7e\n" \
  "v_bitop3_b32 v73, v73, v75, s44 bitop3:0x7e\n" \
  "v_bitop3_b32 v77, v77, v79, s44 bitop3:0x7e\n" \
  "v_bitop3_b32 v81, v81, v83, s44 bitop3:0x7e\n" \
  "v_or3_b32 v49, v49, v53, v57\n" \
  "v_or3_b32 v61, v61, v65, v69\n" \
  "v_or3_b32 v73, v73, v77, v81\n" \
  "v_or3_b32 v49, v49, v61, v73\n" \
  "v_cmp_ne_u32 vcc, 0, v49\n" \
  "s_cbranch_vccnz Mps1%=\n" \
  "Mpk1%=:\n" \
  "v_add_f32 v86, v48, v52\n" \
  "v_add_f32 v87, v50, v54\n" \
  "v_add_f32 v84, v60, v64\n" \
  "v_add_f32 v85, v62, v66\n" \
  "v_add_f32 v88, v72, v76\n" \
  "v_add_f32 v89, v74, v78\n" \
  "v_add_f32 v86, v86, v56\n" \
  "v_add_f32 v87, v87, v58\n" \
  "v_add_f32 v84, v84, v68\n" \
  "v_add_f32 v85, v85, v70\n" \
  "v_add_f32 v88, v88, v80\n" \
  "v_add_f32 v89, v89, v82\n" \
  "v_mov_b32 v90, v93\n" \
  "v_cvt_pk_bf16_f32 v84, 0, v84\n" \
  "v_cvt_pk_bf16_f32 v85, 0, v85\n" \
  "v_cvt_pk_bf16_f32 v86, 0, v86\n" \
  "v_cvt_pk_bf16_f32 v87, 0, v87\n" \
  "v_cvt_pk_bf16_f32 v88, v88, 0\n" \
  "v_cvt_pk_bf16_f32 v89, 0, v89\n" \
  "v_add_f32 v84, 1.0, v84\n" \
  "v_add_f32 v85, 1.0, v85\n" \
  "v_cvt_pk_bf16_f32 v84, 0, v84\n" \
  "v_cvt_pk_bf16_f32 v85, 0, v85\n" \
  "v_lshlrev_b32 v8, 16, v90\n" \
  "v_and_b32 v9, 0xffff0000, v90\n" \
  "v_mul_f32 v8, v8, v84\n" \
  "v_mul_f32 v9, v9, v85\n" \
  "v_cvt_pk_bf16_f32 v8, 0, v8\n" \
  "v_cvt_pk_bf16_f32 v9, 0, v9\n" \
  "v_add_f32 v92, v92, v8\n" \
  "v_add_f32 v92, v92, v9\n" \
  "v_lshrrev_b32 v8, 16, v8\n" \
  "v_or_b32 v91, v8, v9\n" \
  "ds_write_b128 v7, v[84:87] offset:1536\n" \
  "ds_write_b128 v7, v[88:91] offset:1552\n" \
  "s_mov_b64 exec, -1\n" \
  "s_lshl_b32 s50, s50, 4\n" \
  "v_lshrrev_b32 v5, 2, v4\n" \
  "v_add_f32_dpp v92, v92, v92 quad_perm:[1,0,3,2] row_mask:0xf bank_mask:0xf bound_ctrl:1\n" \
  "v_and_b32 v5, 12, v5\n" \
  "v_add3_u32 v5, v5, v1, s50\n" \
  "v_add_f32_dpp v92, v92, v92 quad_perm:[2,3,0,1] row_mask:0xf bank_mask:0xf bound_ctrl:1\n" \
  "s_nop 1\n" \
  "v_add_f32_dpp v92, v92, v92 row_half_mirror row_mask:0xf bank_mask:0xf bound_ctrl:1\n" \
  "s_nop 1\n" \
  "v_add_f32_dpp v92, v92, v92 row_mirror row_mask:0xf bank_mask:0xf bound_ctrl:1\n" \
  "ds_write_b32 v5, v92 offset:32\n" \
  "s_setprio 0\n" \
  "Mlb%=:\n" \
  "v_mov_b32 v5, v95\n" \
  "v_mul_u32_u24 v7, 96, v5\n" \
  "v_add_u32 v7, 147456, v7\n" \
  "v_lshrrev_b32 v9, 5, v4\n" \
  "v_lshl_add_u32 v8, v3, 1, v9\n" \
  "v_mul_u32_u24 v10, 48, v8\n" \
  "s_lshl2_add_u32 s52, %[w], 124192\n" \
  "v_add_u32 v10, s52, v10\n" \
  "v_mul_u32_u24 v8, 208, v8\n" \
  "v_bfe_u32 v9, v4, 4, 1\n" \
  "v_lshl_add_u32 v8, v9, 2, v8\n" \
  "s_lshl4_add_u32 s52, %[w], 120864\n" \
  "v_add_u32 v8, s52, v8\n" \
  "v_mul_u32_u24 v9, 2336, v3\n" \
  "v_mul_u32_u24 v11, 12, v5\n" \
  "v_add_u32 v9, v9, v11\n" \
  "v_lshrrev_b32 v12, 3, v4\n" \
  "v_lshl_add_u32 v11, v3, 3, v12\n" \
  "v_lshlrev_b32 v11, 2, v11\n" \
  "v_mul_u32_u24 v12, 3072, v12\n" \
  "v_lshl_add_u32 v12, v3, 4, v12\n" \
  "s_mul_i32 s52, %[b], 24576\n" \
  "s_lshl_b32 s53, %[w], 7\n" \
  "s_add_u32 s52, s52, s53\n" \
  "s_add_u32 s52, s52, 3309568\n" \
  "v_lshl_add_u32 v13, v3, 4, v1\n" \
  "s_mov_b32 s54, 0x3f80\n" \
  "s_mov_b32 s55, 0x3f800000\n" \
  "v_mov_b64 v[48:49], 0\n" \
  "v_mov_b64 v[50:51], 0\n" \
  "v_mov_b64 v[52:53], 0\n" \
  "v_mov_b64 v[54:55], 0\n" \
  "s_mov_b32 s56, 0x05040100\n" \
  "s_mov_b32 s57, 0x07060302\n" \
  LSTAMP8 \
  "s_waitcnt lgkmcnt(0)\n" \
  "s_barrier\n" \
  LSTAMP9 \
  "buffer_load_dwordx4 v[96:99], v94, s[40:43], s58 offen sc1\n" \
  "buffer_load_dwordx4 v[100:103], v94, s[40:43], s59 offen sc1\n" \
  "ds_read_b128 v[24:27], v7 offset:0\n" \
  "ds_read_b128 v[28:31], v7 offset:16\n" \
  "ds_read_b128 v[32:35], v7 offset:32\n" \
  "ds_read_b128 v[36:39], v7 offset:48\n" \
  "ds_read_b128 v[40:43], v7 offset:64\n" \
  "ds_read_b128 v[44:47], v7 offset:80\n" \
  "s_waitcnt vmcnt(0)\n" \
  "v_bitop3_b32 v14, v99, v103, s44 bitop3:0x7e\n" \
  "v_cmp_ne_u32 vcc, 0, v14\n" \
  "s_cbranch_vccnz Mxs%=\n" \
  "Mxb%=:\n" \
  LSTAMP10 \
  "v_perm_b32 v99, v102, v98, s56\n" \
  "v_perm_b32 v103, v102, v98, s57\n" \
  "s_waitcnt lgkmcnt(0)\n" \
  "v_dot2c_f32_bf16 v48, 0x3f803f80, v96\n" \
  "v_dot2c_f32_bf16 v50, v96, v96\n" \
  "v_dot2c_f32_bf16 v52, v96, v31\n" \
  "v_dot2c_f32_bf16 v49, 0x3f803f80, v97\n" \
  "v_dot2c_f32_bf16 v51, v97, v97\n" \
  "v_dot2c_f32_bf16 v53, v97, v31\n" \
  "v_dot2c_f32_bf16 v48, 0x3f803f80, v100\n" \
  "v_dot2c_f32_bf16 v50, v100, v100\n" \
  "v_dot2c_f32_bf16 v52, v100, v47\n" \
  "v_dot2c_f32_bf16 v49, 0x3f803f80, v101\n" \
  "v_dot2c_f32_bf16 v51, v101, v101\n" \
  "v_dot2c_f32_bf16 v53, v101, v47\n" \
  "v_dot2c_f32_bf16 v48, 0x3f803f80, v99\n" \
  "v_dot2c_f32_bf16 v50, v99, v99\n" \
  "v_dot2c_f32_bf16 v52, v99, v39\n" \
  "v_dot2c_f32_bf16 v49, 0x3f803f80, v103\n" \
  "v_dot2c_f32_bf16 v51, v103, v103\n" \
  "v_dot2c_f32_bf16 v53, v103, v39\n" \
  "s_nop 0\n" \
  "v_permlane32_swap_b32 v48, v49\n" \
  "v_permlane32_swap_b32 v50, v51\n" \
  "v_permlane32_swap_b32 v52, v53\n" \
  "v_add_f32 v48, v48, v49\n" \
  "v_add_f32 v50, v50, v51\n" \
  "v_add_f32 v52, v52, v53\n" \
  "s_nop 0\n" \
  "v_permlane16_swap_b32 v48, v50\n" \
  "v_add_f32_dpp v52, v52, v52 row_ror:8 row_mask:0xf bank_mask:0xf bound_ctrl:1\n" \
  "v_add_f32 v48, v48, v50\n" \
  "s_nop 1\n" \
  "v_add_f32_dpp v48, v48, v48 row_ror:8 row_mask:0xf bank_mask:0xf bound_ctrl:1\n" \
  "ds_write2_b32 v8, v48, v52 offset1:2\n" \
  "s_waitcnt lgkmcnt(0)\n" \
  "s_barrier\n" \
  "s_barrier\n" \
  "ds_read_b128 v[56:59], v13 offset:128\n" \
  "s_waitcnt lgkmcnt(0)\n" \
  "v_dot2_f32_bf16 v64, v96, s54, v56\n" \
  "v_dot2_f32_bf16 v65, v96, s55, v56\n" \
  "v_dot2_f32_bf16 v66, v99, s54, v56\n" \
  "v_dot2_f32_bf16 v67, v99, s55, v56\n" \
  "v_dot2_f32_bf16 v68, v100, s54, v56\n" \
  "v_dot2_f32_bf16 v69, v100, s55, v56\n" \
  "v_dot2_f32_bf16 v70, v97, s54, v58\n" \
  "v_dot2_f32_bf16 v71, v97, s55, v58\n" \
  "v_dot2_f32_bf16 v72, v103, s54, v58\n" \
  "v_dot2_f32_bf16 v73, v103, s55, v58\n" \
  "v_dot2_f32_bf16 v74, v101, s54, v58\n" \
  "v_dot2_f32_bf16 v75, v101, s55, v58\n" \
  "v_cvt_pk_bf16_f32 v64, 0, v64\n" \
  "v_cvt_pk_bf16_f32 v65, 0, v65\n" \
  "v_cvt_pk_bf16_f32 v66, 0, v66\n" \
  "v_cvt_pk_bf16_f32 v67, 0, v67\n" \
  "v_cvt_pk_bf16_f32 v68, 0, v68\n" \
  "v_cvt_pk_bf16_f32 v69, 0, v69\n" \
  "v_cvt_pk_bf16_f32 v70, 0, v70\n" \
  "v_cvt_pk_bf16_f32 v71, 0, v71\n" \
  "v_cvt_pk_bf16_f32 v72, 0, v72\n" \
  "v_cvt_pk_bf16_f32 v73, 0, v73\n" \
  "v_cvt_pk_bf16_f32 v74, 0, v74\n" \
  "v_cvt_pk_bf16_f32 v75, 0, v75\n" \
  "v_pk_mul_f32 v[64:65], v[64:65], v[56:57] op_sel:[0,1]\n" \
  "v_pk_mul_f32 v[66:67], v[66:67], v[56:57] op_sel:[0,1]\n" \
  "v_pk_mul_f32 v[68:69], v[68:69], v[56:57] op_sel:[0,1]\n" \
  "v_pk_mul_f32 v[70:71], v[70:71], v[58:59] op_sel:[0,1]\n" \
  "v_pk_mul_f32 v[72:73], v[72:73], v[58:59] op_sel:[0,1]\n" \
  "v_pk_mul_f32 v[74:75], v[74:75], v[58:59] op_sel:[0,1]\n" \
  "v_cvt_pk_bf16_f32 v64, 0, v64\n" \
  "v_cvt_pk_bf16_f32 v65, 0, v65\n" \
  "v_cvt_pk_bf16_f32 v66, 0, v66\n" \
  "v_cvt_pk_bf16_f32 v67, 0, v67\n" \
  "v_cvt_pk_bf16_f32 v68, 0, v68\n" \
  "v_cvt_pk_bf16_f32 v69, 0, v69\n" \
  "v_cvt_pk_bf16_f32 v70, 0, v70\n" \
  "v_cvt_pk_bf16_f32 v71, 0, v71\n" \
  "v_cvt_pk_bf16_f32 v72, 0, v72\n" \
  "v_cvt_pk_bf16_f32 v73, 0, v73\n" \
  "v_cvt_pk_bf16_f32 v74, 0, v74\n" \
  "v_cvt_pk_bf16_f32 v75, 0, v75\n" \
  "v_pk_mul_f32 v[64:65], v[64:65], v[24:25]\n" \
  "v_pk_mul_f32 v[66:67], v[66:67], v[32:33]\n" \
  "v_pk_mul_f32 v[68:69], v[68:69], v[40:41]\n" \
  "v_pk_mul_f32 v[70:71], v[70:71], v[24:25]\n" \
  "v_pk_mul_f32 v[72:73], v[72:73], v[32:33]\n" \
  "v_pk_mul_f32 v[74:75], v[74:75], v[40:41]\n" \
  "v_cvt_pk_bf16_f32 v64, 0, v64\n" \
  "v_cvt_pk_bf16_f32 v65, 0, v65\n" \
  "v_cvt_pk_bf16_f32 v66, 0, v66\n" \
  "v_cvt_pk_bf16_f32 v67, 0, v67\n" \
  "v_cvt_pk_bf16_f32 v68, 0, v68\n" \
  "v_cvt_pk_bf16_f32 v69, 0, v69\n" \
  "v_cvt_pk_bf16_f32 v70, 0, v70\n" \
  "v_cvt_pk_bf16_f32 v71, 0, v71\n" \
  "v_cvt_pk_bf16_f32 v72, 0, v72\n" \
  "v_cvt_pk_bf16_f32 v73, 0, v73\n" \
  "v_cvt_pk_bf16_f32 v74, 0, v74\n" \
  "v_cvt_pk_bf16_f32 v75, 0, v75\n" \
  "v_pk_add_f32 v[64:65], v[64:65], v[26:27]\n" \
  "v_pk_add_f32 v[66:67], v[66:67], v[34:35]\n" \
  "v_pk_add_f32 v[68:69], v[68:69], v[42:43]\n" \
  "v_pk_add_f32 v[70:71], v[70:71], v[26:27]\n" \
  "v_pk_add_f32 v[72:73], v[72:73], v[34:35]\n" \
  "v_pk_add_f32 v[74:75], v[74:75], v[42:43]\n" \
  "v_cvt_pk_bf16_f32 v76, v64, v65\n" \
  "v_cvt_pk_bf16_f32 v77, v66, v67\n" \
  "v_cvt_pk_bf16_f32 v78, v68, v69\n" \
  "v_cvt_pk_bf16_f32 v79, v70, v71\n" \
  "v_cvt_pk_bf16_f32 v80, v72, v73\n" \
  "v_cvt_pk_bf16_f32 v81, v74, v75\n" \
  "ds_bpermute_b32 v84, v11, v76\n" \
  "ds_bpermute_b32 v85, v11, v77\n" \
  "ds_bpermute_b32 v86, v11, v78\n" \
  "ds_bpermute_b32 v88, v11, v79\n" \
  "ds_bpermute_b32 v89, v11, v80\n" \
  "ds_bpermute_b32 v90, v11, v81\n" \
  "v_dot2c_f32_bf16 v54, v76, v30\n" \
  "v_dot2c_f32_bf16 v55, v79, v30\n" \
  "v_dot2c_f32_bf16 v54, v77, v38\n" \
  "v_dot2c_f32_bf16 v55, v80, v38\n" \
  "v_dot2c_f32_bf16 v54, v78, v46\n" \
  "v_dot2c_f32_bf16 v55, v81, v46\n" \
  "v_mov_b32 v87, s44\n" \
  "v_mov_b32 v91, s44\n" \
  "s_nop 0\n" \
  "v_permlane32_swap_b32 v54, v55\n" \
  "v_add_f32 v54, v54, v55\n" \
  "v_mov_b32 v55, v54\n" \
  "s_waitcnt lgkmcnt(0)\n" \
  "buffer_store_dwordx4 v[84:87], v12, s[40:43], s52 offen sc1\n" \
  "v_permlane16_swap_b32 v54, v55\n" \
  "buffer_store_dwordx4 v[88:91], v12, s[40:43], s52 offen offset:1536 sc1\n" \
  "v_add_f32 v54, v54, v55\n" \
  "s_nop 1\n" \
  "v_add_f32_dpp v54, v54, v54 row_ror:8 row_mask:0xf bank_mask:0xf bound_ctrl:1\n" \
  "ds_write_b32 v10, v54\n" \
  LSTAMP11 \
  "s_waitcnt lgkmcnt(0)\n" \
  "s_barrier\n" \
  "v_dot2_f32_bf16 v64, v76, v28, 0\n" \
  "v_dot2_f32_bf16 v65, v76, v29, 0\n" \
  "v_dot2_f32_bf16 v66, v77, v36, 0\n" \
  "v_dot2_f32_bf16 v67, v77, v37, 0\n" \
  "v_dot2_f32_bf16 v68, v78, v44, 0\n" \
  "v_dot2_f32_bf16 v69, v78, v45, 0\n" \
  "v_dot2_f32_bf16 v70, v79, v28, 0\n" \
  "v_dot2_f32_bf16 v71, v79, v29, 0\n" \
  "v_dot2_f32_bf16 v72, v80, v36, 0\n" \
  "v_dot2_f32_bf16 v73, v80, v37, 0\n" \
  "v_dot2_f32_bf16 v74, v81, v44, 0\n" \
  "v_dot2_f32_bf16 v75, v81, v45, 0\n" \
  "v_cvt_pk_bf16_f32 v64, 0, v64\n" \
  "v_cvt_pk_bf16_f32 v65, 0, v65\n" \
  "v_cvt_pk_bf16_f32 v66, 0, v66\n" \
  "v_cvt_pk_bf16_f32 v67, 0, v67\n" \
  "v_cvt_pk_bf16_f32 v68, 0, v68\n" \
  "v_cvt_pk_bf16_f32 v69, 0, v69\n" \
  "v_cvt_pk_bf16_f32 v70, 0, v70\n" \
  "v_cvt_pk_bf16_f32 v71, 0, v71\n" \
  "v_cvt_pk_bf16_f32 v72, 0, v72\n" \
  "v_cvt_pk_bf16_f32 v73, 0, v73\n" \
  "v_cvt_pk_bf16_f32 v74, 0, v74\n" \
  "v_cvt_pk_bf16_f32 v75, 0, v75\n" \
  "v_dot2_f32_bf16 v64, v96, s54, v64\n" \
  "v_dot2_f32_bf16 v65, v96, s55, v65\n" \
  "v_dot2_f32_bf16 v66, v99, s54, v66\n" \
  "v_dot2_f32_bf16 v67, v99, s55, v67\n" \
  "v_dot2_f32_bf16 v68, v100, s54, v68\n" \
  "v_dot2_f32_bf16 v69, v100, s55, v69\n" \
  "v_dot2_f32_bf16 v70, v97, s54, v70\n" \
  "v_dot2_f32_bf16 v71, v97, s55, v71\n" \
  "v_dot2_f32_bf16 v72, v103, s54, v72\n" \
  "v_dot2_f32_bf16 v73, v103, s55, v73\n" \
  "v_dot2_f32_bf16 v74, v101, s54, v74\n" \
  "v_dot2_f32_bf16 v75, v101, s55, v75\n" \
  "v_cvt_pk_bf16_f32 v76, v64, v65\n" \
  "v_cvt_pk_bf16_f32 v77, v66, v67\n" \
  "v_cvt_pk_bf16_f32 v78, v68, v69\n" \
  "v_cvt_pk_bf16_f32 v79, v70, v71\n" \
  "v_cvt_pk_bf16_f32 v80, v72, v73\n" \
  "v_cvt_pk_bf16_f32 v81, v74, v75\n" \
  "ds_write2_b32 v9, v76, v77 offset1:1\n" \
  "ds_write_b32 v9, v78 offset:8\n" \
  "ds_write_b32 v9, v79 offset:1168\n" \
  "ds_write_b32 v9, v80 offset:1172\n" \
  "ds_write_b32 v9, v81 offset:1176\n" \
  "v_mul_u32_u24 v5, 911, %[t]\n" \
  "v_lshrrev_b32 v5, 16, v5\n" \
  "v_add_lshl_u32 v5, v5, %[t], 4\n" \
  "v_lshlrev_b32 v6, 4, %[t]\n" \
  "s_mul_i32 s50, %[b], 18432\n" \
  "s_add_u32 s48, s48, s50\n" \
  "s_addc_u32 s49, s49, 0\n" \
  "v_add_u32 v7, 9216, v6\n" \
  "s_waitcnt lgkmcnt(0)\n" \
  "s_barrier\n" \
  "s_cmp_ge_u32 %[w], 9\n" \
  "s_cbranch_scc1 Mlz%=\n" \
  ".if " STR(OSLP) "\n" \
  "s_sleep " STR(OSLP) "\n" \
  ".endif\n" \
  "ds_read_b128 v[16:19], v5\n" \
  "ds_read_b128 v[20:23], v5 offset:9344\n" \
  "s_waitcnt lgkmcnt(1)\n" \
  "global_store_dwordx4 v6, v[16:19], s[48:49]" OFLAG "\n" \
  "s_waitcnt lgkmcnt(0)\n" \
  "global_store_dwordx4 v7, v[20:23], s[48:49]" OFLAG "\n" \
  "s_branch Mlz%=\n" \
  "Mxs%=:\n" \
  "s_movk_i32 s97, 0x7fff\n" \
  "Mxr%=:\n" \
  LSTAMPC \
  "s_mov_b64 exec, vcc\n" \
  "buffer_load_dwordx4 v[96:99], v94, s[40:43], s58 offen sc1\n" \
  "buffer_load_dwordx4 v[100:103], v94, s[40:43], s59 offen sc1\n" \
  "s_mov_b64 exec, -1\n" \
  "s_waitcnt vmcnt(0)\n" \
  "v_bitop3_b32 v14, v99, v103, s44 bitop3:0x7e\n" \
  "v_cmp_ne_u32 vcc, 0, v14\n" \
  "s_cbranch_vccz Mxb%=\n" \
  "s_sub_u32 s97, s97, 1\n" \
  "s_cbranch_scc0 Mxr%=\n" \
  "s_branch Mxb%=\n" \
  "Mps0%=:\n" \
  LSTAMPD \
  "s_sub_u32 s97, s97, 1\n" \
  "s_cbranch_scc1 Mpk0%=\n" \
  "s_mov_b32 s51, 17920\n" \
  "buffer_load_dwordx4 v[12:15], v6, s[40:43], s51 offen sc1\n" \
  "s_mov_b32 s51, 45568\n" \
  "buffer_load_dwordx4 v[16:19], v6, s[40:43], s51 offen sc1\n" \
  "s_mov_b32 s51, 73216\n" \
  "buffer_load_dwordx4 v[20:23], v6, s[40:43], s51 offen sc1\n" \
  "s_mov_b32 s51, 22528\n" \
  "buffer_load_dwordx4 v[24:27], v6, s[40:43], s51 offen sc1\n" \
  "s_mov_b32 s51, 50176\n" \
  "buffer_load_dwordx4 v[28:31], v6, s[40:43], s51 offen sc1\n" \
  "s_mov_b32 s51, 77824\n" \
  "buffer_load_dwordx4 v[32:35], v6, s[40:43], s51 offen sc1\n" \
  "s_mov_b32 s51, 27136\n" \
  "buffer_load_dwordx4 v[36:39], v6, s[40:43], s51 offen sc1\n" \
  "s_mov_b32 s51, 54784\n" \
  "buffer_load_dwordx4 v[40:43], v6, s[40:43], s51 offen sc1\n" \
  "s_mov_b32 s51, 82432\n" \
  "buffer_load_dwordx4 v[44:47], v6, s[40:43], s51 offen sc1\n" \
  "s_waitcnt vmcnt(0)\n" \
  "s_branch Mpr0%=\n" \
  "Mps1%=:\n" \
  LSTAMPD \
  "s_sub_u32 s97, s97, 1\n" \
  "s_cbranch_scc1 Mpk1%=\n" \
  "s_mov_b32 s51, 17920\n" \
  "buffer_load_dwordx4 v[48:51], v6, s[40:43], s51 offen offset:768 sc1\n" \
  "s_mov_b32 s51, 45568\n" \
  "buffer_load_dwordx4 v[52:55], v6, s[40:43], s51 offen offset:768 sc1\n" \
  "s_mov_b32 s51, 73216\n" \
  "buffer_load_dwordx4 v[56:59], v6, s[40:43], s51 offen offset:768 sc1\n" \
  "s_mov_b32 s51, 22528\n" \
  "buffer_load_dwordx4 v[60:63], v6, s[40:43], s51 offen offset:768 sc1\n" \
  "s_mov_b32 s51, 50176\n" \
  "buffer_load_dwordx4 v[64:67], v6, s[40:43], s51 offen offset:768 sc1\n" \
  "s_mov_b32 s51, 77824\n" \
  "buffer_load_dwordx4 v[68:71], v6, s[40:43], s51 offen offset:768 sc1\n" \
  "s_mov_b32 s51, 27136\n" \
  "buffer_load_dwordx4 v[72:75], v6, s[40:43], s51 offen offset:768 sc1\n" \
  "s_mov_b32 s51, 54784\n" \
  "buffer_load_dwordx4 v[76:79], v6, s[40:43], s51 offen offset:768 sc1\n" \
  "s_mov_b32 s51, 82432\n" \
  "buffer_load_dwordx4 v[80:83], v6, s[40:43], s51 offen offset:768 sc1\n" \
  "s_waitcnt vmcnt(0)\n" \
  "s_branch Mpr1%=\n" \
  "Mls%=:\n" \
  "s_lshr_b32 s50, %[b], 4\n" \
  "s_cmp_lt_u32 s50, " STR(X1T) "\n" \
  "s_cbranch_scc1 Mld%=\n" \
  "s_sleep " STR(X1SLP) "\n" \
  "Mld%=:\n" \
  "s_cmp_eq_u32 %[w], 15\n" \
  "s_cbranch_scc1 Mlk%=\n" \
  "s_barrier\n" \
  "s_barrier\n" \
  "s_barrier\n" \
  "s_barrier\n" \
  "s_barrier\n" \
  "s_branch Mlz%=\n" \
  "Mlk%=:\n" \
  "s_barrier\n" \
  "s_setprio 3\n" \
  "ds_read_b128 v[16:19], v1 offset:32\n" \
  "ds_read_b128 v[20:23], v1 offset:48\n" \
  "ds_read_b128 v[24:27], v1 offset:64\n" \
  "v_lshrrev_b32 v5, 2, v4\n" \
  "v_mul_u32_u24 v6, 208, v5\n" \
  "v_and_b32 v7, 3, v4\n" \
  "v_mul_u32_u24 v8, 48, v7\n" \
  "s_mov_b32 s50, 120864\n" \
  "v_add3_u32 v6, v6, v8, s50\n" \
  "v_lshl_add_u32 v9, v5, 3, v1\n" \
  "s_lshl_b32 s50, %[b], 4\n" \
  "v_add_u32 v10, s50, v5\n" \
  "v_lshlrev_b32 v11, 3, v10\n" \
  "v_sub_u32 v10, 2047, v10\n" \
  "v_mov_b32 v13, s44\n" \
  "v_mul_u32_u24 v14, 48, v5\n" \
  "v_mul_u32_u24 v7, 12, v7\n" \
  "s_mov_b32 s50, 124192\n" \
  "v_add3_u32 v14, v14, v7, s50\n" \
  "s_mov_b32 s50, 10403840\n" \
  "s_mov_b32 s51, 103424\n" \
  "s_mov_b32 s55, 10469888\n" \
  "v_and_b32 v28, 15, v4\n" \
  "v_lshrrev_b32 v7, 4, v4\n" \
  "v_mul_u32_u24 v7, 16512, v7\n" \
  "v_lshl_add_u32 v7, v28, 3, v7\n" \
  "s_lshl_b32 s56, %[b], 7\n" \
  "v_add_u32 v7, s56, v7\n" \
  "v_lshlrev_b32 v28, 4, v28\n" \
  "s_waitcnt lgkmcnt(0)\n" \
  "v_pk_add_f32 v[16:17], v[16:17], v[18:19]\n" \
  "v_pk_add_f32 v[20:21], v[20:21], v[22:23]\n" \
  "v_pk_add_f32 v[24:25], v[24:25], v[26:27]\n" \
  "v_pk_add_f32 v[16:17], v[16:17], v[20:21]\n" \
  "v_pk_add_f32 v[16:17], v[16:17], v[24:25]\n" \
  "v_add_f32 v15, v16, v17\n" \
  "s_barrier\n" \
  "ds_read_b128 v[16:19], v6\n" \
  "ds_read_b128 v[20:23], v6 offset:16\n" \
  "ds_read_b128 v[24:27], v6 offset:32\n" \
  "s_waitcnt lgkmcnt(1)\n" \
  "v_pk_add_f32 v[16:17], v[16:17], v[20:21]\n" \
  "v_pk_add_f32 v[18:19], v[18:19], v[22:23]\n" \
  "s_waitcnt lgkmcnt(0)\n" \
  "v_pk_add_f32 v[16:17], v[16:17], v[24:25]\n" \
  "v_pk_add_f32 v[18:19], v[18:19], v[26:27]\n" \
  "v_add_f32 v18, v18, v19\n" \
  "v_add_f32_dpp v16, v16, v16 quad_perm:[1,0,3,2] row_mask:0xf bank_mask:0xf bound_ctrl:1\n" \
  "v_add_f32_dpp v17, v17, v17 quad_perm:[1,0,3,2] row_mask:0xf bank_mask:0xf bound_ctrl:1\n" \
  "v_add_f32_dpp v18, v18, v18 quad_perm:[1,0,3,2] row_mask:0xf bank_mask:0xf bound_ctrl:1\n" \
  "v_add_f32_dpp v16, v16, v16 quad_perm:[2,3,0,1] row_mask:0xf bank_mask:0xf bound_ctrl:1\n" \
  "v_add_f32_dpp v17, v17, v17 quad_perm:[2,3,0,1] row_mask:0xf bank_mask:0xf bound_ctrl:1\n" \
  "v_add_f32_dpp v18, v18, v18 quad_perm:[2,3,0,1] row_mask:0xf bank_mask:0xf bound_ctrl:1\n" \
  "v_mul_f32 v16, 0xbae38e39, v16\n" \
  "v_mul_f32 v17, 0x3ae38e39, v17\n" \
  "v_fma_f32 v17, -v16, v16, v17\n" \
  "v_max_f32 v17, 0, v17\n" \
  "v_cvt_pk_bf16_f32 v17, 0, v17\n" \
  "v_add_f32 v17, 0x3727c5ac, v17\n" \
  "v_cvt_pk_bf16_f32 v17, 0, v17\n" \
  "v_sqrt_f32 v17, v17\n" \
  "v_cvt_pk_bf16_f32 v16, 0, v16\n" \
  "v_cvt_pk_bf16_f32 v17, 0, v17\n" \
  "v_rcp_f32 v17, v17\n" \
  "v_fma_f32 v12, v16, v15, v18\n" \
  "v_mul_f32 v12, v12, v17\n" \
  "ds_write_b64 v9, v[16:17] offset:128\n" \
  "v_and_b32 v18, 0x7ff, v12\n" \
  "v_add_u32 v18, 0x7ff, v18\n" \
  "v_or_b32 v12, v12, v18\n" \
  "v_ashrrev_i32 v18, 31, v12\n" \
  "v_or_b32 v18, 0x80000000, v18\n" \
  "v_xor_b32 v12, v12, v18\n" \
  "v_and_b32 v12, 0xfffff800, v12\n" \
  "v_or_b32 v12, v12, v10\n" \
  "ds_bpermute_b32 v12, v28, v12\n" \
  "s_waitcnt lgkmcnt(0)\n" \
  "buffer_store_dwordx2 v[12:13], v7, s[40:43], s50 offen sc1\n" \
  "buffer_store_dwordx2 v[12:13], v7, s[40:43], s55 offen sc1\n" \
  LSTAMP13 \
  "s_waitcnt lgkmcnt(0)\n" \
  "s_barrier\n" \
  "s_barrier\n" \
  "ds_read2_b32 v[16:17], v14 offset1:1\n" \
  "ds_read_b32 v18, v14 offset:8\n" \
  "s_waitcnt lgkmcnt(0)\n" \
  "v_add_f32 v12, v16, v17\n" \
  "v_add_f32 v12, v12, v18\n" \
  "s_nop 1\n" \
  "v_add_f32_dpp v12, v12, v12 quad_perm:[1,0,3,2] row_mask:0xf bank_mask:0xf bound_ctrl:1\n" \
  "s_nop 1\n" \
  "v_add_f32_dpp v12, v12, v12 quad_perm:[2,3,0,1] row_mask:0xf bank_mask:0xf bound_ctrl:1\n" \
  "v_and_b32 v18, 0x7ff, v12\n" \
  "v_add_u32 v18, 0x7ff, v18\n" \
  "v_or_b32 v12, v12, v18\n" \
  "v_ashrrev_i32 v18, 31, v12\n" \
  "v_or_b32 v18, 0x80000000, v18\n" \
  "v_xor_b32 v12, v12, v18\n" \
  "v_and_b32 v12, 0xfffff800, v12\n" \
  "v_or_b32 v12, v12, v10\n" \
  "s_mov_b32 exec_lo, 0x11111111\n" \
  "s_mov_b32 exec_hi, 0x11111111\n" \
  "buffer_store_dwordx2 v[12:13], v11, s[40:43], s51 offen sc1\n" \
  "s_mov_b64 exec, -1\n" \
  LSTAMP14 \
  "s_barrier\n" \
  "Mlz%=:\n" \
  "s_waitcnt vmcnt(0)\n" \
  "s_barrier\n" \
  "s_cmp_lg_u32 %[w], 15\n" \
  "s_cbranch_scc1 Mly%=\n" \
  "s_lshl2_add_u32 s50, %[b], 136192\n" \
  "s_mov_b64 exec, 1\n" \
  "buffer_store_dword v13, off, s[40:43], s50 sc1\n" \
  "s_mov_b64 exec, -1\n" \
  "Mly%=:\n"

#define EW_HEAD \
  "s_load_dwordx4 s[52:55], %[k], 0x58\n" \
  "s_load_dwordx2 s[40:41], %[k], 0x70\n" \
  "s_lshr_b32 s50, %[w], 2\n" \
  "s_mov_b32 s42, 10668032\n" \
  "s_mov_b32 s43, 0x20000\n" \
  "s_cmp_eq_u32 s50, 3\n" \
  "s_cbranch_scc1 Eez%=\n" \
  "s_cmpk_ge_u32 %[b], 246\n" \
  "s_cbranch_scc1 Een%=\n" \
  "s_sub_u32 s51, %[b], 192\n" \
  "s_mul_i32 s56, s51, 43\n" \
  "s_lshr_b32 s56, s56, 7\n" \
  "s_mul_i32 s57, s56, 3\n" \
  "s_sub_u32 s57, s51, s57\n" \
  "s_mul_i32 s51, s57, 192\n" \
  "s_cmp_eq_u32 s50, 2\n" \
  "s_cbranch_scc1 Eec%=\n" \
  "v_bfe_u32 v1, %[t], 3, 5\n" \
  "v_add_u32 v2, s51, v1\n" \
  "v_mul_u32_u24 v2, 6912, v2\n" \
  "v_and_b32 v3, 2, v1\n" \
  "v_lshrrev_b32 v1, 1, v1\n" \
  "v_and_or_b32 v3, v1, 4, v3\n" \
  "v_xor_b32 v3, v3, %[t]\n" \
  "v_and_b32 v3, 7, v3\n" \
  "s_lshl_b32 s58, s56, 7\n" \
  "v_lshl_add_u32 v2, v3, 4, v2\n" \
  "v_add_u32 v2, s58, v2\n" \
  "s_cmp_lg_u32 s50, 0\n" \
  "s_cbranch_scc1 Eeb%=\n" \
  "s_lshl_b32 s51, %[w], 10\n" \
  "s_add_u32 m0, s51, 73728\n" \
  GSTAMP1 \
  "s_waitcnt lgkmcnt(0)\n" \
  "global_load_lds_dwordx4 v2, s[54:55]\n" \
  "s_add_u32 m0, m0, 4096\n" \
  "v_add_u32 v2, 221184, v2\n" \
  "global_load_lds_dwordx4 v2, s[54:55]\n" \
  "s_add_u32 m0, m0, 4096\n" \
  "v_add_u32 v2, 221184, v2\n" \
  "global_load_lds_dwordx4 v2, s[54:55]\n" \
  "s_add_u32 m0, m0, 4096\n" \
  "v_add_u32 v2, 221184, v2\n" \
  "global_load_lds_dwordx4 v2, s[54:55]\n" \
  "s_add_u32 m0, m0, 4096\n" \
  "v_add_u32 v2, 221184, v2\n" \
  "global_load_lds_dwordx4 v2, s[54:55]\n" \
  "s_add_u32 m0, m0, 4096\n" \
  "v_add_u32 v2, 221184, v2\n" \
  "global_load_lds_dwordx4 v2, s[54:55]\n" \
  "s_branch Eeb%=\n" \
  "Een%=:\n" \
  "s_mov_b32 s51, 0\n" \
  "s_cmp_eq_u32 s50, 2\n" \
  "s_cbranch_scc1 Eec%=\n" \
  "s_cmpk_lt_u32 %[b], 248\n" \
  "s_cbranch_scc1 Eeb%=\n" \
  "s_cmp_lg_u32 s50, 0\n" \
  "s_cbranch_scc1 Eeb%=\n" \
  "s_getpc_b64 s[48:49]\n" \
  "Epc%=:\n" \
  "v_lshlrev_b32 v1, 7, %[t]\n" \
  "v_min_u32 v1, Ez%=-Epc%=, v1\n" \
  "s_mov_b32 m0, 160032\n" \
  "s_nop 0\n" \
  "global_load_lds_dword v1, s[48:49]\n" \
  "s_branch Eeb%=\n" \
  "Eez%=:\n" \
  "v_and_b32 v5, 63, %[t]\n" \
  "v_mov_b32 v6, 159488\n" \
  "v_lshl_add_u32 v6, v5, 2, v6\n" \
  "v_mov_b32 v8, 0\n" \
  "ds_write_b32 v6, v8\n" \
  "s_branch Eeb%=\n" \
  "Eec%=:\n" \
  "v_and_b32 v5, 255, %[t]\n" \
  "v_min_u32 v5, 191, v5\n" \
  "v_add_lshl_u32 v6, v5, s51, 1\n" \
  GSTAMP1 \
  "s_waitcnt lgkmcnt(0)\n" \
  "global_load_short_d16_hi v7, v6, s[52:53]\n" \
  "buffer_load_dword v8, off, s[40:43], 0 sc1\n" \
  "Eeb%=:\n"

#define EW_BODY \
  "v_and_b32 v10, 15, %[t]\n" \
  "v_bfe_u32 v11, %[t], 4, 2\n" \
  "v_and_b32 v12, 63, %[t]\n" \
  "s_cmp_lg_u32 s50, 2\n" \
  "s_cbranch_scc1 Em0%=\n" \
  "s_setprio 2\n" \
  "v_bfe_u32 v1, %[t], 2, 2\n" \
  "v_lshl_add_u32 v1, v11, 3, v1\n" \
  "v_lshlrev_b32 v1, 7, v1\n" \
  "v_bfe_u32 v2, %[t], 3, 2\n" \
  "v_xor_b32 v2, %[w], v2\n" \
  "v_and_b32 v2, 3, v2\n" \
  "v_lshl_add_u32 v1, v2, 5, v1\n" \
  "v_and_b32 v2, 3, %[t]\n" \
  "v_lshl_add_u32 v1, v2, 3, v1\n" \
  "v_add_u32 v1, 73728, v1\n" \
  "v_mov_b32 v9, 160416\n" \
  "v_lshl_add_u32 v5, v5, 1, v9\n" \
  "v_lshl_add_u32 v9, v11, 4, v9\n" \
  "v_lshlrev_b32 v3, 3, v10\n" \
  "s_mul_i32 s58, s57, 27648\n" \
  "s_lshl_b32 s59, s56, 9\n" \
  "s_add_u32 s58, s58, s59\n" \
  "s_lshl_b32 s59, %[w], 7\n" \
  "s_add_u32 s58, s58, s59\n" \
  "s_add_u32 s58, s58, 3072\n" \
  "s_cmp_ge_u32 s56, 9\n" \
  "s_cselect_b32 s59, 4608, 0\n" \
  "v_mov_b32 v4, 158208\n" \
  "s_waitcnt vmcnt(0)\n" \
  GSTAMP2 \
  "v_and_b32 v7, 0xffff0000, v7\n" \
  "v_mul_f32 v6, 0xbfb8aa3b, v7\n" \
  "v_exp_f32 v6, v6\n" \
  "v_add_u32 v8, 1, v8\n" \
  "v_add_f32 v6, 1.0, v6\n" \
  "v_rcp_f32 v6, v6\n" \
  "v_readfirstlane_b32 s44, v8\n" \
  "v_mul_f32 v6, v6, v7\n" \
  "v_cvt_pk_bf16_f32 v6, 0, v6\n" \
  "ds_write_b32 v4, v8\n" \
  "s_cmpk_ge_u32 %[b], 246\n" \
  "s_cbranch_scc1 Ew0%=\n" \
  "ds_write_b16_d16_hi v5, v6\n" \
  "s_waitcnt lgkmcnt(0)\n" \
  "s_barrier\n" \
  GSTAMP3 \
  "ds_read_b64_tr_b16 v[40:41], v1 offset:0\n" \
  "ds_read_b64_tr_b16 v[42:43], v1 offset:512\n" \
  "ds_read_b64_tr_b16 v[44:45], v1 offset:4096\n" \
  "ds_read_b64_tr_b16 v[46:47], v1 offset:4608\n" \
  "ds_read_b64_tr_b16 v[48:49], v1 offset:8192\n" \
  "ds_read_b64_tr_b16 v[50:51], v1 offset:8704\n" \
  "ds_read_b64_tr_b16 v[52:53], v1 offset:12288\n" \
  "ds_read_b64_tr_b16 v[54:55], v1 offset:12800\n" \
  "ds_read_b64_tr_b16 v[56:57], v1 offset:16384\n" \
  "ds_read_b64_tr_b16 v[58:59], v1 offset:16896\n" \
  "ds_read_b64_tr_b16 v[60:61], v1 offset:20480\n" \
  "ds_read_b64_tr_b16 v[62:63], v1 offset:20992\n" \
  "ds_read_b128 v[64:67], v9 offset:0\n" \
  "ds_read_b128 v[68:71], v9 offset:64\n" \
  "ds_read_b128 v[72:75], v9 offset:128\n" \
  "ds_read_b128 v[76:79], v9 offset:192\n" \
  "ds_read_b128 v[80:83], v9 offset:256\n" \
  "ds_read_b128 v[84:87], v9 offset:320\n" \
  "s_mov_b64 s[46:47], 0xffff\n" \
  "s_waitcnt lgkmcnt(0)\n" \
  "v_mfma_f32_16x16x32_bf16 v[16:19], v[64:67], v[40:43], 0\n" \
  "v_mfma_f32_16x16x32_bf16 v[16:19], v[68:71], v[44:47], v[16:19]\n" \
  "v_mfma_f32_16x16x32_bf16 v[16:19], v[72:75], v[48:51], v[16:19]\n" \
  "v_mfma_f32_16x16x32_bf16 v[16:19], v[76:79], v[52:55], v[16:19]\n" \
  "v_mfma_f32_16x16x32_bf16 v[16:19], v[80:83], v[56:59], v[16:19]\n" \
  "v_mfma_f32_16x16x32_bf16 v[16:19], v[84:87], v[60:63], v[16:19]\n" \
  "s_nop 7\n" \
  "s_mov_b64 exec, s[46:47]\n" \
  "v_mov_b32 v17, s44\n" \
  "buffer_store_dwordx2 v[16:17], v3, s[40:43], s58 offen sc1\n" \
  "s_mov_b64 exec, -1\n" \
  GSTAMP4 \
  "s_add_u32 s58, s58, 9216\n" \
  "s_add_u32 s58, s58, s59\n" \
  "s_barrier\n" \
  "ds_read_b64_tr_b16 v[40:41], v1 offset:24576\n" \
  "ds_read_b64_tr_b16 v[42:43], v1 offset:25088\n" \
  "ds_read_b64_tr_b16 v[44:45], v1 offset:28672\n" \
  "ds_read_b64_tr_b16 v[46:47], v1 offset:29184\n" \
  "ds_read_b64_tr_b16 v[48:49], v1 offset:32768\n" \
  "ds_read_b64_tr_b16 v[50:51], v1 offset:33280\n" \
  "ds_read_b64_tr_b16 v[52:53], v1 offset:36864\n" \
  "ds_read_b64_tr_b16 v[54:55], v1 offset:37376\n" \
  "ds_read_b64_tr_b16 v[56:57], v1 offset:40960\n" \
  "ds_read_b64_tr_b16 v[58:59], v1 offset:41472\n" \
  "ds_read_b64_tr_b16 v[60:61], v1 offset:45056\n" \
  "ds_read_b64_tr_b16 v[62:63], v1 offset:45568\n" \
  "s_mov_b64 s[46:47], 0xffff\n" \
  "s_waitcnt lgkmcnt(0)\n" \
  "v_mfma_f32_16x16x32_bf16 v[16:19], v[64:67], v[40:43], 0\n" \
  "v_mfma_f32_16x16x32_bf16 v[16:19], v[68:71], v[44:47], v[16:19]\n" \
  "v_mfma_f32_16x16x32_bf16 v[16:19], v[72:75], v[48:51], v[16:19]\n" \
  "v_mfma_f32_16x16x32_bf16 v[16:19], v[76:79], v[52:55], v[16:19]\n" \
  "v_mfma_f32_16x16x32_bf16 v[16:19], v[80:83], v[56:59], v[16:19]\n" \
  "v_mfma_f32_16x16x32_bf16 v[16:19], v[84:87], v[60:63], v[16:19]\n" \
  "s_nop 7\n" \
  "s_mov_b64 exec, s[46:47]\n" \
  "v_mov_b32 v17, s44\n" \
  "buffer_store_dwordx2 v[16:17], v3, s[40:43], s58 offen sc1\n" \
  "s_mov_b64 exec, -1\n" \
  GSTAMP5 \
  "s_add_u32 s58, s58, 4608\n" \
  "v_add_u32 v1, 49152, v1\n" \
  "s_barrier\n" \
  "ds_read_b64_tr_b16 v[40:41], v1 offset:0\n" \
  "ds_read_b64_tr_b16 v[42:43], v1 offset:512\n" \
  "ds_read_b64_tr_b16 v[44:45], v1 offset:4096\n" \
  "ds_read_b64_tr_b16 v[46:47], v1 offset:4608\n" \
  "ds_read_b64_tr_b16 v[48:49], v1 offset:8192\n" \
  "ds_read_b64_tr_b16 v[50:51], v1 offset:8704\n" \
  "ds_read_b64_tr_b16 v[52:53], v1 offset:12288\n" \
  "ds_read_b64_tr_b16 v[54:55], v1 offset:12800\n" \
  "ds_read_b64_tr_b16 v[56:57], v1 offset:16384\n" \
  "ds_read_b64_tr_b16 v[58:59], v1 offset:16896\n" \
  "ds_read_b64_tr_b16 v[60:61], v1 offset:20480\n" \
  "ds_read_b64_tr_b16 v[62:63], v1 offset:20992\n" \
  "s_mov_b64 s[46:47], 0xffff\n" \
  "s_waitcnt lgkmcnt(0)\n" \
  "s_barrier\n" \
  "v_mfma_f32_16x16x32_bf16 v[16:19], v[64:67], v[40:43], 0\n" \
  "v_mfma_f32_16x16x32_bf16 v[16:19], v[68:71], v[44:47], v[16:19]\n" \
  "v_mfma_f32_16x16x32_bf16 v[16:19], v[72:75], v[48:51], v[16:19]\n" \
  "v_mfma_f32_16x16x32_bf16 v[16:19], v[76:79], v[52:55], v[16:19]\n" \
  "v_mfma_f32_16x16x32_bf16 v[16:19], v[80:83], v[56:59], v[16:19]\n" \
  "v_mfma_f32_16x16x32_bf16 v[16:19], v[84:87], v[60:63], v[16:19]\n" \
  "s_nop 7\n" \
  "s_mov_b64 exec, s[46:47]\n" \
  "v_mov_b32 v17, s44\n" \
  "buffer_store_dwordx2 v[16:17], v3, s[40:43], s58 offen sc1\n" \
  "s_mov_b64 exec, -1\n" \
  GSTAMP6 \
  "s_load_dwordx4 s[52:55], %[k], 0x40\n" \
  "s_load_dwordx2 s[48:49], %[k], 0x50\n" \
  "s_load_dwordx2 s[84:85], %[k], 0x68\n" \
  "s_setprio 0\n" \
  "s_waitcnt lgkmcnt(0)\n" \
  "s_branch Ew2%=\n" \
  "Em0%=:\n" \
  "s_cmpk_ge_u32 %[b], 246\n" \
  "s_cbranch_scc1 Em2%=\n" \
  "s_cmp_eq_u32 s50, 3\n" \
  "s_cbranch_scc1 Em1%=\n" \
  "s_cmp_ge_u32 s56, 9\n" \
  "s_cselect_b32 s59, 1152, 0\n" \
  "s_cmp_eq_u32 s50, 0\n" \
  "s_cselect_b32 s58, -1102464, 0\n" \
  "s_cselect_b32 s51, 0, 2304\n" \
  "s_add_u32 s58, s58, s51\n" \
  "s_add_u32 s58, s58, s59\n" \
  "v_add_u32 v2, s58, v2\n" \
  "s_mul_i32 s51, s50, -28672\n" \
  "s_lshl_b32 s58, %[w], 10\n" \
  "s_add_u32 s51, s51, s58\n" \
  "s_add_u32 m0, s51, 122880\n" \
  "s_waitcnt vmcnt(0) lgkmcnt(0)\n" \
  GSTAMP3 \
  "s_cmp_eq_u32 s50, 0\n" \
  "s_cbranch_scc1 Em3%=\n" \
  "s_sleep " STR(P1D) "\n" \
  "global_load_lds_dwordx4 v2, s[54:55]\n" \
  "s_add_u32 m0, m0, 4096\n" \
  "v_add_u32 v2, 221184, v2\n" \
  "global_load_lds_dwordx4 v2, s[54:55]\n" \
  "s_add_u32 m0, m0, 4096\n" \
  "v_add_u32 v2, 221184, v2\n" \
  "global_load_lds_dwordx4 v2, s[54:55]\n" \
  "s_add_u32 m0, m0, 4096\n" \
  "v_add_u32 v2, 221184, v2\n" \
  "global_load_lds_dwordx4 v2, s[54:55]\n" \
  "s_add_u32 m0, m0, 4096\n" \
  "v_add_u32 v2, 221184, v2\n" \
  "global_load_lds_dwordx4 v2, s[54:55]\n" \
  "s_add_u32 m0, m0, 4096\n" \
  "v_add_u32 v2, 221184, v2\n" \
  "global_load_lds_dwordx4 v2, s[54:55]\n" \
  "s_load_dwordx4 s[52:55], %[k], 0x40\n" \
  "s_load_dwordx2 s[48:49], %[k], 0x50\n" \
  "s_load_dwordx2 s[84:85], %[k], 0x68\n" \
  "s_barrier\n" \
  "s_waitcnt lgkmcnt(0)\n" \
  "s_and_b32 s56, %[b], 7\n" \
  "s_bfe_u32 s57, %[b], 0x30003\n" \
  "s_lshr_b32 s58, s52, 7\n" \
  "s_add_u32 s58, s58, s56\n" \
  "s_sub_u32 s58, s57, s58\n" \
  "s_add_u32 s58, s58, 4\n" \
  "s_and_b32 s58, s58, 7\n" \
  "s_mul_i32 s59, s56, 9\n" \
  "s_add_u32 s58, s58, s59\n" \
  "s_lshl_b32 s58, s58, 6\n" \
  "s_add_u32 s59, s59, 8\n" \
  "s_lshl_b32 s59, s59, 6\n" \
  "s_lshl3_add_u32 s59, s57, s59\n" \
  "s_lshl3_add_u32 s57, s56, s57\n" \
  "v_mov_b32 v2, s52\n" \
  "v_mov_b32 v3, s53\n" \
  "v_mov_b32 v4, s54\n" \
  "v_mov_b32 v5, s55\n" \
  "s_mov_b32 s46, 294912\n" \
  "s_mov_b32 s47, 0\n" \
  "v_bfe_u32 v7, %[t], 2, 2\n" \
  "s_mov_b32 vcc_lo, 0xff00ff00\n" \
  "s_mov_b32 vcc_hi, 0xff00ff00\n" \
  "v_cndmask_b32 v2, v2, v4, vcc\n" \
  "v_cndmask_b32 v3, v3, v5, vcc\n" \
  "v_lshrrev_b32 v4, 4, %[t]\n" \
  "v_mul_u32_u24 v4, 9216, v4\n" \
  "s_lshl_b32 s51, %[w], 1\n" \
  "s_and_b32 s51, s51, 4\n" \
  "v_or_b32 v5, s51, v11\n" \
  "v_xor_b32 v5, v5, %[t]\n" \
  "v_and_b32 v5, 7, v5\n" \
  "v_lshl_add_u32 v4, v5, 4, v4\n" \
  "s_lshl_b32 s51, s58, 1\n" \
  "v_add_u32 v4, s51, v4\n" \
  "v_mov_b32 v5, 0\n" \
  "v_lshl_add_u64 v[2:3], v[2:3], 0, v[4:5]\n" \
  "s_lshl_b32 m0, %[w], 10\n" \
  "s_waitcnt vmcnt(0)\n" \
  GSTAMP5 \
  "s_barrier\n" \
  "s_sleep " STR(GED) "\n" \
  "global_load_lds_dwordx4 v[2:3], off" WNT "\n" \
  "s_add_u32 m0, m0, 8192\n" \
  "v_lshl_add_u64 v[2:3], v[2:3], 0, s[46:47]\n" \
  "global_load_lds_dwordx4 v[2:3], off" WNT "\n" \
  "s_add_u32 m0, m0, 8192\n" \
  "v_lshl_add_u64 v[2:3], v[2:3], 0, s[46:47]\n" \
  "global_load_lds_dwordx4 v[2:3], off" WNT "\n" \
  "s_add_u32 m0, m0, 8192\n" \
  "v_lshl_add_u64 v[2:3], v[2:3], 0, s[46:47]\n" \
  "global_load_lds_dwordx4 v[2:3], off" WNT "\n" \
  "s_add_u32 m0, m0, 8192\n" \
  "v_lshl_add_u64 v[2:3], v[2:3], 0, s[46:47]\n" \
  "global_load_lds_dwordx4 v[2:3], off" WNT "\n" \
  "s_add_u32 m0, m0, 8192\n" \
  "v_lshl_add_u64 v[2:3], v[2:3], 0, s[46:47]\n" \
  "global_load_lds_dwordx4 v[2:3], off" WNT "\n" \
  "s_add_u32 m0, m0, 8192\n" \
  "v_lshl_add_u64 v[2:3], v[2:3], 0, s[46:47]\n" \
  "s_barrier\n" \
  "s_barrier\n" \
  "v_mov_b32 v8, 158208\n" \
  "ds_read_b32 v8, v8\n" \
  "s_waitcnt lgkmcnt(0)\n" \
  "v_readfirstlane_b32 s44, v8\n" \
  "s_branch Ew1%=\n" \
  "Em3%=:\n" \
  "s_barrier\n" \
  "s_sleep " STR(P2D) "\n" \
  "global_load_lds_dwordx4 v2, s[54:55]\n" \
  "s_add_u32 m0, m0, 4096\n" \
  "v_add_u32 v2, 221184, v2\n" \
  "global_load_lds_dwordx4 v2, s[54:55]\n" \
  "s_add_u32 m0, m0, 4096\n" \
  "v_add_u32 v2, 221184, v2\n" \
  "global_load_lds_dwordx4 v2, s[54:55]\n" \
  "s_add_u32 m0, m0, 4096\n" \
  "v_add_u32 v2, 221184, v2\n" \
  "global_load_lds_dwordx4 v2, s[54:55]\n" \
  "s_add_u32 m0, m0, 4096\n" \
  "v_add_u32 v2, 221184, v2\n" \
  "global_load_lds_dwordx4 v2, s[54:55]\n" \
  "s_add_u32 m0, m0, 4096\n" \
  "v_add_u32 v2, 221184, v2\n" \
  "s_barrier\n" \
  "s_sleep " STR(P2E) "\n" \
  "global_load_lds_dwordx4 v2, s[54:55]\n" \
  "s_load_dwordx4 s[52:55], %[k], 0x40\n" \
  "s_load_dwordx2 s[48:49], %[k], 0x50\n" \
  "s_load_dwordx2 s[84:85], %[k], 0x68\n" \
  "s_waitcnt lgkmcnt(0)\n" \
  "s_and_b32 s56, %[b], 7\n" \
  "s_bfe_u32 s57, %[b], 0x30003\n" \
  "s_lshr_b32 s58, s52, 7\n" \
  "s_add_u32 s58, s58, s56\n" \
  "s_sub_u32 s58, s57, s58\n" \
  "s_add_u32 s58, s58, 4\n" \
  "s_and_b32 s58, s58, 7\n" \
  "s_mul_i32 s59, s56, 9\n" \
  "s_add_u32 s58, s58, s59\n" \
  "s_lshl_b32 s58, s58, 6\n" \
  "s_add_u32 s59, s59, 8\n" \
  "s_lshl_b32 s59, s59, 6\n" \
  "s_lshl3_add_u32 s59, s57, s59\n" \
  "s_lshl3_add_u32 s57, s56, s57\n" \
  "v_mov_b32 v2, s52\n" \
  "v_mov_b32 v3, s53\n" \
  "v_mov_b32 v4, s54\n" \
  "v_mov_b32 v5, s55\n" \
  "s_mov_b32 s46, 294912\n" \
  "s_mov_b32 s47, 0\n" \
  "v_bfe_u32 v7, %[t], 2, 2\n" \
  "s_mov_b32 vcc_lo, 0xff00ff00\n" \
  "s_mov_b32 vcc_hi, 0xff00ff00\n" \
  "v_cndmask_b32 v2, v2, v4, vcc\n" \
  "v_cndmask_b32 v3, v3, v5, vcc\n" \
  "v_lshrrev_b32 v4, 4, %[t]\n" \
  "v_mul_u32_u24 v4, 9216, v4\n" \
  "s_lshl_b32 s51, %[w], 1\n" \
  "s_and_b32 s51, s51, 4\n" \
  "v_or_b32 v5, s51, v11\n" \
  "v_xor_b32 v5, v5, %[t]\n" \
  "v_and_b32 v5, 7, v5\n" \
  "v_lshl_add_u32 v4, v5, 4, v4\n" \
  "s_lshl_b32 s51, s58, 1\n" \
  "v_add_u32 v4, s51, v4\n" \
  "v_mov_b32 v5, 0\n" \
  "v_lshl_add_u64 v[2:3], v[2:3], 0, v[4:5]\n" \
  "s_lshl_b32 m0, %[w], 10\n" \
  "v_mov_b32 v8, 158208\n" \
  "ds_read_b32 v8, v8\n" \
  "s_waitcnt lgkmcnt(0)\n" \
  "v_readfirstlane_b32 s44, v8\n" \
  "s_waitcnt vmcnt(0)\n" \
  GSTAMP5 \
  "s_barrier\n" \
  "global_load_lds_dwordx4 v[2:3], off" WNT "\n" \
  "s_add_u32 m0, m0, 8192\n" \
  "v_lshl_add_u64 v[2:3], v[2:3], 0, s[46:47]\n" \
  "global_load_lds_dwordx4 v[2:3], off" WNT "\n" \
  "s_add_u32 m0, m0, 8192\n" \
  "v_lshl_add_u64 v[2:3], v[2:3], 0, s[46:47]\n" \
  "s_barrier\n" \
  "s_branch Ew3%=\n" \
  "Em1%=:\n" \
  "s_waitcnt lgkmcnt(0)\n" \
  "s_barrier\n" \
  "s_barrier\n" \
  "s_barrier\n" \
  "s_branch Ew0%=\n" \
  "Em2%=:\n" \
  "s_waitcnt lgkmcnt(0)\n" \
  "s_sleep " STR(WDLY) "\n" \
  "s_cmpk_lt_u32 %[b], 248\n" \
  "s_cbranch_scc1 Ew0%=\n" \
  "s_sleep " STR(WSLOW) "\n" \
  "Ew0%=:\n" \
  "s_setprio 0\n" \
  "s_load_dwordx4 s[52:55], %[k], 0x40\n" \
  "s_load_dwordx2 s[48:49], %[k], 0x50\n" \
  "s_load_dwordx2 s[84:85], %[k], 0x68\n" \
  "s_waitcnt lgkmcnt(0)\n" \
  "s_barrier\n" \
  "Ew2%=:\n" \
  "s_and_b32 s56, %[b], 7\n" \
  "s_bfe_u32 s57, %[b], 0x30003\n" \
  "s_lshr_b32 s58, s52, 7\n" \
  "s_add_u32 s58, s58, s56\n" \
  "s_sub_u32 s58, s57, s58\n" \
  "s_add_u32 s58, s58, 4\n" \
  "s_and_b32 s58, s58, 7\n" \
  "s_mul_i32 s59, s56, 9\n" \
  "s_add_u32 s58, s58, s59\n" \
  "s_lshl_b32 s58, s58, 6\n" \
  "s_add_u32 s59, s59, 8\n" \
  "s_lshl_b32 s59, s59, 6\n" \
  "s_lshl3_add_u32 s59, s57, s59\n" \
  "s_lshl3_add_u32 s57, s56, s57\n" \
  "v_mov_b32 v8, 158208\n" \
  "ds_read_b32 v8, v8\n" \
  "s_waitcnt lgkmcnt(0)\n" \
  "v_readfirstlane_b32 s44, v8\n" \
  "s_cmp_gt_u32 %[w], 8\n" \
  "s_cbranch_scc1 Ewd%=\n" \
  "v_mov_b32 v2, s52\n" \
  "v_mov_b32 v3, s53\n" \
  "v_mov_b32 v4, s54\n" \
  "v_mov_b32 v5, s55\n" \
  "s_mov_b32 s46, 294912\n" \
  "s_mov_b32 s47, 0\n" \
  "v_bfe_u32 v7, %[t], 2, 2\n" \
  "s_cmp_eq_u32 %[w], 8\n" \
  "s_cbranch_scc1 Ewx%=\n" \
  "s_mov_b32 vcc_lo, 0xff00ff00\n" \
  "s_mov_b32 vcc_hi, 0xff00ff00\n" \
  "v_cndmask_b32 v2, v2, v4, vcc\n" \
  "v_cndmask_b32 v3, v3, v5, vcc\n" \
  "v_lshrrev_b32 v4, 4, %[t]\n" \
  "v_mul_u32_u24 v4, 9216, v4\n" \
  "s_lshl_b32 s51, %[w], 1\n" \
  "s_and_b32 s51, s51, 4\n" \
  "v_or_b32 v5, s51, v11\n" \
  "v_xor_b32 v5, v5, %[t]\n" \
  "v_and_b32 v5, 7, v5\n" \
  "v_lshl_add_u32 v4, v5, 4, v4\n" \
  "s_lshl_b32 s51, s58, 1\n" \
  "v_add_u32 v4, s51, v4\n" \
  "v_mov_b32 v5, 0\n" \
  "v_lshl_add_u64 v[2:3], v[2:3], 0, v[4:5]\n" \
  "s_lshl_b32 m0, %[w], 10\n" \
  "s_nop 0\n" \
  "global_load_lds_dwordx4 v[2:3], off" WNT "\n" \
  "s_add_u32 m0, m0, 8192\n" \
  "v_lshl_add_u64 v[2:3], v[2:3], 0, s[46:47]\n" \
  "global_load_lds_dwordx4 v[2:3], off" WNT "\n" \
  "s_add_u32 m0, m0, 8192\n" \
  "v_lshl_add_u64 v[2:3], v[2:3], 0, s[46:47]\n" \
  "Ew3%=:\n" \
  "global_load_lds_dwordx4 v[2:3], off" WNT "\n" \
  "s_add_u32 m0, m0, 8192\n" \
  "v_lshl_add_u64 v[2:3], v[2:3], 0, s[46:47]\n" \
  "global_load_lds_dwordx4 v[2:3], off" WNT "\n" \
  "s_add_u32 m0, m0, 8192\n" \
  "v_lshl_add_u64 v[2:3], v[2:3], 0, s[46:47]\n" \
  "global_load_lds_dwordx4 v[2:3], off" WNT "\n" \
  "s_add_u32 m0, m0, 8192\n" \
  "v_lshl_add_u64 v[2:3], v[2:3], 0, s[46:47]\n" \
  "global_load_lds_dwordx4 v[2:3], off" WNT "\n" \
  "s_add_u32 m0, m0, 8192\n" \
  "v_lshl_add_u64 v[2:3], v[2:3], 0, s[46:47]\n" \
  "Ew1%=:\n" \
  "global_load_lds_dwordx4 v[2:3], off" WNT "\n" \
  "s_add_u32 m0, m0, 8192\n" \
  "v_lshl_add_u64 v[2:3], v[2:3], 0, s[46:47]\n" \
  "global_load_lds_dwordx4 v[2:3], off" WNT "\n" \
  "s_add_u32 m0, m0, 8192\n" \
  "v_lshl_add_u64 v[2:3], v[2:3], 0, s[46:47]\n" \
  "global_load_lds_dwordx4 v[2:3], off" WNT "\n" \
  "s_add_u32 m0, m0, 8192\n" \
  "v_lshl_add_u64 v[2:3], v[2:3], 0, s[46:47]\n" \
  "s_barrier\n" \
  "global_load_lds_dwordx4 v[2:3], off" WNT "\n" \
  "s_add_u32 m0, m0, 8192\n" \
  "v_lshl_add_u64 v[2:3], v[2:3], 0, s[46:47]\n" \
  "global_load_lds_dwordx4 v[2:3], off" WNT "\n" \
  "s_add_u32 m0, m0, 8192\n" \
  "v_lshl_add_u64 v[2:3], v[2:3], 0, s[46:47]\n" \
  "global_load_lds_dwordx4 v[2:3], off" WNT "\n" \
  "s_add_u32 m0, m0, 8192\n" \
  "v_lshl_add_u64 v[2:3], v[2:3], 0, s[46:47]\n" \
  "global_load_lds_dwordx4 v[2:3], off" WNT "\n" \
  "s_add_u32 m0, m0, 8192\n" \
  "v_lshl_add_u64 v[2:3], v[2:3], 0, s[46:47]\n" \
  "global_load_lds_dwordx4 v[2:3], off" WNT "\n" \
  "s_add_u32 m0, m0, 8192\n" \
  "v_lshl_add_u64 v[2:3], v[2:3], 0, s[46:47]\n" \
  "s_waitcnt vmcnt(5)\n" \
  "s_barrier\n" \
  "global_load_lds_dwordx4 v[2:3], off" WNT "\n" \
  "s_add_u32 m0, m0, 8192\n" \
  "v_lshl_add_u64 v[2:3], v[2:3], 0, s[46:47]\n" \
  "global_load_lds_dwordx4 v[2:3], off" WNT "\n" \
  "s_add_u32 m0, m0, 8192\n" \
  "v_lshl_add_u64 v[2:3], v[2:3], 0, s[46:47]\n" \
  "global_load_lds_dwordx4 v[2:3], off" WNT "\n" \
  "s_add_u32 m0, m0, 8192\n" \
  "v_lshl_add_u64 v[2:3], v[2:3], 0, s[46:47]\n" \
  "global_load_lds_dwordx4 v[2:3], off" WNT "\n" \
  "v_lshl_add_u32 v1, v11, 3, v7\n" \
  "v_lshlrev_b32 v1, 8, v1\n" \
  "v_and_b32 v4, 2, %[t]\n" \
  "v_lshl_add_u32 v1, v4, 6, v1\n" \
  "v_and_b32 v4, 1, v11\n" \
  "v_lshl_or_b32 v4, v4, 2, v7\n" \
  "v_xor_b32 v4, %[w], v4\n" \
  "v_lshl_add_u32 v1, v4, 4, v1\n" \
  "v_and_b32 v4, 1, %[t]\n" \
  "v_lshl_add_u32 v1, v4, 3, v1\n" \
  "ds_read_b64_tr_b16 v[40:41], v1 offset:0\n" \
  "ds_read_b64_tr_b16 v[42:43], v1 offset:1024\n" \
  "ds_read_b64_tr_b16 v[44:45], v1 offset:8192\n" \
  "ds_read_b64_tr_b16 v[46:47], v1 offset:9216\n" \
  "ds_read_b64_tr_b16 v[48:49], v1 offset:16384\n" \
  "ds_read_b64_tr_b16 v[50:51], v1 offset:17408\n" \
  "ds_read_b64_tr_b16 v[52:53], v1 offset:24576\n" \
  "ds_read_b64_tr_b16 v[54:55], v1 offset:25600\n" \
  "ds_read_b64_tr_b16 v[56:57], v1 offset:32768\n" \
  "ds_read_b64_tr_b16 v[58:59], v1 offset:33792\n" \
  "ds_read_b64_tr_b16 v[60:61], v1 offset:40960\n" \
  "ds_read_b64_tr_b16 v[62:63], v1 offset:41984\n" \
  "ds_read_b64_tr_b16 v[64:65], v1 offset:49152\n" \
  "ds_read_b64_tr_b16 v[66:67], v1 offset:50176\n" \
  "ds_read_b64_tr_b16 v[68:69], v1 offset:57344\n" \
  "ds_read_b64_tr_b16 v[70:71], v1 offset:58368\n" \
  "v_add_u32 v1, 0x10000, v1\n" \
  "ds_read_b64_tr_b16 v[72:73], v1\n" \
  "ds_read_b64_tr_b16 v[74:75], v1 offset:1024\n" \
  "v_add_u32 v1, 0x2000, v1\n" \
  "s_waitcnt vmcnt(0) lgkmcnt(0)\n" \
  GSTAMP6 \
  "s_barrier\n" \
  "ds_read_b64_tr_b16 v[76:77], v1 offset:0\n" \
  "ds_read_b64_tr_b16 v[78:79], v1 offset:1024\n" \
  "ds_read_b64_tr_b16 v[80:81], v1 offset:8192\n" \
  "ds_read_b64_tr_b16 v[82:83], v1 offset:9216\n" \
  "ds_read_b64_tr_b16 v[84:85], v1 offset:16384\n" \
  "ds_read_b64_tr_b16 v[86:87], v1 offset:17408\n" \
  "ds_read_b64_tr_b16 v[88:89], v1 offset:24576\n" \
  "ds_read_b64_tr_b16 v[90:91], v1 offset:25600\n" \
  "ds_read_b64_tr_b16 v[92:93], v1 offset:32768\n" \
  "ds_read_b64_tr_b16 v[94:95], v1 offset:33792\n" \
  "ds_read_b64_tr_b16 v[96:97], v1 offset:40960\n" \
  "ds_read_b64_tr_b16 v[98:99], v1 offset:41984\n" \
  "ds_read_b64_tr_b16 v[100:101], v1 offset:49152\n" \
  "ds_read_b64_tr_b16 v[102:103], v1 offset:50176\n" \
  "ds_read_b64_tr_b16 v[104:105], v1 offset:57344\n" \
  "ds_read_b64_tr_b16 v[106:107], v1 offset:58368\n" \
  "v_add_u32 v1, 0x10000, v1\n" \
  "ds_read_b64_tr_b16 v[108:109], v1\n" \
  "ds_read_b64_tr_b16 v[110:111], v1 offset:1024\n" \
  "v_add_u32 v1, 0x2000, v1\n" \
  "s_waitcnt lgkmcnt(0)\n" \
  "s_barrier\n" \
  GSTAMP7 \
  "s_branch Ek0%=\n" \
  "Ewx%=:\n" \
  "s_mov_b32 vcc_lo, 0xaaaaaaaa\n" \
  "s_mov_b32 vcc_hi, 0xaaaaaaaa\n" \
  "v_cndmask_b32 v2, v2, v4, vcc\n" \
  "v_cndmask_b32 v3, v3, v5, vcc\n" \
  "v_lshrrev_b32 v4, 1, v12\n" \
  "v_mul_u32_u24 v4, 9216, v4\n" \
  "s_lshl_b32 s51, s59, 1\n" \
  "v_add_u32 v4, s51, v4\n" \
  "v_mov_b32 v5, 0\n" \
  "v_lshl_add_u64 v[2:3], v[2:3], 0, v[4:5]\n" \
  "s_mov_b32 m0, 46080\n" \
  "v_lshlrev_b32 v1, 3, v10\n" \
  "v_lshl_add_u32 v1, v11, 8, v1\n" \
  "s_barrier\n" \
  "s_barrier\n" \
  "s_barrier\n" \
  "s_waitcnt vmcnt(" STR(CTH) ")\n" \
  "global_load_lds_dwordx4 v[2:3], off\n" \
  "s_add_u32 m0, m0, 1024\n" \
  "v_lshl_add_u64 v[2:3], v[2:3], 0, s[46:47]\n" \
  "s_waitcnt vmcnt(" STR(CTH) ")\n" \
  "global_load_lds_dwordx4 v[2:3], off\n" \
  "s_add_u32 m0, m0, 1024\n" \
  "v_lshl_add_u64 v[2:3], v[2:3], 0, s[46:47]\n" \
  "s_waitcnt vmcnt(" STR(CTH) ")\n" \
  "global_load_lds_dwordx4 v[2:3], off\n" \
  "s_add_u32 m0, m0, 1024\n" \
  "v_lshl_add_u64 v[2:3], v[2:3], 0, s[46:47]\n" \
  "s_barrier\n" \
  "s_waitcnt vmcnt(" STR(CTH) ")\n" \
  "global_load_lds_dwordx4 v[2:3], off\n" \
  "s_add_u32 m0, m0, 1024\n" \
  "v_lshl_add_u64 v[2:3], v[2:3], 0, s[46:47]\n" \
  "s_waitcnt vmcnt(" STR(CTH) ")\n" \
  "global_load_lds_dwordx4 v[2:3], off\n" \
  "s_add_u32 m0, m0, 1024\n" \
  "v_lshl_add_u64 v[2:3], v[2:3], 0, s[46:47]\n" \
  "s_waitcnt vmcnt(" STR(CTH) ")\n" \
  "global_load_lds_dwordx4 v[2:3], off\n" \
  "s_add_u32 m0, m0, 1024\n" \
  "v_lshl_add_u64 v[2:3], v[2:3], 0, s[46:47]\n" \
  "s_waitcnt vmcnt(" STR(CTH) ")\n" \
  "global_load_lds_dwordx4 v[2:3], off\n" \
  "s_add_u32 m0, m0, 1024\n" \
  "v_lshl_add_u64 v[2:3], v[2:3], 0, s[46:47]\n" \
  "s_waitcnt vmcnt(" STR(CTH) ")\n" \
  "global_load_lds_dwordx4 v[2:3], off\n" \
  "s_add_u32 m0, m0, 1024\n" \
  "v_lshl_add_u64 v[2:3], v[2:3], 0, s[46:47]\n" \
  "s_waitcnt vmcnt(" STR(CTH) ")\n" \
  "global_load_lds_dwordx4 v[2:3], off\n" \
  "s_add_u32 m0, m0, 1024\n" \
  "v_lshl_add_u64 v[2:3], v[2:3], 0, s[46:47]\n" \
  "s_waitcnt vmcnt(" STR(CTH) ")\n" \
  "global_load_lds_dwordx4 v[2:3], off\n" \
  "s_add_u32 m0, m0, 1024\n" \
  "v_lshl_add_u64 v[2:3], v[2:3], 0, s[46:47]\n" \
  "s_waitcnt vmcnt(" STR(CTH) ")\n" \
  "global_load_lds_dwordx4 v[2:3], off\n" \
  "s_add_u32 m0, m0, 1024\n" \
  "v_lshl_add_u64 v[2:3], v[2:3], 0, s[46:47]\n" \
  "s_waitcnt vmcnt(" STR(CTH) ")\n" \
  "global_load_lds_dwordx4 v[2:3], off\n" \
  "s_add_u32 m0, m0, 1024\n" \
  "v_lshl_add_u64 v[2:3], v[2:3], 0, s[46:47]\n" \
  "s_waitcnt vmcnt(" STR(CTH) ")\n" \
  "global_load_lds_dwordx4 v[2:3], off\n" \
  "s_add_u32 m0, m0, 1024\n" \
  "v_lshl_add_u64 v[2:3], v[2:3], 0, s[46:47]\n" \
  "s_waitcnt vmcnt(" STR(CTH) ")\n" \
  "global_load_lds_dwordx4 v[2:3], off\n" \
  "s_add_u32 m0, m0, 1024\n" \
  "v_lshl_add_u64 v[2:3], v[2:3], 0, s[46:47]\n" \
  "s_waitcnt vmcnt(" STR(CTH) ")\n" \
  "global_load_lds_dwordx4 v[2:3], off\n" \
  "s_add_u32 m0, m0, 1024\n" \
  "v_lshl_add_u64 v[2:3], v[2:3], 0, s[46:47]\n" \
  "s_waitcnt vmcnt(" STR(CTH) ")\n" \
  "global_load_lds_dwordx4 v[2:3], off\n" \
  "s_add_u32 m0, m0, 1024\n" \
  "v_lshl_add_u64 v[2:3], v[2:3], 0, s[46:47]\n" \
  "s_waitcnt vmcnt(" STR(CTH) ")\n" \
  "global_load_lds_dwordx4 v[2:3], off\n" \
  "s_add_u32 m0, m0, 1024\n" \
  "v_lshl_add_u64 v[2:3], v[2:3], 0, s[46:47]\n" \
  "s_waitcnt vmcnt(" STR(CTH) ")\n" \
  "global_load_lds_dwordx4 v[2:3], off\n" \
  "s_add_u32 m0, m0, 1024\n" \
  "v_lshl_add_u64 v[2:3], v[2:3], 0, s[46:47]\n" \
  GSTAMP6 \
  "s_barrier\n" \
  "s_barrier\n" \
  "s_barrier\n" \
  "s_waitcnt vmcnt(17)\n" \
  "ds_read_b64_tr_b16 v[40:41], v1 offset:46080\n" \
  "ds_read_b64_tr_b16 v[42:43], v1 offset:46208\n" \
  "s_waitcnt vmcnt(16)\n" \
  "ds_read_b64_tr_b16 v[44:45], v1 offset:47104\n" \
  "ds_read_b64_tr_b16 v[46:47], v1 offset:47232\n" \
  "s_waitcnt vmcnt(15)\n" \
  "ds_read_b64_tr_b16 v[48:49], v1 offset:48128\n" \
  "ds_read_b64_tr_b16 v[50:51], v1 offset:48256\n" \
  "s_waitcnt vmcnt(14)\n" \
  "ds_read_b64_tr_b16 v[52:53], v1 offset:49152\n" \
  "ds_read_b64_tr_b16 v[54:55], v1 offset:49280\n" \
  "s_waitcnt vmcnt(13)\n" \
  "ds_read_b64_tr_b16 v[56:57], v1 offset:50176\n" \
  "ds_read_b64_tr_b16 v[58:59], v1 offset:50304\n" \
  "s_waitcnt vmcnt(12)\n" \
  "ds_read_b64_tr_b16 v[60:61], v1 offset:51200\n" \
  "ds_read_b64_tr_b16 v[62:63], v1 offset:51328\n" \
  "s_waitcnt vmcnt(11)\n" \
  "ds_read_b64_tr_b16 v[64:65], v1 offset:52224\n" \
  "ds_read_b64_tr_b16 v[66:67], v1 offset:52352\n" \
  "s_waitcnt vmcnt(10)\n" \
  "ds_read_b64_tr_b16 v[68:69], v1 offset:53248\n" \
  "ds_read_b64_tr_b16 v[70:71], v1 offset:53376\n" \
  "s_waitcnt vmcnt(9)\n" \
  "ds_read_b64_tr_b16 v[72:73], v1 offset:54272\n" \
  "ds_read_b64_tr_b16 v[74:75], v1 offset:54400\n" \
  "s_waitcnt vmcnt(8)\n" \
  "ds_read_b64_tr_b16 v[76:77], v1 offset:55296\n" \
  "ds_read_b64_tr_b16 v[78:79], v1 offset:55424\n" \
  "s_waitcnt vmcnt(7)\n" \
  "ds_read_b64_tr_b16 v[80:81], v1 offset:56320\n" \
  "ds_read_b64_tr_b16 v[82:83], v1 offset:56448\n" \
  "s_waitcnt vmcnt(6)\n" \
  "ds_read_b64_tr_b16 v[84:85], v1 offset:57344\n" \
  "ds_read_b64_tr_b16 v[86:87], v1 offset:57472\n" \
  "s_waitcnt vmcnt(5)\n" \
  "ds_read_b64_tr_b16 v[88:89], v1 offset:58368\n" \
  "ds_read_b64_tr_b16 v[90:91], v1 offset:58496\n" \
  "s_waitcnt vmcnt(4)\n" \
  "ds_read_b64_tr_b16 v[92:93], v1 offset:59392\n" \
  "ds_read_b64_tr_b16 v[94:95], v1 offset:59520\n" \
  "s_waitcnt vmcnt(3)\n" \
  "ds_read_b64_tr_b16 v[96:97], v1 offset:60416\n" \
  "ds_read_b64_tr_b16 v[98:99], v1 offset:60544\n" \
  "s_waitcnt vmcnt(2)\n" \
  "ds_read_b64_tr_b16 v[100:101], v1 offset:61440\n" \
  "ds_read_b64_tr_b16 v[102:103], v1 offset:61568\n" \
  "s_waitcnt vmcnt(1)\n" \
  "ds_read_b64_tr_b16 v[104:105], v1 offset:62464\n" \
  "ds_read_b64_tr_b16 v[106:107], v1 offset:62592\n" \
  "s_waitcnt vmcnt(0)\n" \
  "ds_read_b64_tr_b16 v[108:109], v1 offset:63488\n" \
  "ds_read_b64_tr_b16 v[110:111], v1 offset:63616\n" \
  GSTAMP7 \
  "v_mul_u32_u24 v1, 1184, v10\n" \
  "v_lshl_add_u32 v1, v11, 4, v1\n" \
  "v_add_u32 v1, 120832, v1\n" \
  "v_lshrrev_b32 v2, 3, v12\n" \
  "v_mul_u32_u24 v2, 320, v2\n" \
  "v_and_b32 v4, 7, %[t]\n" \
  "v_lshl_add_u32 v2, v4, 1, v2\n" \
  "s_lshl4_add_u32 s46, %[w], 141312\n" \
  "v_add_u32 v2, s46, v2\n" \
  "s_branch Eg0%=\n" \
  "Ewd%=:\n" \
  "s_cmp_eq_u32 %[w], 15\n" \
  "s_cbranch_scc1 Ewz%=\n" \
  "s_sub_u32 s56, %[w], 9\n" \
  "s_mul_i32 s51, s56, 192\n" \
  "s_mul_i32 s46, s58, 1152\n" \
  "s_add_u32 s51, s51, s46\n" \
  "s_sub_u32 s47, s59, s58\n" \
  "s_mul_i32 s47, s47, 1152\n" \
  "s_sub_u32 s47, s47, 55296\n" \
  "v_add_u32 v4, 0, v12\n" \
  "v_mul_u32_u24 v5, 171, v4\n" \
  "v_lshrrev_b32 v5, 11, v5\n" \
  "v_lshrrev_b32 v6, 2, v5\n" \
  "v_and_b32 v6, 2, v6\n" \
  "v_xor_b32 v4, v4, v6\n" \
  "v_mul_u32_u24 v5, 960, v5\n" \
  "v_lshl_add_u32 v4, v4, 4, v5\n" \
  "v_add_u32 v13, s51, v4\n" \
  "v_add_u32 v4, 64, v12\n" \
  "v_mul_u32_u24 v5, 171, v4\n" \
  "v_lshrrev_b32 v5, 11, v5\n" \
  "v_lshrrev_b32 v6, 2, v5\n" \
  "v_and_b32 v6, 2, v6\n" \
  "v_xor_b32 v4, v4, v6\n" \
  "v_mul_u32_u24 v5, 960, v5\n" \
  "v_lshl_add_u32 v4, v4, 4, v5\n" \
  "v_add_u32 v14, s51, v4\n" \
  "v_add_u32 v4, 128, v12\n" \
  "v_mul_u32_u24 v5, 171, v4\n" \
  "v_lshrrev_b32 v5, 11, v5\n" \
  "v_lshrrev_b32 v6, 2, v5\n" \
  "v_and_b32 v6, 2, v6\n" \
  "v_xor_b32 v4, v4, v6\n" \
  "v_mul_u32_u24 v5, 960, v5\n" \
  "v_lshl_add_u32 v4, v4, 4, v5\n" \
  "v_add_u32 v15, s51, v4\n" \
  "s_mul_i32 s51, s56, 15360\n" \
  "s_cmp_ge_u32 s56, 3\n" \
  "s_cselect_b32 s46, 27648, 0\n" \
  "s_add_u32 s51, s51, s46\n" \
  "s_mov_b32 m0, s51\n" \
  "v_and_b32 v4, 1, v11\n" \
  "v_bfe_u32 v7, %[t], 2, 2\n" \
  "v_mul_u32_u24 v7, 192, v7\n" \
  "v_lshrrev_b32 v5, 1, v11\n" \
  "v_and_b32 v6, 3, %[t]\n" \
  "v_lshl_add_u32 v7, v6, 3, v7\n" \
  "v_add_u32 v7, s51, v7\n" \
  "v_lshl_add_u32 v2, v5, 5, v7\n" \
  "v_xor_b32 v5, v5, v4\n" \
  "v_lshl_add_u32 v1, v5, 5, v7\n" \
  "v_mul_u32_u24 v5, 1536, v4\n" \
  "v_add_u32 v1, v1, v5\n" \
  "s_barrier\n" \
  "s_barrier\n" \
  "s_barrier\n" \
  "s_cmp_lt_u32 s56, 3\n" \
  "s_cbranch_scc1 Ewe%=\n" \
  "s_barrier\n" \
  "Ewe%=:\n" \
  "s_waitcnt vmcnt(" STR(DTH) ")\n" \
  "global_load_lds_dwordx4 v13, s[48:49]" DNTF "\n" \
  "s_add_u32 m0, m0, 1024\n" \
  "v_add_u32 v13, 18432, v13\n" \
  "s_waitcnt vmcnt(" STR(DTH) ")\n" \
  "global_load_lds_dwordx4 v14, s[48:49]" DNTF "\n" \
  "s_add_u32 m0, m0, 1024\n" \
  "v_add_u32 v14, 18432, v14\n" \
  "s_waitcnt vmcnt(" STR(DTH) ")\n" \
  "global_load_lds_dwordx4 v15, s[48:49]" DNTF "\n" \
  "s_add_u32 m0, m0, 1024\n" \
  "v_add_u32 v15, 18432, v15\n" \
  "s_cmp_ge_u32 s56, 3\n" \
  "s_cbranch_scc1 Ewf%=\n" \
  "s_barrier\n" \
  "Ewf%=:\n" \
  "s_waitcnt vmcnt(" STR(DTH) ")\n" \
  "global_load_lds_dwordx4 v13, s[48:49]" DNTF "\n" \
  "s_add_u32 m0, m0, 1024\n" \
  "v_add_u32 v13, 18432, v13\n" \
  "s_waitcnt vmcnt(" STR(DTH) ")\n" \
  "global_load_lds_dwordx4 v14, s[48:49]" DNTF "\n" \
  "s_add_u32 m0, m0, 1024\n" \
  "v_add_u32 v14, 18432, v14\n" \
  "s_waitcnt vmcnt(" STR(DTH) ")\n" \
  "global_load_lds_dwordx4 v15, s[48:49]" DNTF "\n" \
  "s_add_u32 m0, m0, 1024\n" \
  "v_add_u32 v15, 18432, v15\n" \
  "s_waitcnt vmcnt(" STR(DTH) ")\n" \
  "global_load_lds_dwordx4 v13, s[48:49]" DNTF "\n" \
  "s_add_u32 m0, m0, 1024\n" \
  "v_add_u32 v13, 18432, v13\n" \
  "s_waitcnt vmcnt(" STR(DTH) ")\n" \
  "global_load_lds_dwordx4 v14, s[48:49]" DNTF "\n" \
  "s_add_u32 m0, m0, 1024\n" \
  "v_add_u32 v14, 18432, v14\n" \
  "s_waitcnt vmcnt(" STR(DTH) ")\n" \
  "global_load_lds_dwordx4 v15, s[48:49]" DNTF "\n" \
  "s_add_u32 m0, m0, 1024\n" \
  "v_add_u32 v15, 18432, v15\n" \
  "s_waitcnt vmcnt(" STR(DTH) ")\n" \
  "global_load_lds_dwordx4 v13, s[48:49]" DNTF "\n" \
  "s_add_u32 m0, m0, 1024\n" \
  "v_add_u32 v13, s47, v13\n" \
  "s_waitcnt vmcnt(" STR(DTH) ")\n" \
  "global_load_lds_dwordx4 v14, s[48:49]" DNTF "\n" \
  "s_add_u32 m0, m0, 1024\n" \
  "v_add_u32 v14, s47, v14\n" \
  "s_waitcnt vmcnt(" STR(DTH) ")\n" \
  "global_load_lds_dwordx4 v15, s[48:49]" DNTF "\n" \
  "s_add_u32 m0, m0, 1024\n" \
  "v_add_u32 v15, s47, v15\n" \
  "s_waitcnt vmcnt(" STR(DTH) ")\n" \
  "global_load_lds_dwordx4 v13, s[48:49]" DNTF "\n" \
  "s_add_u32 m0, m0, 1024\n" \
  "v_add_u32 v13, s47, v13\n" \
  "s_mov_b32 exec_hi, 0\n" \
  "s_waitcnt vmcnt(" STR(DTH) ")\n" \
  "global_load_lds_dwordx4 v14, s[48:49]" DNTF "\n" \
  "s_add_u32 m0, m0, 1024\n" \
  "v_add_u32 v14, s47, v14\n" \
  "s_mov_b64 exec, -1\n" \
  GSTAMP6 \
  "s_barrier\n" \
  "s_barrier\n" \
  "s_barrier\n" \
  "s_waitcnt vmcnt(11)\n" \
  "ds_read_b64_tr_b16 v[40:41], v1 offset:0\n" \
  "ds_read_b64_tr_b16 v[42:43], v1 offset:768\n" \
  "ds_read_b64_tr_b16 v[44:45], v1 offset:64\n" \
  "ds_read_b64_tr_b16 v[46:47], v1 offset:832\n" \
  "ds_read_b64_tr_b16 v[48:49], v1 offset:128\n" \
  "ds_read_b64_tr_b16 v[50:51], v1 offset:896\n" \
  "s_waitcnt vmcnt(8)\n" \
  "ds_read_b64_tr_b16 v[52:53], v1 offset:3072\n" \
  "ds_read_b64_tr_b16 v[54:55], v1 offset:3840\n" \
  "ds_read_b64_tr_b16 v[56:57], v1 offset:3136\n" \
  "ds_read_b64_tr_b16 v[58:59], v1 offset:3904\n" \
  "ds_read_b64_tr_b16 v[60:61], v1 offset:3200\n" \
  "ds_read_b64_tr_b16 v[62:63], v1 offset:3968\n" \
  "s_waitcnt vmcnt(5)\n" \
  "ds_read_b64_tr_b16 v[64:65], v1 offset:6144\n" \
  "ds_read_b64_tr_b16 v[66:67], v1 offset:6912\n" \
  "ds_read_b64_tr_b16 v[68:69], v1 offset:6208\n" \
  "ds_read_b64_tr_b16 v[70:71], v1 offset:6976\n" \
  "ds_read_b64_tr_b16 v[72:73], v1 offset:6272\n" \
  "ds_read_b64_tr_b16 v[74:75], v1 offset:7040\n" \
  "s_waitcnt vmcnt(2)\n" \
  "ds_read_b64_tr_b16 v[76:77], v1 offset:9216\n" \
  "ds_read_b64_tr_b16 v[78:79], v1 offset:9984\n" \
  "ds_read_b64_tr_b16 v[80:81], v1 offset:9280\n" \
  "ds_read_b64_tr_b16 v[82:83], v1 offset:10048\n" \
  "ds_read_b64_tr_b16 v[84:85], v1 offset:9344\n" \
  "ds_read_b64_tr_b16 v[86:87], v1 offset:10112\n" \
  "s_waitcnt vmcnt(0)\n" \
  "ds_read_b64_tr_b16 v[88:89], v2 offset:12288\n" \
  "ds_read_b64_tr_b16 v[90:91], v2 offset:13056\n" \
  "ds_read_b64_tr_b16 v[92:93], v2 offset:12352\n" \
  "ds_read_b64_tr_b16 v[94:95], v2 offset:13120\n" \
  "ds_read_b64_tr_b16 v[96:97], v2 offset:12416\n" \
  "ds_read_b64_tr_b16 v[98:99], v2 offset:13184\n" \
  "s_lshl_b32 s51, s56, 5\n" \
  "v_lshrrev_b32 v5, 1, v11\n" \
  "v_lshl_add_u32 v5, v5, 4, v10\n" \
  "v_add_u32 v5, s51, v5\n" \
  "v_mul_u32_u24 v6, 171, v5\n" \
  "v_lshrrev_b32 v6, 11, v6\n" \
  "v_mul_u32_u24 v6, 12096, v6\n" \
  "v_lshl_add_u32 v6, v5, 4, v6\n" \
  "v_mul_u32_u24 v4, 393216, v4\n" \
  "s_mul_i32 s51, s57, 192\n" \
  "v_add3_u32 v38, v6, v4, s51\n" \
  "s_mov_b32 s86, 6455296\n" \
  "s_mov_b32 s87, 6651904\n" \
  "v_mov_b32 v31, s44\n" \
  "v_mov_b32 v35, s44\n" \
  "v_mov_b32 v3, 159488\n" \
  GSTAMP7 \
  "s_barrier\n" \
  "s_branch Ed0%=\n" \
  "Ewz%=:\n" \
  "v_mov_b32 v4, 147456\n" \
  "v_lshl_add_u32 v4, v12, 4, v4\n" \
  "v_mov_b64 v[16:17], 0\n" \
  "v_mov_b64 v[18:19], 0\n" \
  "ds_write_b128 v4, v[16:19] offset:0\n" \
  "ds_write_b128 v4, v[16:19] offset:1024\n" \
  "ds_write_b128 v4, v[16:19] offset:2048\n" \
  "ds_write_b128 v4, v[16:19] offset:3072\n" \
  "ds_write_b128 v4, v[16:19] offset:4096\n" \
  "ds_write_b128 v4, v[16:19] offset:5120\n" \
  "ds_write_b128 v4, v[16:19] offset:6144\n" \
  "ds_write_b128 v4, v[16:19] offset:7168\n" \
  "ds_write_b128 v4, v[16:19] offset:8192\n" \
  "s_barrier\n" \
  "s_barrier\n" \
  "s_barrier\n" \
  "s_barrier\n" \
  "v_add_u32 v4, -6144, v4\n" \
  "ds_write_b128 v4, v[16:19] offset:0\n" \
  "ds_write_b128 v4, v[16:19] offset:1024\n" \
  "ds_write_b128 v4, v[16:19] offset:2048\n" \
  "s_setprio 3\n" \
  "v_lshlrev_b32 v4, 7, v12\n" \
  "s_mov_b32 s46, 87040\n" \
  "s_memrealtime s[88:89]\n" \
  "s_movk_i32 s97, 0x7fff\n" \
  "Etr%=:\n" \
  "buffer_load_dwordx2 v[20:21], v4, s[40:43], s46 offen sc1\n" \
  "s_waitcnt vmcnt(0)\n" \
  "v_cmp_ne_u32 vcc, s44, v21\n" \
  "s_cbranch_vccz Etx%=\n" \
  "s_sleep 4\n" \
  "s_sub_u32 s97, s97, 1\n" \
  "s_cbranch_scc0 Etr%=\n" \
  "Etx%=:\n" \
  "s_waitcnt lgkmcnt(0)\n" \
  "v_subrev_u32 v20, s88, v20\n" \
  "s_nop 1\n" \
  "v_max_i32_dpp v20, v20, v20 quad_perm:[1,0,3,2] row_mask:0xf bank_mask:0xf\n" \
  "s_nop 1\n" \
  "v_max_i32_dpp v20, v20, v20 quad_perm:[2,3,0,1] row_mask:0xf bank_mask:0xf\n" \
  "s_nop 1\n" \
  "v_max_i32_dpp v20, v20, v20 row_half_mirror row_mask:0xf bank_mask:0xf\n" \
  "s_nop 1\n" \
  "v_max_i32_dpp v20, v20, v20 row_mirror row_mask:0xf bank_mask:0xf\n" \
  "s_nop 1\n" \
  "v_max_i32_dpp v20, v20, v20 row_bcast:15 row_mask:0xa bank_mask:0xf\n" \
  "s_nop 1\n" \
  "v_max_i32_dpp v20, v20, v20 row_bcast:31 row_mask:0xc bank_mask:0xf\n" \
  "v_mov_b32 v3, 159488\n" \
  "v_readlane_b32 s46, v20, 63\n" \
  "s_add_u32 s46, s46, s88\n" \
  "s_or_b32 s46, s46, 1\n" \
  "v_mov_b32 v20, s46\n" \
  "ds_write_b32 v3, v20\n" \
  GSTAMP7 \
  "v_mov_b32 v13, 160800\n" \
  "s_waitcnt lgkmcnt(0)\n" \
  "s_barrier\n" \
  "s_barrier\n" \
  "s_barrier\n" \
  "s_branch Eh1%=\n" \
  "Ek0%=:\n" \
  "v_lshlrev_b32 v1, 4, %[t]\n" \
  "s_and_b32 s51, %[b], 7\n" \
  "s_mul_i32 s51, s51, 16512\n" \
  "s_add_u32 s51, s51, 10403840\n" \
  "s_add_u32 s52, s51, 8192\n" \
  "v_and_b32 v2, 7, %[t]\n" \
  "v_mov_b32 v3, 159488\n" \
  "v_lshl_add_u32 v4, v2, 2, v3\n" \
  "v_and_b32 v5, 3, %[t]\n" \
  "v_lshl_add_u32 v5, v5, 4, v3\n" \
  "v_mov_b32 v6, 1\n" \
  "v_mov_b32 v7, 147456\n" \
  "v_lshrrev_b32 v8, 3, %[t]\n" \
  "v_lshl_add_u32 v8, v8, 2, v7\n" \
  "v_lshl_add_u32 v9, v2, 4, v7\n" \
  "s_lshl_b32 s56, %[w], 3\n" \
  "v_mov_b32 v13, 160800\n" \
  "v_lshrrev_b32 v14, 5, %[t]\n" \
  "v_and_b32 v15, 31, %[t]\n" \
  "v_mul_u32_u24 v36, 1184, v14\n" \
  "v_mad_u32_u24 v36, v15, 12, v36\n" \
  "v_add_u32 v36, 120832, v36\n" \
  "v_lshl_add_u32 v14, v14, 2, v13\n" \
  "v_lshlrev_b32 v15, 4, v15\n" \
  "v_add_u32 v15, 6453760, v15\n" \
  "s_mov_b32 s45, -1536\n" \
  "s_movk_i32 s97, 0x7fff\n" \
  "Ekt%=:\n" \
  "ds_read_b32 v16, v3\n" \
  "s_memrealtime s[46:47]\n" \
  "s_waitcnt lgkmcnt(0)\n" \
  "v_readfirstlane_b32 s47, v16\n" \
  "s_sub_u32 s46, s46, s47\n" \
  "s_cmp_eq_u32 s47, 0\n" \
  "s_cselect_b32 s46, -1, s46\n" \
  "s_cmp_ge_i32 s46, 0\n" \
  "s_cbranch_scc1 Eku%=\n" \
  "s_sleep 1\n" \
  "s_sub_u32 s97, s97, 1\n" \
  "s_cbranch_scc0 Ekt%=\n" \
  "Eku%=:\n" \
  "s_setprio " STR(KPR) "\n" \
  GSTAMP3 \
  "buffer_load_dwordx4 v[20:23], v1, s[40:43], s51 offen sc1\n" \
  "buffer_load_dwordx4 v[24:27], v1, s[40:43], s52 offen sc1\n" \
  "s_waitcnt vmcnt(1)\n" \
  "v_bitop3_b32 v28, v21, v23, s44 bitop3:0x7e\n" \
  "v_cmp_ne_u32 vcc, 0, v28\n" \
  "s_cbranch_vccz Ekax%=\n" \
  "s_movk_i32 s97, 0x7fff\n" \
  "Ekar%=:\n" \
  "buffer_load_dwordx4 v[20:23], v1, s[40:43], s51 offen sc1\n" \
  GSTAMPK \
  "s_waitcnt vmcnt(0)\n" \
  "v_bitop3_b32 v28, v21, v23, s44 bitop3:0x7e\n" \
  "v_cmp_ne_u32 vcc, 0, v28\n" \
  "s_cbranch_vccz Ekax%=\n" \
  "s_sleep " STR(KSLP) "\n" \
  "s_sub_u32 s97, s97, 1\n" \
  "s_cbranch_scc0 Ekar%=\n" \
  "Ekax%=:\n" \
  "ds_max_u32 v4, v20 offset:96\n" \
  "ds_max_u32 v4, v22 offset:128\n" \
  "s_waitcnt vmcnt(0)\n" \
  "v_bitop3_b32 v28, v25, v27, s44 bitop3:0x7e\n" \
  "v_cmp_ne_u32 vcc, 0, v28\n" \
  "s_cbranch_vccz Ekbx%=\n" \
  "s_movk_i32 s97, 0x7fff\n" \
  "Ekbr%=:\n" \
  "buffer_load_dwordx4 v[24:27], v1, s[40:43], s52 offen sc1\n" \
  GSTAMPK \
  "s_waitcnt vmcnt(0)\n" \
  "v_bitop3_b32 v28, v25, v27, s44 bitop3:0x7e\n" \
  "v_cmp_ne_u32 vcc, 0, v28\n" \
  "s_cbranch_vccz Ekbx%=\n" \
  "s_sleep " STR(KSLP) "\n" \
  "s_sub_u32 s97, s97, 1\n" \
  "s_cbranch_scc0 Ekbr%=\n" \
  "Ekbx%=:\n" \
  GSTAMP8 \
  "ds_max_u32 v4, v24 offset:96\n" \
  "ds_max_u32 v4, v26 offset:128\n" \
  "s_waitcnt lgkmcnt(0)\n" \
  "s_barrier\n" \
  GSTAMP2 \
  "ds_read_b128 v[28:31], v5 offset:96\n" \
  "s_waitcnt lgkmcnt(0)\n" \
  "v_min3_u32 v28, v28, v29, v30\n" \
  "v_min_u32 v28, v28, v31\n" \
  "s_nop 1\n" \
  "v_min_u32_dpp v28, v28, v28 quad_perm:[1,0,3,2] row_mask:0xf bank_mask:0xf\n" \
  "s_nop 1\n" \
  "v_min_u32_dpp v28, v28, v28 quad_perm:[2,3,0,1] row_mask:0xf bank_mask:0xf\n" \
  "v_cmp_ge_u32 s[88:89], v20, v28\n" \
  "v_cmp_ge_u32 s[90:91], v22, v28\n" \
  "v_cmp_ge_u32 s[94:95], v24, v28\n" \
  "v_cmp_ge_u32 s[96:97], v26, v28\n" \
  "s_mov_b64 exec, s[88:89]\n" \
  "ds_add_rtn_u32 v21, v3, v6 offset:24\n" \
  "s_mov_b64 exec, s[90:91]\n" \
  "ds_add_rtn_u32 v23, v3, v6 offset:24\n" \
  "s_mov_b64 exec, s[94:95]\n" \
  "ds_add_rtn_u32 v25, v3, v6 offset:24\n" \
  "s_mov_b64 exec, s[96:97]\n" \
  "ds_add_rtn_u32 v27, v3, v6 offset:24\n" \
  "s_waitcnt lgkmcnt(0)\n" \
  "v_lshl_add_u32 v27, v27, 2, v7\n" \
  "ds_write_b32 v27, v26\n" \
  "s_mov_b64 exec, s[94:95]\n" \
  "v_lshl_add_u32 v25, v25, 2, v7\n" \
  "ds_write_b32 v25, v24\n" \
  "s_mov_b64 exec, s[90:91]\n" \
  "v_lshl_add_u32 v23, v23, 2, v7\n" \
  "ds_write_b32 v23, v22\n" \
  "s_mov_b64 exec, s[88:89]\n" \
  "v_lshl_add_u32 v21, v21, 2, v7\n" \
  "ds_write_b32 v21, v20\n" \
  "s_mov_b64 exec, -1\n" \
  "s_waitcnt lgkmcnt(0)\n" \
  "s_barrier\n" \
  GSTAMP4 \
  "ds_read_b32 v18, v3 offset:24\n" \
  "Ec1%=:\n" \
  "ds_read_b32 v17, v8\n" \
  "ds_read_b128 v[20:23], v9\n" \
  "ds_read_b128 v[24:27], v9 offset:128\n" \
  "v_mov_b32 v16, 0\n" \
  "s_waitcnt lgkmcnt(3)\n" \
  "v_readfirstlane_b32 s46, v18\n" \
  "s_cmp_ge_u32 s56, s46\n" \
  "s_cbranch_scc1 Ec4%=\n" \
  "s_mov_b32 s47, 64\n" \
  "v_mov_b32 v19, v9\n" \
  "Ec2%=:\n" \
  "s_waitcnt lgkmcnt(0)\n" \
  "v_cmp_gt_u32 s[88:89], v20, v17\n" \
  "v_cmp_gt_u32 s[90:91], v21, v17\n" \
  "v_cmp_gt_u32 s[94:95], v22, v17\n" \
  "v_cmp_gt_u32 s[96:97], v23, v17\n" \
  "v_addc_co_u32 v16, vcc, 0, v16, s[88:89]\n" \
  "v_addc_co_u32 v16, vcc, 0, v16, s[90:91]\n" \
  "v_addc_co_u32 v16, vcc, 0, v16, s[94:95]\n" \
  "v_addc_co_u32 v16, vcc, 0, v16, s[96:97]\n" \
  "v_cmp_gt_u32 s[88:89], v24, v17\n" \
  "v_cmp_gt_u32 s[90:91], v25, v17\n" \
  "v_cmp_gt_u32 s[94:95], v26, v17\n" \
  "v_cmp_gt_u32 s[96:97], v27, v17\n" \
  "v_addc_co_u32 v16, vcc, 0, v16, s[88:89]\n" \
  "v_addc_co_u32 v16, vcc, 0, v16, s[90:91]\n" \
  "v_addc_co_u32 v16, vcc, 0, v16, s[94:95]\n" \
  "v_addc_co_u32 v16, vcc, 0, v16, s[96:97]\n" \
  "s_cmp_ge_u32 s47, s46\n" \
  "s_cbranch_scc1 Ec3%=\n" \
  "s_add_u32 s47, s47, 64\n" \
  "v_add_u32 v19, 256, v19\n" \
  "ds_read_b128 v[20:23], v19\n" \
  "ds_read_b128 v[24:27], v19 offset:128\n" \
  "s_branch Ec2%=\n" \
  "Ec3%=:\n" \
  "v_add_u32_dpp v16, v16, v16 quad_perm:[1,0,3,2] row_mask:0xf bank_mask:0xf\n" \
  "s_nop 1\n" \
  "v_add_u32_dpp v16, v16, v16 quad_perm:[2,3,0,1] row_mask:0xf bank_mask:0xf\n" \
  "s_addk_i32 s56, 64\n" \
  "s_cmp_lt_u32 s56, s46\n" \
  "v_add_u32_dpp v16, v16, v16 row_half_mirror row_mask:0xf bank_mask:0xf\n" \
  "v_min_u32 v16, 16, v16\n" \
  "v_lshl_add_u32 v16, v16, 2, v13\n" \
  "ds_write_b32 v16, v17\n" \
  "s_cbranch_scc0 Ec4%=\n" \
  "v_add_u32 v8, 256, v8\n" \
  "s_branch Ec1%=\n" \
  "Ec4%=:\n" \
  "s_waitcnt lgkmcnt(0)\n" \
  "s_barrier\n" \
  GSTAMP9 \
  "ds_read_b32 v16, v14\n" \
  "s_waitcnt lgkmcnt(0)\n" \
  "v_and_b32 v16, 0x7ff, v16\n" \
  "v_mad_i32_i24 v15, v16, s45, v15\n" \
  "s_movk_i32 s97, 0x7fff\n" \
  "Ehr%=:\n" \
  "buffer_load_dwordx4 v[20:23], v15, s[40:43], 0 offen offset:0 sc1\n" \
  "buffer_load_dwordx4 v[24:27], v15, s[40:43], 0 offen offset:512 sc1\n" \
  "buffer_load_dwordx4 v[28:31], v15, s[40:43], 0 offen offset:1024 sc1\n" \
  "v_mul_u32_u24 v1, 1184, v10\n" \
  "v_lshl_add_u32 v1, v11, 4, v1\n" \
  "v_add_u32 v1, 120832, v1\n" \
  "v_lshrrev_b32 v2, 3, v12\n" \
  "v_mul_u32_u24 v2, 320, v2\n" \
  "v_and_b32 v4, 7, %[t]\n" \
  "v_lshl_add_u32 v2, v4, 1, v2\n" \
  "s_lshl4_add_u32 s46, %[w], 141312\n" \
  "v_add_u32 v2, s46, v2\n" \
  "s_waitcnt vmcnt(0)\n" \
  "v_bitop3_b32 v16, v23, v27, s44 bitop3:0x7e\n" \
  "v_xor_b32 v31, s44, v31\n" \
  "v_or_b32 v16, v16, v31\n" \
  "v_cmp_ne_u32 vcc, 0, v16\n" \
  "s_cbranch_vccz Ehx%=\n" \
  "s_sub_u32 s97, s97, 1\n" \
  "s_cbranch_scc0 Ehr%=\n" \
  "Ehx%=:\n" \
  GSTAMP10 \
  "ds_write2_b32 v36, v20, v24 offset0:0 offset1:96\n" \
  "ds_write2_b32 v36, v21, v25 offset0:1 offset1:97\n" \
  "ds_write2_b32 v36, v22, v26 offset0:2 offset1:98\n" \
  "ds_write2_b32 v36, v28, v29 offset0:192 offset1:193\n" \
  "ds_write_b32 v36, v30 offset:776\n" \
  "Eg0%=:\n" \
  "s_waitcnt lgkmcnt(0)\n" \
  "s_barrier\n" \
  GSTAMP5 \
  "ds_read_b128 v[112:115], v1 offset:0\n" \
  "ds_read_b128 v[116:119], v1 offset:64\n" \
  "ds_read_b128 v[120:123], v1 offset:128\n" \
  "ds_read_b128 v[124:127], v1 offset:192\n" \
  "ds_read_b128 v[20:23], v1 offset:256\n" \
  "ds_read_b128 v[24:27], v1 offset:320\n" \
  "ds_read_b128 v[28:31], v1 offset:384\n" \
  "ds_read_b128 v[32:35], v1 offset:448\n" \
  "s_waitcnt lgkmcnt(7)\n" \
  "v_mfma_f32_16x16x32_bf16 v[16:19], v[112:115], v[40:43], 0\n" \
  "ds_read_b128 v[112:115], v1 offset:512\n" \
  "s_waitcnt lgkmcnt(7)\n" \
  "v_mfma_f32_16x16x32_bf16 v[16:19], v[116:119], v[44:47], v[16:19]\n" \
  "ds_read_b128 v[116:119], v1 offset:576\n" \
  "s_waitcnt lgkmcnt(7)\n" \
  "v_mfma_f32_16x16x32_bf16 v[16:19], v[120:123], v[48:51], v[16:19]\n" \
  "ds_read_b128 v[120:123], v1 offset:640\n" \
  "s_waitcnt lgkmcnt(7)\n" \
  "v_mfma_f32_16x16x32_bf16 v[16:19], v[124:127], v[52:55], v[16:19]\n" \
  "ds_read_b128 v[124:127], v1 offset:704\n" \
  "s_waitcnt lgkmcnt(7)\n" \
  "v_mfma_f32_16x16x32_bf16 v[16:19], v[20:23], v[56:59], v[16:19]\n" \
  "ds_read_b128 v[20:23], v1 offset:768\n" \
  "s_waitcnt lgkmcnt(7)\n" \
  "v_mfma_f32_16x16x32_bf16 v[16:19], v[24:27], v[60:63], v[16:19]\n" \
  "ds_read_b128 v[24:27], v1 offset:832\n" \
  "s_waitcnt lgkmcnt(7)\n" \
  "v_mfma_f32_16x16x32_bf16 v[16:19], v[28:31], v[64:67], v[16:19]\n" \
  "ds_read_b128 v[28:31], v1 offset:896\n" \
  "s_waitcnt lgkmcnt(7)\n" \
  "v_mfma_f32_16x16x32_bf16 v[16:19], v[32:35], v[68:71], v[16:19]\n" \
  "ds_read_b128 v[32:35], v1 offset:960\n" \
  "s_waitcnt lgkmcnt(7)\n" \
  "v_mfma_f32_16x16x32_bf16 v[16:19], v[112:115], v[72:75], v[16:19]\n" \
  "ds_read_b128 v[112:115], v1 offset:1024\n" \
  "s_waitcnt lgkmcnt(7)\n" \
  "v_mfma_f32_16x16x32_bf16 v[16:19], v[116:119], v[76:79], v[16:19]\n" \
  "ds_read_b128 v[116:119], v1 offset:1088\n" \
  "s_waitcnt lgkmcnt(7)\n" \
  "v_mfma_f32_16x16x32_bf16 v[16:19], v[120:123], v[80:83], v[16:19]\n" \
  "s_waitcnt lgkmcnt(6)\n" \
  "v_mfma_f32_16x16x32_bf16 v[16:19], v[124:127], v[84:87], v[16:19]\n" \
  "s_waitcnt lgkmcnt(5)\n" \
  "v_mfma_f32_16x16x32_bf16 v[16:19], v[20:23], v[88:91], v[16:19]\n" \
  "s_waitcnt lgkmcnt(4)\n" \
  "v_mfma_f32_16x16x32_bf16 v[16:19], v[24:27], v[92:95], v[16:19]\n" \
  "s_waitcnt lgkmcnt(3)\n" \
  "v_mfma_f32_16x16x32_bf16 v[16:19], v[28:31], v[96:99], v[16:19]\n" \
  "s_waitcnt lgkmcnt(2)\n" \
  "v_mfma_f32_16x16x32_bf16 v[16:19], v[32:35], v[100:103], v[16:19]\n" \
  "s_waitcnt lgkmcnt(1)\n" \
  "v_mfma_f32_16x16x32_bf16 v[16:19], v[112:115], v[104:107], v[16:19]\n" \
  "s_waitcnt lgkmcnt(0)\n" \
  "v_mfma_f32_16x16x32_bf16 v[16:19], v[116:119], v[108:111], v[16:19]\n" \
  "s_nop 7\n" \
  "v_mov_b64 v[20:21], v[16:17]\n" \
  "v_mov_b32_dpp v16, v18 row_ror:8 row_mask:0xf bank_mask:0xc\n" \
  "v_mov_b32_dpp v17, v19 row_ror:8 row_mask:0xf bank_mask:0xc\n" \
  "v_mov_b32_dpp v18, v20 row_ror:8 row_mask:0xf bank_mask:0x3\n" \
  "v_mov_b32_dpp v19, v21 row_ror:8 row_mask:0xf bank_mask:0x3\n" \
  "v_cvt_pk_bf16_f32 v16, 0, v16\n" \
  "v_cvt_pk_bf16_f32 v17, 0, v17\n" \
  "v_mul_f32 v20, 0xbfb8aa3b, v16\n" \
  "v_mul_f32 v21, 0xbfb8aa3b, v17\n" \
  "v_exp_f32 v20, v20\n" \
  "v_exp_f32 v21, v21\n" \
  "v_cvt_pk_bf16_f32 v18, 0, v18\n" \
  "v_pk_add_f32 v[20:21], v[20:21], 1.0 op_sel_hi:[1,0]\n" \
  "v_rcp_f32 v20, v20\n" \
  "v_rcp_f32 v21, v21\n" \
  "v_cvt_pk_bf16_f32 v19, 0, v19\n" \
  "v_pk_mul_f32 v[16:17], v[16:17], v[20:21]\n" \
  "v_cvt_pk_bf16_f32 v16, 0, v16\n" \
  "v_cvt_pk_bf16_f32 v17, 0, v17\n" \
  "v_pk_mul_f32 v[16:17], v[16:17], v[18:19]\n" \
  "v_cvt_pk_bf16_f32 v16, v16, v17\n" \
  "ds_write_b16 v2, v16\n" \
  "ds_write_b16_d16_hi v2, v16 offset:160\n" \
  GSTAMP11 \
  "s_setprio 0\n" \
  "s_waitcnt lgkmcnt(0)\n" \
  "s_barrier\n" \
  "s_cmp_gt_u32 %[w], 2\n" \
  "s_cbranch_scc1 Ez%=\n" \
  "s_lshr_b32 s46, s57, 4\n" \
  "v_bfe_u32 v1, %[t], 3, 1\n" \
  "v_lshl_add_u32 v1, v1, 3, v3\n" \
  "s_lshl_b32 s46, s46, 4\n" \
  "v_add_u32 v1, s46, v1\n" \
  "ds_read_b64 v[4:5], v1 offset:32\n" \
  "s_and_b32 s46, s57, 15\n" \
  "s_mul_i32 s46, s46, 12\n" \
  "s_lshl2_add_u32 s46, %[w], s46\n" \
  "v_and_b32 v2, 3, %[t]\n" \
  "v_add_u32 v6, s46, v2\n" \
  "v_lshrrev_b32 v7, 5, v6\n" \
  "v_lshl_add_u32 v6, v7, 6, v6\n" \
  "v_min_u32 v7, 2, v11\n" \
  "v_lshl_add_u32 v6, v7, 5, v6\n" \
  "v_mul_u32_u24 v7, 683, v6\n" \
  "v_lshrrev_b32 v7, 11, v7\n" \
  "v_mul_u32_u24 v8, 3, v7\n" \
  "v_sub_u32 v8, v6, v8\n" \
  "v_and_b32 v9, 1, v7\n" \
  "v_sub_u32 v8, v8, v9\n" \
  "v_min_u32 v8, 2, v8\n" \
  "v_mul_u32_u24 v7, 16512, v7\n" \
  "v_lshlrev_b32 v9, 3, v6\n" \
  "s_waitcnt lgkmcnt(0)\n" \
  "v_not_b32 v13, v5\n" \
  "v_and_b32 v13, 0x7ff, v13\n" \
  "v_lshrrev_b32 v14, 1, v13\n" \
  "v_lshl_add_u32 v7, v14, 4, v7\n" \
  "v_lshrrev_b32 v14, 4, v13\n" \
  "v_lshlrev_b32 v14, 2, v14\n" \
  "v_mul_u32_u24 v4, 1184, v4\n" \
  "v_lshl_add_u32 v4, v6, 1, v4\n" \
  "v_add_u32 v4, 101888, v4\n" \
  "v_mov_b32 v15, 0\n" \
  "ds_read_u16_d16_hi v15, v4\n" \
  "s_mov_b32 s90, 139264\n" \
  "s_mov_b32 s91, 136192\n" \
  "s_mov_b32 s86, 27136\n" \
  "s_mov_b32 s87, 54784\n" \
  "s_mov_b32 s88, 82432\n" \
  "buffer_load_dwordx4 v[16:19], v7, s[40:43], s90 offen sc1\n" \
  "buffer_load_dword v20, v14, s[40:43], s91 offen sc1\n" \
  "buffer_load_dwordx2 v[22:23], v9, s[40:43], s86 offen sc1\n" \
  "buffer_load_dwordx2 v[24:25], v9, s[40:43], s87 offen sc1\n" \
  "buffer_load_dwordx2 v[26:27], v9, s[40:43], s88 offen sc1\n" \
  "v_lshrrev_b32 v28, 2, v12\n" \
  "v_mul_u32_u24 v28, 192, v28\n" \
  "v_lshl_add_u32 v28, v2, 4, v28\n" \
  "s_mul_i32 s46, s57, 12288\n" \
  "s_lshl_b32 s47, %[w], 6\n" \
  "s_add_u32 s46, s46, s47\n" \
  "s_add_u32 s46, s46, 6455296\n" \
  "s_add_u32 s47, s46, 6144\n" \
  ".if " STR(FSLP) "\n" \
  "s_sleep " STR(FSLP) "\n" \
  ".endif\n" \
  GSTAMP12 \
  "buffer_load_dwordx4 v[40:43], v28, s[40:43], s46 offen sc1\n" \
  "buffer_load_dwordx4 v[44:47], v28, s[40:43], s46 offen offset:3072 sc1\n" \
  "buffer_load_dwordx4 v[48:51], v28, s[40:43], s47 offen sc1\n" \
  "buffer_load_dwordx4 v[52:55], v28, s[40:43], s47 offen offset:3072 sc1\n" \
  "s_waitcnt vmcnt(4)\n" \
  "v_bitop3_b32 v21, v19, v20, s44 bitop3:0x7e\n" \
  "v_bitop3_b32 v30, v23, v25, s44 bitop3:0x7e\n" \
  "v_xor_b32 v29, s44, v27\n" \
  "v_or3_b32 v21, v21, v30, v29\n" \
  "v_cmp_ne_u32 vcc, 0, v21\n" \
  "s_cbranch_vccz Efx%=\n" \
  "s_movk_i32 s97, 0x7fff\n" \
  "Efr%=:\n" \
  "buffer_load_dwordx4 v[16:19], v7, s[40:43], s90 offen sc1\n" \
  "buffer_load_dword v20, v14, s[40:43], s91 offen sc1\n" \
  "buffer_load_dwordx2 v[22:23], v9, s[40:43], s86 offen sc1\n" \
  "buffer_load_dwordx2 v[24:25], v9, s[40:43], s87 offen sc1\n" \
  "buffer_load_dwordx2 v[26:27], v9, s[40:43], s88 offen sc1\n" \
  "s_waitcnt vmcnt(0)\n" \
  "v_bitop3_b32 v21, v19, v20, s44 bitop3:0x7e\n" \
  "v_bitop3_b32 v30, v23, v25, s44 bitop3:0x7e\n" \
  "v_xor_b32 v29, s44, v27\n" \
  "v_or3_b32 v21, v21, v30, v29\n" \
  "v_cmp_ne_u32 vcc, 0, v21\n" \
  "s_cbranch_vccz Efx%=\n" \
  "s_sub_u32 s97, s97, 1\n" \
  "s_cbranch_scc0 Efr%=\n" \
  "Efx%=:\n" \
  "v_add_f32 v22, v22, v24\n" \
  "v_add_f32 v22, v22, v26\n" \
  "v_cvt_pk_bf16_f32 v22, 0, v22\n" \
  "v_and_b32 v20, 1, v13\n" \
  "v_cmp_eq_u32 vcc, 1, v20\n" \
  "v_cmp_eq_u32 s[94:95], 2, v8\n" \
  "v_mul_u32_u24 v13, 1152, v13\n" \
  "v_cndmask_b32 v16, v16, v17, vcc\n" \
  "v_cndmask_b32 v8, v8, v20, s[94:95]\n" \
  "v_cndmask_b32 v16, v16, v18, s[94:95]\n" \
  "v_lshlrev_b32 v8, 4, v8\n" \
  "v_lshrrev_b32 v16, v8, v16\n" \
  "v_lshlrev_b32 v16, 16, v16\n" \
  "v_lshl_add_u32 v13, v6, 1, v13\n" \
  "v_ashrrev_i32 v17, 31, v5\n" \
  "v_not_b32 v17, v17\n" \
  "v_or_b32 v17, 0x80000000, v17\n" \
  "v_xor_b32 v5, v5, v17\n" \
  "v_and_b32 v5, 0xfffff800, v5\n" \
  "v_cvt_pk_bf16_f32 v5, 0, v5\n" \
  "v_mul_f32 v5, 0xbfb8aa3b, v5\n" \
  "v_exp_f32 v5, v5\n" \
  "v_and_b32 v17, 2, v10\n" \
  "v_add_f32 v5, 1.0, v5\n" \
  "v_rcp_f32 v5, v5\n" \
  "v_cmp_eq_u32 vcc, 0, v17\n" \
  "v_cvt_pk_bf16_f32 v5, 0, v5\n" \
  "v_and_b32 v17, 1, v10\n" \
  "v_lshlrev_b32 v17, 4, v17\n" \
  "v_mov_b32 v18, 0x3f80\n" \
  "v_lshlrev_b32 v17, v17, v18\n" \
  "v_cndmask_b32 v17, 0, v17, vcc\n" \
  "v_lshrrev_b32 v18, 2, v10\n" \
  "v_cmp_eq_u32 vcc, 0, v18\n" \
  "s_nop 1\n" \
  "v_cndmask_b32 v32, 0, v17, vcc\n" \
  "v_cmp_eq_u32 vcc, 1, v18\n" \
  "s_nop 1\n" \
  "v_cndmask_b32 v33, 0, v17, vcc\n" \
  "v_cmp_eq_u32 vcc, 2, v18\n" \
  "s_nop 1\n" \
  "v_cndmask_b32 v34, 0, v17, vcc\n" \
  "v_mov_b32 v35, 0\n" \
  "s_waitcnt vmcnt(0) lgkmcnt(0)\n" \
  "v_xor_b32 v43, s44, v43\n" \
  "v_xor_b32 v47, s44, v47\n" \
  "v_xor_b32 v51, s44, v51\n" \
  "v_xor_b32 v55, s44, v55\n" \
  "v_or3_b32 v17, v43, v47, v51\n" \
  "v_or_b32 v17, v17, v55\n" \
  "v_cmp_ne_u32 vcc, 0, v17\n" \
  "s_cbranch_vccnz Eps%=\n" \
  "Epb%=:\n" \
  GSTAMP13 \
  "v_mfma_f32_16x16x32_bf16 v[24:27], v[32:35], v[40:43], 0\n" \
  "v_mfma_f32_16x16x32_bf16 v[24:27], v[32:35], v[44:47], v[24:27]\n" \
  "v_mfma_f32_16x16x32_bf16 v[24:27], v[32:35], v[48:51], v[24:27]\n" \
  "v_mfma_f32_16x16x32_bf16 v[24:27], v[32:35], v[52:55], v[24:27]\n" \
  "s_mov_b32 s46, 0x0f0f0f0f\n" \
  "s_mov_b32 s47, 0x0f0f\n" \
  "s_nop 5\n" \
  "v_add_f32_dpp v17, v24, v24 row_ror:8 row_mask:0xf bank_mask:0x3\n" \
  "v_add_f32_dpp v17, v25, v25 row_ror:8 row_mask:0xf bank_mask:0xc\n" \
  "s_nop 1\n" \
  "v_add_f32_dpp v17, v17, v17 row_shl:4 row_mask:0xf bank_mask:0xf bound_ctrl:1\n" \
  "v_cvt_pk_bf16_f32 v17, 0, v17\n" \
  "v_mul_f32 v17, v17, v5\n" \
  "v_cvt_pk_bf16_f32 v17, 0, v17\n" \
  "v_add_f32 v17, v17, v15\n" \
  "v_cvt_pk_bf16_f32 v17, 0, v17\n" \
  "v_mul_f32 v17, v17, v22\n" \
  "v_cvt_pk_bf16_f32 v17, 0, v17\n" \
  "v_add_f32 v17, v17, v16\n" \
  "v_cvt_pk_bf16_f32 v17, 0, v17\n" \
  "s_mov_b64 exec, s[46:47]\n" \
  "global_store_short_d16_hi v13, v17, s[84:85]\n" \
  "s_or_b32 s46, s57, %[w]\n" \
  "s_cbranch_scc1 Ez%=\n" \
  "s_mov_b64 exec, 1\n" \
  "v_mov_b32 v17, s44\n" \
  "buffer_store_dword v17, off, s[40:43], 0 sc1\n" \
  "s_branch Ez%=\n" \
  "Eps%=:\n" \
  "s_movk_i32 s97, 0x7fff\n" \
  "Epr%=:\n" \
  GSTAMPC \
  "s_mov_b64 exec, vcc\n" \
  "buffer_load_dwordx4 v[40:43], v28, s[40:43], s46 offen sc1\n" \
  "buffer_load_dwordx4 v[44:47], v28, s[40:43], s46 offen offset:3072 sc1\n" \
  "buffer_load_dwordx4 v[48:51], v28, s[40:43], s47 offen sc1\n" \
  "buffer_load_dwordx4 v[52:55], v28, s[40:43], s47 offen offset:3072 sc1\n" \
  "s_waitcnt vmcnt(0)\n" \
  "v_xor_b32 v43, s44, v43\n" \
  "v_xor_b32 v47, s44, v47\n" \
  "v_xor_b32 v51, s44, v51\n" \
  "v_xor_b32 v55, s44, v55\n" \
  "v_or3_b32 v17, v43, v47, v51\n" \
  "v_or_b32 v17, v17, v55\n" \
  "v_cmp_ne_u32 vcc, 0, v17\n" \
  "s_mov_b64 exec, -1\n" \
  "s_cbranch_vccz Epb%=\n" \
  "s_sub_u32 s97, s97, 1\n" \
  "s_cbranch_scc0 Epr%=\n" \
  "s_branch Epb%=\n" \
  "Eh1%=:\n" \
  "s_setprio 3\n" \
  "v_lshl_add_u32 v1, v10, 2, v13\n" \
  "ds_read_b32 v16, v1\n" \
  "s_mov_b32 s46, 103424\n" \
  "v_lshl_add_u32 v4, v10, 2, v3\n" \
  "v_lshl_add_u32 v5, v11, 4, v3\n" \
  "v_add_u32 v18, 16, v10\n" \
  "s_waitcnt lgkmcnt(0)\n" \
  "v_not_b32 v16, v16\n" \
  "v_and_b32 v16, 0x7ff, v16\n" \
  "v_lshlrev_b32 v1, 3, v16\n" \
  "buffer_load_dwordx2 v[16:17], v1, s[40:43], s46 offen sc1\n" \
  "s_barrier\n" \
  "s_waitcnt vmcnt(0)\n" \
  "v_cmp_ne_u32 vcc, s44, v17\n" \
  "s_cbranch_vccz Eqx%=\n" \
  "s_movk_i32 s97, 0x7fff\n" \
  "Eqr%=:\n" \
  "buffer_load_dwordx2 v[16:17], v1, s[40:43], s46 offen sc1\n" \
  "s_waitcnt vmcnt(0)\n" \
  "v_cmp_ne_u32 vcc, s44, v17\n" \
  "s_cbranch_vccz Eqx%=\n" \
  "s_sub_u32 s97, s97, 1\n" \
  "s_cbranch_scc0 Eqr%=\n" \
  "Eqx%=:\n" \
  "ds_write_b32 v4, v16 offset:96\n" \
  "ds_read_b128 v[20:23], v5 offset:96\n" \
  "v_mov_b32 v17, v16\n" \
  "v_mov_b32 v19, v16\n" \
  "v_mov_b32 v16, 0\n" \
  "s_waitcnt lgkmcnt(0)\n" \
  "v_cmp_gt_u32 s[88:89], v20, v17\n" \
  "v_cmp_gt_u32 s[90:91], v21, v17\n" \
  "v_cmp_gt_u32 s[94:95], v22, v17\n" \
  "v_cmp_gt_u32 s[96:97], v23, v17\n" \
  "v_addc_co_u32 v16, vcc, 0, v16, s[88:89]\n" \
  "v_addc_co_u32 v16, vcc, 0, v16, s[90:91]\n" \
  "v_addc_co_u32 v16, vcc, 0, v16, s[94:95]\n" \
  "v_addc_co_u32 v16, vcc, 0, v16, s[96:97]\n" \
  "v_mov_b32 v17, v16\n" \
  "s_nop 1\n" \
  "v_permlane16_swap_b32 v16, v17\n" \
  "v_add_u32 v16, v16, v17\n" \
  "v_mov_b32 v17, v16\n" \
  "s_nop 1\n" \
  "v_permlane32_swap_b32 v16, v17\n" \
  "v_add_u32 v16, v16, v17\n" \
  "v_cmp_gt_u32 vcc, 8, v16\n" \
  "v_lshl_add_u32 v16, v16, 3, v3\n" \
  "s_mov_b64 exec, vcc\n" \
  "ds_write_b64 v16, v[18:19] offset:32\n" \
  "s_mov_b64 exec, -1\n" \
  GSTAMP11 \
  "s_waitcnt lgkmcnt(0)\n" \
  "s_barrier\n" \
  "s_branch Ez%=\n" \
  "Ed0%=:\n" \
  "v_and_b32 v2, 7, %[t]\n" \
  "v_lshl_add_u32 v2, v2, 3, v3\n" \
  "s_mov_b32 s97, 0x1fffff\n" \
  "Ed1%=:\n" \
  "ds_read_b32 v1, v2 offset:32\n" \
  "s_waitcnt lgkmcnt(0)\n" \
  "v_cmp_eq_u32 vcc, 0, v1\n" \
  "s_cbranch_vccz Ed2%=\n" \
  "s_sleep 1\n" \
  "s_sub_u32 s97, s97, 1\n" \
  "s_cbranch_scc0 Ed1%=\n" \
  "Ed2%=:\n" \
  "s_mov_b32 vcc_lo, 0x00ff00ff\n" \
  "s_mov_b32 vcc_hi, 0xff00ff00\n" \
  "v_cndmask_b32 v1, 32, v1, vcc\n" \
  "v_mul_u32_u24 v1, 160, v1\n" \
  "v_and_b32 v2, 16, %[t]\n" \
  "v_add_u32 v2, 138752, v2\n" \
  "v_add_u32 v1, v1, v2\n" \
  "s_setprio 3\n" \
  GSTAMP11 \
  "s_barrier\n" \
  "ds_read_b128 v[100:103], v1 offset:0\n" \
  "ds_read_b128 v[104:107], v1 offset:32\n" \
  "ds_read_b128 v[108:111], v1 offset:64\n" \
  "ds_read_b128 v[112:115], v1 offset:96\n" \
  "ds_read_b128 v[116:119], v1 offset:128\n" \
  "s_waitcnt lgkmcnt(4)\n" \
  "v_mfma_f32_16x16x32_bf16 v[16:19], v[100:103], v[40:43], 0\n" \
  "v_mfma_f32_16x16x32_bf16 v[20:23], v[100:103], v[44:47], 0\n" \
  "v_mfma_f32_16x16x32_bf16 v[24:27], v[100:103], v[48:51], 0\n" \
  "s_waitcnt lgkmcnt(3)\n" \
  "v_mfma_f32_16x16x32_bf16 v[16:19], v[104:107], v[52:55], v[16:19]\n" \
  "v_mfma_f32_16x16x32_bf16 v[20:23], v[104:107], v[56:59], v[20:23]\n" \
  "v_mfma_f32_16x16x32_bf16 v[24:27], v[104:107], v[60:63], v[24:27]\n" \
  "s_waitcnt lgkmcnt(2)\n" \
  "v_mfma_f32_16x16x32_bf16 v[16:19], v[108:111], v[64:67], v[16:19]\n" \
  "v_mfma_f32_16x16x32_bf16 v[20:23], v[108:111], v[68:71], v[20:23]\n" \
  "v_mfma_f32_16x16x32_bf16 v[24:27], v[108:111], v[72:75], v[24:27]\n" \
  "s_waitcnt lgkmcnt(1)\n" \
  "v_mfma_f32_16x16x32_bf16 v[16:19], v[112:115], v[76:79], v[16:19]\n" \
  "v_mfma_f32_16x16x32_bf16 v[20:23], v[112:115], v[80:83], v[20:23]\n" \
  "v_mfma_f32_16x16x32_bf16 v[24:27], v[112:115], v[84:87], v[24:27]\n" \
  "s_waitcnt lgkmcnt(0)\n" \
  "v_mfma_f32_16x16x32_bf16 v[16:19], v[116:119], v[88:91], v[16:19]\n" \
  "v_mfma_f32_16x16x32_bf16 v[20:23], v[116:119], v[92:95], v[20:23]\n" \
  "v_mfma_f32_16x16x32_bf16 v[24:27], v[116:119], v[96:99], v[24:27]\n" \
  "s_nop 5\n" \
  "v_cvt_pk_bf16_f32 v28, v16, v17\n" \
  "v_cvt_pk_bf16_f32 v32, v18, v19\n" \
  "v_cvt_pk_bf16_f32 v29, v20, v21\n" \
  "v_cvt_pk_bf16_f32 v33, v22, v23\n" \
  "v_cvt_pk_bf16_f32 v30, v24, v25\n" \
  "v_cvt_pk_bf16_f32 v34, v26, v27\n" \
  "buffer_store_dwordx4 v[28:31], v38, s[40:43], s86 offen sc1\n" \
  "buffer_store_dwordx4 v[32:35], v38, s[40:43], s87 offen sc1\n" \
  GSTAMP12 \
  "Ez%=:\n" \
  "s_mov_b64 exec, -1\n"

#define V4(a) "v" #a "0", "v" #a "1", "v" #a "2", "v" #a "3", "v" #a "4", "v" #a "5", "v" #a "6", "v" #a "7", "v" #a "8", "v" #a "9"
#define S4(a) "s" #a "0", "s" #a "1", "s" #a "2", "s" #a "3", "s" #a "4", "s" #a "5", "s" #a "6", "s" #a "7", "s" #a "8", "s" #a "9"
#define WAVE_CLOB "v1", "v2", "v3", "v4", "v5", "v6", "v7", "v8", "v9", V4(1), V4(2), V4(3), V4(4), V4(5), V4(6), V4(7), V4(8), V4(9), V4(10), V4(11), \
                  "v120", "v121", "v122", "v123", "v124", "v125", "v126", "v127", S4(4), S4(5), "s84", "s85", "s86", "s87", "s88", "s89", "s90", "s91", "s92", "s93", "s94", "s95", "s96", "s97", "s98", "s99", "vcc", "scc", "memory"

__global__ void __launch_bounds__(1024) __attribute__((aligned(KALIGN))) mega(const u16*, const u16*, const u16*, const u16*, const u16*, const u16*, const u16*, const u16*, const u16*, const u16*, const u16*,
                                             const u16*, const u16*, u16*, char* ws) {
  TS(0);
  int tid = threadIdx.x, blk = blockIdx.x, wave;
  const auto ka = __builtin_amdgcn_kernarg_segment_ptr();
  asm volatile("s_load_dword s99, %[k], 0x0\n\ts_load_dword s99, %[k], 0x40\n\tv_readfirstlane_b32 %[w], %[t]\n\ts_lshr_b32 %[w], %[w], 6\n\ts_cmpk_lt_u32 %[b], 192\n\ts_cbranch_scc1 Lcc%=\n" EW_HEAD "s_branch Lcm%=\n"
               "Lcc%=:\n.if " STR(ROT) "\nv_add_u32 v0, 768, v0\nv_and_b32 v0, 1023, v0\ns_add_u32 %[w], %[w], 12\ns_and_b32 %[w], %[w], 15\n.endif\n\ts_cmp_lt_u32 %[w], 12\n\ts_cbranch_scc1 Lcg%=\n" SW_HEAD "s_branch Lcs%=\nLcg%=:\n" GW_HEAD GW_BODY "s_branch Lcl%=\nLcs%=:\n" SW_BODY
               "Lcl%=:\n\ts_cmpk_ge_u32 %[b], 128\n\ts_cbranch_scc1 Lce%=\n" LW_ASM "s_branch Lce%=\nLcm%=:\n" EW_BODY
               "Lce%=:\n\ts_waitcnt lgkmcnt(0)"
               : [w] "=&s"(wave) STAMP_IO : [t] "{v0}"(tid), [b] "s"(blk), [k] "s"(ka) : WAVE_CLOB);
  if (blk >= 192) TS(15);
}

extern "C" void kernel_launch(void* const* d_in, const int* in_sizes, int n_in, void* d_out, int out_size, void* d_ws, size_t ws_size, hipStream_t stream) {
  (void)hipFuncSetAttribute((const void*)mega, hipFuncAttributeMaxDynamicSharedMemorySize, LDS_BYTES);
  mega<<<256, 1024, LDS_BYTES, stream>>>((const u16*)d_in[0], (const u16*)d_in[1], (const u16*)d_in[2], (const u16*)d_in[3], (const u16*)d_in[4],
                                         (const u16*)d_in[5], (const u16*)d_in[6], (const u16*)d_in[8], (const u16*)d_in[9], (const u16*)d_in[10],
                                         (const u16*)d_in[11], (const u16*)d_in[12], (const u16*)d_in[13], (u16*)d_out, (char*)d_ws);
}
```

```cpp
#include <hip/hip_runtime.h>

typedef unsigned short u16;
constexpr int LDS_BYTES = 163840;

#define V4(a) "v" #a "0", "v" #a "1", "v" #a "2", "v" #a "3", "v" #a "4", "v" #a "5", "v" #a "6", "v" #a "7", "v" #a "8", "v" #a "9"
#define S4(a) "s" #a "0", "s" #a "1", "s" #a "2", "s" #a "3", "s" #a "4", "s" #a "5", "s" #a "6", "s" #a "7", "s" #a "8", "s" #a "9"
#define WAVE_CLOB "v1", "v2", "v3", "v4", "v5", "v6", "v7", "v8", "v9", V4(1), V4(2), V4(3), V4(4), V4(5), V4(6), V4(7), V4(8), V4(9), V4(10), V4(11), \
                  "v120", "v121", "v122", "v123", "v124", "v125", "v126", "v127", S4(4), S4(5), "s84", "s85", "s86", "s87", "s88", "s89", "s90", "s91", "s92", "s93", "s94", "s95", "s96", "s97", "s98", "s99", "vcc", "scc", "memory"

__global__ void __launch_bounds__(1024) __attribute__((aligned(4096)))
mega(const u16*, const u16*, const u16*, const u16*, const u16*, const u16*, const u16*, const u16*, const u16*, const u16*, const u16*,
     const u16*, const u16*, u16*, char*) {
  int tid = threadIdx.x, blk = blockIdx.x, wave;
  const auto ka = __builtin_amdgcn_kernarg_segment_ptr();
  asm volatile(
    "s_load_dword s99, %[k], 0x0\n"
    "s_load_dword s99, %[k], 0x40\n"
    "v_readfirstlane_b32 %[w], %[t]\n"
    "s_lshr_b32 %[w], %[w], 6\n"
    "s_cmpk_lt_u32 %[b], 192\n"
    "s_cbranch_scc1 Lcc%=\n"
    "s_load_dwordx4 s[52:55], %[k], 0x58\n"
    "s_load_dwordx2 s[40:41], %[k], 0x70\n"
    "s_lshr_b32 s50, %[w], 2\n"
    "s_mov_b32 s42, 10668032\n"
    "s_mov_b32 s43, 0x20000\n"
    "s_cmp_eq_u32 s50, 3\n"
    "s_cbranch_scc1 Eez%=\n"
    "s_cmpk_ge_u32 %[b], 246\n"
    "s_cbranch_scc1 Een%=\n"
    "s_sub_u32 s51, %[b], 192\n"
    "s_mul_i32 s56, s51, 43\n"
    "s_lshr_b32 s56, s56, 7\n"
    "s_mul_i32 s57, s56, 3\n"
    "s_sub_u32 s57, s51, s57\n"
    "s_mul_i32 s51, s57, 192\n"
    "s_cmp_eq_u32 s50, 2\n"
    "s_cbranch_scc1 Eec%=\n"
    "v_bfe_u32 v1, %[t], 3, 5\n"
    "v_add_u32 v2, s51, v1\n"
    "v_mul_u32_u24 v2, 6912, v2\n"
    "v_and_b32 v3, 2, v1\n"
    "v_lshrrev_b32 v1, 1, v1\n"
    "v_and_or_b32 v3, v1, 4, v3\n"
    "v_xor_b32 v3, v3, %[t]\n"
    "v_and_b32 v3, 7, v3\n"
    "s_lshl_b32 s58, s56, 7\n"
    "v_lshl_add_u32 v2, v3, 4, v2\n"
    "v_add_u32 v2, s58, v2\n"
    "s_cmp_lg_u32 s50, 0\n"
    "s_cbranch_scc1 Eeb%=\n"
    "s_lshl_b32 s51, %[w], 10\n"
    "s_add_u32 m0, s51, 73728\n"
    "s_waitcnt lgkmcnt(0)\n"
    "global_load_lds_dwordx4 v2, s[54:55]\n"
    "s_add_u32 m0, m0, 4096\n"
    "v_add_u32 v2, 221184, v2\n"
    "global_load_lds_dwordx4 v2, s[54:55]\n"
    "s_add_u32 m0, m0, 4096\n"
    "v_add_u32 v2, 221184, v2\n"
    "global_load_lds_dwordx4 v2, s[54:55]\n"
    "s_add_u32 m0, m0, 4096\n"
    "v_add_u32 v2, 221184, v2\n"
    "global_load_lds_dwordx4 v2, s[54:55]\n"
    "s_add_u32 m0, m0, 4096\n"
    "v_add_u32 v2, 221184, v2\n"
    "global_load_lds_dwordx4 v2, s[54:55]\n"
    "s_add_u32 m0, m0, 4096\n"
    "v_add_u32 v2, 221184, v2\n"
    "global_load_lds_dwordx4 v2, s[54:55]\n"
    "s_branch Eeb%=\n"
    "Een%=:\n"
    "s_mov_b32 s51, 0\n"
    "s_cmp_eq_u32 s50, 2\n"
    "s_cbranch_scc1 Eec%=\n"
    "s_cmpk_lt_u32 %[b], 248\n"
    "s_cbranch_scc1 Eeb%=\n"
    "s_cmp_lg_u32 s50, 0\n"
    "s_cbranch_scc1 Eeb%=\n"
    "s_getpc_b64 s[48:49]\n"
    "Epc%=:\n"
    "v_lshlrev_b32 v1, 7, %[t]\n"
    "v_min_u32 v1, Ez%=-Epc%=, v1\n"
    "s_mov_b32 m0, 160032\n"
    "s_nop 0\n"
    "global_load_lds_dword v1, s[48:49]\n"
    "s_branch Eeb%=\n"
    "Eez%=:\n"
    "v_and_b32 v5, 63, %[t]\n"
    "v_mov_b32 v6, 159488\n"
    "v_lshl_add_u32 v6, v5, 2, v6\n"
    "v_mov_b32 v8, 0\n"
    "ds_write_b32 v6, v8\n"
    "s_branch Eeb%=\n"
    "Eec%=:\n"
    "v_and_b32 v5, 255, %[t]\n"
    "v_min_u32 v5, 191, v5\n"
    "v_add_lshl_u32 v6, v5, s51, 1\n"
    "s_waitcnt lgkmcnt(0)\n"
    "global_load_short_d16_hi v7, v6, s[52:53]\n"
    "buffer_load_dword v8, off, s[40:43], 0 sc1\n"
    "Eeb%=:\n"
    "s_branch Lcm%=\n"
    "Lcc%=:\n"
    "s_cmp_lt_u32 %[w], 12\n"
    "s_cbranch_scc1 Lcg%=\n"
    "s_load_dwordx2 s[46:47], %[k], 0x0\n"
    "s_load_dwordx2 s[48:49], %[k], 0x30\n"
    "s_load_dwordx2 s[40:41], %[k], 0x70\n"
    "s_setprio 3\n"
    "s_and_b32 s50, %[w], 3\n"
    "s_mul_i32 s51, %[b], 9216\n"
    "s_mul_i32 s52, s50, 288\n"
    "s_add_u32 s51, s51, s52\n"
    "v_bfe_u32 v1, %[t], 3, 3\n"
    "v_and_b32 v2, 7, %[t]\n"
    "v_mul_u32_u24 v3, 1152, v1\n"
    "v_mul_u32_u24 v4, 12, v2\n"
    "v_add3_u32 v3, v3, v4, s51\n"
    "v_and_b32 v5, 255, %[t]\n"
    "v_mov_b32 v6, 158464\n"
    "v_lshl_add_u32 v6, v5, 3, v6\n"
    "v_mov_b64 v[8:9], 0\n"
    "ds_write_b64 v6, v[8:9]\n"
    "s_mov_b32 s42, 10668032\n"
    "s_mov_b32 s43, 0x20000\n"
    "s_mul_i32 s52, %[b], 6\n"
    "s_and_b32 s52, s52, -4\n"
    "s_cmp_lt_u32 %[b], 128\n"
    "s_waitcnt lgkmcnt(0)\n"
    "s_memrealtime s[58:59]\n"
    "s_sleep 1\n"
    "global_load_dwordx3 v[16:18], v3, s[46:47] offset:0\n"
    "global_load_dwordx3 v[20:22], v3, s[46:47] offset:96\n"
    "global_load_dwordx3 v[24:26], v3, s[46:47] offset:192\n"
    "buffer_load_dword v7, off, s[40:43], 0 sc1\n"
    "s_load_dwordx2 s[54:55], s[48:49], s52\n"
    "s_cbranch_scc1 Lone%=\n"
    "v_add_u32 v3, 589824, v3\n"
    "global_load_dwordx3 v[28:30], v3, s[46:47] offset:0\n"
    "global_load_dwordx3 v[32:34], v3, s[46:47] offset:96\n"
    "global_load_dwordx3 v[36:38], v3, s[46:47] offset:192\n"
    "Lone%=:\n"
    "s_barrier\n"
    "s_branch Lcs%=\n"
    "Lcg%=:\n"
    "s_load_dwordx8 s[40:47], %[k], 0x8\n"
    "s_load_dwordx2 s[48:49], %[k], 0x28\n"
    "s_setprio 1\n"
    "s_lshr_b32 s50, %[w], 2\n"
    "s_and_b32 s54, %[b], 1\n"
    "s_add_u32 s54, s54, s50\n"
    "s_cmp_eq_u32 s54, 3\n"
    "s_cselect_b32 s54, 0, s54\n"
    "s_mul_i32 s53, %[b], 3\n"
    "s_add_u32 s53, s53, s54\n"
    "s_add_u32 s52, %[w], s50\n"
    "s_lshl_b32 s55, s52, 7\n"
    "s_and_b32 s55, s55, 128\n"
    "s_lshl_b32 s54, s53, 8\n"
    "s_add_u32 s54, s54, s55\n"
    "v_and_b32 v9, 63, %[t]\n"
    "v_lshl_add_u32 v17, v9, 1, s54\n"
    "v_mov_b64 v[108:109], 0\n"
    "v_mov_b64 v[110:111], 0\n"
    "s_mul_i32 s57, s50, 32096\n"
    "s_lshl1_add_u32 m0, s55, s57\n"
    "s_lshl_b32 s84, s53, 1\n"
    "s_and_b32 s84, s84, -4\n"
    "s_cmp_ge_u32 s50, 2\n"
    "s_cbranch_scc1 Lnc%=\n"
    "s_setprio 3\n"
    "s_bitcmp1_b32 s52, 1\n"
    "s_waitcnt lgkmcnt(0)\n"
    "global_load_short_d16_hi v108, v17, s[40:41]\n"
    "global_load_short_d16_hi v109, v17, s[42:43]\n"
    "s_load_dword s98, s[48:49], s84\n"
    "s_cbranch_scc1 Lnd%=\n"
    "global_load_short_d16_hi v110, v17, s[44:45]\n"
    "global_load_short_d16_hi v111, v17, s[46:47]\n"
    "Lnd%=:\n"
    "s_setprio 1\n"
    "Lnc%=:\n"
    "s_and_b32 s51, %[w], 3\n"
    "s_lshl_b32 s58, s50, 13\n"
    "s_add_u32 s58, s58, 96288\n"
    "v_and_b32 v1, 15, %[t]\n"
    "v_and_b32 v2, 48, %[t]\n"
    "v_mov_b64 v[12:13], 0\n"
    "v_mov_b64 v[14:15], 0\n"
    "v_and_b32 v5, 255, %[t]\n"
    "v_lshl_add_u32 v5, v5, 4, s58\n"
    "ds_write_b128 v5, v[12:15] offset:4096\n"
    "v_min_u32 v4, 1, v9\n"
    "v_mul_u32_u24 v4, 40, v4\n"
    "v_add_u32 v4, v4, v9\n"
    "v_mov_b32 v5, 159488\n"
    "v_lshl_add_u32 v4, v4, 2, v5\n"
    "s_movk_i32 s54, 544\n"
    "v_mad_u32_u24 v5, v1, s54, v2\n"
    "s_and_b32 s54, s51, 1\n"
    "s_mul_i32 s55, s54, 8704\n"
    "s_add_u32 s55, s55, 14688\n"
    "s_cmp_lt_u32 s51, 2\n"
    "s_cselect_b32 s56, 0, s55\n"
    "s_cselect_b32 s55, s55, 8704\n"
    "s_cselect_b32 s85, 16, 64\n"
    "s_cselect_b32 s86, 512, 16\n"
    "s_cselect_b32 s87, 8, 5\n"
    "s_cselect_b32 s88, -2912, 0\n"
    "v_add_u32 v5, s57, v5\n"
    "v_add_u32 v6, s55, v5\n"
    "v_add_u32 v5, s56, v5\n"
    "s_lshl_b32 s54, s54, s87\n"
    "s_add_u32 s54, s54, s88\n"
    "s_add_u32 s54, s54, s58\n"
    "v_lshrrev_b32 v7, 1, v2\n"
    "v_and_b32 v7, 8, v7\n"
    "v_mad_u32_u24 v7, v1, s85, v7\n"
    "v_lshrrev_b32 v9, 5, v9\n"
    "v_mad_u32_u24 v7, v9, s86, v7\n"
    "v_add_u32 v7, s54, v7\n"
    "s_lshl_b32 s59, s50, 3\n"
    "s_lshl_b32 s59, 1, s59\n"
    "v_mov_b32 v16, s59\n"
    "s_not_b32 s53, s53\n"
    "s_lshl_b32 s53, s53, 4\n"
    "s_cmp_eq_u32 s50, 0\n"
    "s_cbranch_scc0 Lp1%=\n"
    "s_setprio 2\n"
    "Lp1%=:\n"
    "s_bitcmp1_b32 s52, 1\n"
    "s_cbranch_scc1 Lgb%=\n"
    "s_barrier\n"
    "s_cmp_lt_u32 s50, 2\n"
    "s_cbranch_scc1 Lne1%=\n"
    "s_waitcnt lgkmcnt(0)\n"
    "global_load_short_d16_hi v108, v17, s[40:41]\n"
    "global_load_short_d16_hi v109, v17, s[42:43]\n"
    "s_load_dword s98, s[48:49], s84\n"
    "global_load_short_d16_hi v110, v17, s[44:45]\n"
    "global_load_short_d16_hi v111, v17, s[46:47]\n"
    "Lne1%=:\n"
    "v_lshrrev_b32 v3, 1, v2\n"
    "v_add3_u32 v3, v3, v1, 1\n"
    "v_and_b32 v8, 1, v3\n"
    "v_mul_u32_u24 v8, 1470, v8\n"
    "v_lshl_add_u32 v3, v3, 1, v8\n"
    "s_add_u32 s55, s58, 4544\n"
    "v_add_u32 v3, s55, v3\n"
    "v_lshlrev_b32 v8, 5, v1\n"
    "v_sub_u32 v10, v2, v8\n"
    "s_add_u32 s55, s58, 480\n"
    "v_add_u32 v10, s55, v10\n"
    "v_mul_u32_u24 v11, 20, v2\n"
    "v_lshl_add_u32 v11, v1, 4, v11\n"
    "v_add_u32 v11, s57, v11\n"
    "v_lshlrev_b32 v8, 2, v1\n"
    "v_sub_u32 v8, v8, v2\n"
    "s_add_u32 s55, s58, 7540\n"
    "v_add_u32 v8, s55, v8\n"
    "s_bitcmp0_b32 s51, 0\n"
    "s_cbranch_scc1 Lo1%=\n"
    "s_lshr_b32 s55, s51, 1\n"
    "s_mul_i32 s55, s55, 1470\n"
    "s_add_u32 s55, s55, s58\n"
    "v_lshrrev_b32 v19, 3, v2\n"
    "v_mul_u32_u24 v19, 29, v19\n"
    "v_lshl_add_u32 v19, v1, 1, v19\n"
    "v_add_u32 v19, s55, v19\n"
    "v_mov_b32 v20, 0x3c00\n"
    "s_mov_b32 exec_lo, 0x001f0007\n"
    "s_mov_b32 exec_hi, 0x01ff007f\n"
    "ds_write_b16 v19, v20 offset:5798\n"
    "s_mov_b64 exec, -1\n"
    "Lo1%=:\n"
    "s_lshl_b32 s54, s59, 2\n"
    "v_mov_b32 v9, s57\n"
    "s_sub_u32 s55, s51, s50\n"
    "s_cmp_eq_u32 s51, 3\n"
    "s_cselect_b32 s55, 3, s55\n"
    "s_waitcnt vmcnt(0) lgkmcnt(0)\n"
    "s_lshl_b32 s98, s98, s53\n"
    "s_and_b32 s98, s98, 0xffff0000\n"
    "v_mul_f32 v108, 0x3fb8aa3b, v108\n"
    "v_mov_b32 v112, s98\n"
    "v_mul_f32 v112, 0x3fb8aa3b, v112\n"
    "v_exp_f32 v108, v108\n"
    "v_exp_f32 v112, v112\n"
    "v_max_f32 v122, |v110|, |v111|\n"
    "v_cvt_pk_bf16_f32 v108, 0, v108\n"
    "v_cvt_pk_bf16_f32 v112, 0, v112\n"
    "v_max_u32_dpp v122, v122, v122 quad_perm:[1,0,3,2] row_mask:0xf bank_mask:0xf\n"
    "v_pk_mul_f32 v[108:109], v[108:109], v[112:113] op_sel_hi:[1,0]\n"
    "v_mul_f32 v115, 0x3e22f983, v109\n"
    "v_max_u32_dpp v122, v122, v122 quad_perm:[2,3,0,1] row_mask:0xf bank_mask:0xf\n"
    "v_mul_f32 v117, 0x4222f983, v109\n"
    "v_mul_f32 v114, 0xbfb8aa3b, v108\n"
    "v_max_u32_dpp v122, v122, v122 row_half_mirror row_mask:0xf bank_mask:0xf\n"
    "v_mul_f32 v116, 0xc3b8aa3b, v108\n"
    "v_fract_f32 v115, v115\n"
    "v_max_u32_dpp v122, v122, v122 row_mirror row_mask:0xf bank_mask:0xf\n"
    "v_fract_f32 v117, v117\n"
    "v_exp_f32 v114, v114\n"
    "v_max_u32_dpp v122, v122, v122 row_bcast:15 row_mask:0xa bank_mask:0xf\n"
    "v_cos_f32 v118, v115\n"
    "v_sin_f32 v119, v115\n"
    "v_max_u32_dpp v122, v122, v122 row_bcast:31 row_mask:0xc bank_mask:0xf\n"
    "v_exp_f32 v116, v116\n"
    "v_cos_f32 v120, v117\n"
    "v_readlane_b32 s94, v122, 63\n"
    "v_sin_f32 v121, v117\n"
    "v_pk_mul_f32 v[118:119], v[118:119], v[114:115] op_sel_hi:[1,0]\n"
    "s_lshr_b32 s94, s94, 23\n"
    "v_pk_mul_f32 v[120:121], v[120:121], v[116:117] op_sel_hi:[1,0]\n"
    "s_max_u32 s94, s94, 26\n"
    "s_min_u32 s94, s94, 226\n"
    "v_mov_b32 v113, s94\n"
    "s_sub_u32 s94, 266, s94\n"
    "s_lshl_b32 s94, s94, 23\n"
    "ds_write_addtid_b32 v113 offset:8704\n"
    "ds_write_addtid_b32 v112 offset:10336\n"
    "v_pk_mul_f32 v[110:111], v[110:111], s[94:95] op_sel_hi:[1,0]\n"
    "v_pk_mul_f32 v[104:105], v[110:111], v[118:119] op_sel:[1,1] op_sel_hi:[1,0] neg_lo:[0,1]\n"
    "v_pk_mul_f32 v[106:107], v[110:111], v[120:121] op_sel:[1,1] op_sel_hi:[1,0] neg_lo:[0,1]\n"
    "v_cvt_pk_f16_f32 v112, v110, v111\n"
    "v_pk_fma_f32 v[114:115], v[110:111], v[118:119], v[104:105] op_sel_hi:[0,1,1]\n"
    "v_pk_fma_f32 v[116:117], v[110:111], v[120:121], v[106:107] op_sel_hi:[0,1,1]\n"
    "ds_write_addtid_b32 v112 offset:0\n"
    "v_pk_mul_f32 v[104:105], v[114:115], v[118:119] op_sel:[1,1] op_sel_hi:[1,0] neg_lo:[0,1]\n"
    "v_cvt_pk_f16_f32 v112, v114, v115\n"
    "v_cvt_pk_f16_f32 v103, v116, v117\n"
    "v_pk_fma_f32 v[114:115], v[114:115], v[118:119], v[104:105] op_sel_hi:[0,1,1]\n"
    "ds_write_addtid_b32 v112 offset:544\n"
    "ds_write_addtid_b32 v103 offset:4352\n"
    "ds_write_addtid_b32 v103 offset:4896\n"
    "v_pk_mul_f32 v[104:105], v[114:115], v[118:119] op_sel:[1,1] op_sel_hi:[1,0] neg_lo:[0,1]\n"
    "v_pk_mul_f32 v[106:107], v[114:115], v[120:121] op_sel:[1,1] op_sel_hi:[1,0] neg_lo:[0,1]\n"
    "v_cvt_pk_f16_f32 v112, v114, v115\n"
    "v_pk_fma_f32 v[122:123], v[114:115], v[118:119], v[104:105] op_sel_hi:[0,1,1]\n"
    "v_pk_fma_f32 v[116:117], v[114:115], v[120:121], v[106:107] op_sel_hi:[0,1,1]\n"
    "ds_write_addtid_b32 v112 offset:1088\n"
    "v_pk_mul_f32 v[104:105], v[122:123], v[118:119] op_sel:[1,1] op_sel_hi:[1,0] neg_lo:[0,1]\n"
    "v_cvt_pk_f16_f32 v112, v122, v123\n"
    "v_cvt_pk_f16_f32 v103, v116, v117\n"
    "v_pk_fma_f32 v[114:115], v[122:123], v[118:119], v[104:105] op_sel_hi:[0,1,1]\n"
    "ds_write_addtid_b32 v112 offset:1632\n"
    "ds_write_addtid_b32 v103 offset:5440\n"
    "v_pk_mul_f32 v[104:105], v[114:115], v[118:119] op_sel:[1,1] op_sel_hi:[1,0] neg_lo:[0,1]\n"
    "v_pk_mul_f32 v[106:107], v[114:115], v[120:121] op_sel:[1,1] op_sel_hi:[1,0] neg_lo:[0,1]\n"
    "v_cvt_pk_f16_f32 v112, v114, v115\n"
    "v_pk_fma_f32 v[122:123], v[114:115], v[118:119], v[104:105] op_sel_hi:[0,1,1]\n"
    "v_pk_fma_f32 v[116:117], v[114:115], v[120:121], v[106:107] op_sel_hi:[0,1,1]\n"
    "ds_write_addtid_b32 v112 offset:2176\n"
    "v_pk_mul_f32 v[104:105], v[122:123], v[118:119] op_sel:[1,1] op_sel_hi:[1,0] neg_lo:[0,1]\n"
    "v_pk_mul_f32 v[106:107], v[116:117], v[120:121] op_sel:[1,1] op_sel_hi:[1,0] neg_lo:[0,1]\n"
    "v_cvt_pk_f16_f32 v112, v122, v123\n"
    "v_cvt_pk_f16_f32 v103, v116, v117\n"
    "v_pk_fma_f32 v[114:115], v[122:123], v[118:119], v[104:105] op_sel_hi:[0,1,1]\n"
    "v_pk_fma_f32 v[116:117], v[116:117], v[120:121], v[106:107] op_sel_hi:[0,1,1]\n"
    "ds_write_addtid_b32 v112 offset:2720\n"
    "ds_write_addtid_b32 v103 offset:5984\n"
    "ds_write_addtid_b32 v103 offset:6528\n"
    "ds_write_addtid_b32 v103 offset:7072\n"
    "v_pk_mul_f32 v[104:105], v[114:115], v[118:119] op_sel:[1,1] op_sel_hi:[1,0] neg_lo:[0,1]\n"
    "v_pk_mul_f32 v[106:107], v[116:117], v[120:121] op_sel:[1,1] op_sel_hi:[1,0] neg_lo:[0,1]\n"
    "v_cvt_pk_f16_f32 v112, v114, v115\n"
    "v_cvt_pk_f16_f32 v103, v116, v117\n"
    "v_pk_fma_f32 v[122:123], v[114:115], v[118:119], v[104:105] op_sel_hi:[0,1,1]\n"
    "v_pk_fma_f32 v[116:117], v[116:117], v[120:121], v[106:107] op_sel_hi:[0,1,1]\n"
    "ds_write_addtid_b32 v112 offset:3264\n"
    "ds_write_addtid_b32 v103 offset:9248\n"
    "v_pk_mul_f32 v[104:105], v[122:123], v[118:119] op_sel:[1,1] op_sel_hi:[1,0] neg_lo:[0,1]\n"
    "v_pk_mul_f32 v[106:107], v[114:115], v[120:121] op_sel:[1,1] op_sel_hi:[1,0] neg_lo:[0,1]\n"
    "v_cvt_pk_f16_f32 v112, v122, v123\n"
    "v_cvt_pk_f16_f32 v103, v116, v117\n"
    "v_pk_fma_f32 v[122:123], v[122:123], v[118:119], v[104:105] op_sel_hi:[0,1,1]\n"
    "v_pk_fma_f32 v[116:117], v[114:115], v[120:121], v[106:107] op_sel_hi:[0,1,1]\n"
    "ds_write_addtid_b32 v112 offset:3808\n"
    "ds_write_addtid_b32 v103 offset:9792\n"
    "v_pk_mul_f32 v[104:105], v[122:123], v[120:121] op_sel:[1,1] op_sel_hi:[1,0] neg_lo:[0,1]\n"
    "v_pk_mul_f32 v[106:107], v[120:121], v[120:121] op_sel:[1,1] op_sel_hi:[1,0] neg_lo:[0,1]\n"
    "v_cvt_pk_f16_f32 v112, v116, v117\n"
    "v_pk_fma_f32 v[114:115], v[122:123], v[120:121], v[104:105] op_sel_hi:[0,1,1]\n"
    "v_pk_fma_f32 v[116:117], v[120:121], v[120:121], v[106:107] op_sel_hi:[0,1,1]\n"
    "ds_write_addtid_b32 v112 offset:7616\n"
    "v_pk_mul_f32 v[104:105], v[122:123], v[116:117] op_sel:[1,1] op_sel_hi:[1,0] neg_lo:[0,1]\n"
    "v_pk_mul_f32 v[106:107], v[114:115], v[116:117] op_sel:[1,1] op_sel_hi:[1,0] neg_lo:[0,1]\n"
    "v_cvt_pk_f16_f32 v112, v114, v115\n"
    "v_pk_fma_f32 v[122:123], v[122:123], v[116:117], v[104:105] op_sel_hi:[0,1,1]\n"
    "v_pk_fma_f32 v[114:115], v[114:115], v[116:117], v[106:107] op_sel_hi:[0,1,1]\n"
    "ds_write_addtid_b32 v112 offset:8160\n"
    "v_pk_mul_f32 v[104:105], v[122:123], v[116:117] op_sel:[1,1] op_sel_hi:[1,0] neg_lo:[0,1]\n"
    "v_pk_mul_f32 v[106:107], v[114:115], v[116:117] op_sel:[1,1] op_sel_hi:[1,0] neg_lo:[0,1]\n"
    "v_cvt_pk_f16_f32 v112, v122, v123\n"
    "v_cvt_pk_f16_f32 v103, v114, v115\n"
    "v_pk_fma_f32 v[122:123], v[122:123], v[116:117], v[104:105] op_sel_hi:[0,1,1]\n"
    "v_pk_fma_f32 v[114:115], v[114:115], v[116:117], v[106:107] op_sel_hi:[0,1,1]\n"
    "ds_write_addtid_b32 v112 offset:10880\n"
    "ds_write_addtid_b32 v103 offset:11424\n"
    "v_pk_mul_f32 v[104:105], v[122:123], v[116:117] op_sel:[1,1] op_sel_hi:[1,0] neg_lo:[0,1]\n"
    "v_pk_mul_f32 v[106:107], v[114:115], v[116:117] op_sel:[1,1] op_sel_hi:[1,0] neg_lo:[0,1]\n"
    "v_cvt_pk_f16_f32 v112, v122, v123\n"
    "v_cvt_pk_f16_f32 v103, v114, v115\n"
    "v_pk_fma_f32 v[122:123], v[122:123], v[116:117], v[104:105] op_sel_hi:[0,1,1]\n"
    "v_pk_fma_f32 v[114:115], v[114:115], v[116:117], v[106:107] op_sel_hi:[0,1,1]\n"
    "ds_write_addtid_b32 v112 offset:12512\n"
    "ds_write_addtid_b32 v103 offset:13056\n"
    "v_cvt_pk_f16_f32 v112, v122, v123\n"
    "v_cvt_pk_f16_f32 v103, v114, v115\n"
    "ds_write_addtid_b32 v112 offset:13600\n"
    "ds_write_addtid_b32 v103 offset:14144\n"
    "s_branch Lgk%=\n"
    "Lgb%=:\n"
    "s_barrier\n"
    "s_cmp_lt_u32 s50, 2\n"
    "s_cbranch_scc1 Lne8%=\n"
    "s_waitcnt lgkmcnt(0)\n"
    "global_load_short_d16_hi v108, v17, s[40:41]\n"
    "global_load_short_d16_hi v109, v17, s[42:43]\n"
    "s_load_dword s98, s[48:49], s84\n"
    "Lne8%=:\n"
    "v_lshrrev_b32 v3, 1, v2\n"
    "v_add3_u32 v3, v3, v1, 1\n"
    "v_and_b32 v8, 1, v3\n"
    "v_mul_u32_u24 v8, 1470, v8\n"
    "v_lshl_add_u32 v3, v3, 1, v8\n"
    "s_add_u32 s55, s58, 4544\n"
    "v_add_u32 v3, s55, v3\n"
    "v_lshlrev_b32 v8, 5, v1\n"
    "v_sub_u32 v10, v2, v8\n"
    "s_add_u32 s55, s58, 480\n"
    "v_add_u32 v10, s55, v10\n"
    "v_mul_u32_u24 v11, 20, v2\n"
    "v_lshl_add_u32 v11, v1, 4, v11\n"
    "v_add_u32 v11, s57, v11\n"
    "v_lshlrev_b32 v8, 2, v1\n"
    "v_sub_u32 v8, v8, v2\n"
    "s_add_u32 s55, s58, 7540\n"
    "v_add_u32 v8, s55, v8\n"
    "s_bitcmp0_b32 s51, 0\n"
    "s_cbranch_scc1 Lo8%=\n"
    "s_lshr_b32 s55, s51, 1\n"
    "s_mul_i32 s55, s55, 1470\n"
    "s_add_u32 s55, s55, s58\n"
    "v_lshrrev_b32 v19, 3, v2\n"
    "v_mul_u32_u24 v19, 29, v19\n"
    "v_lshl_add_u32 v19, v1, 1, v19\n"
    "v_add_u32 v19, s55, v19\n"
    "v_mov_b32 v20, 0x3c00\n"
    "s_mov_b32 exec_lo, 0x001f0007\n"
    "s_mov_b32 exec_hi, 0x01ff007f\n"
    "ds_write_b16 v19, v20 offset:5798\n"
    "s_mov_b64 exec, -1\n"
    "Lo8%=:\n"
    "s_lshl_b32 s54, s59, 2\n"
    "v_mov_b32 v9, s57\n"
    "s_sub_u32 s55, s51, s50\n"
    "s_cmp_eq_u32 s51, 3\n"
    "s_cselect_b32 s55, 3, s55\n"
    "s_waitcnt vmcnt(0) lgkmcnt(0)\n"
    "s_lshl_b32 s98, s98, s53\n"
    "s_and_b32 s98, s98, 0xffff0000\n"
    "v_mul_f32 v108, 0x3fb8aa3b, v108\n"
    "v_mov_b32 v112, s98\n"
    "v_mul_f32 v112, 0x3fb8aa3b, v112\n"
    "v_exp_f32 v108, v108\n"
    "v_exp_f32 v112, v112\n"
    "v_mov_b32 v103, 0x3c00\n"
    "v_cvt_pk_bf16_f32 v108, 0, v108\n"
    "v_cvt_pk_bf16_f32 v112, 0, v112\n"
    "v_pk_mul_f32 v[108:109], v[108:109], v[112:113] op_sel_hi:[1,0]\n"
    "v_mul_f32 v115, 0xbfa2f983, v109\n"
    "v_mul_f32 v117, 0xc1a2f983, v109\n"
    "v_mul_f32 v114, 0xc138aa3b, v108\n"
    "v_mul_f32 v116, 0xc338aa3b, v108\n"
    "v_fract_f32 v115, v115\n"
    "v_fract_f32 v117, v117\n"
    "v_exp_f32 v114, v114\n"
    "v_cos_f32 v118, v115\n"
    "v_sin_f32 v119, v115\n"
    "v_exp_f32 v116, v116\n"
    "v_cos_f32 v120, v117\n"
    "v_sin_f32 v121, v117\n"
    "v_pk_mul_f32 v[118:119], v[118:119], v[114:115] op_sel_hi:[1,0]\n"
    "v_pk_mul_f32 v[120:121], v[120:121], v[116:117] op_sel_hi:[1,0]\n"
    "ds_write_addtid_b32 v103 offset:14688\n"
    "v_pk_mul_f32 v[104:105], v[118:119], v[118:119] op_sel:[1,1] op_sel_hi:[1,0] neg_lo:[0,1]\n"
    "v_pk_mul_f32 v[106:107], v[120:121], v[118:119] op_sel:[1,1] op_sel_hi:[1,0] neg_lo:[0,1]\n"
    "v_cvt_pk_f16_f32 v112, v118, v119\n"
    "v_cvt_pk_f16_f32 v103, v120, v121\n"
    "v_pk_fma_f32 v[110:111], v[118:119], v[118:119], v[104:105] op_sel_hi:[0,1,1]\n"
    "v_pk_fma_f32 v[122:123], v[120:121], v[118:119], v[106:107] op_sel_hi:[0,1,1]\n"
    "ds_write_addtid_b32 v112 offset:15232\n"
    "ds_write_addtid_b32 v103 offset:23392\n"
    "v_pk_mul_f32 v[104:105], v[110:111], v[118:119] op_sel:[1,1] op_sel_hi:[1,0] neg_lo:[0,1]\n"
    "v_pk_mul_f32 v[106:107], v[122:123], v[118:119] op_sel:[1,1] op_sel_hi:[1,0] neg_lo:[0,1]\n"
    "v_cvt_pk_f16_f32 v112, v110, v111\n"
    "v_cvt_pk_f16_f32 v103, v122, v123\n"
    "v_pk_fma_f32 v[110:111], v[110:111], v[118:119], v[104:105] op_sel_hi:[0,1,1]\n"
    "v_pk_fma_f32 v[122:123], v[122:123], v[118:119], v[106:107] op_sel_hi:[0,1,1]\n"
    "ds_write_addtid_b32 v112 offset:15776\n"
    "ds_write_addtid_b32 v103 offset:23936\n"
    "v_pk_mul_f32 v[104:105], v[110:111], v[118:119] op_sel:[1,1] op_sel_hi:[1,0] neg_lo:[0,1]\n"
    "v_pk_mul_f32 v[106:107], v[122:123], v[118:119] op_sel:[1,1] op_sel_hi:[1,0] neg_lo:[0,1]\n"
    "v_cvt_pk_f16_f32 v112, v110, v111\n"
    "v_cvt_pk_f16_f32 v103, v122, v123\n"
    "v_pk_fma_f32 v[110:111], v[110:111], v[118:119], v[104:105] op_sel_hi:[0,1,1]\n"
    "v_pk_fma_f32 v[122:123], v[122:123], v[118:119], v[106:107] op_sel_hi:[0,1,1]\n"
    "ds_write_addtid_b32 v112 offset:16320\n"
    "ds_write_addtid_b32 v103 offset:24480\n"
    "v_pk_mul_f32 v[104:105], v[110:111], v[118:119] op_sel:[1,1] op_sel_hi:[1,0] neg_lo:[0,1]\n"
    "v_pk_mul_f32 v[106:107], v[122:123], v[118:119] op_sel:[1,1] op_sel_hi:[1,0] neg_lo:[0,1]\n"
    "v_cvt_pk_f16_f32 v112, v110, v111\n"
    "v_cvt_pk_f16_f32 v103, v122, v123\n"
    "v_pk_fma_f32 v[110:111], v[110:111], v[118:119], v[104:105] op_sel_hi:[0,1,1]\n"
    "v_pk_fma_f32 v[122:123], v[122:123], v[118:119], v[106:107] op_sel_hi:[0,1,1]\n"
    "ds_write_addtid_b32 v112 offset:16864\n"
    "ds_write_addtid_b32 v103 offset:25024\n"
    "v_pk_mul_f32 v[104:105], v[110:111], v[118:119] op_sel:[1,1] op_sel_hi:[1,0] neg_lo:[0,1]\n"
    "v_pk_mul_f32 v[106:107], v[122:123], v[118:119] op_sel:[1,1] op_sel_hi:[1,0] neg_lo:[0,1]\n"
    "v_cvt_pk_f16_f32 v112, v110, v111\n"
    "v_cvt_pk_f16_f32 v103, v122, v123\n"
    "v_pk_fma_f32 v[110:111], v[110:111], v[118:119], v[104:105] op_sel_hi:[0,1,1]\n"
    "v_pk_fma_f32 v[122:123], v[122:123], v[118:119], v[106:107] op_sel_hi:[0,1,1]\n"
    "ds_write_addtid_b32 v112 offset:17408\n"
    "ds_write_addtid_b32 v103 offset:25568\n"
    "v_pk_mul_f32 v[104:105], v[110:111], v[118:119] op_sel:[1,1] op_sel_hi:[1,0] neg_lo:[0,1]\n"
    "v_pk_mul_f32 v[106:107], v[122:123], v[118:119] op_sel:[1,1] op_sel_hi:[1,0] neg_lo:[0,1]\n"
    "v_cvt_pk_f16_f32 v112, v110, v111\n"
    "v_cvt_pk_f16_f32 v103, v122, v123\n"
    "v_pk_fma_f32 v[110:111], v[110:111], v[118:119], v[104:105] op_sel_hi:[0,1,1]\n"
    "v_pk_fma_f32 v[122:123], v[122:123], v[118:119], v[106:107] op_sel_hi:[0,1,1]\n"
    "ds_write_addtid_b32 v112 offset:17952\n"
    "ds_write_addtid_b32 v103 offset:26112\n"
    "v_pk_mul_f32 v[104:105], v[110:111], v[118:119] op_sel:[1,1] op_sel_hi:[1,0] neg_lo:[0,1]\n"
    "v_pk_mul_f32 v[106:107], v[122:123], v[118:119] op_sel:[1,1] op_sel_hi:[1,0] neg_lo:[0,1]\n"
    "v_cvt_pk_f16_f32 v112, v110, v111\n"
    "v_cvt_pk_f16_f32 v103, v122, v123\n"
    "v_pk_fma_f32 v[110:111], v[110:111], v[118:119], v[104:105] op_sel_hi:[0,1,1]\n"
    "v_pk_fma_f32 v[122:123], v[122:123], v[118:119], v[106:107] op_sel_hi:[0,1,1]\n"
    "ds_write_addtid_b32 v112 offset:18496\n"
    "ds_write_addtid_b32 v103 offset:26656\n"
    "v_pk_mul_f32 v[104:105], v[110:111], v[118:119] op_sel:[1,1] op_sel_hi:[1,0] neg_lo:[0,1]\n"
    "v_pk_mul_f32 v[106:107], v[122:123], v[118:119] op_sel:[1,1] op_sel_hi:[1,0] neg_lo:[0,1]\n"
    "v_cvt_pk_f16_f32 v112, v110, v111\n"
    "v_cvt_pk_f16_f32 v103, v122, v123\n"
    "v_pk_fma_f32 v[110:111], v[110:111], v[118:119], v[104:105] op_sel_hi:[0,1,1]\n"
    "v_pk_fma_f32 v[122:123], v[122:123], v[118:119], v[106:107] op_sel_hi:[0,1,1]\n"
    "ds_write_addtid_b32 v112 offset:19040\n"
    "ds_write_addtid_b32 v103 offset:27200\n"
    "v_pk_mul_f32 v[104:105], v[110:111], v[118:119] op_sel:[1,1] op_sel_hi:[1,0] neg_lo:[0,1]\n"
    "v_pk_mul_f32 v[106:107], v[122:123], v[118:119] op_sel:[1,1] op_sel_hi:[1,0] neg_lo:[0,1]\n"
    "v_cvt_pk_f16_f32 v112, v110, v111\n"
    "v_cvt_pk_f16_f32 v103, v122, v123\n"
    "v_pk_fma_f32 v[110:111], v[110:111], v[118:119], v[104:105] op_sel_hi:[0,1,1]\n"
    "v_pk_fma_f32 v[122:123], v[122:123], v[118:119], v[106:107] op_sel_hi:[0,1,1]\n"
    "ds_write_addtid_b32 v112 offset:19584\n"
    "ds_write_addtid_b32 v103 offset:27744\n"
    "v_pk_mul_f32 v[104:105], v[110:111], v[118:119] op_sel:[1,1] op_sel_hi:[1,0] neg_lo:[0,1]\n"
    "v_pk_mul_f32 v[106:107], v[122:123], v[118:119] op_sel:[1,1] op_sel_hi:[1,0] neg_lo:[0,1]\n"
    "v_cvt_pk_f16_f32 v112, v110, v111\n"
    "v_cvt_pk_f16_f32 v103, v122, v123\n"
    "v_pk_fma_f32 v[110:111], v[110:111], v[118:119], v[104:105] op_sel_hi:[0,1,1]\n"
    "v_pk_fma_f32 v[122:123], v[122:123], v[118:119], v[106:107] op_sel_hi:[0,1,1]\n"
    "ds_write_addtid_b32 v112 offset:20128\n"
    "ds_write_addtid_b32 v103 offset:28288\n"
    "v_pk_mul_f32 v[104:105], v[110:111], v[118:119] op_sel:[1,1] op_sel_hi:[1,0] neg_lo:[0,1]\n"
    "v_pk_mul_f32 v[106:107], v[122:123], v[118:119] op_sel:[1,1] op_sel_hi:[1,0] neg_lo:[0,1]\n"
    "v_cvt_pk_f16_f32 v112, v110, v111\n"
    "v_cvt_pk_f16_f32 v103, v122, v123\n"
    "v_pk_fma_f32 v[110:111], v[110:111], v[118:119], v[104:105] op_sel_hi:[0,1,1]\n"
    "v_pk_fma_f32 v[122:123], v[122:123], v[118:119], v[106:107] op_sel_hi:[0,1,1]\n"
    "ds_write_addtid_b32 v112 offset:20672\n"
    "ds_write_addtid_b32 v103 offset:28832\n"
    "v_pk_mul_f32 v[104:105], v[110:111], v[118:119] op_sel:[1,1] op_sel_hi:[1,0] neg_lo:[0,1]\n"
    "v_pk_mul_f32 v[106:107], v[122:123], v[118:119] op_sel:[1,1] op_sel_hi:[1,0] neg_lo:[0,1]\n"
    "v_cvt_pk_f16_f32 v112, v110, v111\n"
    "v_cvt_pk_f16_f32 v103, v122, v123\n"
    "v_pk_fma_f32 v[110:111], v[110:111], v[118:119], v[104:105] op_sel_hi:[0,1,1]\n"
    "v_pk_fma_f32 v[122:123], v[122:123], v[118:119], v[106:107] op_sel_hi:[0,1,1]\n"
    "ds_write_addtid_b32 v112 offset:21216\n"
    "ds_write_addtid_b32 v103 offset:29376\n"
    "v_pk_mul_f32 v[104:105], v[110:111], v[118:119] op_sel:[1,1] op_sel_hi:[1,0] neg_lo:[0,1]\n"
    "v_pk_mul_f32 v[106:107], v[122:123], v[118:119] op_sel:[1,1] op_sel_hi:[1,0] neg_lo:[0,1]\n"
    "v_cvt_pk_f16_f32 v112, v110, v111\n"
    "v_cvt_pk_f16_f32 v103, v122, v123\n"
    "v_pk_fma_f32 v[110:111], v[110:111], v[118:119], v[104:105] op_sel_hi:[0,1,1]\n"
    "v_pk_fma_f32 v[122:123], v[122:123], v[118:119], v[106:107] op_sel_hi:[0,1,1]\n"
    "ds_write_addtid_b32 v112 offset:21760\n"
    "ds_write_addtid_b32 v103 offset:29920\n"
    "v_pk_mul_f32 v[104:105], v[110:111], v[118:119] op_sel:[1,1] op_sel_hi:[1,0] neg_lo:[0,1]\n"
    "v_pk_mul_f32 v[106:107], v[122:123], v[118:119] op_sel:[1,1] op_sel_hi:[1,0] neg_lo:[0,1]\n"
    "v_cvt_pk_f16_f32 v112, v110, v111\n"
    "v_cvt_pk_f16_f32 v103, v122, v123\n"
    "v_pk_fma_f32 v[110:111], v[110:111], v[118:119], v[104:105] op_sel_hi:[0,1,1]\n"
    "v_pk_fma_f32 v[122:123], v[122:123], v[118:119], v[106:107] op_sel_hi:[0,1,1]\n"
    "ds_write_addtid_b32 v112 offset:22304\n"
    "ds_write_addtid_b32 v103 offset:30464\n"
    "v_pk_mul_f32 v[104:105], v[122:123], v[118:119] op_sel:[1,1] op_sel_hi:[1,0] neg_lo:[0,1]\n"
    "v_cvt_pk_f16_f32 v112, v110, v111\n"
    "v_cvt_pk_f16_f32 v103, v122, v123\n"
    "v_pk_fma_f32 v[122:123], v[122:123], v[118:119], v[104:105] op_sel_hi:[0,1,1]\n"
    "ds_write_addtid_b32 v112 offset:22848\n"
    "ds_write_addtid_b32 v103 offset:31008\n"
    "v_cvt_pk_f16_f32 v112, v122, v123\n"
    "ds_write_addtid_b32 v112 offset:31552\n"
    "Lgk%=:\n"
    "ds_add_u32 v4, v16 offset:8\n"
    "s_mov_b32 s97, 0x1fffff\n"
    "Lkp%=:\n"
    "ds_read_b32 v17, v4 offset:8\n"
    "s_waitcnt lgkmcnt(0)\n"
    "v_readfirstlane_b32 s96, v17\n"
    "s_and_b32 s96, s96, s54\n"
    "s_cmp_eq_u32 s96, s54\n"
    "s_cbranch_scc1 Lkp%=x\n"
    "s_sleep 1\n"
    "s_sub_u32 s97, s97, 1\n"
    "s_cbranch_scc0 Lkp%=\n"
    "Lkp%=x:\n"
    "ds_read2st64_b32 v[18:19], v9 offset0:34 offset1:35\n"
    "ds_read_b128 v[24:27], v5 offset:0\n"
    "ds_read_b128 v[28:31], v6 offset:0\n"
    "ds_read_b128 v[56:59], v5 offset:256\n"
    "ds_read_b128 v[60:63], v6 offset:256\n"
    "ds_read_b128 v[32:35], v5 offset:64\n"
    "ds_read_b128 v[36:39], v6 offset:64\n"
    "ds_read_b128 v[64:67], v5 offset:320\n"
    "ds_read_b128 v[68:71], v6 offset:320\n"
    "ds_read_b128 v[40:43], v5 offset:128\n"
    "ds_read_b128 v[44:47], v6 offset:128\n"
    "ds_read_b128 v[72:75], v5 offset:384\n"
    "ds_read_b128 v[76:79], v6 offset:384\n"
    "ds_read_b128 v[48:51], v5 offset:192\n"
    "ds_read_b128 v[52:55], v6 offset:192\n"
    "ds_read_b128 v[80:83], v5 offset:448\n"
    "ds_read_b128 v[84:87], v6 offset:448\n"
    "s_waitcnt lgkmcnt(14)\n"
    "v_max_u32 v17, v18, v19\n"
    "v_sub_u32 v20, 121, v17\n"
    "s_waitcnt lgkmcnt(14)\n"
    "v_mfma_f32_16x16x32_f16 v[88:91], v[24:27], v[28:31], 0\n"
    "v_add_u32 v18, v18, v20\n"
    "v_add_u32 v19, v19, v20\n"
    "s_waitcnt lgkmcnt(12)\n"
    "v_mfma_f32_16x16x32_f16 v[92:95], v[56:59], v[60:63], 0\n"
    "v_max_i32 v18, 0, v18\n"
    "v_max_i32 v19, 0, v19\n"
    "s_waitcnt lgkmcnt(10)\n"
    "v_mfma_f32_16x16x32_f16 v[88:91], v[32:35], v[36:39], v[88:91]\n"
    "v_lshlrev_b32 v18, 23, v18\n"
    "v_lshlrev_b32 v19, 23, v19\n"
    "s_waitcnt lgkmcnt(8)\n"
    "v_mfma_f32_16x16x32_f16 v[92:95], v[64:67], v[68:71], v[92:95]\n"
    "s_waitcnt lgkmcnt(6)\n"
    "v_mfma_f32_16x16x32_f16 v[88:91], v[40:43], v[44:47], v[88:91]\n"
    "s_waitcnt lgkmcnt(4)\n"
    "v_mfma_f32_16x16x32_f16 v[92:95], v[72:75], v[76:79], v[92:95]\n"
    "s_waitcnt lgkmcnt(2)\n"
    "v_mfma_f32_16x16x32_f16 v[88:91], v[48:51], v[52:55], v[88:91]\n"
    "s_waitcnt lgkmcnt(0)\n"
    "v_mfma_f32_16x16x32_f16 v[92:95], v[80:83], v[84:87], v[92:95]\n"
    "s_cmp_lt_u32 s51, 2\n"
    "v_mov_b32 v21, 1\n"
    "s_nop 3\n"
    "v_pk_mul_f32 v[88:89], v[88:89], v[18:19] op_sel_hi:[1,0]\n"
    "v_pk_mul_f32 v[90:91], v[90:91], v[18:19] op_sel_hi:[1,0]\n"
    "v_pk_fma_f32 v[88:89], v[92:93], v[18:19], v[88:89] op_sel:[0,1,0]\n"
    "v_pk_fma_f32 v[90:91], v[94:95], v[18:19], v[90:91] op_sel:[0,1,0]\n"
    "v_cvt_pk_f16_f32 v88, v88, v89\n"
    "v_cvt_pk_f16_f32 v89, v90, v91\n"
    "s_cbranch_scc0 Lkf%=\n"
    "ds_write_b64 v7, v[88:89] offset:7488\n"
    "v_alignbit_b32 v90, v89, v88, 16\n"
    "ds_write_b16 v7, v88 offset:8958\n"
    "ds_write_b16_d16_hi v7, v89 offset:8964\n"
    "ds_write_b32 v7, v90 offset:8960\n"
    "s_cmp_eq_u32 s51, 0\n"
    "s_cbranch_scc1 Lkd%=\n"
    "s_mov_b32 exec_lo, 0\n"
    "s_brev_b32 exec_hi, 1\n"
    "ds_write_b16_d16_hi v7, v89 offset:7496\n"
    "ds_write_b16_d16_hi v7, v89 offset:7498\n"
    "ds_write_b16_d16_hi v7, v89 offset:7500\n"
    "ds_write_b16_d16_hi v7, v89 offset:8966\n"
    "ds_write_b16_d16_hi v7, v89 offset:8968\n"
    "ds_write_b16_d16_hi v7, v89 offset:8970\n"
    "s_branch Lke%=\n"
    "Lkf%=:\n"
    "s_mov_b32 exec_lo, 0x07b607b6\n"
    "s_mov_b32 exec_hi, 0x07b607b6\n"
    "ds_write_b64 v7, v[88:89] offset:7488\n"
    "s_cmp_eq_u32 s51, 2\n"
    "s_cbranch_scc1 Lk2%=\n"
    "s_mov_b32 exec_lo, 0\n"
    "s_mov_b32 exec_hi, 0x200000\n"
    "ds_write_b16_d16_hi v7, v89 offset:5358\n"
    "ds_write_b16_d16_hi v7, v89 offset:5360\n"
    "ds_write_b16_d16_hi v7, v89 offset:6828\n"
    "ds_write_b16_d16_hi v7, v89 offset:6830\n"
    "s_branch Lke%=\n"
    "Lk2%=:\n"
    "s_mov_b64 exec, -1\n"
    "ds_add_u32 v4, v16 offset:12\n"
    "ds_read_b32 v18, v9 offset:10336\n"
    "v_add_u32 v17, -5, v17\n"
    "v_lshlrev_b32 v17, 23, v17\n"
    "s_lshl2_add_u32 s56, s50, 158224\n"
    "v_mov_b32 v19, s56\n"
    "s_waitcnt lgkmcnt(0)\n"
    "v_mul_f32 v17, v17, v18\n"
    "ds_write_b32 v19, v17\n"
    "s_branch Lkg%=\n"
    "Lke%=:\n"
    "s_mov_b64 exec, -1\n"
    "Lkd%=:\n"
    "ds_add_u32 v4, v16 offset:12\n"
    "Lkg%=:\n"
    "ds_add_u32 v4, v21 offset:4\n"
    "s_setprio 0\n"
    "s_cmp_eq_u32 s55, 2\n"
    "s_cbranch_scc1 Lr4%=\n"
    "s_cmp_eq_u32 s55, -1\n"
    "s_cbranch_scc1 Lr4%=\n"
    "s_mov_b32 s97, 0x1fffff\n"
    "Lkq%=:\n"
    "ds_read_b32 v17, v4 offset:12\n"
    "s_waitcnt lgkmcnt(0)\n"
    "v_readfirstlane_b32 s96, v17\n"
    "s_and_b32 s96, s96, s54\n"
    "s_cmp_eq_u32 s96, s54\n"
    "s_cbranch_scc1 Lkq%=x\n"
    "s_sleep 1\n"
    "s_sub_u32 s97, s97, 1\n"
    "s_cbranch_scc0 Lkq%=\n"
    "Lkq%=x:\n"
    "s_cmp_eq_u32 s55, 3\n"
    "s_cbranch_scc1 Lr1%=\n"
    "s_cmp_lg_u32 s55, 0\n"
    "s_cbranch_scc1 Lr2%=\n"
    "ds_read2_b32 v[60:61], v3 offset0:0 offset1:1\n"
    "ds_read2_b32 v[62:63], v3 offset0:2 offset1:3\n"
    "ds_read2_b32 v[92:93], v3 offset0:128 offset1:129\n"
    "ds_read2_b32 v[94:95], v3 offset0:130 offset1:131\n"
    "ds_read2_b32 v[64:65], v3 offset0:16 offset1:17\n"
    "ds_read2_b32 v[66:67], v3 offset0:18 offset1:19\n"
    "ds_read2_b32 v[96:97], v3 offset0:144 offset1:145\n"
    "ds_read2_b32 v[98:99], v3 offset0:146 offset1:147\n"
    "ds_read2_b32 v[68:69], v3 offset0:32 offset1:33\n"
    "ds_read2_b32 v[70:71], v3 offset0:34 offset1:35\n"
    "ds_read2_b32 v[100:101], v3 offset0:160 offset1:161\n"
    "ds_read2_b32 v[102:103], v3 offset0:162 offset1:163\n"
    "ds_read2_b32 v[72:73], v3 offset0:48 offset1:49\n"
    "ds_read2_b32 v[74:75], v3 offset0:50 offset1:51\n"
    "ds_read2_b32 v[104:105], v3 offset0:176 offset1:177\n"
    "ds_read2_b32 v[106:107], v3 offset0:178 offset1:179\n"
    "ds_read2_b32 v[76:77], v3 offset0:64 offset1:65\n"
    "ds_read2_b32 v[78:79], v3 offset0:66 offset1:67\n"
    "ds_read2_b32 v[108:109], v3 offset0:192 offset1:193\n"
    "ds_read2_b32 v[110:111], v3 offset0:194 offset1:195\n"
    "ds_read2_b32 v[80:81], v3 offset0:80 offset1:81\n"
    "ds_read2_b32 v[82:83], v3 offset0:82 offset1:83\n"
    "ds_read2_b32 v[112:113], v3 offset0:208 offset1:209\n"
    "ds_read2_b32 v[114:115], v3 offset0:210 offset1:211\n"
    "ds_read2_b32 v[84:85], v3 offset0:96 offset1:97\n"
    "ds_read2_b32 v[86:87], v3 offset0:98 offset1:99\n"
    "ds_read2_b32 v[116:117], v3 offset0:224 offset1:225\n"
    "ds_read2_b32 v[118:119], v3 offset0:226 offset1:227\n"
    "ds_read2_b32 v[88:89], v3 offset0:112 offset1:113\n"
    "ds_read2_b32 v[90:91], v3 offset0:114 offset1:115\n"
    "ds_read2_b32 v[120:121], v3 offset0:240 offset1:241\n"
    "ds_read2_b32 v[122:123], v3 offset0:242 offset1:243\n"
    "v_add_u32 v9, 1024, v3\n"
    "ds_read2_b32 v[124:125], v9 offset0:0 offset1:1\n"
    "ds_read2_b32 v[126:127], v9 offset0:2 offset1:3\n"
    "s_mov_b32 s97, 0x1fffff\n"
    "Lnp%=:\n"
    "ds_read_b32 v8, v4 offset:4\n"
    "s_waitcnt lgkmcnt(0)\n"
    "v_readfirstlane_b32 s96, v8\n"
    "s_and_b32 s96, s96, 0xff\n"
    "s_cmp_eq_u32 s96, 16\n"
    "s_cbranch_scc1 Lnp%=x\n"
    "s_sleep 1\n"
    "s_sub_u32 s97, s97, 1\n"
    "s_cbranch_scc0 Lnp%=\n"
    "Lnp%=x:\n"
    "s_setprio 3\n"
    "ds_read_b128 v[28:31], v10 offset:3584\n"
    "ds_read_b128 v[32:35], v10 offset:3648\n"
    "ds_read_b128 v[36:39], v10 offset:3712\n"
    "ds_read_b128 v[40:43], v10 offset:3776\n"
    "ds_read_b128 v[44:47], v10 offset:3840\n"
    "ds_read_b128 v[48:51], v10 offset:3904\n"
    "ds_read_b128 v[52:55], v10 offset:3968\n"
    "ds_read_b128 v[56:59], v10 offset:4032\n"
    "s_waitcnt lgkmcnt(6)\n"
    "v_mfma_f32_16x16x32_f16 v[12:15], v[60:63], v[28:31], 0\n"
    "v_mfma_f32_16x16x32_f16 v[16:19], v[92:95], v[28:31], 0\n"
    "v_mfma_f32_16x16x32_f16 v[20:23], v[124:127], v[28:31], 0\n"
    "ds_read_b128 v[28:31], v10 offset:3072\n"
    "v_mfma_f32_16x16x32_f16 v[12:15], v[64:67], v[32:35], v[12:15]\n"
    "v_mfma_f32_16x16x32_f16 v[16:19], v[96:99], v[32:35], v[16:19]\n"
    "ds_read_b128 v[32:35], v10 offset:3136\n"
    "s_waitcnt lgkmcnt(6)\n"
    "v_mfma_f32_16x16x32_f16 v[12:15], v[68:71], v[36:39], v[12:15]\n"
    "v_mfma_f32_16x16x32_f16 v[16:19], v[100:103], v[36:39], v[16:19]\n"
    "ds_read_b128 v[36:39], v10 offset:3200\n"
    "v_mfma_f32_16x16x32_f16 v[12:15], v[72:75], v[40:43], v[12:15]\n"
    "v_mfma_f32_16x16x32_f16 v[16:19], v[104:107], v[40:43], v[16:19]\n"
    "ds_read_b128 v[40:43], v10 offset:3264\n"
    "s_waitcnt lgkmcnt(6)\n"
    "v_mfma_f32_16x16x32_f16 v[12:15], v[76:79], v[44:47], v[12:15]\n"
    "v_mfma_f32_16x16x32_f16 v[16:19], v[108:111], v[44:47], v[16:19]\n"
    "ds_read_b128 v[44:47], v10 offset:3328\n"
    "v_mfma_f32_16x16x32_f16 v[12:15], v[80:83], v[48:51], v[12:15]\n"
    "v_mfma_f32_16x16x32_f16 v[16:19], v[112:115], v[48:51], v[16:19]\n"
    "ds_read_b128 v[48:51], v10 offset:3392\n"
    "s_waitcnt lgkmcnt(6)\n"
    "v_mfma_f32_16x16x32_f16 v[12:15], v[84:87], v[52:55], v[12:15]\n"
    "v_mfma_f32_16x16x32_f16 v[16:19], v[116:119], v[52:55], v[16:19]\n"
    "ds_read_b128 v[52:55], v10 offset:3456\n"
    "v_mfma_f32_16x16x32_f16 v[12:15], v[88:91], v[56:59], v[12:15]\n"
    "v_mfma_f32_16x16x32_f16 v[16:19], v[120:123], v[56:59], v[16:19]\n"
    "ds_read_b128 v[56:59], v10 offset:3520\n"
    "s_waitcnt lgkmcnt(6)\n"
    "v_mfma_f32_16x16x32_f16 v[16:19], v[60:63], v[28:31], v[16:19]\n"
    "v_mfma_f32_16x16x32_f16 v[20:23], v[92:95], v[28:31], v[20:23]\n"
    "v_mfma_f32_16x16x32_f16 v[24:27], v[124:127], v[28:31], 0\n"
    "ds_read_b128 v[28:31], v10 offset:2560\n"
    "v_mfma_f32_16x16x32_f16 v[16:19], v[64:67], v[32:35], v[16:19]\n"
    "v_mfma_f32_16x16x32_f16 v[20:23], v[96:99], v[32:35], v[20:23]\n"
    "ds_read_b128 v[32:35], v10 offset:2624\n"
    "s_waitcnt lgkmcnt(6)\n"
    "v_mfma_f32_16x16x32_f16 v[16:19], v[68:71], v[36:39], v[16:19]\n"
    "v_mov_b32 v9, 0x1\n"
    "ds_write_b128 v11, v[12:15] offset:0\n"
    "ds_add_u32 v4, v9 offset:0\n"
    "v_mfma_f32_16x16x32_f16 v[20:23], v[100:103], v[36:39], v[20:23]\n"
    "ds_read_b128 v[36:39], v10 offset:2688\n"
    "v_mfma_f32_16x16x32_f16 v[16:19], v[72:75], v[40:43], v[16:19]\n"
    "v_mfma_f32_16x16x32_f16 v[20:23], v[104:107], v[40:43], v[20:23]\n"
    "ds_read_b128 v[40:43], v10 offset:2752\n"
    "s_waitcnt lgkmcnt(8)\n"
    "v_mfma_f32_16x16x32_f16 v[16:19], v[76:79], v[44:47], v[16:19]\n"
    "v_mfma_f32_16x16x32_f16 v[20:23], v[108:111], v[44:47], v[20:23]\n"
    "ds_read_b128 v[44:47], v10 offset:2816\n"
    "v_mfma_f32_16x16x32_f16 v[16:19], v[80:83], v[48:51], v[16:19]\n"
    "v_mfma_f32_16x16x32_f16 v[20:23], v[112:115], v[48:51], v[20:23]\n"
    "ds_read_b128 v[48:51], v10 offset:2880\n"
    "s_waitcnt lgkmcnt(8)\n"
    "v_mfma_f32_16x16x32_f16 v[16:19], v[84:87], v[52:55], v[16:19]\n"
    "v_mfma_f32_16x16x32_f16 v[20:23], v[116:119], v[52:55], v[20:23]\n"
    "ds_read_b128 v[52:55], v10 offset:2944\n"
    "v_mfma_f32_16x16x32_f16 v[16:19], v[88:91], v[56:59], v[16:19]\n"
    "v_mfma_f32_16x16x32_f16 v[20:23], v[120:123], v[56:59], v[20:23]\n"
    "ds_read_b128 v[56:59], v10 offset:3008\n"
    "s_waitcnt lgkmcnt(8)\n"
    "v_mfma_f32_16x16x32_f16 v[20:23], v[60:63], v[28:31], v[20:23]\n"
    "v_mfma_f32_16x16x32_f16 v[24:27], v[92:95], v[28:31], v[24:27]\n"
    "ds_read_b128 v[28:31], v10 offset:2048\n"
    "v_mfma_f32_16x16x32_f16 v[20:23], v[64:67], v[32:35], v[20:23]\n"
    "v_mfma_f32_16x16x32_f16 v[24:27], v[96:99], v[32:35], v[24:27]\n"
    "ds_read_b128 v[32:35], v10 offset:2112\n"
    "s_waitcnt lgkmcnt(6)\n"
    "v_mfma_f32_16x16x32_f16 v[20:23], v[68:71], v[36:39], v[20:23]\n"
    "v_mov_b32 v9, 0x10\n"
    "ds_write_b128 v11, v[16:19] offset:1280\n"
    "ds_add_u32 v4, v9 offset:0\n"
    "v_mfma_f32_16x16x32_f16 v[24:27], v[100:103], v[36:39], v[24:27]\n"
    "ds_read_b128 v[36:39], v10 offset:2176\n"
    "v_mfma_f32_16x16x32_f16 v[20:23], v[72:75], v[40:43], v[20:23]\n"
    "v_mfma_f32_16x16x32_f16 v[24:27], v[104:107], v[40:43], v[24:27]\n"
    "ds_read_b128 v[40:43], v10 offset:2240\n"
    "s_waitcnt lgkmcnt(8)\n"
    "v_mfma_f32_16x16x32_f16 v[20:23], v[76:79], v[44:47], v[20:23]\n"
    "v_mfma_f32_16x16x32_f16 v[24:27], v[108:111], v[44:47], v[24:27]\n"
    "ds_read_b128 v[44:47], v10 offset:2304\n"
    "v_mfma_f32_16x16x32_f16 v[20:23], v[80:83], v[48:51], v[20:23]\n"
    "v_mfma_f32_16x16x32_f16 v[24:27], v[112:115], v[48:51], v[24:27]\n"
    "ds_read_b128 v[48:51], v10 offset:2368\n"
    "s_waitcnt lgkmcnt(8)\n"
    "v_mfma_f32_16x16x32_f16 v[20:23], v[84:87], v[52:55], v[20:23]\n"
    "v_mfma_f32_16x16x32_f16 v[24:27], v[116:119], v[52:55], v[24:27]\n"
    "ds_read_b128 v[52:55], v10 offset:2432\n"
    "v_mfma_f32_16x16x32_f16 v[20:23], v[88:91], v[56:59], v[20:23]\n"
    "v_mfma_f32_16x16x32_f16 v[24:27], v[120:123], v[56:59], v[24:27]\n"
    "ds_read_b128 v[56:59], v10 offset:2496\n"
    "s_waitcnt lgkmcnt(8)\n"
    "v_mfma_f32_16x16x32_f16 v[24:27], v[60:63], v[28:31], v[24:27]\n"
    "v_mfma_f32_16x16x32_f16 v[24:27], v[64:67], v[32:35], v[24:27]\n"
    "s_waitcnt lgkmcnt(4)\n"
    "v_mfma_f32_16x16x32_f16 v[24:27], v[68:71], v[36:39], v[24:27]\n"
    "v_mov_b32 v9, 0x100\n"
    "ds_write_b128 v11, v[20:23] offset:2560\n"
    "ds_add_u32 v4, v9 offset:0\n"
    "v_mfma_f32_16x16x32_f16 v[24:27], v[72:75], v[40:43], v[24:27]\n"
    "s_waitcnt lgkmcnt(4)\n"
    "v_mfma_f32_16x16x32_f16 v[24:27], v[76:79], v[44:47], v[24:27]\n"
    "v_mfma_f32_16x16x32_f16 v[24:27], v[80:83], v[48:51], v[24:27]\n"
    "s_waitcnt lgkmcnt(2)\n"
    "v_mfma_f32_16x16x32_f16 v[24:27], v[84:87], v[52:55], v[24:27]\n"
    "v_mfma_f32_16x16x32_f16 v[24:27], v[88:91], v[56:59], v[24:27]\n"
    "v_mov_b32 v9, 0x1000\n"
    "s_nop 6\n"
    "ds_write_b128 v11, v[24:27] offset:3840\n"
    "ds_add_u32 v4, v9 offset:0\n"
    "s_branch Lr3%=\n"
    "Lr1%=:\n"
    "v_add_u32 v9, 1024, v3\n"
    "ds_read2_b32 v[12:13], v9 offset0:48 offset1:49\n"
    "ds_read2_b32 v[14:15], v9 offset0:50 offset1:51\n"
    "ds_read2_b32 v[16:17], v9 offset0:44 offset1:45\n"
    "ds_read2_b32 v[18:19], v9 offset0:46 offset1:47\n"
    "ds_read2_b32 v[20:21], v9 offset0:61 offset1:62\n"
    "ds_read2_b32 v[22:23], v9 offset0:63 offset1:64\n"
    "ds_read2_b32 v[24:25], v8 offset0:0 offset1:1\n"
    "ds_read2_b32 v[26:27], v8 offset0:2 offset1:3\n"
    "ds_read2_b32 v[36:37], v8 offset0:48 offset1:49\n"
    "ds_read2_b32 v[38:39], v8 offset0:50 offset1:51\n"
    "ds_read2_b32 v[28:29], v8 offset0:16 offset1:17\n"
    "ds_read2_b32 v[30:31], v8 offset0:18 offset1:19\n"
    "ds_read2_b32 v[40:41], v8 offset0:64 offset1:65\n"
    "ds_read2_b32 v[42:43], v8 offset0:66 offset1:67\n"
    "ds_read2_b32 v[32:33], v8 offset0:32 offset1:33\n"
    "ds_read2_b32 v[34:35], v8 offset0:34 offset1:35\n"
    "ds_read2_b32 v[44:45], v8 offset0:80 offset1:81\n"
    "ds_read2_b32 v[46:47], v8 offset0:82 offset1:83\n"
    "s_mov_b32 s97, 0x1fffff\n"
    "Lfp%=:\n"
    "ds_read_b32 v8, v4 offset:4\n"
    "s_waitcnt lgkmcnt(0)\n"
    "v_readfirstlane_b32 s96, v8\n"
    "s_and_b32 s96, s96, 0xff\n"
    "s_cmp_eq_u32 s96, 16\n"
    "s_cbranch_scc1 Lfp%=x\n"
    "s_sleep 1\n"
    "s_sub_u32 s97, s97, 1\n"
    "s_cbranch_scc0 Lfp%=\n"
    "Lfp%=x:\n"
    "s_setprio 2\n"
    "ds_read_b128 v[48:51], v10 offset:3584\n"
    "ds_read_b128 v[52:55], v10 offset:3600\n"
    "ds_read_b128 v[56:59], v10 offset:3072\n"
    "ds_read_b128 v[60:63], v10 offset:3088\n"
    "s_waitcnt lgkmcnt(3)\n"
    "v_mfma_f32_16x16x32_f16 v[64:67], v[48:51], v[12:15], 0\n"
    "v_mfma_f32_16x16x32_f16 v[68:71], v[48:51], v[16:19], 0\n"
    "v_mfma_f32_16x16x32_f16 v[72:75], v[48:51], v[20:23], 0\n"
    "s_waitcnt lgkmcnt(2)\n"
    "v_mfma_f32_16x16x32_f16 v[76:79], v[52:55], v[20:23], 0\n"
    "ds_read_b128 v[48:51], v10 offset:2560\n"
    "ds_read_b128 v[52:55], v10 offset:2576\n"
    "s_nop 2\n"
    "v_cvt_pk_f16_f32 v80, v67, v71\n"
    "v_cvt_pk_f16_f32 v81, v66, v70\n"
    "v_cvt_pk_f16_f32 v82, v65, v69\n"
    "v_cvt_pk_f16_f32 v83, v64, v68\n"
    "v_cvt_pk_f16_f32 v84, v75, v79\n"
    "v_cvt_pk_f16_f32 v85, v74, v78\n"
    "v_cvt_pk_f16_f32 v86, v73, v77\n"
    "v_cvt_pk_f16_f32 v87, v72, v76\n"
    "s_waitcnt lgkmcnt(3)\n"
    "v_mfma_f32_16x16x32_f16 v[64:67], v[56:59], v[12:15], 0\n"
    "v_mfma_f32_16x16x32_f16 v[68:71], v[56:59], v[16:19], 0\n"
    "v_mfma_f32_16x16x32_f16 v[72:75], v[56:59], v[20:23], 0\n"
    "s_waitcnt lgkmcnt(2)\n"
    "v_mfma_f32_16x16x32_f16 v[76:79], v[60:63], v[20:23], 0\n"
    "ds_read_b128 v[56:59], v10 offset:2048\n"
    "ds_read_b128 v[60:63], v10 offset:2064\n"
    "v_mfma_f32_16x16x32_f16 v[104:107], v[80:83], v[24:27], 0\n"
    "v_mfma_f32_16x16x32_f16 v[108:111], v[80:83], v[28:31], 0\n"
    "s_nop 0\n"
    "v_cvt_pk_f16_f32 v88, v67, v71\n"
    "v_cvt_pk_f16_f32 v89, v66, v70\n"
    "v_mfma_f32_16x16x32_f16 v[104:107], v[84:87], v[36:39], v[104:107]\n"
    "v_cvt_pk_f16_f32 v90, v65, v69\n"
    "v_cvt_pk_f16_f32 v91, v64, v68\n"
    "v_mfma_f32_16x16x32_f16 v[96:99], v[80:83], v[32:35], 0\n"
    "v_cvt_pk_f16_f32 v92, v75, v79\n"
    "v_cvt_pk_f16_f32 v93, v74, v78\n"
    "v_mfma_f32_16x16x32_f16 v[108:111], v[84:87], v[40:43], v[108:111]\n"
    "v_cvt_pk_f16_f32 v94, v73, v77\n"
    "v_cvt_pk_f16_f32 v95, v72, v76\n"
    "v_mfma_f32_16x16x32_f16 v[96:99], v[84:87], v[44:47], v[96:99]\n"
    "s_waitcnt lgkmcnt(3)\n"
    "v_mfma_f32_16x16x32_f16 v[64:67], v[48:51], v[12:15], 0\n"
    "v_mfma_f32_16x16x32_f16 v[68:71], v[48:51], v[16:19], 0\n"
    "v_mfma_f32_16x16x32_f16 v[72:75], v[48:51], v[20:23], 0\n"
    "s_waitcnt lgkmcnt(2)\n"
    "v_mfma_f32_16x16x32_f16 v[76:79], v[52:55], v[20:23], 0\n"
    "s_mov_b32 s97, 0x1fffff\n"
    "Lfq%=:\n"
    "ds_read_b32 v8, v4 offset:4\n"
    "s_waitcnt lgkmcnt(0)\n"
    "v_readfirstlane_b32 s96, v8\n"
    "s_and_b32 s96, s96, 0x400\n"
    "s_cmp_eq_u32 s96, 0x400\n"
    "s_cbranch_scc1 Lfq%=x\n"
    "s_sub_u32 s97, s97, 1\n"
    "s_cbranch_scc0 Lfq%=\n"
    "Lfq%=x:\n"
    "ds_read_b128 v[48:51], v10 offset:1536\n"
    "ds_read_b128 v[52:55], v10 offset:1552\n"
    "v_mov_b32 v9, 0x100\n"
    "ds_write_b128 v11, v[104:107] offset:10240\n"
    "ds_add_u32 v4, v9 offset:0\n"
    "v_mfma_f32_16x16x32_f16 v[108:111], v[88:91], v[24:27], v[108:111]\n"
    "v_mfma_f32_16x16x32_f16 v[96:99], v[88:91], v[28:31], v[96:99]\n"
    "v_cvt_pk_f16_f32 v80, v67, v71\n"
    "v_cvt_pk_f16_f32 v81, v66, v70\n"
    "v_mfma_f32_16x16x32_f16 v[108:111], v[92:95], v[36:39], v[108:111]\n"
    "v_cvt_pk_f16_f32 v82, v65, v69\n"
    "v_cvt_pk_f16_f32 v83, v64, v68\n"
    "v_mfma_f32_16x16x32_f16 v[100:103], v[88:91], v[32:35], 0\n"
    "v_cvt_pk_f16_f32 v84, v75, v79\n"
    "v_cvt_pk_f16_f32 v85, v74, v78\n"
    "v_mfma_f32_16x16x32_f16 v[96:99], v[92:95], v[40:43], v[96:99]\n"
    "v_cvt_pk_f16_f32 v86, v73, v77\n"
    "v_cvt_pk_f16_f32 v87, v72, v76\n"
    "v_mfma_f32_16x16x32_f16 v[100:103], v[92:95], v[44:47], v[100:103]\n"
    "v_mfma_f32_16x16x32_f16 v[64:67], v[56:59], v[12:15], 0\n"
    "v_mfma_f32_16x16x32_f16 v[68:71], v[56:59], v[16:19], 0\n"
    "v_mfma_f32_16x16x32_f16 v[72:75], v[56:59], v[20:23], 0\n"
    "v_mfma_f32_16x16x32_f16 v[76:79], v[60:63], v[20:23], 0\n"
    "ds_read_b128 v[56:59], v10 offset:1024\n"
    "ds_read_b128 v[60:63], v10 offset:1040\n"
    "v_mov_b32 v9, 0x1000\n"
    "ds_write_b128 v11, v[108:111] offset:11520\n"
    "ds_add_u32 v4, v9 offset:0\n"
    "v_mfma_f32_16x16x32_f16 v[96:99], v[80:83], v[24:27], v[96:99]\n"
    "v_mfma_f32_16x16x32_f16 v[100:103], v[80:83], v[28:31], v[100:103]\n"
    "v_cvt_pk_f16_f32 v88, v67, v71\n"
    "v_cvt_pk_f16_f32 v89, v66, v70\n"
    "v_mfma_f32_16x16x32_f16 v[96:99], v[84:87], v[36:39], v[96:99]\n"
    "v_cvt_pk_f16_f32 v90, v65, v69\n"
    "v_cvt_pk_f16_f32 v91, v64, v68\n"
    "v_mfma_f32_16x16x32_f16 v[104:107], v[80:83], v[32:35], 0\n"
    "v_cvt_pk_f16_f32 v92, v75, v79\n"
    "v_cvt_pk_f16_f32 v93, v74, v78\n"
    "v_mfma_f32_16x16x32_f16 v[100:103], v[84:87], v[40:43], v[100:103]\n"
    "v_cvt_pk_f16_f32 v94, v73, v77\n"
    "v_cvt_pk_f16_f32 v95, v72, v76\n"
    "v_mfma_f32_16x16x32_f16 v[104:107], v[84:87], v[44:47], v[104:107]\n"
    "s_waitcnt lgkmcnt(7)\n"
    "v_mfma_f32_16x16x32_f16 v[64:67], v[48:51], v[12:15], 0\n"
    "v_mfma_f32_16x16x32_f16 v[68:71], v[48:51], v[16:19], 0\n"
    "v_mfma_f32_16x16x32_f16 v[72:75], v[48:51], v[20:23], 0\n"
    "s_waitcnt lgkmcnt(6)\n"
    "v_mfma_f32_16x16x32_f16 v[76:79], v[52:55], v[20:23], 0\n"
    "v_mov_b32 v9, 0x10000\n"
    "ds_write_b128 v11, v[96:99] offset:12800\n"
    "ds_add_u32 v4, v9 offset:0\n"
    "v_mfma_f32_16x16x32_f16 v[100:103], v[88:91], v[24:27], v[100:103]\n"
    "v_mfma_f32_16x16x32_f16 v[104:107], v[88:91], v[28:31], v[104:107]\n"
    "v_cvt_pk_f16_f32 v80, v67, v71\n"
    "v_cvt_pk_f16_f32 v81, v66, v70\n"
    "v_mfma_f32_16x16x32_f16 v[100:103], v[92:95], v[36:39], v[100:103]\n"
    "v_cvt_pk_f16_f32 v82, v65, v69\n"
    "v_cvt_pk_f16_f32 v83, v64, v68\n"
    "v_mfma_f32_16x16x32_f16 v[108:111], v[88:91], v[32:35], 0\n"
    "v_cvt_pk_f16_f32 v84, v75, v79\n"
    "v_cvt_pk_f16_f32 v85, v74, v78\n"
    "v_mfma_f32_16x16x32_f16 v[104:107], v[92:95], v[40:43], v[104:107]\n"
    "v_cvt_pk_f16_f32 v86, v73, v77\n"
    "v_cvt_pk_f16_f32 v87, v72, v76\n"
    "v_mfma_f32_16x16x32_f16 v[108:111], v[92:95], v[44:47], v[108:111]\n"
    "s_waitcnt lgkmcnt(5)\n"
    "v_mfma_f32_16x16x32_f16 v[64:67], v[56:59], v[12:15], 0\n"
    "v_mfma_f32_16x16x32_f16 v[68:71], v[56:59], v[16:19], 0\n"
    "v_mfma_f32_16x16x32_f16 v[72:75], v[56:59], v[20:23], 0\n"
    "s_waitcnt lgkmcnt(4)\n"
    "v_mfma_f32_16x16x32_f16 v[76:79], v[60:63], v[20:23], 0\n"
    "v_mov_b32 v9, 0x100000\n"
    "ds_write_b128 v11, v[100:103] offset:14080\n"
    "ds_add_u32 v4, v9 offset:0\n"
    "v_mfma_f32_16x16x32_f16 v[104:107], v[80:83], v[24:27], v[104:107]\n"
    "s_nop 0\n"
    "v_cvt_pk_f16_f32 v88, v67, v71\n"
    "v_cvt_pk_f16_f32 v89, v66, v70\n"
    "v_mfma_f32_16x16x32_f16 v[108:111], v[80:83], v[28:31], v[108:111]\n"
    "v_cvt_pk_f16_f32 v90, v65, v69\n"
    "v_cvt_pk_f16_f32 v91, v64, v68\n"
    "v_mfma_f32_16x16x32_f16 v[104:107], v[84:87], v[36:39], v[104:107]\n"
    "v_cvt_pk_f16_f32 v92, v75, v79\n"
    "v_cvt_pk_f16_f32 v93, v74, v78\n"
    "v_mfma_f32_16x16x32_f16 v[108:111], v[84:87], v[40:43], v[108:111]\n"
    "v_cvt_pk_f16_f32 v94, v73, v77\n"
    "v_cvt_pk_f16_f32 v95, v72, v76\n"
    "v_mov_b32 v9, 0x1000000\n"
    "s_nop 1\n"
    "ds_write_b128 v11, v[104:107] offset:15360\n"
    "ds_add_u32 v4, v9 offset:0\n"
    "v_mfma_f32_16x16x32_f16 v[108:111], v[88:91], v[24:27], v[108:111]\n"
    "v_mfma_f32_16x16x32_f16 v[108:111], v[92:95], v[36:39], v[108:111]\n"
    "v_mov_b32 v9, 0x10000000\n"
    "s_nop 6\n"
    "ds_write_b128 v11, v[108:111] offset:16640\n"
    "ds_add_u32 v4, v9 offset:0\n"
    "s_branch Lr3%=\n"
    "Lr2%=:\n"
    "ds_read2_b32 v[24:25], v8 offset0:96 offset1:97\n"
    "ds_read2_b32 v[26:27], v8 offset0:98 offset1:99\n"
    "ds_read2_b32 v[28:29], v8 offset0:112 offset1:113\n"
    "ds_read2_b32 v[30:31], v8 offset0:114 offset1:115\n"
    "ds_read2_b32 v[32:33], v8 offset0:128 offset1:129\n"
    "ds_read2_b32 v[34:35], v8 offset0:130 offset1:131\n"
    "ds_read2_b32 v[36:37], v8 offset0:144 offset1:145\n"
    "ds_read2_b32 v[38:39], v8 offset0:146 offset1:147\n"
    "v_add_u32 v9, 1024, v3\n"
    "ds_read2_b32 v[124:125], v9 offset0:0 offset1:1\n"
    "ds_read2_b32 v[126:127], v9 offset0:2 offset1:3\n"
    "ds_read2_b32 v[20:21], v9 offset0:32 offset1:33\n"
    "ds_read2_b32 v[22:23], v9 offset0:34 offset1:35\n"
    "ds_read2_b32 v[12:13], v9 offset0:76 offset1:77\n"
    "ds_read2_b32 v[14:15], v9 offset0:78 offset1:79\n"
    "ds_read2_b32 v[16:17], v9 offset0:91 offset1:92\n"
    "ds_read2_b32 v[18:19], v9 offset0:93 offset1:94\n"
    "ds_read2_b32 v[40:41], v3 offset0:112 offset1:113\n"
    "ds_read2_b32 v[42:43], v3 offset0:114 offset1:115\n"
    "ds_read2_b32 v[44:45], v3 offset0:128 offset1:129\n"
    "ds_read2_b32 v[46:47], v3 offset0:130 offset1:131\n"
    "ds_read2_b32 v[48:49], v3 offset0:144 offset1:145\n"
    "ds_read2_b32 v[50:51], v3 offset0:146 offset1:147\n"
    "ds_read2_b32 v[52:53], v3 offset0:160 offset1:161\n"
    "ds_read2_b32 v[54:55], v3 offset0:162 offset1:163\n"
    "ds_read2_b32 v[56:57], v3 offset0:176 offset1:177\n"
    "ds_read2_b32 v[58:59], v3 offset0:178 offset1:179\n"
    "ds_read2_b32 v[60:61], v3 offset0:192 offset1:193\n"
    "ds_read2_b32 v[62:63], v3 offset0:194 offset1:195\n"
    "ds_read2_b32 v[64:65], v3 offset0:208 offset1:209\n"
    "ds_read2_b32 v[66:67], v3 offset0:210 offset1:211\n"
    "ds_read2_b32 v[68:69], v3 offset0:224 offset1:225\n"
    "ds_read2_b32 v[70:71], v3 offset0:226 offset1:227\n"
    "ds_read2_b32 v[72:73], v3 offset0:240 offset1:241\n"
    "ds_read2_b32 v[74:75], v3 offset0:242 offset1:243\n"
    "s_mov_b32 s97, 0x1fffff\n"
    "Lgp%=:\n"
    "ds_read_b32 v8, v4 offset:4\n"
    "s_waitcnt lgkmcnt(0)\n"
    "v_readfirstlane_b32 s96, v8\n"
    "s_and_b32 s96, s96, 0xff\n"
    "s_cmp_eq_u32 s96, 16\n"
    "s_cbranch_scc1 Lgp%=x\n"
    "s_sleep 1\n"
    "s_sub_u32 s97, s97, 1\n"
    "s_cbranch_scc0 Lgp%=\n"
    "Lgp%=x:\n"
    "s_setprio 2\n"
    "ds_read_b128 v[76:79], v10 offset:3584\n"
    "ds_read_b128 v[80:83], v10 offset:3600\n"
    "ds_read_b128 v[84:87], v10 offset:3072\n"
    "ds_read_b128 v[88:91], v10 offset:3088\n"
    "s_waitcnt lgkmcnt(3)\n"
    "v_mfma_f32_16x16x32_f16 v[92:95], v[76:79], v[12:15], 0\n"
    "s_waitcnt lgkmcnt(2)\n"
    "v_mfma_f32_16x16x32_f16 v[96:99], v[80:83], v[16:19], 0\n"
    "v_mfma_f32_16x16x32_f16 v[108:111], v[20:23], v[76:79], 0\n"
    "ds_read_b128 v[76:79], v10 offset:2560\n"
    "ds_read_b128 v[80:83], v10 offset:2576\n"
    "s_nop 4\n"
    "v_cvt_pk_f16_f32 v100, v95, v99\n"
    "v_cvt_pk_f16_f32 v101, v94, v98\n"
    "v_cvt_pk_f16_f32 v102, v93, v97\n"
    "v_cvt_pk_f16_f32 v103, v92, v96\n"
    "s_waitcnt lgkmcnt(3)\n"
    "v_mfma_f32_16x16x32_f16 v[92:95], v[84:87], v[12:15], 0\n"
    "s_waitcnt lgkmcnt(2)\n"
    "v_mfma_f32_16x16x32_f16 v[96:99], v[88:91], v[16:19], 0\n"
    "v_mfma_f32_16x16x32_f16 v[112:115], v[20:23], v[84:87], 0\n"
    "ds_read_b128 v[84:87], v10 offset:2048\n"
    "ds_read_b128 v[88:91], v10 offset:2064\n"
    "v_mfma_f32_16x16x32_f16 v[108:111], v[100:103], v[24:27], v[108:111]\n"
    "v_mfma_f32_16x16x32_f16 v[112:115], v[100:103], v[28:31], v[112:115]\n"
    "v_mfma_f32_16x16x32_f16 v[116:119], v[100:103], v[32:35], 0\n"
    "v_mfma_f32_16x16x32_f16 v[120:123], v[100:103], v[36:39], 0\n"
    "ds_read2_b32 v[36:37], v3 offset0:96 offset1:97\n"
    "ds_read2_b32 v[38:39], v3 offset0:98 offset1:99\n"
    "v_cvt_pk_f16_f32 v104, v95, v99\n"
    "v_cvt_pk_f16_f32 v105, v94, v98\n"
    "v_cvt_pk_f16_f32 v106, v93, v97\n"
    "v_cvt_pk_f16_f32 v107, v92, v96\n"
    "s_waitcnt lgkmcnt(5)\n"
    "v_mfma_f32_16x16x32_f16 v[92:95], v[76:79], v[12:15], 0\n"
    "s_waitcnt lgkmcnt(4)\n"
    "v_mfma_f32_16x16x32_f16 v[96:99], v[80:83], v[16:19], 0\n"
    "v_mfma_f32_16x16x32_f16 v[116:119], v[20:23], v[76:79], v[116:119]\n"
    "v_mfma_f32_16x16x32_f16 v[108:111], v[124:127], v[76:79], v[108:111]\n"
    "ds_read_b128 v[76:79], v10 offset:2048\n"
    "ds_read_b128 v[80:83], v10 offset:2112\n"
    "v_mfma_f32_16x16x32_f16 v[112:115], v[104:107], v[24:27], v[112:115]\n"
    "v_mfma_f32_16x16x32_f16 v[116:119], v[104:107], v[28:31], v[116:119]\n"
    "v_mfma_f32_16x16x32_f16 v[120:123], v[104:107], v[32:35], v[120:123]\n"
    "ds_read2_b32 v[32:33], v3 offset0:80 offset1:81\n"
    "ds_read2_b32 v[34:35], v3 offset0:82 offset1:83\n"
    "v_cvt_pk_f16_f32 v100, v95, v99\n"
    "v_cvt_pk_f16_f32 v101, v94, v98\n"
    "v_cvt_pk_f16_f32 v102, v93, v97\n"
    "v_cvt_pk_f16_f32 v103, v92, v96\n"
    "s_waitcnt lgkmcnt(7)\n"
    "v_mfma_f32_16x16x32_f16 v[92:95], v[84:87], v[12:15], 0\n"
    "s_waitcnt lgkmcnt(6)\n"
    "v_mfma_f32_16x16x32_f16 v[96:99], v[88:91], v[16:19], 0\n"
    "v_mfma_f32_16x16x32_f16 v[120:123], v[20:23], v[84:87], v[120:123]\n"
    "ds_read_b128 v[84:87], v10 offset:2176\n"
    "ds_read_b128 v[88:91], v10 offset:2240\n"
    "ds_read2_b32 v[12:13], v3 offset0:0 offset1:1\n"
    "ds_read2_b32 v[14:15], v3 offset0:2 offset1:3\n"
    "ds_read2_b32 v[16:17], v3 offset0:16 offset1:17\n"
    "ds_read2_b32 v[18:19], v3 offset0:18 offset1:19\n"
    "ds_read2_b32 v[20:21], v3 offset0:32 offset1:33\n"
    "ds_read2_b32 v[22:23], v3 offset0:34 offset1:35\n"
    "v_mfma_f32_16x16x32_f16 v[116:119], v[100:103], v[24:27], v[116:119]\n"
    "v_mfma_f32_16x16x32_f16 v[120:123], v[100:103], v[28:31], v[120:123]\n"
    "ds_read2_b32 v[28:29], v3 offset0:64 offset1:65\n"
    "ds_read2_b32 v[30:31], v3 offset0:66 offset1:67\n"
    "v_cvt_pk_f16_f32 v104, v95, v99\n"
    "v_cvt_pk_f16_f32 v105, v94, v98\n"
    "v_cvt_pk_f16_f32 v106, v93, v97\n"
    "v_cvt_pk_f16_f32 v107, v92, v96\n"
    "s_mov_b32 s97, 0x1fffff\n"
    "Lgq%=:\n"
    "ds_read_b32 v8, v4 offset:4\n"
    "s_waitcnt lgkmcnt(0)\n"
    "v_readfirstlane_b32 s96, v8\n"
    "s_and_b32 s96, s96, 0x400\n"
    "s_cmp_eq_u32 s96, 0x400\n"
    "s_cbranch_scc1 Lgq%=x\n"
    "s_sub_u32 s97, s97, 1\n"
    "s_cbranch_scc0 Lgq%=\n"
    "Lgq%=x:\n"
    "ds_read_b128 v[92:95], v10 offset:2304\n"
    "ds_read_b128 v[96:99], v10 offset:2368\n"
    "ds_read_b128 v[100:103], v10 offset:2432\n"
    "v_mfma_f32_16x16x32_f16 v[120:123], v[104:107], v[24:27], v[120:123]\n"
    "ds_read2_b32 v[24:25], v3 offset0:48 offset1:49\n"
    "ds_read2_b32 v[26:27], v3 offset0:50 offset1:51\n"
    "ds_read_b128 v[104:107], v10 offset:2496\n"
    "v_mfma_f32_16x16x32_f16 v[108:111], v[44:47], v[76:79], v[108:111]\n"
    "v_mfma_f32_16x16x32_f16 v[112:115], v[124:127], v[76:79], v[112:115]\n"
    "ds_read_b128 v[76:79], v10 offset:1536\n"
    "v_mfma_f32_16x16x32_f16 v[108:111], v[48:51], v[80:83], v[108:111]\n"
    "ds_read_b128 v[80:83], v10 offset:1600\n"
    "v_mfma_f32_16x16x32_f16 v[108:111], v[52:55], v[84:87], v[108:111]\n"
    "ds_read_b128 v[84:87], v10 offset:1664\n"
    "v_mfma_f32_16x16x32_f16 v[108:111], v[56:59], v[88:91], v[108:111]\n"
    "ds_read_b128 v[88:91], v10 offset:1728\n"
    "s_waitcnt lgkmcnt(8)\n"
    "v_mfma_f32_16x16x32_f16 v[108:111], v[60:63], v[92:95], v[108:111]\n"
    "ds_read_b128 v[92:95], v10 offset:1792\n"
    "v_mfma_f32_16x16x32_f16 v[108:111], v[64:67], v[96:99], v[108:111]\n"
    "ds_read_b128 v[96:99], v10 offset:1856\n"
    "s_waitcnt lgkmcnt(6)\n"
    "v_mfma_f32_16x16x32_f16 v[108:111], v[68:71], v[100:103], v[108:111]\n"
    "ds_read_b128 v[100:103], v10 offset:1920\n"
    "v_mfma_f32_16x16x32_f16 v[108:111], v[72:75], v[104:107], v[108:111]\n"
    "ds_read_b128 v[104:107], v10 offset:1984\n"
    "s_waitcnt lgkmcnt(6)\n"
    "v_mfma_f32_16x16x32_f16 v[108:111], v[12:15], v[76:79], v[108:111]\n"
    "v_mfma_f32_16x16x32_f16 v[112:115], v[44:47], v[76:79], v[112:115]\n"
    "v_mfma_f32_16x16x32_f16 v[116:119], v[124:127], v[76:79], v[116:119]\n"
    "ds_read_b128 v[76:79], v10 offset:1024\n"
    "v_mfma_f32_16x16x32_f16 v[108:111], v[16:19], v[80:83], v[108:111]\n"
    "v_mfma_f32_16x16x32_f16 v[112:115], v[48:51], v[80:83], v[112:115]\n"
    "ds_read_b128 v[80:83], v10 offset:1088\n"
    "s_waitcnt lgkmcnt(6)\n"
    "v_mfma_f32_16x16x32_f16 v[108:111], v[20:23], v[84:87], v[108:111]\n"
    "v_mfma_f32_16x16x32_f16 v[112:115], v[52:55], v[84:87], v[112:115]\n"
    "ds_read_b128 v[84:87], v10 offset:1152\n"
    "v_mfma_f32_16x16x32_f16 v[108:111], v[24:27], v[88:91], v[108:111]\n"
    "v_mfma_f32_16x16x32_f16 v[112:115], v[56:59], v[88:91], v[112:115]\n"
    "ds_read_b128 v[88:91], v10 offset:1216\n"
    "s_waitcnt lgkmcnt(6)\n"
    "v_mfma_f32_16x16x32_f16 v[108:111], v[28:31], v[92:95], v[108:111]\n"
    "v_mfma_f32_16x16x32_f16 v[112:115], v[60:63], v[92:95], v[112:115]\n"
    "ds_read_b128 v[92:95], v10 offset:1280\n"
    "v_mfma_f32_16x16x32_f16 v[108:111], v[32:35], v[96:99], v[108:111]\n"
    "v_mfma_f32_16x16x32_f16 v[112:115], v[64:67], v[96:99], v[112:115]\n"
    "ds_read_b128 v[96:99], v10 offset:1344\n"
    "s_waitcnt lgkmcnt(6)\n"
    "v_mfma_f32_16x16x32_f16 v[108:111], v[36:39], v[100:103], v[108:111]\n"
    "v_mfma_f32_16x16x32_f16 v[112:115], v[68:71], v[100:103], v[112:115]\n"
    "ds_read_b128 v[100:103], v10 offset:1408\n"
    "v_mfma_f32_16x16x32_f16 v[108:111], v[40:43], v[104:107], v[108:111]\n"
    "v_mfma_f32_16x16x32_f16 v[112:115], v[72:75], v[104:107], v[112:115]\n"
    "ds_read_b128 v[104:107], v10 offset:1472\n"
    "s_waitcnt lgkmcnt(6)\n"
    "v_mfma_f32_16x16x32_f16 v[112:115], v[12:15], v[76:79], v[112:115]\n"
    "v_mfma_f32_16x16x32_f16 v[116:119], v[44:47], v[76:79], v[116:119]\n"
    "v_mfma_f32_16x16x32_f16 v[120:123], v[124:127], v[76:79], v[120:123]\n"
    "ds_read_b128 v[76:79], v10 offset:512\n"
    "v_mfma_f32_16x16x32_f16 v[112:115], v[16:19], v[80:83], v[112:115]\n"
    "v_mfma_f32_16x16x32_f16 v[116:119], v[48:51], v[80:83], v[116:119]\n"
    "ds_read_b128 v[80:83], v10 offset:576\n"
    "s_waitcnt lgkmcnt(6)\n"
    "v_mfma_f32_16x16x32_f16 v[112:115], v[20:23], v[84:87], v[112:115]\n"
    "v_mov_b32 v9, 0x10000\n"
    "ds_write_b128 v11, v[108:111] offset:5120\n"
    "ds_add_u32 v4, v9 offset:0\n"
    "v_mfma_f32_16x16x32_f16 v[116:119], v[52:55], v[84:87], v[116:119]\n"
    "ds_read_b128 v[84:87], v10 offset:640\n"
    "v_mfma_f32_16x16x32_f16 v[112:115], v[24:27], v[88:91], v[112:115]\n"
    "v_mfma_f32_16x16x32_f16 v[116:119], v[56:59], v[88:91], v[116:119]\n"
    "ds_read_b128 v[88:91], v10 offset:704\n"
    "s_waitcnt lgkmcnt(8)\n"
    "v_mfma_f32_16x16x32_f16 v[112:115], v[28:31], v[92:95], v[112:115]\n"
    "v_mfma_f32_16x16x32_f16 v[116:119], v[60:63], v[92:95], v[116:119]\n"
    "ds_read_b128 v[92:95], v10 offset:768\n"
    "v_mfma_f32_16x16x32_f16 v[112:115], v[32:35], v[96:99], v[112:115]\n"
    "v_mfma_f32_16x16x32_f16 v[116:119], v[64:67], v[96:99], v[116:119]\n"
    "ds_read_b128 v[96:99], v10 offset:832\n"
    "s_waitcnt lgkmcnt(8)\n"
    "v_mfma_f32_16x16x32_f16 v[112:115], v[36:39], v[100:103], v[112:115]\n"
    "v_mfma_f32_16x16x32_f16 v[116:119], v[68:71], v[100:103], v[116:119]\n"
    "ds_read_b128 v[100:103], v10 offset:896\n"
    "v_mfma_f32_16x16x32_f16 v[112:115], v[40:43], v[104:107], v[112:115]\n"
    "v_mfma_f32_16x16x32_f16 v[116:119], v[72:75], v[104:107], v[116:119]\n"
    "ds_read_b128 v[104:107], v10 offset:960\n"
    "s_waitcnt lgkmcnt(8)\n"
    "v_mfma_f32_16x16x32_f16 v[116:119], v[12:15], v[76:79], v[116:119]\n"
    "v_mfma_f32_16x16x32_f16 v[120:123], v[44:47], v[76:79], v[120:123]\n"
    "ds_read_b128 v[76:79], v10 offset:0\n"
    "v_mfma_f32_16x16x32_f16 v[116:119], v[16:19], v[80:83], v[116:119]\n"
    "v_mfma_f32_16x16x32_f16 v[120:123], v[48:51], v[80:83], v[120:123]\n"
    "ds_read_b128 v[80:83], v10 offset:64\n"
    "s_waitcnt lgkmcnt(6)\n"
    "v_mfma_f32_16x16x32_f16 v[116:119], v[20:23], v[84:87], v[116:119]\n"
    "v_mov_b32 v9, 0x100000\n"
    "ds_write_b128 v11, v[112:115] offset:6400\n"
    "ds_add_u32 v4, v9 offset:0\n"
    "v_mfma_f32_16x16x32_f16 v[120:123], v[52:55], v[84:87], v[120:123]\n"
    "ds_read_b128 v[84:87], v10 offset:128\n"
    "v_mfma_f32_16x16x32_f16 v[116:119], v[24:27], v[88:91], v[116:119]\n"
    "v_mfma_f32_16x16x32_f16 v[120:123], v[56:59], v[88:91], v[120:123]\n"
    "ds_read_b128 v[88:91], v10 offset:192\n"
    "s_waitcnt lgkmcnt(8)\n"
    "v_mfma_f32_16x16x32_f16 v[116:119], v[28:31], v[92:95], v[116:119]\n"
    "v_mfma_f32_16x16x32_f16 v[120:123], v[60:63], v[92:95], v[120:123]\n"
    "ds_read_b128 v[92:95], v10 offset:256\n"
    "v_mfma_f32_16x16x32_f16 v[116:119], v[32:35], v[96:99], v[116:119]\n"
    "v_mfma_f32_16x16x32_f16 v[120:123], v[64:67], v[96:99], v[120:123]\n"
    "ds_read_b128 v[96:99], v10 offset:320\n"
    "s_waitcnt lgkmcnt(8)\n"
    "v_mfma_f32_16x16x32_f16 v[116:119], v[36:39], v[100:103], v[116:119]\n"
    "v_mfma_f32_16x16x32_f16 v[120:123], v[68:71], v[100:103], v[120:123]\n"
    "ds_read_b128 v[100:103], v10 offset:384\n"
    "v_mfma_f32_16x16x32_f16 v[116:119], v[40:43], v[104:107], v[116:119]\n"
    "v_mfma_f32_16x16x32_f16 v[120:123], v[72:75], v[104:107], v[120:123]\n"
    "ds_read_b128 v[104:107], v10 offset:448\n"
    "s_waitcnt lgkmcnt(8)\n"
    "v_mfma_f32_16x16x32_f16 v[120:123], v[12:15], v[76:79], v[120:123]\n"
    "v_mfma_f32_16x16x32_f16 v[120:123], v[16:19], v[80:83], v[120:123]\n"
    "s_waitcnt lgkmcnt(4)\n"
    "v_mfma_f32_16x16x32_f16 v[120:123], v[20:23], v[84:87], v[120:123]\n"
    "v_mov_b32 v9, 0x1000000\n"
    "ds_write_b128 v11, v[116:119] offset:7680\n"
    "ds_add_u32 v4, v9 offset:0\n"
    "v_mfma_f32_16x16x32_f16 v[120:123], v[24:27], v[88:91], v[120:123]\n"
    "s_waitcnt lgkmcnt(4)\n"
    "v_mfma_f32_16x16x32_f16 v[120:123], v[28:31], v[92:95], v[120:123]\n"
    "v_mfma_f32_16x16x32_f16 v[120:123], v[32:35], v[96:99], v[120:123]\n"
    "s_waitcnt lgkmcnt(2)\n"
    "v_mfma_f32_16x16x32_f16 v[120:123], v[36:39], v[100:103], v[120:123]\n"
    "v_mfma_f32_16x16x32_f16 v[120:123], v[40:43], v[104:107], v[120:123]\n"
    "v_mov_b32 v9, 0x10000000\n"
    "s_nop 6\n"
    "ds_write_b128 v11, v[120:123] offset:8960\n"
    "ds_add_u32 v4, v9 offset:0\n"
    "s_branch Lr3%=\n"
    "Lr4%=:\n"
    "s_load_dwordx4 s[84:87], %[k], 0x40\n"
    "s_load_dwordx2 s[88:89], %[k], 0x50\n"
    "s_and_b32 s46, %[b], 7\n"
    "s_lshr_b32 s47, %[b], 3\n"
    "s_mul_i32 s47, s47, 3\n"
    "s_add_u32 s47, s47, s50\n"
    "v_and_b32 v9, 63, %[t]\n"
    "s_mul_i32 s48, s46, 72\n"
    "s_add_u32 s48, s48, s47\n"
    "s_mul_i32 s48, s48, 72\n"
    "v_min_u32 v5, 35, v9\n"
    "v_add_lshl_u32 v121, v5, s48, 7\n"
    "v_add_u32 v122, 4608, v121\n"
    "s_mul_i32 s48, s47, 0\n"
    "v_add_u32 v6, s48, v9\n"
    "v_mul_u32_u24 v7, 7282, v6\n"
    "v_lshrrev_b32 v7, 16, v7\n"
    "v_mul_u32_u24 v7, 8064, v7\n"
    "v_lshl_add_u32 v6, v6, 7, v7\n"
    "v_add_u32 v6, 5308416, v6\n"
    "v_cmp_gt_u32 vcc, 0, v9\n"
    "v_subrev_u32 v9, 0, v9\n"
    "v_min_u32 v9, 15, v9\n"
    "s_mul_i32 s47, s47, 8\n"
    "v_lshrrev_b32 v7, 1, v9\n"
    "v_add_u32 v7, s47, v7\n"
    "v_mul_u32_u24 v7, 9216, v7\n"
    "v_and_b32 v9, 1, v9\n"
    "s_cmp_lt_u32 s47, 288\n"
    "s_cselect_b32 s48, 1, 0\n"
    "v_or_b32 v9, s48, v9\n"
    "s_waitcnt lgkmcnt(0)\n"
    "s_lshr_b32 s47, s84, 7\n"
    "s_add_u32 s47, s47, s46\n"
    "s_sub_u32 s47, 3, s47\n"
    "s_and_b32 s47, s47, 7\n"
    "s_sub_u32 s48, 8, s47\n"
    "v_mul_u32_u24 v9, s48, v9\n"
    "v_add_lshl_u32 v9, v9, s47, 7\n"
    "v_add_u32 v7, v7, v9\n"
    "v_cndmask_b32 v6, v7, v6, vcc\n"
    "s_mul_i32 s46, s46, 1152\n"
    "v_add_u32 v120, s46, v6\n"
    "s_setprio 3\n"
    "s_mov_b32 s97, 0x1fffff\n"
    "Lkr%=:\n"
    "ds_read_b32 v17, v4 offset:4\n"
    "s_waitcnt lgkmcnt(0)\n"
    "v_readfirstlane_b32 s96, v17\n"
    "s_and_b32 s96, s96, 0x40000\n"
    "s_cmp_eq_u32 s96, 0x40000\n"
    "s_cbranch_scc1 Lkr%=x\n"
    "s_sleep 2\n"
    "s_sub_u32 s97, s97, 1\n"
    "s_cbranch_scc0 Lkr%=\n"
    "Lkr%=x:\n"
    "s_lshl_b64 exec, -1, 0\n"
    "global_load_dword v126, v120, s[84:85]\n"
    "global_load_dword v126, v120, s[86:87]\n"
    "s_mov_b64 exec, -1\n"
    "global_load_dword v126, v121, s[88:89]\n"
    "global_load_dword v126, v122, s[88:89]\n"
    "Lr3%=:\n"
    "s_setprio 0\n"
    "Lr5%=:\n"
    "s_branch Lcl%=\n"
    "Lcs%=:\n"
    "s_mul_i32 s51, s50, 3200\n"
    "s_add_u32 s51, s51, 125088\n"
    "v_mul_u32_u24 v3, 400, v1\n"
    "v_lshlrev_b32 v4, 4, v2\n"
    "v_add3_u32 v3, v3, v4, s51\n"
    "v_and_b32 v10, 3, %[t]\n"
    "v_mul_u32_u24 v4, 800, v10\n"
    "v_lshl_add_u32 v4, v1, 4, v4\n"
    "v_add_u32 v4, s51, v4\n"
    "v_and_b32 v11, 4, %[t]\n"
    "v_lshl_add_u32 v6, v11, 1, v4\n"
    "v_mul_u32_u24 v12, 8421504, v11\n"
    "v_lshrrev_b32 v13, 1, v11\n"
    "v_add_u32 v12, v12, v13\n"
    "v_add_u32 v12, 0x05040100, v12\n"
    "s_lshr_b32 s51, %[b], 1\n"
    "s_mul_i32 s51, s51, 24704\n"
    "s_and_b32 s52, %[b], 1\n"
    "s_lshl_b32 s52, s52, 6\n"
    "s_add_u32 s51, s51, s52\n"
    "s_mul_i32 s52, s50, 6144\n"
    "s_add_u32 s51, s51, s52\n"
    "s_add_u32 s51, s51, 7241728\n"
    "v_bfe_u32 v13, %[t], 2, 4\n"
    "v_lshlrev_b32 v13, 7, v13\n"
    "v_lshl_add_u32 v13, v10, 4, v13\n"
    "v_add_u32 v13, s51, v13\n"
    "s_lshl4_add_u32 s51, s50, 158464\n"
    "v_lshl_add_u32 v14, v1, 6, s51\n"
    "v_mov_b32 v42, 1\n"
    "s_mov_b32 s53, 3\n"
    "s_movk_i32 s56, 0x1000\n"
    "v_and_b32 v15, 63, %[t]\n"
    "v_min_u32 v111, 1, v15\n"
    "v_mul_u32_u24 v111, 40, v111\n"
    "v_add_u32 v111, v111, v15\n"
    "v_mov_b32 v8, 159488\n"
    "v_lshl_add_u32 v111, v111, 2, v8\n"
    "v_lshlrev_b32 v8, 2, v5\n"
    "v_sub_u32 v105, 97308, v8\n"
    "v_lshrrev_b32 v8, 3, v5\n"
    "v_mul_u32_u24 v106, 24704, v8\n"
    "v_lshl_add_u32 v106, v2, 4, v106\n"
    "s_lshl_b32 s51, %[b], 7\n"
    "s_add_u32 s51, s51, 7241728\n"
    "v_add_u32 v106, s51, v106\n"
    "v_lshlrev_b32 v107, 4, v5\n"
    "s_mul_i32 s51, %[b], 16512\n"
    "s_add_u32 s51, s51, 139264\n"
    "v_add_u32 v117, s51, v107\n"
    "s_and_b32 s51, %[b], 0\n"
    "s_mul_i32 s51, s51, 16512\n"
    "s_add_u32 s51, s51, 10535936\n"
    "v_add_u32 v107, s51, v107\n"
    "v_and_b32 v9, 4, v1\n"
    "v_add_u32 v9, v9, v10\n"
    "v_mul_u32_u24 v122, 16512, v9\n"
    "v_and_b32 v9, 3, v1\n"
    "v_lshl_add_u32 v122, v9, 4, v122\n"
    "v_and_b32 v8, 15, v8\n"
    "v_bfe_u32 v9, v5, 1, 2\n"
    "v_mul_u32_u24 v9, 320, v9\n"
    "v_lshl_add_u32 v115, v8, 4, v9\n"
    "v_and_b32 v9, 1, v5\n"
    "v_lshl_add_u32 v115, v9, 3, v115\n"
    "s_lshr_b32 s84, s50, 1\n"
    "s_mul_i32 s51, s84, 1280\n"
    "v_add_u32 v115, s51, v115\n"
    "v_add_u32 v116, 64192, v115\n"
    "s_lshl_b32 s84, s84, 2\n"
    "s_and_b32 s51, %[b], 1\n"
    "v_min_u32 v8, 2, v1\n"
    "v_add_u32 v8, s51, v8\n"
    "v_cmp_ne_u32 vcc, 3, v8\n"
    "v_min_u32 v9, 2, v10\n"
    "v_mul_u32_u24 v9, 27648, v9\n"
    "v_cndmask_b32 v8, 0, v8, vcc\n"
    "v_lshl_add_u32 v9, v8, 3, v9\n"
    "s_mul_i32 s52, %[b], 24\n"
    "s_add_u32 s52, s52, 4096\n"
    "v_add_u32 v9, s52, v9\n"
    "v_mul_u32_u24 v108, 1152, v11\n"
    "v_add_u32 v108, v108, v9\n"
    "v_add_u32 v114, 9216, v9\n"
    "v_add_lshl_u32 v109, v8, s51, 4\n"
    "v_cmp_ne_u32 vcc, 3, v10\n"
    "s_nop 1\n"
    "v_cndmask_b32 v110, 0, 1.0, vcc\n"
    "s_waitcnt vmcnt(0) lgkmcnt(0)\n"
    "v_readfirstlane_b32 s44, v7\n"
    "Lun%=:\n"
    "ds_write_b96 v3, v[16:18] offset:0\n"
    "ds_write_b96 v3, v[20:22] offset:128\n"
    "ds_write_b96 v3, v[24:26] offset:256\n"
    "ds_read2_b32 v[44:45], v6 offset0:0 offset1:100\n"
    "ds_read2_b32 v[56:57], v4 offset0:1 offset1:101\n"
    "ds_read2_b32 v[48:49], v6 offset0:32 offset1:132\n"
    "ds_read2_b32 v[58:59], v4 offset0:33 offset1:133\n"
    "ds_read2_b32 v[52:53], v6 offset0:64 offset1:164\n"
    "ds_read2_b32 v[60:61], v4 offset0:65 offset1:165\n"
    "v_mov_b64 v[40:41], 0\n"
    "s_add_u32 s45, s44, 1\n"
    "v_dot2c_f32_bf16 v40, 0x3f803f80, v16\n"
    "v_dot2c_f32_bf16 v41, v16, v16\n"
    "v_dot2c_f32_bf16 v40, 0x3f803f80, v17\n"
    "v_dot2c_f32_bf16 v41, v17, v17\n"
    "v_dot2c_f32_bf16 v40, 0x3f803f80, v18\n"
    "v_dot2c_f32_bf16 v41, v18, v18\n"
    "v_dot2c_f32_bf16 v40, 0x3f803f80, v20\n"
    "v_dot2c_f32_bf16 v41, v20, v20\n"
    "v_dot2c_f32_bf16 v40, 0x3f803f80, v21\n"
    "v_dot2c_f32_bf16 v41, v21, v21\n"
    "v_dot2c_f32_bf16 v40, 0x3f803f80, v22\n"
    "v_dot2c_f32_bf16 v41, v22, v22\n"
    "v_dot2c_f32_bf16 v40, 0x3f803f80, v24\n"
    "v_dot2c_f32_bf16 v41, v24, v24\n"
    "v_dot2c_f32_bf16 v40, 0x3f803f80, v25\n"
    "v_dot2c_f32_bf16 v41, v25, v25\n"
    "v_dot2c_f32_bf16 v40, 0x3f803f80, v26\n"
    "v_dot2c_f32_bf16 v41, v26, v26\n"
    "v_mov_b32 v47, s45\n"
    "v_mov_b32 v51, s45\n"
    "v_mov_b32 v55, s45\n"
    "s_waitcnt lgkmcnt(0)\n"
    "v_perm_b32 v46, v57, v56, v12\n"
    "v_add_f32_dpp v40, v40, v40 quad_perm:[1,0,3,2] row_mask:0xf bank_mask:0xf bound_ctrl:1\n"
    "v_add_f32_dpp v41, v41, v41 quad_perm:[1,0,3,2] row_mask:0xf bank_mask:0xf bound_ctrl:1\n"
    "buffer_store_dwordx4 v[44:47], v13, s[40:43], 0 offen sc1\n"
    "v_perm_b32 v50, v59, v58, v12\n"
    "v_add_f32_dpp v40, v40, v40 quad_perm:[2,3,0,1] row_mask:0xf bank_mask:0xf bound_ctrl:1\n"
    "v_add_f32_dpp v41, v41, v41 quad_perm:[2,3,0,1] row_mask:0xf bank_mask:0xf bound_ctrl:1\n"
    "buffer_store_dwordx4 v[48:51], v13, s[40:43], 0 offen offset:2048 sc1\n"
    "v_perm_b32 v54, v61, v60, v12\n"
    "v_add_f32_dpp v40, v40, v40 row_half_mirror row_mask:0xf bank_mask:0xf bound_ctrl:1\n"
    "v_add_f32_dpp v41, v41, v41 row_half_mirror row_mask:0xf bank_mask:0xf bound_ctrl:1\n"
    "buffer_store_dwordx4 v[52:55], v13, s[40:43], s56 offen sc1\n"
    "ds_write_b96 v14, v[40:42]\n"
    "s_cmp_lt_u32 %[b], 128\n"
    "s_cbranch_scc1 Lsn%=\n"
    "s_cmp_eq_u32 s53, 2\n"
    "s_cbranch_scc1 Lsn%=\n"
    "s_mov_b32 s53, 2\n"
    "v_add_u32 v13, 790528, v13\n"
    "v_add_u32 v14, 512, v14\n"
    "v_mov_b64 v[16:17], v[28:29]\n"
    "v_mov_b32 v18, v30\n"
    "v_mov_b64 v[20:21], v[32:33]\n"
    "v_mov_b32 v22, v34\n"
    "v_mov_b64 v[24:25], v[36:37]\n"
    "v_mov_b32 v26, v38\n"
    "s_branch Lun%=\n"
    "Lsn%=:\n"
    "s_cmp_lt_u32 s50, s53\n"
    "s_cbranch_scc1 Lsd%=\n"
    "s_sub_u32 s57, 3, s50\n"
    "s_lshl_b32 s57, s57, 9\n"
    "v_and_b32 v15, 31, %[t]\n"
    "s_add_u32 s51, s57, 158464\n"
    "v_lshl_add_u32 v15, v15, 4, s51\n"
    "v_mov_b32 v43, 158208\n"
    "ds_write_b32 v43, v55\n"
    "s_movk_i32 s97, 0x7fff\n"
    "Lsp%=:\n"
    "ds_read_b96 v[56:58], v15\n"
    "s_waitcnt lgkmcnt(0)\n"
    "v_cmp_ne_u32 vcc, 1, v58\n"
    "s_cbranch_vccz Lsq%=\n"
    "s_sub_u32 s97, s97, 1\n"
    "s_cbranch_scc0 Lsp%=\n"
    "Lsq%=:\n"
    "v_add_f32_dpp v56, v56, v56 quad_perm:[1,0,3,2] row_mask:0xf bank_mask:0xf bound_ctrl:1\n"
    "v_add_f32_dpp v57, v57, v57 quad_perm:[1,0,3,2] row_mask:0xf bank_mask:0xf bound_ctrl:1\n"
    "s_lshl_b32 s51, %[b], 6\n"
    "v_add_f32_dpp v56, v56, v56 quad_perm:[2,3,0,1] row_mask:0xf bank_mask:0xf bound_ctrl:1\n"
    "v_add_f32_dpp v57, v57, v57 quad_perm:[2,3,0,1] row_mask:0xf bank_mask:0xf bound_ctrl:1\n"
    "v_mul_f32 v56, 0x3ae38e39, v56\n"
    "v_mul_f32 v57, 0x3ae38e39, v57\n"
    "v_fma_f32 v57, -v56, v56, v57\n"
    "v_max_f32 v57, 0, v57\n"
    "v_cvt_pk_bf16_f32 v57, 0, v57\n"
    "v_add_f32 v57, 0x3727c5ac, v57\n"
    "v_cvt_pk_bf16_f32 v57, 0, v57\n"
    "v_sqrt_f32 v57, v57\n"
    "v_mov_b32_dpp v58, v56 row_shl:4 row_mask:0xf bank_mask:0xf bound_ctrl:1\n"
    "v_cvt_pk_bf16_f32 v57, 0, v57\n"
    "v_rcp_f32 v60, v57\n"
    "v_cvt_pk_bf16_f32 v62, v56, v58\n"
    "v_mov_b32 v63, s45\n"
    "s_lshl3_add_u32 s51, s57, s51\n"
    "v_mov_b32_dpp v61, v60 row_shl:4 row_mask:0xf bank_mask:0xf bound_ctrl:1\n"
    "s_add_u32 s51, s51, 10535936\n"
    "s_mov_b32 exec_lo, 16843009\n"
    "s_mov_b32 exec_hi, 0\n"
    "buffer_store_dwordx4 v[60:63], v122, s[40:43], s51 offen sc1\n"
    "s_mov_b64 exec, -1\n"
    "Lsd%=:\n"
    "s_mov_b32 s44, s45\n"
    "s_mov_b32 s45, s84\n"
    "v_lshrrev_b64 v[8:9], v109, s[54:55]\n"
    "v_lshlrev_b32 v109, 16, v8\n"
    "s_movk_i32 s97, 128\n"
    "Lxw%=:\n"
    "s_memrealtime s[46:47]\n"
    "s_waitcnt lgkmcnt(0)\n"
    "s_sub_u32 s46, s46, s58\n"
    "s_cmp_gt_u32 s46, 200\n"
    "s_cbranch_scc1 Lxv%=\n"
    "s_nop 7\n"
    "s_sub_u32 s97, s97, 1\n"
    "s_cbranch_scc0 Lxw%=\n"
    "Lxv%=:\n"
    "s_setprio 3\n"
    "buffer_load_dwordx2 v[102:103], v108, s[40:43], 0 offen sc1\n"
    "s_mov_b32 s94, 0\n"
    "s_mov_b32 s95, 0\n"
    "buffer_load_dwordx4 v[64:67], v106, s[40:43], s94 offen sc1\n"
    "buffer_load_dwordx4 v[68:71], v107, s[40:43], s95 offen sc1\n"
    "s_mov_b32 s94, 790528\n"
    "s_mov_b32 s95, 4096\n"
    "buffer_load_dwordx4 v[72:75], v106, s[40:43], s94 offen sc1\n"
    "buffer_load_dwordx4 v[76:79], v107, s[40:43], s95 offen sc1\n"
    "s_waitcnt vmcnt(4)\n"
    "v_cmp_ne_u32 vcc, s44, v103\n"
    "s_cmp_lg_u64 vcc, 0\n"
    "s_cbranch_scc1 Lms%=\n"
    "Lmb%=:\n"
    "v_mul_f32 v102, v102, v110\n"
    "v_mov_b32 v104, 226\n"
    "s_mov_b32 s94, 0x42000000\n"
    "v_add_f32_dpp v102, v102, v102 quad_perm:[1,0,3,2] row_mask:0xf bank_mask:0xf bound_ctrl:1\n"
    "v_mov_b32 v112, 1\n"
    "s_nop 0\n"
    "v_add_f32_dpp v102, v102, v102 quad_perm:[2,3,0,1] row_mask:0xf bank_mask:0xf bound_ctrl:1\n"
    "v_cvt_pk_bf16_f32 v102, 0, v102\n"
    "s_nop 1\n"
    "v_mov_b32_dpp v103, v102 row_shl:4 row_mask:0xf bank_mask:0xf bound_ctrl:1\n"
    "v_add_f32 v103, 1.0, v103\n"
    "v_cvt_pk_bf16_f32 v103, 0, v103\n"
    "v_fma_f32 v96, |v103|, s94, |v102|\n"
    "v_lshrrev_b32 v96, 23, v96\n"
    "v_med3_u32 v96, v96, 26, v104\n"
    "v_lshlrev_b32 v96, 23, v96\n"
    "v_sub_u32 v97, 0x84000000, v96\n"
    "v_add_u32 v104, 0xfb000000, v96\n"
    "v_mul_f32 v103, v103, v97\n"
    "v_mul_f32 v102, v102, v97\n"
    "v_mul_f32 v113, v109, v104\n"
    "v_readlane_b32 s84, v103, 0\n"
    "v_readlane_b32 s87, v102, 0\n"
    "v_readlane_b32 s85, v103, 8\n"
    "v_readlane_b32 s88, v102, 8\n"
    "v_readlane_b32 s86, v103, 16\n"
    "v_readlane_b32 s89, v102, 16\n"
    "v_mov_b32 v96, s84\n"
    "v_mov_b32 v97, s87\n"
    "v_mov_b32 v98, s85\n"
    "v_mov_b32 v99, s88\n"
    "v_mov_b32 v100, s86\n"
    "v_mov_b32 v101, s89\n"
    "v_readlane_b32 s84, v113, 0\n"
    "v_readlane_b32 s87, v104, 0\n"
    "v_readlane_b32 s85, v113, 8\n"
    "v_readlane_b32 s88, v104, 8\n"
    "v_readlane_b32 s86, v113, 16\n"
    "v_readlane_b32 s89, v104, 16\n"
    "s_waitcnt vmcnt(2)\n"
    "v_cmp_ne_u32 vcc, s44, v67\n"
    "v_cmp_ne_u32 s[94:95], s44, v71\n"
    "s_or_b64 vcc, vcc, s[94:95]\n"
    "s_cbranch_scc1 Lu0s%=\n"
    "Lu0b%=:\n"
    "v_lshlrev_b32 v102, 16, v70\n"
    "v_and_b32 v103, 0xffff0000, v70\n"
    "v_lshlrev_b32 v16, 16, v64\n"
    "v_lshlrev_b32 v17, 16, v65\n"
    "v_and_b32 v18, 0xffff0000, v64\n"
    "v_and_b32 v19, 0xffff0000, v65\n"
    "v_lshlrev_b32 v20, 16, v66\n"
    "v_and_b32 v21, 0xffff0000, v66\n"
    "v_pk_add_f32 v[40:41], v[16:17], v[102:103] neg_lo:[0,1] neg_hi:[0,1]\n"
    "v_pk_add_f32 v[42:43], v[18:19], v[102:103] neg_lo:[0,1] neg_hi:[0,1]\n"
    "v_pk_add_f32 v[44:45], v[20:21], v[102:103] neg_lo:[0,1] neg_hi:[0,1]\n"
    "v_cvt_pk_bf16_f32 v40, 0, v40\n"
    "v_cvt_pk_bf16_f32 v41, 0, v41\n"
    "v_cvt_pk_bf16_f32 v42, 0, v42\n"
    "v_cvt_pk_bf16_f32 v43, 0, v43\n"
    "v_cvt_pk_bf16_f32 v44, 0, v44\n"
    "v_cvt_pk_bf16_f32 v45, 0, v45\n"
    "v_pk_mul_f32 v[40:41], v[40:41], v[68:69]\n"
    "v_pk_mul_f32 v[42:43], v[42:43], v[68:69]\n"
    "v_pk_mul_f32 v[44:45], v[44:45], v[68:69]\n"
    "v_cvt_pk_bf16_f32 v40, 0, v40\n"
    "v_cvt_pk_bf16_f32 v41, 0, v41\n"
    "v_cvt_pk_bf16_f32 v42, 0, v42\n"
    "v_cvt_pk_bf16_f32 v43, 0, v43\n"
    "v_cvt_pk_bf16_f32 v44, 0, v44\n"
    "v_cvt_pk_bf16_f32 v45, 0, v45\n"
    "v_pk_mul_f32 v[40:41], v[40:41], v[96:97] op_sel_hi:[1,0]\n"
    "v_pk_mul_f32 v[42:43], v[42:43], v[98:99] op_sel_hi:[1,0]\n"
    "v_pk_mul_f32 v[44:45], v[44:45], v[100:101] op_sel_hi:[1,0]\n"
    "v_cvt_pk_bf16_f32 v40, 0, v40\n"
    "v_cvt_pk_bf16_f32 v41, 0, v41\n"
    "v_cvt_pk_bf16_f32 v42, 0, v42\n"
    "v_cvt_pk_bf16_f32 v43, 0, v43\n"
    "v_cvt_pk_bf16_f32 v44, 0, v44\n"
    "v_cvt_pk_bf16_f32 v45, 0, v45\n"
    "v_pk_add_f32 v[40:41], v[40:41], v[96:97] op_sel:[0,1]\n"
    "v_pk_add_f32 v[42:43], v[42:43], v[98:99] op_sel:[0,1]\n"
    "v_pk_add_f32 v[44:45], v[44:45], v[100:101] op_sel:[0,1]\n"
    "v_cvt_pk_bf16_f32 v40, 0, v40\n"
    "v_cvt_pk_bf16_f32 v41, 0, v41\n"
    "v_cvt_pk_bf16_f32 v42, 0, v42\n"
    "v_cvt_pk_bf16_f32 v43, 0, v43\n"
    "v_cvt_pk_bf16_f32 v44, 0, v44\n"
    "v_cvt_pk_bf16_f32 v45, 0, v45\n"
    "v_cvt_pkrtz_f16_f32 v102, v41, v40\n"
    "v_cvt_pkrtz_f16_f32 v103, v43, v42\n"
    "v_cvt_pkrtz_f16_f32 v104, v45, v44\n"
    "ds_write_b32 v105, v102 offset:3072\n"
    "ds_write_b32 v105, v103 offset:11264\n"
    "ds_write_b32 v105, v104 offset:19456\n"
    "s_mov_b32 s94, 1581056\n"
    "s_mov_b32 s95, 8192\n"
    "buffer_load_dwordx4 v[80:83], v106, s[40:43], s94 offen sc1\n"
    "buffer_load_dwordx4 v[84:87], v107, s[40:43], s95 offen sc1\n"
    "s_mov_b32 s94, 2371584\n"
    "s_mov_b32 s95, 12288\n"
    "buffer_load_dwordx4 v[88:91], v106, s[40:43], s94 offen sc1\n"
    "buffer_load_dwordx4 v[92:95], v107, s[40:43], s95 offen sc1\n"
    "buffer_load_dwordx2 v[118:119], v114, s[40:43], 0 offen sc1\n"
    "v_mov_b32 v102, 0x10000\n"
    "ds_add_u32 v111, v102 offset:4\n"
    "s_waitcnt vmcnt(5)\n"
    "v_cmp_ne_u32 vcc, s44, v75\n"
    "v_cmp_ne_u32 s[94:95], s44, v79\n"
    "s_or_b64 vcc, vcc, s[94:95]\n"
    "s_cbranch_scc1 Lu1s%=\n"
    "Lu1b%=:\n"
    "v_lshlrev_b32 v102, 16, v78\n"
    "v_and_b32 v103, 0xffff0000, v78\n"
    "v_lshlrev_b32 v22, 16, v72\n"
    "v_lshlrev_b32 v23, 16, v73\n"
    "v_and_b32 v24, 0xffff0000, v72\n"
    "v_and_b32 v25, 0xffff0000, v73\n"
    "v_lshlrev_b32 v26, 16, v74\n"
    "v_and_b32 v27, 0xffff0000, v74\n"
    "v_pk_add_f32 v[46:47], v[22:23], v[102:103] neg_lo:[0,1] neg_hi:[0,1]\n"
    "v_pk_add_f32 v[48:49], v[24:25], v[102:103] neg_lo:[0,1] neg_hi:[0,1]\n"
    "v_pk_add_f32 v[50:51], v[26:27], v[102:103] neg_lo:[0,1] neg_hi:[0,1]\n"
    "v_cvt_pk_bf16_f32 v46, 0, v46\n"
    "v_cvt_pk_bf16_f32 v47, 0, v47\n"
    "v_cvt_pk_bf16_f32 v48, 0, v48\n"
    "v_cvt_pk_bf16_f32 v49, 0, v49\n"
    "v_cvt_pk_bf16_f32 v50, 0, v50\n"
    "v_cvt_pk_bf16_f32 v51, 0, v51\n"
    "v_pk_mul_f32 v[46:47], v[46:47], v[76:77]\n"
    "v_pk_mul_f32 v[48:49], v[48:49], v[76:77]\n"
    "v_pk_mul_f32 v[50:51], v[50:51], v[76:77]\n"
    "v_cvt_pk_bf16_f32 v46, 0, v46\n"
    "v_cvt_pk_bf16_f32 v47, 0, v47\n"
    "v_cvt_pk_bf16_f32 v48, 0, v48\n"
    "v_cvt_pk_bf16_f32 v49, 0, v49\n"
    "v_cvt_pk_bf16_f32 v50, 0, v50\n"
    "v_cvt_pk_bf16_f32 v51, 0, v51\n"
    "v_pk_mul_f32 v[46:47], v[46:47], v[96:97] op_sel_hi:[1,0]\n"
    "v_pk_mul_f32 v[48:49], v[48:49], v[98:99] op_sel_hi:[1,0]\n"
    "v_pk_mul_f32 v[50:51], v[50:51], v[100:101] op_sel_hi:[1,0]\n"
    "v_cvt_pk_bf16_f32 v46, 0, v46\n"
    "v_cvt_pk_bf16_f32 v47, 0, v47\n"
    "v_cvt_pk_bf16_f32 v48, 0, v48\n"
    "v_cvt_pk_bf16_f32 v49, 0, v49\n"
    "v_cvt_pk_bf16_f32 v50, 0, v50\n"
    "v_cvt_pk_bf16_f32 v51, 0, v51\n"
    "v_pk_add_f32 v[46:47], v[46:47], v[96:97] op_sel:[0,1]\n"
    "v_pk_add_f32 v[48:49], v[48:49], v[98:99] op_sel:[0,1]\n"
    "v_pk_add_f32 v[50:51], v[50:51], v[100:101] op_sel:[0,1]\n"
    "v_cvt_pk_bf16_f32 v46, 0, v46\n"
    "v_cvt_pk_bf16_f32 v47, 0, v47\n"
    "v_cvt_pk_bf16_f32 v48, 0, v48\n"
    "v_cvt_pk_bf16_f32 v49, 0, v49\n"
    "v_cvt_pk_bf16_f32 v50, 0, v50\n"
    "v_cvt_pk_bf16_f32 v51, 0, v51\n"
    "v_cvt_pkrtz_f16_f32 v102, v47, v46\n"
    "v_cvt_pkrtz_f16_f32 v103, v49, v48\n"
    "v_cvt_pkrtz_f16_f32 v104, v51, v50\n"
    "ds_write_b32 v105, v102 offset:2048\n"
    "ds_write_b32 v105, v103 offset:10240\n"
    "ds_write_b32 v105, v104 offset:18432\n"
    "ds_add_u32 v111, v112 offset:4\n"
    "v_mov_b32 v112, 0x100\n"
    "s_waitcnt vmcnt(0)\n"
    "v_cmp_ne_u32 vcc, s44, v83\n"
    "v_cmp_ne_u32 s[94:95], s44, v87\n"
    "s_or_b64 vcc, vcc, s[94:95]\n"
    "s_cbranch_scc1 Lu2s%=\n"
    "Lu2b%=:\n"
    "v_cmp_ne_u32 vcc, s44, v91\n"
    "v_cmp_ne_u32 s[94:95], s44, v95\n"
    "s_or_b64 vcc, vcc, s[94:95]\n"
    "s_cbranch_scc1 Lu3s%=\n"
    "Lu3b%=:\n"
    "v_cmp_ne_u32 vcc, s44, v119\n"
    "s_cmp_lg_u64 vcc, 0\n"
    "s_cbranch_scc1 Las%=\n"
    "Lab%=:\n"
    "v_lshlrev_b32 v102, 16, v86\n"
    "v_and_b32 v103, 0xffff0000, v86\n"
    "v_lshlrev_b32 v28, 16, v80\n"
    "v_lshlrev_b32 v29, 16, v81\n"
    "v_and_b32 v30, 0xffff0000, v80\n"
    "v_and_b32 v31, 0xffff0000, v81\n"
    "v_lshlrev_b32 v32, 16, v82\n"
    "v_and_b32 v33, 0xffff0000, v82\n"
    "v_sub_f32 v52, v28, v102\n"
    "v_sub_f32 v53, v29, v103\n"
    "v_sub_f32 v54, v30, v102\n"
    "v_sub_f32 v55, v31, v103\n"
    "v_sub_f32 v56, v32, v102\n"
    "v_sub_f32 v57, v33, v103\n"
    "v_cvt_pk_bf16_f32 v52, 0, v52\n"
    "v_cvt_pk_bf16_f32 v53, 0, v53\n"
    "v_cvt_pk_bf16_f32 v54, 0, v54\n"
    "v_cvt_pk_bf16_f32 v55, 0, v55\n"
    "v_cvt_pk_bf16_f32 v56, 0, v56\n"
    "v_cvt_pk_bf16_f32 v57, 0, v57\n"
    "v_mul_f32 v52, v52, v84\n"
    "v_mul_f32 v53, v53, v85\n"
    "v_mul_f32 v54, v54, v84\n"
    "v_mul_f32 v55, v55, v85\n"
    "v_mul_f32 v56, v56, v84\n"
    "v_mul_f32 v57, v57, v85\n"
    "v_cvt_pk_bf16_f32 v52, 0, v52\n"
    "v_cvt_pk_bf16_f32 v53, 0, v53\n"
    "v_cvt_pk_bf16_f32 v54, 0, v54\n"
    "v_cvt_pk_bf16_f32 v55, 0, v55\n"
    "v_cvt_pk_bf16_f32 v56, 0, v56\n"
    "v_cvt_pk_bf16_f32 v57, 0, v57\n"
    "v_mul_f32 v52, v52, v96\n"
    "v_mul_f32 v53, v53, v96\n"
    "v_mul_f32 v54, v54, v98\n"
    "v_mul_f32 v55, v55, v98\n"
    "v_mul_f32 v56, v56, v100\n"
    "v_mul_f32 v57, v57, v100\n"
    "v_cvt_pk_bf16_f32 v52, 0, v52\n"
    "v_cvt_pk_bf16_f32 v53, 0, v53\n"
    "v_cvt_pk_bf16_f32 v54, 0, v54\n"
    "v_cvt_pk_bf16_f32 v55, 0, v55\n"
    "v_cvt_pk_bf16_f32 v56, 0, v56\n"
    "v_cvt_pk_bf16_f32 v57, 0, v57\n"
    "v_add_f32 v52, v52, v97\n"
    "v_add_f32 v53, v53, v97\n"
    "v_add_f32 v54, v54, v99\n"
    "v_add_f32 v55, v55, v99\n"
    "v_add_f32 v56, v56, v101\n"
    "v_add_f32 v57, v57, v101\n"
    "v_cvt_pk_bf16_f32 v52, 0, v52\n"
    "v_cvt_pk_bf16_f32 v53, 0, v53\n"
    "v_cvt_pk_bf16_f32 v54, 0, v54\n"
    "v_cvt_pk_bf16_f32 v55, 0, v55\n"
    "v_cvt_pk_bf16_f32 v56, 0, v56\n"
    "v_cvt_pk_bf16_f32 v57, 0, v57\n"
    "v_cvt_pkrtz_f16_f32 v102, v53, v52\n"
    "v_cvt_pkrtz_f16_f32 v103, v55, v54\n"
    "v_cvt_pkrtz_f16_f32 v104, v57, v56\n"
    "ds_write_b32 v105, v102 offset:1024\n"
    "ds_write_b32 v105, v103 offset:9216\n"
    "ds_write_b32 v105, v104 offset:17408\n"
    "v_lshlrev_b32 v102, 16, v94\n"
    "v_and_b32 v103, 0xffff0000, v94\n"
    "v_lshlrev_b32 v34, 16, v88\n"
    "v_lshlrev_b32 v35, 16, v89\n"
    "v_and_b32 v36, 0xffff0000, v88\n"
    "v_and_b32 v37, 0xffff0000, v89\n"
    "v_lshlrev_b32 v38, 16, v90\n"
    "v_and_b32 v39, 0xffff0000, v90\n"
    "v_sub_f32 v58, v34, v102\n"
    "v_sub_f32 v59, v35, v103\n"
    "v_sub_f32 v60, v36, v102\n"
    "v_sub_f32 v61, v37, v103\n"
    "v_sub_f32 v62, v38, v102\n"
    "v_sub_f32 v63, v39, v103\n"
    "v_cvt_pk_bf16_f32 v58, 0, v58\n"
    "v_cvt_pk_bf16_f32 v59, 0, v59\n"
    "v_cvt_pk_bf16_f32 v60, 0, v60\n"
    "v_cvt_pk_bf16_f32 v61, 0, v61\n"
    "v_cvt_pk_bf16_f32 v62, 0, v62\n"
    "v_cvt_pk_bf16_f32 v63, 0, v63\n"
    "v_mul_f32 v58, v58, v92\n"
    "v_mul_f32 v59, v59, v93\n"
    "v_mul_f32 v60, v60, v92\n"
    "v_mul_f32 v61, v61, v93\n"
    "v_mul_f32 v62, v62, v92\n"
    "v_mul_f32 v63, v63, v93\n"
    "v_cvt_pk_bf16_f32 v58, 0, v58\n"
    "v_cvt_pk_bf16_f32 v59, 0, v59\n"
    "v_cvt_pk_bf16_f32 v60, 0, v60\n"
    "v_cvt_pk_bf16_f32 v61, 0, v61\n"
    "v_cvt_pk_bf16_f32 v62, 0, v62\n"
    "v_cvt_pk_bf16_f32 v63, 0, v63\n"
    "v_mul_f32 v58, v58, v96\n"
    "v_mul_f32 v59, v59, v96\n"
    "v_mul_f32 v60, v60, v98\n"
    "v_mul_f32 v61, v61, v98\n"
    "v_mul_f32 v62, v62, v100\n"
    "v_mul_f32 v63, v63, v100\n"
    "v_cvt_pk_bf16_f32 v58, 0, v58\n"
    "v_cvt_pk_bf16_f32 v59, 0, v59\n"
    "v_cvt_pk_bf16_f32 v60, 0, v60\n"
    "v_cvt_pk_bf16_f32 v61, 0, v61\n"
    "v_cvt_pk_bf16_f32 v62, 0, v62\n"
    "v_cvt_pk_bf16_f32 v63, 0, v63\n"
    "v_add_f32 v58, v58, v97\n"
    "v_add_f32 v59, v59, v97\n"
    "v_add_f32 v60, v60, v99\n"
    "v_add_f32 v61, v61, v99\n"
    "v_add_f32 v62, v62, v101\n"
    "v_add_f32 v63, v63, v101\n"
    "v_cvt_pk_bf16_f32 v58, 0, v58\n"
    "v_cvt_pk_bf16_f32 v59, 0, v59\n"
    "v_cvt_pk_bf16_f32 v60, 0, v60\n"
    "v_cvt_pk_bf16_f32 v61, 0, v61\n"
    "v_cvt_pk_bf16_f32 v62, 0, v62\n"
    "v_cvt_pk_bf16_f32 v63, 0, v63\n"
    "v_cvt_pkrtz_f16_f32 v102, v59, v58\n"
    "v_cvt_pkrtz_f16_f32 v103, v61, v60\n"
    "v_cvt_pkrtz_f16_f32 v104, v63, v62\n"
    "ds_write_b32 v105, v102 offset:0\n"
    "ds_write_b32 v105, v103 offset:8192\n"
    "ds_write_b32 v105, v104 offset:16384\n"
    "ds_add_u32 v111, v112 offset:4\n"
    "s_setprio 1\n"
    "v_mul_f32 v118, v118, v110\n"
    "v_mov_b32 v90, 158224\n"
    "v_mov_b32 v85, s44\n"
    "v_add_f32_dpp v118, v118, v118 quad_perm:[1,0,3,2] row_mask:0xf bank_mask:0xf bound_ctrl:1\n"
    "s_nop 1\n"
    "v_add_f32_dpp v118, v118, v118 quad_perm:[2,3,0,1] row_mask:0xf bank_mask:0xf bound_ctrl:1\n"
    "v_cvt_pk_bf16_f32 v118, 0, v118\n"
    "s_nop 0\n"
    "v_readlane_b32 s94, v118, 0\n"
    "v_readlane_b32 s95, v118, 8\n"
    "v_readlane_b32 s96, v118, 16\n"
    "v_mov_b32 v77, s94\n"
    "v_mov_b32 v79, s95\n"
    "v_mov_b32 v81, s96\n"
    "ds_read_b32 v86, v111\n"
    "ds_read_b64 v[64:65], v115 offset:0\n"
    "ds_read_b64 v[66:67], v115 offset:32096\n"
    "ds_read_b64 v[68:69], v116 offset:0\n"
    "ds_read_b128 v[92:95], v90\n"
    "v_mul_f32 v40, s84, v40\n"
    "v_mul_f32 v41, s84, v41\n"
    "v_mul_f32 v42, s85, v42\n"
    "v_mul_f32 v43, s85, v43\n"
    "v_mul_f32 v44, s86, v44\n"
    "v_mul_f32 v45, s86, v45\n"
    "v_cvt_pk_bf16_f32 v40, 0, v40\n"
    "v_cvt_pk_bf16_f32 v41, 0, v41\n"
    "v_cvt_pk_bf16_f32 v42, 0, v42\n"
    "v_cvt_pk_bf16_f32 v43, 0, v43\n"
    "v_cvt_pk_bf16_f32 v44, 0, v44\n"
    "v_cvt_pk_bf16_f32 v45, 0, v45\n"
    "s_waitcnt lgkmcnt(0)\n"
    "v_readfirstlane_b32 s94, v86\n"
    "s_lshr_b32 s94, s94, s45\n"
    "s_bfe_u32 s94, s94, 0x40000\n"
    "s_cmp_lg_u32 s94, 3\n"
    "s_cbranch_scc1 Le0s%=\n"
    "Le0b%=:\n"
    "v_mul_f32 v76, s87, v92\n"
    "v_mul_f32 v78, s88, v93\n"
    "v_mul_f32 v80, s89, v94\n"
    "v_mul_f32 v64, v64, v76\n"
    "v_mul_f32 v65, v65, v76\n"
    "v_mul_f32 v66, v66, v78\n"
    "v_mul_f32 v67, v67, v78\n"
    "v_mul_f32 v68, v68, v80\n"
    "v_mul_f32 v69, v69, v80\n"
    "v_cvt_pk_bf16_f32 v64, 0, v64\n"
    "v_cvt_pk_bf16_f32 v65, 0, v65\n"
    "v_cvt_pk_bf16_f32 v66, 0, v66\n"
    "v_cvt_pk_bf16_f32 v67, 0, v67\n"
    "v_cvt_pk_bf16_f32 v68, 0, v68\n"
    "v_cvt_pk_bf16_f32 v69, 0, v69\n"
    "v_add_f32 v64, v64, v40\n"
    "v_add_f32 v65, v65, v41\n"
    "v_add_f32 v66, v66, v42\n"
    "v_add_f32 v67, v67, v43\n"
    "v_add_f32 v68, v68, v44\n"
    "v_add_f32 v69, v69, v45\n"
    "v_cvt_pk_bf16_f32 v64, 0, v64\n"
    "v_cvt_pk_bf16_f32 v65, 0, v65\n"
    "v_cvt_pk_bf16_f32 v66, 0, v66\n"
    "v_cvt_pk_bf16_f32 v67, 0, v67\n"
    "v_cvt_pk_bf16_f32 v68, 0, v68\n"
    "v_cvt_pk_bf16_f32 v69, 0, v69\n"
    "v_mul_f32 v64, v64, v77\n"
    "v_mul_f32 v65, v65, v77\n"
    "v_mul_f32 v66, v66, v79\n"
    "v_mul_f32 v67, v67, v79\n"
    "v_mul_f32 v68, v68, v81\n"
    "v_mul_f32 v69, v69, v81\n"
    "v_cvt_pk_bf16_f32 v64, 0, v64\n"
    "v_cvt_pk_bf16_f32 v65, 0, v65\n"
    "v_cvt_pk_bf16_f32 v66, 0, v66\n"
    "v_cvt_pk_bf16_f32 v67, 0, v67\n"
    "v_cvt_pk_bf16_f32 v68, 0, v68\n"
    "v_cvt_pk_bf16_f32 v69, 0, v69\n"
    "v_add_f32 v64, v64, v16\n"
    "v_add_f32 v65, v65, v17\n"
    "v_add_f32 v66, v66, v18\n"
    "v_add_f32 v67, v67, v19\n"
    "v_add_f32 v68, v68, v20\n"
    "v_add_f32 v69, v69, v21\n"
    "v_cvt_pk_bf16_f32 v82, v64, v66\n"
    "v_cvt_pk_bf16_f32 v83, v65, v67\n"
    "v_cvt_pk_bf16_f32 v84, v68, v69\n"
    "s_movk_i32 s94, 0\n"
    "buffer_store_dwordx4 v[82:85], v117, s[40:43], s94 offen sc1\n"
    "ds_read_b32 v86, v111\n"
    "ds_read_b64 v[64:65], v115 offset:2560\n"
    "ds_read_b64 v[70:71], v115 offset:10240\n"
    "ds_read_b64 v[66:67], v115 offset:34656\n"
    "ds_read_b64 v[72:73], v115 offset:42336\n"
    "ds_read_b64 v[68:69], v116 offset:2560\n"
    "ds_read_b64 v[74:75], v116 offset:10240\n"
    "v_mul_f32 v46, s84, v46\n"
    "v_mul_f32 v47, s84, v47\n"
    "v_mul_f32 v48, s85, v48\n"
    "v_mul_f32 v49, s85, v49\n"
    "v_mul_f32 v50, s86, v50\n"
    "v_mul_f32 v51, s86, v51\n"
    "v_cvt_pk_bf16_f32 v46, 0, v46\n"
    "v_cvt_pk_bf16_f32 v47, 0, v47\n"
    "v_cvt_pk_bf16_f32 v48, 0, v48\n"
    "v_cvt_pk_bf16_f32 v49, 0, v49\n"
    "v_cvt_pk_bf16_f32 v50, 0, v50\n"
    "v_cvt_pk_bf16_f32 v51, 0, v51\n"
    "s_waitcnt lgkmcnt(0)\n"
    "v_readfirstlane_b32 s94, v86\n"
    "s_lshr_b32 s94, s94, s45\n"
    "s_bfe_u32 s94, s94, 0x40008\n"
    "s_cmp_lg_u32 s94, 6\n"
    "s_cbranch_scc1 Le1s%=\n"
    "Le1b%=:\n"
    "v_add_f32 v64, v64, v70\n"
    "v_add_f32 v65, v65, v71\n"
    "v_add_f32 v66, v66, v72\n"
    "v_add_f32 v67, v67, v73\n"
    "v_add_f32 v68, v68, v74\n"
    "v_add_f32 v69, v69, v75\n"
    "v_mul_f32 v64, v64, v76\n"
    "v_mul_f32 v65, v65, v76\n"
    "v_mul_f32 v66, v66, v78\n"
    "v_mul_f32 v67, v67, v78\n"
    "v_mul_f32 v68, v68, v80\n"
    "v_mul_f32 v69, v69, v80\n"
    "v_cvt_pk_bf16_f32 v64, 0, v64\n"
    "v_cvt_pk_bf16_f32 v65, 0, v65\n"
    "v_cvt_pk_bf16_f32 v66, 0, v66\n"
    "v_cvt_pk_bf16_f32 v67, 0, v67\n"
    "v_cvt_pk_bf16_f32 v68, 0, v68\n"
    "v_cvt_pk_bf16_f32 v69, 0, v69\n"
    "v_add_f32 v64, v64, v46\n"
    "v_add_f32 v65, v65, v47\n"
    "v_add_f32 v66, v66, v48\n"
    "v_add_f32 v67, v67, v49\n"
    "v_add_f32 v68, v68, v50\n"
    "v_add_f32 v69, v69, v51\n"
    "v_cvt_pk_bf16_f32 v64, 0, v64\n"
    "v_cvt_pk_bf16_f32 v65, 0, v65\n"
    "v_cvt_pk_bf16_f32 v66, 0, v66\n"
    "v_cvt_pk_bf16_f32 v67, 0, v67\n"
    "v_cvt_pk_bf16_f32 v68, 0, v68\n"
    "v_cvt_pk_bf16_f32 v69, 0, v69\n"
    "v_mul_f32 v64, v64, v77\n"
    "v_mul_f32 v65, v65, v77\n"
    "v_mul_f32 v66, v66, v79\n"
    "v_mul_f32 v67, v67, v79\n"
    "v_mul_f32 v68, v68, v81\n"
    "v_mul_f32 v69, v69, v81\n"
    "v_cvt_pk_bf16_f32 v64, 0, v64\n"
    "v_cvt_pk_bf16_f32 v65, 0, v65\n"
    "v_cvt_pk_bf16_f32 v66, 0, v66\n"
    "v_cvt_pk_bf16_f32 v67, 0, v67\n"
    "v_cvt_pk_bf16_f32 v68, 0, v68\n"
    "v_cvt_pk_bf16_f32 v69, 0, v69\n"
    "v_add_f32 v64, v64, v22\n"
    "v_add_f32 v65, v65, v23\n"
    "v_add_f32 v66, v66, v24\n"
    "v_add_f32 v67, v67, v25\n"
    "v_add_f32 v68, v68, v26\n"
    "v_add_f32 v69, v69, v27\n"
    "v_cvt_pk_bf16_f32 v82, v64, v66\n"
    "v_cvt_pk_bf16_f32 v83, v65, v67\n"
    "v_cvt_pk_bf16_f32 v84, v68, v69\n"
    "s_movk_i32 s94, 4096\n"
    "buffer_store_dwordx4 v[82:85], v117, s[40:43], s94 offen sc1\n"
    "ds_read_b32 v86, v111\n"
    "ds_read_b64 v[64:65], v115 offset:5120\n"
    "ds_read_b64 v[70:71], v115 offset:12800\n"
    "ds_read_b64 v[66:67], v115 offset:37216\n"
    "ds_read_b64 v[72:73], v115 offset:44896\n"
    "ds_read_b64 v[68:69], v116 offset:5120\n"
    "ds_read_b64 v[74:75], v116 offset:12800\n"
    "v_mul_f32 v52, s84, v52\n"
    "v_mul_f32 v53, s84, v53\n"
    "v_mul_f32 v54, s85, v54\n"
    "v_mul_f32 v55, s85, v55\n"
    "v_mul_f32 v56, s86, v56\n"
    "v_mul_f32 v57, s86, v57\n"
    "v_cvt_pk_bf16_f32 v52, 0, v52\n"
    "v_cvt_pk_bf16_f32 v53, 0, v53\n"
    "v_cvt_pk_bf16_f32 v54, 0, v54\n"
    "v_cvt_pk_bf16_f32 v55, 0, v55\n"
    "v_cvt_pk_bf16_f32 v56, 0, v56\n"
    "v_cvt_pk_bf16_f32 v57, 0, v57\n"
    "s_waitcnt lgkmcnt(0)\n"
    "v_readfirstlane_b32 s94, v86\n"
    "s_lshr_b32 s94, s94, s45\n"
    "s_bfe_u32 s94, s94, 0x40010\n"
    "s_cmp_lg_u32 s94, 6\n"
    "s_cbranch_scc1 Le2s%=\n"
    "Le2b%=:\n"
    "v_add_f32 v64, v64, v70\n"
    "v_add_f32 v65, v65, v71\n"
    "v_add_f32 v66, v66, v72\n"
    "v_add_f32 v67, v67, v73\n"
    "v_add_f32 v68, v68, v74\n"
    "v_add_f32 v69, v69, v75\n"
    "v_mul_f32 v64, v64, v76\n"
    "v_mul_f32 v65, v65, v76\n"
    "v_mul_f32 v66, v66, v78\n"
    "v_mul_f32 v67, v67, v78\n"
    "v_mul_f32 v68, v68, v80\n"
    "v_mul_f32 v69, v69, v80\n"
    "v_cvt_pk_bf16_f32 v64, 0, v64\n"
    "v_cvt_pk_bf16_f32 v65, 0, v65\n"
    "v_cvt_pk_bf16_f32 v66, 0, v66\n"
    "v_cvt_pk_bf16_f32 v67, 0, v67\n"
    "v_cvt_pk_bf16_f32 v68, 0, v68\n"
    "v_cvt_pk_bf16_f32 v69, 0, v69\n"
    "v_add_f32 v64, v64, v52\n"
    "v_add_f32 v65, v65, v53\n"
    "v_add_f32 v66, v66, v54\n"
    "v_add_f32 v67, v67, v55\n"
    "v_add_f32 v68, v68, v56\n"
    "v_add_f32 v69, v69, v57\n"
    "v_cvt_pk_bf16_f32 v64, 0, v64\n"
    "v_cvt_pk_bf16_f32 v65, 0, v65\n"
    "v_cvt_pk_bf16_f32 v66, 0, v66\n"
    "v_cvt_pk_bf16_f32 v67, 0, v67\n"
    "v_cvt_pk_bf16_f32 v68, 0, v68\n"
    "v_cvt_pk_bf16_f32 v69, 0, v69\n"
    "v_mul_f32 v64, v64, v77\n"
    "v_mul_f32 v65, v65, v77\n"
    "v_mul_f32 v66, v66, v79\n"
    "v_mul_f32 v67, v67, v79\n"
    "v_mul_f32 v68, v68, v81\n"
    "v_mul_f32 v69, v69, v81\n"
    "v_cvt_pk_bf16_f32 v64, 0, v64\n"
    "v_cvt_pk_bf16_f32 v65, 0, v65\n"
    "v_cvt_pk_bf16_f32 v66, 0, v66\n"
    "v_cvt_pk_bf16_f32 v67, 0, v67\n"
    "v_cvt_pk_bf16_f32 v68, 0, v68\n"
    "v_cvt_pk_bf16_f32 v69, 0, v69\n"
    "v_add_f32 v64, v64, v28\n"
    "v_add_f32 v65, v65, v29\n"
    "v_add_f32 v66, v66, v30\n"
    "v_add_f32 v67, v67, v31\n"
    "v_add_f32 v68, v68, v32\n"
    "v_add_f32 v69, v69, v33\n"
    "v_cvt_pk_bf16_f32 v82, v64, v66\n"
    "v_cvt_pk_bf16_f32 v83, v65, v67\n"
    "v_cvt_pk_bf16_f32 v84, v68, v69\n"
    "s_movk_i32 s94, 8192\n"
    "buffer_store_dwordx4 v[82:85], v117, s[40:43], s94 offen sc1\n"
    "ds_read_b32 v86, v111\n"
    "ds_read_b64 v[64:65], v115 offset:7680\n"
    "ds_read_b64 v[70:71], v115 offset:15360\n"
    "ds_read_b64 v[66:67], v115 offset:39776\n"
    "ds_read_b64 v[72:73], v115 offset:47456\n"
    "ds_read_b64 v[68:69], v116 offset:7680\n"
    "ds_read_b64 v[74:75], v116 offset:15360\n"
    "v_mul_f32 v58, s84, v58\n"
    "v_mul_f32 v59, s84, v59\n"
    "v_mul_f32 v60, s85, v60\n"
    "v_mul_f32 v61, s85, v61\n"
    "v_mul_f32 v62, s86, v62\n"
    "v_mul_f32 v63, s86, v63\n"
    "v_cvt_pk_bf16_f32 v58, 0, v58\n"
    "v_cvt_pk_bf16_f32 v59, 0, v59\n"
    "v_cvt_pk_bf16_f32 v60, 0, v60\n"
    "v_cvt_pk_bf16_f32 v61, 0, v61\n"
    "v_cvt_pk_bf16_f32 v62, 0, v62\n"
    "v_cvt_pk_bf16_f32 v63, 0, v63\n"
    "s_waitcnt lgkmcnt(0)\n"
    "v_readfirstlane_b32 s94, v86\n"
    "s_lshr_b32 s94, s94, s45\n"
    "s_bfe_u32 s94, s94, 0x40018\n"
    "s_cmp_lg_u32 s94, 6\n"
    "s_cbranch_scc1 Le3s%=\n"
    "Le3b%=:\n"
    "v_pk_add_f32 v[64:65], v[64:65], v[70:71]\n"
    "v_pk_add_f32 v[66:67], v[66:67], v[72:73]\n"
    "v_pk_add_f32 v[68:69], v[68:69], v[74:75]\n"
    "v_pk_mul_f32 v[64:65], v[64:65], v[76:77] op_sel_hi:[1,0]\n"
    "v_pk_mul_f32 v[66:67], v[66:67], v[78:79] op_sel_hi:[1,0]\n"
    "v_pk_mul_f32 v[68:69], v[68:69], v[80:81] op_sel_hi:[1,0]\n"
    "v_cvt_pk_bf16_f32 v64, 0, v64\n"
    "v_cvt_pk_bf16_f32 v65, 0, v65\n"
    "v_cvt_pk_bf16_f32 v66, 0, v66\n"
    "v_cvt_pk_bf16_f32 v67, 0, v67\n"
    "v_cvt_pk_bf16_f32 v68, 0, v68\n"
    "v_cvt_pk_bf16_f32 v69, 0, v69\n"
    "v_pk_add_f32 v[64:65], v[64:65], v[58:59]\n"
    "v_pk_add_f32 v[66:67], v[66:67], v[60:61]\n"
    "v_pk_add_f32 v[68:69], v[68:69], v[62:63]\n"
    "v_cvt_pk_bf16_f32 v64, 0, v64\n"
    "v_cvt_pk_bf16_f32 v65, 0, v65\n"
    "v_cvt_pk_bf16_f32 v66, 0, v66\n"
    "v_cvt_pk_bf16_f32 v67, 0, v67\n"
    "v_cvt_pk_bf16_f32 v68, 0, v68\n"
    "v_cvt_pk_bf16_f32 v69, 0, v69\n"
    "v_pk_mul_f32 v[64:65], v[64:65], v[76:77] op_sel:[0,1]\n"
    "v_pk_mul_f32 v[66:67], v[66:67], v[78:79] op_sel:[0,1]\n"
    "v_pk_mul_f32 v[68:69], v[68:69], v[80:81] op_sel:[0,1]\n"
    "v_cvt_pk_bf16_f32 v64, 0, v64\n"
    "v_cvt_pk_bf16_f32 v65, 0, v65\n"
    "v_cvt_pk_bf16_f32 v66, 0, v66\n"
    "v_cvt_pk_bf16_f32 v67, 0, v67\n"
    "v_cvt_pk_bf16_f32 v68, 0, v68\n"
    "v_cvt_pk_bf16_f32 v69, 0, v69\n"
    "v_pk_add_f32 v[64:65], v[64:65], v[34:35]\n"
    "v_pk_add_f32 v[66:67], v[66:67], v[36:37]\n"
    "v_pk_add_f32 v[68:69], v[68:69], v[38:39]\n"
    "v_cvt_pk_bf16_f32 v82, v64, v66\n"
    "v_cvt_pk_bf16_f32 v83, v65, v67\n"
    "v_cvt_pk_bf16_f32 v84, v68, v69\n"
    "s_movk_i32 s94, 12288\n"
    "buffer_store_dwordx4 v[82:85], v117, s[40:43], s94 offen sc1\n"
    "s_cmpk_lt_u32 %[b], 128\n"
    "s_cbranch_scc1 Lhe%=\n"
    "s_cmp_lg_u32 s50, 3\n"
    "s_cbranch_scc1 Lhe%=\n"
    "s_memrealtime s[46:47]\n"
    "s_lshl_b32 s94, %[b], 7\n"
    "s_add_u32 s94, s94, 70656\n"
    "s_mov_b64 exec, 1\n"
    "v_mov_b32 v87, s44\n"
    "s_waitcnt lgkmcnt(0)\n"
    "s_add_u32 s46, s46, 170\n"
    "v_mov_b32 v86, s46\n"
    "buffer_store_dwordx2 v[86:87], off, s[40:43], s94 sc1\n"
    "s_mov_b64 exec, -1\n"
    "s_branch Lhe%=\n"
    "Lms%=:\n"
    "s_mov_b32 s97, 0x7fff\n"
    "Lmr%=:\n"
    "s_mov_b64 exec, vcc\n"
    "buffer_load_dwordx2 v[102:103], v108, s[40:43], 0 offen sc1\n"
    "s_mov_b64 exec, -1\n"
    "s_waitcnt vmcnt(0)\n"
    "v_cmp_ne_u32 vcc, s44, v103\n"
    "s_cmp_lg_u64 vcc, 0\n"
    "s_cbranch_scc0 Lmb%=\n"
    "s_sub_u32 s97, s97, 1\n"
    "s_cbranch_scc0 Lmr%=\n"
    "s_branch Lmb%=\n"
    "Lu0s%=:\n"
    "s_mov_b32 s97, 0x7fff\n"
    "Lu0r%=:\n"
    "s_mov_b64 exec, vcc\n"
    "s_mov_b32 s94, 0\n"
    "s_mov_b32 s95, 0\n"
    "buffer_load_dwordx4 v[64:67], v106, s[40:43], s94 offen sc1\n"
    "buffer_load_dwordx4 v[68:71], v107, s[40:43], s95 offen sc1\n"
    "s_mov_b64 exec, -1\n"
    "s_waitcnt vmcnt(0)\n"
    "v_cmp_ne_u32 vcc, s44, v67\n"
    "v_cmp_ne_u32 s[94:95], s44, v71\n"
    "s_or_b64 vcc, vcc, s[94:95]\n"
    "s_cbranch_scc0 Lu0b%=\n"
    "s_sub_u32 s97, s97, 1\n"
    "s_cbranch_scc0 Lu0r%=\n"
    "s_branch Lu0b%=\n"
    "Lu1s%=:\n"
    "s_mov_b32 s97, 0x7fff\n"
    "Lu1r%=:\n"
    "s_mov_b64 exec, vcc\n"
    "s_mov_b32 s94, 790528\n"
    "s_mov_b32 s95, 4096\n"
    "buffer_load_dwordx4 v[72:75], v106, s[40:43], s94 offen sc1\n"
    "buffer_load_dwordx4 v[76:79], v107, s[40:43], s95 offen sc1\n"
    "s_mov_b64 exec, -1\n"
    "s_waitcnt vmcnt(0)\n"
    "v_cmp_ne_u32 vcc, s44, v75\n"
    "v_cmp_ne_u32 s[94:95], s44, v79\n"
    "s_or_b64 vcc, vcc, s[94:95]\n"
    "s_cbranch_scc0 Lu1b%=\n"
    "s_sub_u32 s97, s97, 1\n"
    "s_cbranch_scc0 Lu1r%=\n"
    "s_branch Lu1b%=\n"
    "Lu2s%=:\n"
    "s_mov_b32 s97, 0x7fff\n"
    "Lu2r%=:\n"
    "s_mov_b64 exec, vcc\n"
    "s_mov_b32 s94, 1581056\n"
    "s_mov_b32 s95, 8192\n"
    "buffer_load_dwordx4 v[80:83], v106, s[40:43], s94 offen sc1\n"
    "buffer_load_dwordx4 v[84:87], v107, s[40:43], s95 offen sc1\n"
    "s_mov_b64 exec, -1\n"
    "s_waitcnt vmcnt(0)\n"
    "v_cmp_ne_u32 vcc, s44, v83\n"
    "v_cmp_ne_u32 s[94:95], s44, v87\n"
    "s_or_b64 vcc, vcc, s[94:95]\n"
    "s_cbranch_scc0 Lu2b%=\n"
    "s_sub_u32 s97, s97, 1\n"
    "s_cbranch_scc0 Lu2r%=\n"
    "s_branch Lu2b%=\n"
    "Lu3s%=:\n"
    "s_mov_b32 s97, 0x7fff\n"
    "Lu3r%=:\n"
    "s_mov_b64 exec, vcc\n"
    "s_mov_b32 s94, 2371584\n"
    "s_mov_b32 s95, 12288\n"
    "buffer_load_dwordx4 v[88:91], v106, s[40:43], s94 offen sc1\n"
    "buffer_load_dwordx4 v[92:95], v107, s[40:43], s95 offen sc1\n"
    "s_mov_b64 exec, -1\n"
    "s_waitcnt vmcnt(0)\n"
    "v_cmp_ne_u32 vcc, s44, v91\n"
    "v_cmp_ne_u32 s[94:95], s44, v95\n"
    "s_or_b64 vcc, vcc, s[94:95]\n"
    "s_cbranch_scc0 Lu3b%=\n"
    "s_sub_u32 s97, s97, 1\n"
    "s_cbranch_scc0 Lu3r%=\n"
    "s_branch Lu3b%=\n"
    "Las%=:\n"
    "s_mov_b32 s97, 0x7fff\n"
    "Lar%=:\n"
    "s_mov_b64 exec, vcc\n"
    "buffer_load_dwordx2 v[118:119], v114, s[40:43], 0 offen sc1\n"
    "s_mov_b64 exec, -1\n"
    "s_waitcnt vmcnt(0)\n"
    "v_cmp_ne_u32 vcc, s44, v119\n"
    "s_cmp_lg_u64 vcc, 0\n"
    "s_cbranch_scc0 Lab%=\n"
    "s_sub_u32 s97, s97, 1\n"
    "s_cbranch_scc0 Lar%=\n"
    "s_branch Lab%=\n"
    "Le0s%=:\n"
    "s_mov_b32 s97, 0x1fffff\n"
    "Le0r%=:\n"
    "s_sleep 1\n"
    "ds_read_b32 v86, v111\n"
    "s_waitcnt lgkmcnt(0)\n"
    "v_readfirstlane_b32 s94, v86\n"
    "s_lshr_b32 s94, s94, s45\n"
    "s_bfe_u32 s94, s94, 0x40000\n"
    "s_cmp_lg_u32 s94, 3\n"
    "s_cbranch_scc0 Le0x%=\n"
    "s_sub_u32 s97, s97, 1\n"
    "s_cbranch_scc0 Le0r%=\n"
    "Le0x%=:\n"
    "ds_read_b64 v[64:65], v115 offset:0\n"
    "ds_read_b64 v[66:67], v115 offset:32096\n"
    "ds_read_b64 v[68:69], v116 offset:0\n"
    "ds_read_b128 v[92:95], v90\n"
    "s_waitcnt lgkmcnt(0)\n"
    "s_branch Le0b%=\n"
    "Le1s%=:\n"
    "s_mov_b32 s97, 0x1fffff\n"
    "Le1r%=:\n"
    "s_sleep 1\n"
    "ds_read_b32 v86, v111\n"
    "s_waitcnt lgkmcnt(0)\n"
    "v_readfirstlane_b32 s94, v86\n"
    "s_lshr_b32 s94, s94, s45\n"
    "s_bfe_u32 s94, s94, 0x40008\n"
    "s_cmp_lg_u32 s94, 6\n"
    "s_cbranch_scc0 Le1x%=\n"
    "s_sub_u32 s97, s97, 1\n"
    "s_cbranch_scc0 Le1r%=\n"
    "Le1x%=:\n"
    "ds_read_b64 v[64:65], v115 offset:2560\n"
    "ds_read_b64 v[70:71], v115 offset:10240\n"
    "ds_read_b64 v[66:67], v115 offset:34656\n"
    "ds_read_b64 v[72:73], v115 offset:42336\n"
    "ds_read_b64 v[68:69], v116 offset:2560\n"
    "ds_read_b64 v[74:75], v116 offset:10240\n"
    "s_waitcnt lgkmcnt(0)\n"
    "s_branch Le1b%=\n"
    "Le2s%=:\n"
    "s_mov_b32 s97, 0x1fffff\n"
    "Le2r%=:\n"
    "s_sleep 1\n"
    "ds_read_b32 v86, v111\n"
    "s_waitcnt lgkmcnt(0)\n"
    "v_readfirstlane_b32 s94, v86\n"
    "s_lshr_b32 s94, s94, s45\n"
    "s_bfe_u32 s94, s94, 0x40010\n"
    "s_cmp_lg_u32 s94, 6\n"
    "s_cbranch_scc0 Le2x%=\n"
    "s_sub_u32 s97, s97, 1\n"
    "s_cbranch_scc0 Le2r%=\n"
    "Le2x%=:\n"
    "ds_read_b64 v[64:65], v115 offset:5120\n"
    "ds_read_b64 v[70:71], v115 offset:12800\n"
    "ds_read_b64 v[66:67], v115 offset:37216\n"
    "ds_read_b64 v[72:73], v115 offset:44896\n"
    "ds_read_b64 v[68:69], v116 offset:5120\n"
    "ds_read_b64 v[74:75], v116 offset:12800\n"
    "s_waitcnt lgkmcnt(0)\n"
    "s_branch Le2b%=\n"
    "Le3s%=:\n"
    "s_mov_b32 s97, 0x1fffff\n"
    "Le3r%=:\n"
    "ds_read_b32 v86, v111\n"
    "ds_read_b64 v[64:65], v115 offset:7680\n"
    "ds_read_b64 v[70:71], v115 offset:15360\n"
    "ds_read_b64 v[66:67], v115 offset:39776\n"
    "ds_read_b64 v[72:73], v115 offset:47456\n"
    "ds_read_b64 v[68:69], v116 offset:7680\n"
    "ds_read_b64 v[74:75], v116 offset:15360\n"
    "s_waitcnt lgkmcnt(0)\n"
    "v_readfirstlane_b32 s94, v86\n"
    "s_lshr_b32 s94, s94, s45\n"
    "s_bfe_u32 s94, s94, 0x40018\n"
    "s_cmp_lg_u32 s94, 6\n"
    "s_cbranch_scc0 Le3b%=\n"
    "s_sub_u32 s97, s97, 1\n"
    "s_cbranch_scc0 Le3r%=\n"
    "s_branch Le3b%=\n"
    "Lhe%=:\n"
    "s_setprio 0\n"
    "Lcl%=:\n"
    "s_cmpk_ge_u32 %[b], 128\n"
    "s_cbranch_scc1 Lce%=\n"
    "s_load_dwordx2 s[40:41], %[k], 0x70\n"
    "s_load_dwordx2 s[46:47], %[k], 0x38\n"
    "s_load_dwordx2 s[48:49], %[k], 0x68\n"
    "v_mov_b32 v1, 158208\n"
    "ds_read_b32 v2, v1\n"
    "s_mov_b32 s42, 10668032\n"
    "s_mov_b32 s43, 0x20000\n"
    "v_and_b32 v3, 7, %[t]\n"
    "v_and_b32 v4, 63, %[t]\n"
    "s_waitcnt lgkmcnt(0)\n"
    "v_readfirstlane_b32 s44, v2\n"
    "s_cmp_ge_u32 %[w], 12\n"
    "s_cbranch_scc1 Mls%=\n"
    "s_lshr_b32 s50, 0x214, %[w]\n"
    "s_bitcmp0_b32 s50, 0\n"
    "s_cbranch_scc1 Mla%=\n"
    "s_sleep 0\n"
    "s_lshr_b32 s50, %[w], 2\n"
    "s_mul_i32 s51, s50, 96\n"
    "v_add_u32 v5, s51, v4\n"
    "v_mov_b32 v92, 0\n"
    "s_mov_b32 exec_hi, 0xffff\n"
    "v_lshlrev_b32 v6, 4, v5\n"
    "v_lshlrev_b32 v7, 2, v5\n"
    "s_waitcnt vmcnt(1)\n"
    "s_setprio 1\n"
    "global_load_dword v90, v7, s[46:47]\n"
    "global_load_dword v93, v7, s[46:47] offset:192\n"
    "s_mov_b32 s51, 17920\n"
    "buffer_load_dwordx4 v[12:15], v6, s[40:43], s51 offen sc1\n"
    "s_mov_b32 s51, 45568\n"
    "buffer_load_dwordx4 v[16:19], v6, s[40:43], s51 offen sc1\n"
    "s_mov_b32 s51, 73216\n"
    "buffer_load_dwordx4 v[20:23], v6, s[40:43], s51 offen sc1\n"
    "s_mov_b32 s51, 22528\n"
    "buffer_load_dwordx4 v[24:27], v6, s[40:43], s51 offen sc1\n"
    "s_mov_b32 s51, 50176\n"
    "buffer_load_dwordx4 v[28:31], v6, s[40:43], s51 offen sc1\n"
    "s_mov_b32 s51, 77824\n"
    "buffer_load_dwordx4 v[32:35], v6, s[40:43], s51 offen sc1\n"
    "s_mov_b32 s51, 27136\n"
    "buffer_load_dwordx4 v[36:39], v6, s[40:43], s51 offen sc1\n"
    "s_mov_b32 s51, 54784\n"
    "buffer_load_dwordx4 v[40:43], v6, s[40:43], s51 offen sc1\n"
    "s_mov_b32 s51, 82432\n"
    "buffer_load_dwordx4 v[44:47], v6, s[40:43], s51 offen sc1\n"
    "s_mov_b32 s51, 17920\n"
    "buffer_load_dwordx4 v[48:51], v6, s[40:43], s51 offen offset:768 sc1\n"
    "s_mov_b32 s51, 45568\n"
    "buffer_load_dwordx4 v[52:55], v6, s[40:43], s51 offen offset:768 sc1\n"
    "s_mov_b32 s51, 73216\n"
    "buffer_load_dwordx4 v[56:59], v6, s[40:43], s51 offen offset:768 sc1\n"
    "s_mov_b32 s51, 22528\n"
    "buffer_load_dwordx4 v[60:63], v6, s[40:43], s51 offen offset:768 sc1\n"
    "s_mov_b32 s51, 50176\n"
    "buffer_load_dwordx4 v[64:67], v6, s[40:43], s51 offen offset:768 sc1\n"
    "s_mov_b32 s51, 77824\n"
    "buffer_load_dwordx4 v[68:71], v6, s[40:43], s51 offen offset:768 sc1\n"
    "s_mov_b32 s51, 27136\n"
    "buffer_load_dwordx4 v[72:75], v6, s[40:43], s51 offen offset:768 sc1\n"
    "s_mov_b32 s51, 54784\n"
    "buffer_load_dwordx4 v[76:79], v6, s[40:43], s51 offen offset:768 sc1\n"
    "s_mov_b32 s51, 82432\n"
    "buffer_load_dwordx4 v[80:83], v6, s[40:43], s51 offen offset:768 sc1\n"
    "s_mov_b64 exec, -1\n"
    "s_setprio 0\n"
    "Mla%=:\n"
    "v_mov_b32 v8, 159488\n"
    "s_mov_b32 s97, 0x1fffff\n"
    "Mwa%=:\n"
    "ds_read_b32 v9, v8\n"
    "s_waitcnt lgkmcnt(0)\n"
    "v_readfirstlane_b32 s51, v9\n"
    "s_cmp_ge_u32 s51, 0x30000000\n"
    "s_cbranch_scc1 Mwb%=\n"
    "s_sleep 3\n"
    "s_sub_u32 s97, s97, 1\n"
    "s_cbranch_scc0 Mwa%=\n"
    "Mwb%=:\n"
    "v_lshrrev_b32 v95, 3, %[t]\n"
    "v_mul_u32_u24 v94, 33024, v95\n"
    "v_lshl_add_u32 v94, v3, 4, v94\n"
    "s_lshl_b32 s58, %[b], 7\n"
    "s_add_u32 s58, s58, 139264\n"
    "s_add_u32 s59, s58, 16512\n"
    "s_lshr_b32 s51, 0x214, %[w]\n"
    "s_bitcmp0_b32 s51, 0\n"
    "s_cbranch_scc1 Mlb%=\n"
    "s_setprio 1\n"
    "s_mov_b32 exec_hi, 0xffff\n"
    "v_mov_b32 v8, 147456\n"
    "v_lshl_add_u32 v7, v6, 1, v8\n"
    "s_movk_i32 s97, 0x7fff\n"
    "s_waitcnt vmcnt(9)\n"
    "Mpr0%=:\n"
    "v_bitop3_b32 v13, v13, v15, s44 bitop3:0x7e\n"
    "v_bitop3_b32 v17, v17, v19, s44 bitop3:0x7e\n"
    "v_bitop3_b32 v21, v21, v23, s44 bitop3:0x7e\n"
    "v_bitop3_b32 v25, v25, v27, s44 bitop3:0x7e\n"
    "v_bitop3_b32 v29, v29, v31, s44 bitop3:0x7e\n"
    "v_bitop3_b32 v33, v33, v35, s44 bitop3:0x7e\n"
    "v_bitop3_b32 v37, v37, v39, s44 bitop3:0x7e\n"
    "v_bitop3_b32 v41, v41, v43, s44 bitop3:0x7e\n"
    "v_bitop3_b32 v45, v45, v47, s44 bitop3:0x7e\n"
    "v_or3_b32 v13, v13, v17, v21\n"
    "v_or3_b32 v25, v25, v29, v33\n"
    "v_or3_b32 v37, v37, v41, v45\n"
    "v_or3_b32 v13, v13, v25, v37\n"
    "v_cmp_ne_u32 vcc, 0, v13\n"
    "s_cbranch_vccnz Mps0%=\n"
    "Mpk0%=:\n"
    "v_add_f32 v86, v12, v16\n"
    "v_add_f32 v87, v14, v18\n"
    "v_add_f32 v84, v24, v28\n"
    "v_add_f32 v85, v26, v30\n"
    "v_add_f32 v88, v36, v40\n"
    "v_add_f32 v89, v38, v42\n"
    "v_add_f32 v86, v86, v20\n"
    "v_add_f32 v87, v87, v22\n"
    "v_add_f32 v84, v84, v32\n"
    "v_add_f32 v85, v85, v34\n"
    "v_add_f32 v88, v88, v44\n"
    "v_add_f32 v89, v89, v46\n"
    "v_cvt_pk_bf16_f32 v84, 0, v84\n"
    "v_cvt_pk_bf16_f32 v85, 0, v85\n"
    "v_cvt_pk_bf16_f32 v86, 0, v86\n"
    "v_cvt_pk_bf16_f32 v87, 0, v87\n"
    "v_cvt_pk_bf16_f32 v88, v88, 0\n"
    "v_cvt_pk_bf16_f32 v89, 0, v89\n"
    "v_add_f32 v84, 1.0, v84\n"
    "v_add_f32 v85, 1.0, v85\n"
    "v_cvt_pk_bf16_f32 v84, 0, v84\n"
    "v_cvt_pk_bf16_f32 v85, 0, v85\n"
    "v_lshlrev_b32 v8, 16, v90\n"
    "v_and_b32 v9, 0xffff0000, v90\n"
    "v_mul_f32 v8, v8, v84\n"
    "v_mul_f32 v9, v9, v85\n"
    "v_cvt_pk_bf16_f32 v8, 0, v8\n"
    "v_cvt_pk_bf16_f32 v9, 0, v9\n"
    "v_add_f32 v92, v92, v8\n"
    "v_add_f32 v92, v92, v9\n"
    "v_lshrrev_b32 v8, 16, v8\n"
    "v_or_b32 v91, v8, v9\n"
    "ds_write_b128 v7, v[84:87] offset:0\n"
    "ds_write_b128 v7, v[88:91] offset:16\n"
    "s_movk_i32 s97, 0x7fff\n"
    "s_waitcnt vmcnt(0)\n"
    "Mpr1%=:\n"
    "v_bitop3_b32 v49, v49, v51, s44 bitop3:0x7e\n"
    "v_bitop3_b32 v53, v53, v55, s44 bitop3:0x7e\n"
    "v_bitop3_b32 v57, v57, v59, s44 bitop3:0x7e\n"
    "v_bitop3_b32 v61, v61, v63, s44 bitop3:0x7e\n"
    "v_bitop3_b32 v65, v65, v67, s44 bitop3:0x7e\n"
    "v_bitop3_b32 v69, v69, v71, s44 bitop3:0x7e\n"
    "v_bitop3_b32 v73, v73, v75, s44 bitop3:0x7e\n"
    "v_bitop3_b32 v77, v77, v79, s44 bitop3:0x7e\n"
    "v_bitop3_b32 v81, v81, v83, s44 bitop3:0x7e\n"
    "v_or3_b32 v49, v49, v53, v57\n"
    "v_or3_b32 v61, v61, v65, v69\n"
    "v_or3_b32 v73, v73, v77, v81\n"
    "v_or3_b32 v49, v49, v61, v73\n"
    "v_cmp_ne_u32 vcc, 0, v49\n"
    "s_cbranch_vccnz Mps1%=\n"
    "Mpk1%=:\n"
    "v_add_f32 v86, v48, v52\n"
    "v_add_f32 v87, v50, v54\n"
    "v_add_f32 v84, v60, v64\n"
    "v_add_f32 v85, v62, v66\n"
    "v_add_f32 v88, v72, v76\n"
    "v_add_f32 v89, v74, v78\n"
    "v_add_f32 v86, v86, v56\n"
    "v_add_f32 v87, v87, v58\n"
    "v_add_f32 v84, v84, v68\n"
    "v_add_f32 v85, v85, v70\n"
    "v_add_f32 v88, v88, v80\n"
    "v_add_f32 v89, v89, v82\n"
    "v_mov_b32 v90, v93\n"
    "v_cvt_pk_bf16_f32 v84, 0, v84\n"
    "v_cvt_pk_bf16_f32 v85, 0, v85\n"
    "v_cvt_pk_bf16_f32 v86, 0, v86\n"
    "v_cvt_pk_bf16_f32 v87, 0, v87\n"
    "v_cvt_pk_bf16_f32 v88, v88, 0\n"
    "v_cvt_pk_bf16_f32 v89, 0, v89\n"
    "v_add_f32 v84, 1.0, v84\n"
    "v_add_f32 v85, 1.0, v85\n"
    "v_cvt_pk_bf16_f32 v84, 0, v84\n"
    "v_cvt_pk_bf16_f32 v85, 0, v85\n"
    "v_lshlrev_b32 v8, 16, v90\n"
    "v_and_b32 v9, 0xffff0000, v90\n"
    "v_mul_f32 v8, v8, v84\n"
    "v_mul_f32 v9, v9, v85\n"
    "v_cvt_pk_bf16_f32 v8, 0, v8\n"
    "v_cvt_pk_bf16_f32 v9, 0, v9\n"
    "v_add_f32 v92, v92, v8\n"
    "v_add_f32 v92, v92, v9\n"
    "v_lshrrev_b32 v8, 16, v8\n"
    "v_or_b32 v91, v8, v9\n"
    "ds_write_b128 v7, v[84:87] offset:1536\n"
    "ds_write_b128 v7, v[88:91] offset:1552\n"
    "s_mov_b64 exec, -1\n"
    "s_lshl_b32 s50, s50, 4\n"
    "v_lshrrev_b32 v5, 2, v4\n"
    "v_add_f32_dpp v92, v92, v92 quad_perm:[1,0,3,2] row_mask:0xf bank_mask:0xf bound_ctrl:1\n"
    "v_and_b32 v5, 12, v5\n"
    "v_add3_u32 v5, v5, v1, s50\n"
    "v_add_f32_dpp v92, v92, v92 quad_perm:[2,3,0,1] row_mask:0xf bank_mask:0xf bound_ctrl:1\n"
    "s_nop 1\n"
    "v_add_f32_dpp v92, v92, v92 row_half_mirror row_mask:0xf bank_mask:0xf bound_ctrl:1\n"
    "s_nop 1\n"
    "v_add_f32_dpp v92, v92, v92 row_mirror row_mask:0xf bank_mask:0xf bound_ctrl:1\n"
    "ds_write_b32 v5, v92 offset:32\n"
    "s_setprio 0\n"
    "Mlb%=:\n"
    "v_mov_b32 v5, v95\n"
    "v_mul_u32_u24 v7, 96, v5\n"
    "v_add_u32 v7, 147456, v7\n"
    "v_lshrrev_b32 v9, 5, v4\n"
    "v_lshl_add_u32 v8, v3, 1, v9\n"
    "v_mul_u32_u24 v10, 48, v8\n"
    "s_lshl2_add_u32 s52, %[w], 124192\n"
    "v_add_u32 v10, s52, v10\n"
    "v_mul_u32_u24 v8, 208, v8\n"
    "v_bfe_u32 v9, v4, 4, 1\n"
    "v_lshl_add_u32 v8, v9, 2, v8\n"
    "s_lshl4_add_u32 s52, %[w], 120864\n"
    "v_add_u32 v8, s52, v8\n"
    "v_mul_u32_u24 v9, 2336, v3\n"
    "v_mul_u32_u24 v11, 12, v5\n"
    "v_add_u32 v9, v9, v11\n"
    "v_lshrrev_b32 v12, 3, v4\n"
    "v_lshl_add_u32 v11, v3, 3, v12\n"
    "v_lshlrev_b32 v11, 2, v11\n"
    "v_mul_u32_u24 v12, 3072, v12\n"
    "v_lshl_add_u32 v12, v3, 4, v12\n"
    "s_mul_i32 s52, %[b], 24576\n"
    "s_lshl_b32 s53, %[w], 7\n"
    "s_add_u32 s52, s52, s53\n"
    "s_add_u32 s52, s52, 3309568\n"
    "v_lshl_add_u32 v13, v3, 4, v1\n"
    "s_mov_b32 s54, 0x3f80\n"
    "s_mov_b32 s55, 0x3f800000\n"
    "v_mov_b64 v[48:49], 0\n"
    "v_mov_b64 v[50:51], 0\n"
    "v_mov_b64 v[52:53], 0\n"
    "v_mov_b64 v[54:55], 0\n"
    "s_mov_b32 s56, 0x05040100\n"
    "s_mov_b32 s57, 0x07060302\n"
    "s_waitcnt lgkmcnt(0)\n"
    "s_barrier\n"
    "buffer_load_dwordx4 v[96:99], v94, s[40:43], s58 offen sc1\n"
    "buffer_load_dwordx4 v[100:103], v94, s[40:43], s59 offen sc1\n"
    "ds_read_b128 v[24:27], v7 offset:0\n"
    "ds_read_b128 v[28:31], v7 offset:16\n"
    "ds_read_b128 v[32:35], v7 offset:32\n"
    "ds_read_b128 v[36:39], v7 offset:48\n"
    "ds_read_b128 v[40:43], v7 offset:64\n"
    "ds_read_b128 v[44:47], v7 offset:80\n"
    "s_waitcnt vmcnt(0)\n"
    "v_bitop3_b32 v14, v99, v103, s44 bitop3:0x7e\n"
    "v_cmp_ne_u32 vcc, 0, v14\n"
    "s_cbranch_vccnz Mxs%=\n"
    "Mxb%=:\n"
    "v_perm_b32 v99, v102, v98, s56\n"
    "v_perm_b32 v103, v102, v98, s57\n"
    "s_waitcnt lgkmcnt(0)\n"
    "v_dot2c_f32_bf16 v48, 0x3f803f80, v96\n"
    "v_dot2c_f32_bf16 v50, v96, v96\n"
    "v_dot2c_f32_bf16 v52, v96, v31\n"
    "v_dot2c_f32_bf16 v49, 0x3f803f80, v97\n"
    "v_dot2c_f32_bf16 v51, v97, v97\n"
    "v_dot2c_f32_bf16 v53, v97, v31\n"
    "v_dot2c_f32_bf16 v48, 0x3f803f80, v100\n"
    "v_dot2c_f32_bf16 v50, v100, v100\n"
    "v_dot2c_f32_bf16 v52, v100, v47\n"
    "v_dot2c_f32_bf16 v49, 0x3f803f80, v101\n"
    "v_dot2c_f32_bf16 v51, v101, v101\n"
    "v_dot2c_f32_bf16 v53, v101, v47\n"
    "v_dot2c_f32_bf16 v48, 0x3f803f80, v99\n"
    "v_dot2c_f32_bf16 v50, v99, v99\n"
    "v_dot2c_f32_bf16 v52, v99, v39\n"
    "v_dot2c_f32_bf16 v49, 0x3f803f80, v103\n"
    "v_dot2c_f32_bf16 v51, v103, v103\n"
    "v_dot2c_f32_bf16 v53, v103, v39\n"
    "s_nop 0\n"
    "v_permlane32_swap_b32 v48, v49\n"
    "v_permlane32_swap_b32 v50, v51\n"
    "v_permlane32_swap_b32 v52, v53\n"
    "v_add_f32 v48, v48, v49\n"
    "v_add_f32 v50, v50, v51\n"
    "v_add_f32 v52, v52, v53\n"
    "s_nop 0\n"
    "v_permlane16_swap_b32 v48, v50\n"
    "v_add_f32_dpp v52, v52, v52 row_ror:8 row_mask:0xf bank_mask:0xf bound_ctrl:1\n"
    "v_add_f32 v48, v48, v50\n"
    "s_nop 1\n"
    "v_add_f32_dpp v48, v48, v48 row_ror:8 row_mask:0xf bank_mask:0xf bound_ctrl:1\n"
    "ds_write2_b32 v8, v48, v52 offset1:2\n"
    "s_waitcnt lgkmcnt(0)\n"
    "s_barrier\n"
    "s_barrier\n"
    "ds_read_b128 v[56:59], v13 offset:128\n"
    "s_waitcnt lgkmcnt(0)\n"
    "v_dot2_f32_bf16 v64, v96, s54, v56\n"
    "v_dot2_f32_bf16 v65, v96, s55, v56\n"
    "v_dot2_f32_bf16 v66, v99, s54, v56\n"
    "v_dot2_f32_bf16 v67, v99, s55, v56\n"
    "v_dot2_f32_bf16 v68, v100, s54, v56\n"
    "v_dot2_f32_bf16 v69, v100, s55, v56\n"
    "v_dot2_f32_bf16 v70, v97, s54, v58\n"
    "v_dot2_f32_bf16 v71, v97, s55, v58\n"
    "v_dot2_f32_bf16 v72, v103, s54, v58\n"
    "v_dot2_f32_bf16 v73, v103, s55, v58\n"
    "v_dot2_f32_bf16 v74, v101, s54, v58\n"
    "v_dot2_f32_bf16 v75, v101, s55, v58\n"
    "v_cvt_pk_bf16_f32 v64, 0, v64\n"
    "v_cvt_pk_bf16_f32 v65, 0, v65\n"
    "v_cvt_pk_bf16_f32 v66, 0, v66\n"
    "v_cvt_pk_bf16_f32 v67, 0, v67\n"
    "v_cvt_pk_bf16_f32 v68, 0, v68\n"
    "v_cvt_pk_bf16_f32 v69, 0, v69\n"
    "v_cvt_pk_bf16_f32 v70, 0, v70\n"
    "v_cvt_pk_bf16_f32 v71, 0, v71\n"
    "v_cvt_pk_bf16_f32 v72, 0, v72\n"
    "v_cvt_pk_bf16_f32 v73, 0, v73\n"
    "v_cvt_pk_bf16_f32 v74, 0, v74\n"
    "v_cvt_pk_bf16_f32 v75, 0, v75\n"
    "v_pk_mul_f32 v[64:65], v[64:65], v[56:57] op_sel:[0,1]\n"
    "v_pk_mul_f32 v[66:67], v[66:67], v[56:57] op_sel:[0,1]\n"
    "v_pk_mul_f32 v[68:69], v[68:69], v[56:57] op_sel:[0,1]\n"
    "v_pk_mul_f32 v[70:71], v[70:71], v[58:59] op_sel:[0,1]\n"
    "v_pk_mul_f32 v[72:73], v[72:73], v[58:59] op_sel:[0,1]\n"
    "v_pk_mul_f32 v[74:75], v[74:75], v[58:59] op_sel:[0,1]\n"
    "v_cvt_pk_bf16_f32 v64, 0, v64\n"
    "v_cvt_pk_bf16_f32 v65, 0, v65\n"
    "v_cvt_pk_bf16_f32 v66, 0, v66\n"
    "v_cvt_pk_bf16_f32 v67, 0, v67\n"
    "v_cvt_pk_bf16_f32 v68, 0, v68\n"
    "v_cvt_pk_bf16_f32 v69, 0, v69\n"
    "v_cvt_pk_bf16_f32 v70, 0, v70\n"
    "v_cvt_pk_bf16_f32 v71, 0, v71\n"
    "v_cvt_pk_bf16_f32 v72, 0, v72\n"
    "v_cvt_pk_bf16_f32 v73, 0, v73\n"
    "v_cvt_pk_bf16_f32 v74, 0, v74\n"
    "v_cvt_pk_bf16_f32 v75, 0, v75\n"
    "v_pk_mul_f32 v[64:65], v[64:65], v[24:25]\n"
    "v_pk_mul_f32 v[66:67], v[66:67], v[32:33]\n"
    "v_pk_mul_f32 v[68:69], v[68:69], v[40:41]\n"
    "v_pk_mul_f32 v[70:71], v[70:71], v[24:25]\n"
    "v_pk_mul_f32 v[72:73], v[72:73], v[32:33]\n"
    "v_pk_mul_f32 v[74:75], v[74:75], v[40:41]\n"
    "v_cvt_pk_bf16_f32 v64, 0, v64\n"
    "v_cvt_pk_bf16_f32 v65, 0, v65\n"
    "v_cvt_pk_bf16_f32 v66, 0, v66\n"
    "v_cvt_pk_bf16_f32 v67, 0, v67\n"
    "v_cvt_pk_bf16_f32 v68, 0, v68\n"
    "v_cvt_pk_bf16_f32 v69, 0, v69\n"
    "v_cvt_pk_bf16_f32 v70, 0, v70\n"
    "v_cvt_pk_bf16_f32 v71, 0, v71\n"
    "v_cvt_pk_bf16_f32 v72, 0, v72\n"
    "v_cvt_pk_bf16_f32 v73, 0, v73\n"
    "v_cvt_pk_bf16_f32 v74, 0, v74\n"
    "v_cvt_pk_bf16_f32 v75, 0, v75\n"
    "v_pk_add_f32 v[64:65], v[64:65], v[26:27]\n"
    "v_pk_add_f32 v[66:67], v[66:67], v[34:35]\n"
    "v_pk_add_f32 v[68:69], v[68:69], v[42:43]\n"
    "v_pk_add_f32 v[70:71], v[70:71], v[26:27]\n"
    "v_pk_add_f32 v[72:73], v[72:73], v[34:35]\n"
    "v_pk_add_f32 v[74:75], v[74:75], v[42:43]\n"
    "v_cvt_pk_bf16_f32 v76, v64, v65\n"
    "v_cvt_pk_bf16_f32 v77, v66, v67\n"
    "v_cvt_pk_bf16_f32 v78, v68, v69\n"
    "v_cvt_pk_bf16_f32 v79, v70, v71\n"
    "v_cvt_pk_bf16_f32 v80, v72, v73\n"
    "v_cvt_pk_bf16_f32 v81, v74, v75\n"
    "ds_bpermute_b32 v84, v11, v76\n"
    "ds_bpermute_b32 v85, v11, v77\n"
    "ds_bpermute_b32 v86, v11, v78\n"
    "ds_bpermute_b32 v88, v11, v79\n"
    "ds_bpermute_b32 v89, v11, v80\n"
    "ds_bpermute_b32 v90, v11, v81\n"
    "v_dot2c_f32_bf16 v54, v76, v30\n"
    "v_dot2c_f32_bf16 v55, v79, v30\n"
    "v_dot2c_f32_bf16 v54, v77, v38\n"
    "v_dot2c_f32_bf16 v55, v80, v38\n"
    "v_dot2c_f32_bf16 v54, v78, v46\n"
    "v_dot2c_f32_bf16 v55, v81, v46\n"
    "v_mov_b32 v87, s44\n"
    "v_mov_b32 v91, s44\n"
    "s_nop 0\n"
    "v_permlane32_swap_b32 v54, v55\n"
    "v_add_f32 v54, v54, v55\n"
    "v_mov_b32 v55, v54\n"
    "s_waitcnt lgkmcnt(0)\n"
    "buffer_store_dwordx4 v[84:87], v12, s[40:43], s52 offen sc1\n"
    "v_permlane16_swap_b32 v54, v55\n"
    "buffer_store_dwordx4 v[88:91], v12, s[40:43], s52 offen offset:1536 sc1\n"
    "v_add_f32 v54, v54, v55\n"
    "s_nop 1\n"
    "v_add_f32_dpp v54, v54, v54 row_ror:8 row_mask:0xf bank_mask:0xf bound_ctrl:1\n"
    "ds_write_b32 v10, v54\n"
    "s_waitcnt lgkmcnt(0)\n"
    "s_barrier\n"
    "v_dot2_f32_bf16 v64, v76, v28, 0\n"
    "v_dot2_f32_bf16 v65, v76, v29, 0\n"
    "v_dot2_f32_bf16 v66, v77, v36, 0\n"
    "v_dot2_f32_bf16 v67, v77, v37, 0\n"
    "v_dot2_f32_bf16 v68, v78, v44, 0\n"
    "v_dot2_f32_bf16 v69, v78, v45, 0\n"
    "v_dot2_f32_bf16 v70, v79, v28, 0\n"
    "v_dot2_f32_bf16 v71, v79, v29, 0\n"
    "v_dot2_f32_bf16 v72, v80, v36, 0\n"
    "v_dot2_f32_bf16 v73, v80, v37, 0\n"
    "v_dot2_f32_bf16 v74, v81, v44, 0\n"
    "v_dot2_f32_bf16 v75, v81, v45, 0\n"
    "v_cvt_pk_bf16_f32 v64, 0, v64\n"
    "v_cvt_pk_bf16_f32 v65, 0, v65\n"
    "v_cvt_pk_bf16_f32 v66, 0, v66\n"
    "v_cvt_pk_bf16_f32 v67, 0, v67\n"
    "v_cvt_pk_bf16_f32 v68, 0, v68\n"
    "v_cvt_pk_bf16_f32 v69, 0, v69\n"
    "v_cvt_pk_bf16_f32 v70, 0, v70\n"
    "v_cvt_pk_bf16_f32 v71, 0, v71\n"
    "v_cvt_pk_bf16_f32 v72, 0, v72\n"
    "v_cvt_pk_bf16_f32 v73, 0, v73\n"
    "v_cvt_pk_bf16_f32 v74, 0, v74\n"
    "v_cvt_pk_bf16_f32 v75, 0, v75\n"
    "v_dot2_f32_bf16 v64, v96, s54, v64\n"
    "v_dot2_f32_bf16 v65, v96, s55, v65\n"
    "v_dot2_f32_bf16 v66, v99, s54, v66\n"
    "v_dot2_f32_bf16 v67, v99, s55, v67\n"
    "v_dot2_f32_bf16 v68, v100, s54, v68\n"
    "v_dot2_f32_bf16 v69, v100, s55, v69\n"
    "v_dot2_f32_bf16 v70, v97, s54, v70\n"
    "v_dot2_f32_bf16 v71, v97, s55, v71\n"
    "v_dot2_f32_bf16 v72, v103, s54, v72\n"
    "v_dot2_f32_bf16 v73, v103, s55, v73\n"
    "v_dot2_f32_bf16 v74, v101, s54, v74\n"
    "v_dot2_f32_bf16 v75, v101, s55, v75\n"
    "v_cvt_pk_bf16_f32 v76, v64, v65\n"
    "v_cvt_pk_bf16_f32 v77, v66, v67\n"
    "v_cvt_pk_bf16_f32 v78, v68, v69\n"
    "v_cvt_pk_bf16_f32 v79, v70, v71\n"
    "v_cvt_pk_bf16_f32 v80, v72, v73\n"
    "v_cvt_pk_bf16_f32 v81, v74, v75\n"
    "ds_write2_b32 v9, v76, v77 offset1:1\n"
    "ds_write_b32 v9, v78 offset:8\n"
    "ds_write_b32 v9, v79 offset:1168\n"
    "ds_write_b32 v9, v80 offset:1172\n"
    "ds_write_b32 v9, v81 offset:1176\n"
    "v_mul_u32_u24 v5, 911, %[t]\n"
    "v_lshrrev_b32 v5, 16, v5\n"
    "v_add_lshl_u32 v5, v5, %[t], 4\n"
    "v_lshlrev_b32 v6, 4, %[t]\n"
    "s_mul_i32 s50, %[b], 18432\n"
    "s_add_u32 s48, s48, s50\n"
    "s_addc_u32 s49, s49, 0\n"
    "v_add_u32 v7, 9216, v6\n"
    "s_waitcnt lgkmcnt(0)\n"
    "s_barrier\n"
    "s_cmp_ge_u32 %[w], 9\n"
    "s_cbranch_scc1 Mlz%=\n"
    "s_sleep 24\n"
    "ds_read_b128 v[16:19], v5\n"
    "ds_read_b128 v[20:23], v5 offset:9344\n"
    "s_waitcnt lgkmcnt(1)\n"
    "global_store_dwordx4 v6, v[16:19], s[48:49] sc1\n"
    "s_waitcnt lgkmcnt(0)\n"
    "global_store_dwordx4 v7, v[20:23], s[48:49] sc1\n"
    "s_branch Mlz%=\n"
    "Mxs%=:\n"
    "s_movk_i32 s97, 0x7fff\n"
    "Mxr%=:\n"
    "s_mov_b64 exec, vcc\n"
    "buffer_load_dwordx4 v[96:99], v94, s[40:43], s58 offen sc1\n"
    "buffer_load_dwordx4 v[100:103], v94, s[40:43], s59 offen sc1\n"
    "s_mov_b64 exec, -1\n"
    "s_waitcnt vmcnt(0)\n"
    "v_bitop3_b32 v14, v99, v103, s44 bitop3:0x7e\n"
    "v_cmp_ne_u32 vcc, 0, v14\n"
    "s_cbranch_vccz Mxb%=\n"
    "s_sub_u32 s97, s97, 1\n"
    "s_cbranch_scc0 Mxr%=\n"
    "s_branch Mxb%=\n"
    "Mps0%=:\n"
    "s_sub_u32 s97, s97, 1\n"
    "s_cbranch_scc1 Mpk0%=\n"
    "s_mov_b32 s51, 17920\n"
    "buffer_load_dwordx4 v[12:15], v6, s[40:43], s51 offen sc1\n"
    "s_mov_b32 s51, 45568\n"
    "buffer_load_dwordx4 v[16:19], v6, s[40:43], s51 offen sc1\n"
    "s_mov_b32 s51, 73216\n"
    "buffer_load_dwordx4 v[20:23], v6, s[40:43], s51 offen sc1\n"
    "s_mov_b32 s51, 22528\n"
    "buffer_load_dwordx4 v[24:27], v6, s[40:43], s51 offen sc1\n"
    "s_mov_b32 s51, 50176\n"
    "buffer_load_dwordx4 v[28:31], v6, s[40:43], s51 offen sc1\n"
    "s_mov_b32 s51, 77824\n"
    "buffer_load_dwordx4 v[32:35], v6, s[40:43], s51 offen sc1\n"
    "s_mov_b32 s51, 27136\n"
    "buffer_load_dwordx4 v[36:39], v6, s[40:43], s51 offen sc1\n"
    "s_mov_b32 s51, 54784\n"
    "buffer_load_dwordx4 v[40:43], v6, s[40:43], s51 offen sc1\n"
    "s_mov_b32 s51, 82432\n"
    "buffer_load_dwordx4 v[44:47], v6, s[40:43], s51 offen sc1\n"
    "s_waitcnt vmcnt(0)\n"
    "s_branch Mpr0%=\n"
    "Mps1%=:\n"
    "s_sub_u32 s97, s97, 1\n"
    "s_cbranch_scc1 Mpk1%=\n"
    "s_mov_b32 s51, 17920\n"
    "buffer_load_dwordx4 v[48:51], v6, s[40:43], s51 offen offset:768 sc1\n"
    "s_mov_b32 s51, 45568\n"
    "buffer_load_dwordx4 v[52:55], v6, s[40:43], s51 offen offset:768 sc1\n"
    "s_mov_b32 s51, 73216\n"
    "buffer_load_dwordx4 v[56:59], v6, s[40:43], s51 offen offset:768 sc1\n"
    "s_mov_b32 s51, 22528\n"
    "buffer_load_dwordx4 v[60:63], v6, s[40:43], s51 offen offset:768 sc1\n"
    "s_mov_b32 s51, 50176\n"
    "buffer_load_dwordx4 v[64:67], v6, s[40:43], s51 offen offset:768 sc1\n"
    "s_mov_b32 s51, 77824\n"
    "buffer_load_dwordx4 v[68:71], v6, s[40:43], s51 offen offset:768 sc1\n"
    "s_mov_b32 s51, 27136\n"
    "buffer_load_dwordx4 v[72:75], v6, s[40:43], s51 offen offset:768 sc1\n"
    "s_mov_b32 s51, 54784\n"
    "buffer_load_dwordx4 v[76:79], v6, s[40:43], s51 offen offset:768 sc1\n"
    "s_mov_b32 s51, 82432\n"
    "buffer_load_dwordx4 v[80:83], v6, s[40:43], s51 offen offset:768 sc1\n"
    "s_waitcnt vmcnt(0)\n"
    "s_branch Mpr1%=\n"
    "Mls%=:\n"
    "s_lshr_b32 s50, %[b], 4\n"
    "s_cmp_lt_u32 s50, 4\n"
    "s_cbranch_scc1 Mld%=\n"
    "s_sleep 15\n"
    "Mld%=:\n"
    "s_cmp_eq_u32 %[w], 15\n"
    "s_cbranch_scc1 Mlk%=\n"
    "s_barrier\n"
    "s_barrier\n"
    "s_barrier\n"
    "s_barrier\n"
    "s_barrier\n"
    "s_branch Mlz%=\n"
    "Mlk%=:\n"
    "s_barrier\n"
    "s_setprio 3\n"
    "ds_read_b128 v[16:19], v1 offset:32\n"
    "ds_read_b128 v[20:23], v1 offset:48\n"
    "ds_read_b128 v[24:27], v1 offset:64\n"
    "v_lshrrev_b32 v5, 2, v4\n"
    "v_mul_u32_u24 v6, 208, v5\n"
    "v_and_b32 v7, 3, v4\n"
    "v_mul_u32_u24 v8, 48, v7\n"
    "s_mov_b32 s50, 120864\n"
    "v_add3_u32 v6, v6, v8, s50\n"
    "v_lshl_add_u32 v9, v5, 3, v1\n"
    "s_lshl_b32 s50, %[b], 4\n"
    "v_add_u32 v10, s50, v5\n"
    "v_lshlrev_b32 v11, 3, v10\n"
    "v_sub_u32 v10, 2047, v10\n"
    "v_mov_b32 v13, s44\n"
    "v_mul_u32_u24 v14, 48, v5\n"
    "v_mul_u32_u24 v7, 12, v7\n"
    "s_mov_b32 s50, 124192\n"
    "v_add3_u32 v14, v14, v7, s50\n"
    "s_mov_b32 s50, 10403840\n"
    "s_mov_b32 s51, 103424\n"
    "s_mov_b32 s55, 10469888\n"
    "v_and_b32 v28, 15, v4\n"
    "v_lshrrev_b32 v7, 4, v4\n"
    "v_mul_u32_u24 v7, 16512, v7\n"
    "v_lshl_add_u32 v7, v28, 3, v7\n"
    "s_lshl_b32 s56, %[b], 7\n"
    "v_add_u32 v7, s56, v7\n"
    "v_lshlrev_b32 v28, 4, v28\n"
    "s_waitcnt lgkmcnt(0)\n"
    "v_pk_add_f32 v[16:17], v[16:17], v[18:19]\n"
    "v_pk_add_f32 v[20:21], v[20:21], v[22:23]\n"
    "v_pk_add_f32 v[24:25], v[24:25], v[26:27]\n"
    "v_pk_add_f32 v[16:17], v[16:17], v[20:21]\n"
    "v_pk_add_f32 v[16:17], v[16:17], v[24:25]\n"
    "v_add_f32 v15, v16, v17\n"
    "s_barrier\n"
    "ds_read_b128 v[16:19], v6\n"
    "ds_read_b128 v[20:23], v6 offset:16\n"
    "ds_read_b128 v[24:27], v6 offset:32\n"
    "s_waitcnt lgkmcnt(1)\n"
    "v_pk_add_f32 v[16:17], v[16:17], v[20:21]\n"
    "v_pk_add_f32 v[18:19], v[18:19], v[22:23]\n"
    "s_waitcnt lgkmcnt(0)\n"
    "v_pk_add_f32 v[16:17], v[16:17], v[24:25]\n"
    "v_pk_add_f32 v[18:19], v[18:19], v[26:27]\n"
    "v_add_f32 v18, v18, v19\n"
    "v_add_f32_dpp v16, v16, v16 quad_perm:[1,0,3,2] row_mask:0xf bank_mask:0xf bound_ctrl:1\n"
    "v_add_f32_dpp v17, v17, v17 quad_perm:[1,0,3,2] row_mask:0xf bank_mask:0xf bound_ctrl:1\n"
    "v_add_f32_dpp v18, v18, v18 quad_perm:[1,0,3,2] row_mask:0xf bank_mask:0xf bound_ctrl:1\n"
    "v_add_f32_dpp v16, v16, v16 quad_perm:[2,3,0,1] row_mask:0xf bank_mask:0xf bound_ctrl:1\n"
    "v_add_f32_dpp v17, v17, v17 quad_perm:[2,3,0,1] row_mask:0xf bank_mask:0xf bound_ctrl:1\n"
    "v_add_f32_dpp v18, v18, v18 quad_perm:[2,3,0,1] row_mask:0xf bank_mask:0xf bound_ctrl:1\n"
    "v_mul_f32 v16, 0xbae38e39, v16\n"
    "v_mul_f32 v17, 0x3ae38e39, v17\n"
    "v_fma_f32 v17, -v16, v16, v17\n"
    "v_max_f32 v17, 0, v17\n"
    "v_cvt_pk_bf16_f32 v17, 0, v17\n"
    "v_add_f32 v17, 0x3727c5ac, v17\n"
    "v_cvt_pk_bf16_f32 v17, 0, v17\n"
    "v_sqrt_f32 v17, v17\n"
    "v_cvt_pk_bf16_f32 v16, 0, v16\n"
    "v_cvt_pk_bf16_f32 v17, 0, v17\n"
    "v_rcp_f32 v17, v17\n"
    "v_fma_f32 v12, v16, v15, v18\n"
    "v_mul_f32 v12, v12, v17\n"
    "ds_write_b64 v9, v[16:17] offset:128\n"
    "v_and_b32 v18, 0x7ff, v12\n"
    "v_add_u32 v18, 0x7ff, v18\n"
    "v_or_b32 v12, v12, v18\n"
    "v_ashrrev_i32 v18, 31, v12\n"
    "v_or_b32 v18, 0x80000000, v18\n"
    "v_xor_b32 v12, v12, v18\n"
    "v_and_b32 v12, 0xfffff800, v12\n"
    "v_or_b32 v12, v12, v10\n"
    "ds_bpermute_b32 v12, v28, v12\n"
    "s_waitcnt lgkmcnt(0)\n"
    "buffer_store_dwordx2 v[12:13], v7, s[40:43], s50 offen sc1\n"
    "buffer_store_dwordx2 v[12:13], v7, s[40:43], s55 offen sc1\n"
    "s_waitcnt lgkmcnt(0)\n"
    "s_barrier\n"
    "s_barrier\n"
    "ds_read2_b32 v[16:17], v14 offset1:1\n"
    "ds_read_b32 v18, v14 offset:8\n"
    "s_waitcnt lgkmcnt(0)\n"
    "v_add_f32 v12, v16, v17\n"
    "v_add_f32 v12, v12, v18\n"
    "s_nop 1\n"
    "v_add_f32_dpp v12, v12, v12 quad_perm:[1,0,3,2] row_mask:0xf bank_mask:0xf bound_ctrl:1\n"
    "s_nop 1\n"
    "v_add_f32_dpp v12, v12, v12 quad_perm:[2,3,0,1] row_mask:0xf bank_mask:0xf bound_ctrl:1\n"
    "v_and_b32 v18, 0x7ff, v12\n"
    "v_add_u32 v18, 0x7ff, v18\n"
    "v_or_b32 v12, v12, v18\n"
    "v_ashrrev_i32 v18, 31, v12\n"
    "v_or_b32 v18, 0x80000000, v18\n"
    "v_xor_b32 v12, v12, v18\n"
    "v_and_b32 v12, 0xfffff800, v12\n"
    "v_or_b32 v12, v12, v10\n"
    "s_mov_b32 exec_lo, 0x11111111\n"
    "s_mov_b32 exec_hi, 0x11111111\n"
    "buffer_store_dwordx2 v[12:13], v11, s[40:43], s51 offen sc1\n"
    "s_mov_b64 exec, -1\n"
    "s_barrier\n"
    "Mlz%=:\n"
    "s_waitcnt vmcnt(0)\n"
    "s_barrier\n"
    "s_cmp_lg_u32 %[w], 15\n"
    "s_cbranch_scc1 Mly%=\n"
    "s_lshl2_add_u32 s50, %[b], 136192\n"
    "s_mov_b64 exec, 1\n"
    "buffer_store_dword v13, off, s[40:43], s50 sc1\n"
    "s_mov_b64 exec, -1\n"
    "Mly%=:\n"
    "s_branch Lce%=\n"
    "Lcm%=:\n"
    "v_and_b32 v10, 15, %[t]\n"
    "v_bfe_u32 v11, %[t], 4, 2\n"
    "v_and_b32 v12, 63, %[t]\n"
    "s_cmp_lg_u32 s50, 2\n"
    "s_cbranch_scc1 Em0%=\n"
    "s_setprio 2\n"
    "v_bfe_u32 v1, %[t], 2, 2\n"
    "v_lshl_add_u32 v1, v11, 3, v1\n"
    "v_lshlrev_b32 v1, 7, v1\n"
    "v_bfe_u32 v2, %[t], 3, 2\n"
    "v_xor_b32 v2, %[w], v2\n"
    "v_and_b32 v2, 3, v2\n"
    "v_lshl_add_u32 v1, v2, 5, v1\n"
    "v_and_b32 v2, 3, %[t]\n"
    "v_lshl_add_u32 v1, v2, 3, v1\n"
    "v_add_u32 v1, 73728, v1\n"
    "v_mov_b32 v9, 160416\n"
    "v_lshl_add_u32 v5, v5, 1, v9\n"
    "v_lshl_add_u32 v9, v11, 4, v9\n"
    "v_lshlrev_b32 v3, 3, v10\n"
    "s_mul_i32 s58, s57, 27648\n"
    "s_lshl_b32 s59, s56, 9\n"
    "s_add_u32 s58, s58, s59\n"
    "s_lshl_b32 s59, %[w], 7\n"
    "s_add_u32 s58, s58, s59\n"
    "s_add_u32 s58, s58, 3072\n"
    "s_cmp_ge_u32 s56, 9\n"
    "s_cselect_b32 s59, 4608, 0\n"
    "v_mov_b32 v4, 158208\n"
    "s_waitcnt vmcnt(0)\n"
    "v_and_b32 v7, 0xffff0000, v7\n"
    "v_mul_f32 v6, 0xbfb8aa3b, v7\n"
    "v_exp_f32 v6, v6\n"
    "v_add_u32 v8, 1, v8\n"
    "v_add_f32 v6, 1.0, v6\n"
    "v_rcp_f32 v6, v6\n"
    "v_readfirstlane_b32 s44, v8\n"
    "v_mul_f32 v6, v6, v7\n"
    "v_cvt_pk_bf16_f32 v6, 0, v6\n"
    "ds_write_b32 v4, v8\n"
    "s_cmpk_ge_u32 %[b], 246\n"
    "s_cbranch_scc1 Ew0%=\n"
    "ds_write_b16_d16_hi v5, v6\n"
    "s_waitcnt lgkmcnt(0)\n"
    "s_barrier\n"
    "ds_read_b64_tr_b16 v[40:41], v1 offset:0\n"
    "ds_read_b64_tr_b16 v[42:43], v1 offset:512\n"
    "ds_read_b64_tr_b16 v[44:45], v1 offset:4096\n"
    "ds_read_b64_tr_b16 v[46:47], v1 offset:4608\n"
    "ds_read_b64_tr_b16 v[48:49], v1 offset:8192\n"
    "ds_read_b64_tr_b16 v[50:51], v1 offset:8704\n"
    "ds_read_b64_tr_b16 v[52:53], v1 offset:12288\n"
    "ds_read_b64_tr_b16 v[54:55], v1 offset:12800\n"
    "ds_read_b64_tr_b16 v[56:57], v1 offset:16384\n"
    "ds_read_b64_tr_b16 v[58:59], v1 offset:16896\n"
    "ds_read_b64_tr_b16 v[60:61], v1 offset:20480\n"
    "ds_read_b64_tr_b16 v[62:63], v1 offset:20992\n"
    "ds_read_b128 v[64:67], v9 offset:0\n"
    "ds_read_b128 v[68:71], v9 offset:64\n"
    "ds_read_b128 v[72:75], v9 offset:128\n"
    "ds_read_b128 v[76:79], v9 offset:192\n"
    "ds_read_b128 v[80:83], v9 offset:256\n"
    "ds_read_b128 v[84:87], v9 offset:320\n"
    "s_mov_b64 s[46:47], 0xffff\n"
    "s_waitcnt lgkmcnt(0)\n"
    "v_mfma_f32_16x16x32_bf16 v[16:19], v[64:67], v[40:43], 0\n"
    "v_mfma_f32_16x16x32_bf16 v[16:19], v[68:71], v[44:47], v[16:19]\n"
    "v_mfma_f32_16x16x32_bf16 v[16:19], v[72:75], v[48:51], v[16:19]\n"
    "v_mfma_f32_16x16x32_bf16 v[16:19], v[76:79], v[52:55], v[16:19]\n"
    "v_mfma_f32_16x16x32_bf16 v[16:19], v[80:83], v[56:59], v[16:19]\n"
    "v_mfma_f32_16x16x32_bf16 v[16:19], v[84:87], v[60:63], v[16:19]\n"
    "s_nop 7\n"
    "s_mov_b64 exec, s[46:47]\n"
    "v_mov_b32 v17, s44\n"
    "buffer_store_dwordx2 v[16:17], v3, s[40:43], s58 offen sc1\n"
    "s_mov_b64 exec, -1\n"
    "s_add_u32 s58, s58, 9216\n"
    "s_add_u32 s58, s58, s59\n"
    "s_barrier\n"
    "ds_read_b64_tr_b16 v[40:41], v1 offset:24576\n"
    "ds_read_b64_tr_b16 v[42:43], v1 offset:25088\n"
    "ds_read_b64_tr_b16 v[44:45], v1 offset:28672\n"
    "ds_read_b64_tr_b16 v[46:47], v1 offset:29184\n"
    "ds_read_b64_tr_b16 v[48:49], v1 offset:32768\n"
    "ds_read_b64_tr_b16 v[50:51], v1 offset:33280\n"
    "ds_read_b64_tr_b16 v[52:53], v1 offset:36864\n"
    "ds_read_b64_tr_b16 v[54:55], v1 offset:37376\n"
    "ds_read_b64_tr_b16 v[56:57], v1 offset:40960\n"
    "ds_read_b64_tr_b16 v[58:59], v1 offset:41472\n"
    "ds_read_b64_tr_b16 v[60:61], v1 offset:45056\n"
    "ds_read_b64_tr_b16 v[62:63], v1 offset:45568\n"
    "s_mov_b64 s[46:47], 0xffff\n"
    "s_waitcnt lgkmcnt(0)\n"
    "v_mfma_f32_16x16x32_bf16 v[16:19], v[64:67], v[40:43], 0\n"
    "v_mfma_f32_16x16x32_bf16 v[16:19], v[68:71], v[44:47], v[16:19]\n"
    "v_mfma_f32_16x16x32_bf16 v[16:19], v[72:75], v[48:51], v[16:19]\n"
    "v_mfma_f32_16x16x32_bf16 v[16:19], v[76:79], v[52:55], v[16:19]\n"
    "v_mfma_f32_16x16x32_bf16 v[16:19], v[80:83], v[56:59], v[16:19]\n"
    "v_mfma_f32_16x16x32_bf16 v[16:19], v[84:87], v[60:63], v[16:19]\n"
    "s_nop 7\n"
    "s_mov_b64 exec, s[46:47]\n"
    "v_mov_b32 v17, s44\n"
    "buffer_store_dwordx2 v[16:17], v3, s[40:43], s58 offen sc1\n"
    "s_mov_b64 exec, -1\n"
    "s_add_u32 s58, s58, 4608\n"
    "v_add_u32 v1, 49152, v1\n"
    "s_barrier\n"
    "ds_read_b64_tr_b16 v[40:41], v1 offset:0\n"
    "ds_read_b64_tr_b16 v[42:43], v1 offset:512\n"
    "ds_read_b64_tr_b16 v[44:45], v1 offset:4096\n"
    "ds_read_b64_tr_b16 v[46:47], v1 offset:4608\n"
    "ds_read_b64_tr_b16 v[48:49], v1 offset:8192\n"
    "ds_read_b64_tr_b16 v[50:51], v1 offset:8704\n"
    "ds_read_b64_tr_b16 v[52:53], v1 offset:12288\n"
    "ds_read_b64_tr_b16 v[54:55], v1 offset:12800\n"
    "ds_read_b64_tr_b16 v[56:57], v1 offset:16384\n"
    "ds_read_b64_tr_b16 v[58:59], v1 offset:16896\n"
    "ds_read_b64_tr_b16 v[60:61], v1 offset:20480\n"
    "ds_read_b64_tr_b16 v[62:63], v1 offset:20992\n"
    "s_mov_b64 s[46:47], 0xffff\n"
    "s_waitcnt lgkmcnt(0)\n"
    "s_barrier\n"
    "v_mfma_f32_16x16x32_bf16 v[16:19], v[64:67], v[40:43], 0\n"
    "v_mfma_f32_16x16x32_bf16 v[16:19], v[68:71], v[44:47], v[16:19]\n"
    "v_mfma_f32_16x16x32_bf16 v[16:19], v[72:75], v[48:51], v[16:19]\n"
    "v_mfma_f32_16x16x32_bf16 v[16:19], v[76:79], v[52:55], v[16:19]\n"
    "v_mfma_f32_16x16x32_bf16 v[16:19], v[80:83], v[56:59], v[16:19]\n"
    "v_mfma_f32_16x16x32_bf16 v[16:19], v[84:87], v[60:63], v[16:19]\n"
    "s_nop 7\n"
    "s_mov_b64 exec, s[46:47]\n"
    "v_mov_b32 v17, s44\n"
    "buffer_store_dwordx2 v[16:17], v3, s[40:43], s58 offen sc1\n"
    "s_mov_b64 exec, -1\n"
    "s_load_dwordx4 s[52:55], %[k], 0x40\n"
    "s_load_dwordx2 s[48:49], %[k], 0x50\n"
    "s_load_dwordx2 s[84:85], %[k], 0x68\n"
    "s_setprio 0\n"
    "s_waitcnt lgkmcnt(0)\n"
    "s_branch Ew2%=\n"
    "Em0%=:\n"
    "s_cmpk_ge_u32 %[b], 246\n"
    "s_cbranch_scc1 Em2%=\n"
    "s_cmp_eq_u32 s50, 3\n"
    "s_cbranch_scc1 Em1%=\n"
    "s_cmp_ge_u32 s56, 9\n"
    "s_cselect_b32 s59, 1152, 0\n"
    "s_cmp_eq_u32 s50, 0\n"
    "s_cselect_b32 s58, -1102464, 0\n"
    "s_cselect_b32 s51, 0, 2304\n"
    "s_add_u32 s58, s58, s51\n"
    "s_add_u32 s58, s58, s59\n"
    "v_add_u32 v2, s58, v2\n"
    "s_mul_i32 s51, s50, -28672\n"
    "s_lshl_b32 s58, %[w], 10\n"
    "s_add_u32 s51, s51, s58\n"
    "s_add_u32 m0, s51, 122880\n"
    "s_waitcnt vmcnt(0) lgkmcnt(0)\n"
    "s_cmp_eq_u32 s50, 0\n"
    "s_cbranch_scc1 Em3%=\n"
    "s_sleep 8\n"
    "global_load_lds_dwordx4 v2, s[54:55]\n"
    "s_add_u32 m0, m0, 4096\n"
    "v_add_u32 v2, 221184, v2\n"
    "global_load_lds_dwordx4 v2, s[54:55]\n"
    "s_add_u32 m0, m0, 4096\n"
    "v_add_u32 v2, 221184, v2\n"
    "global_load_lds_dwordx4 v2, s[54:55]\n"
    "s_add_u32 m0, m0, 4096\n"
    "v_add_u32 v2, 221184, v2\n"
    "global_load_lds_dwordx4 v2, s[54:55]\n"
    "s_add_u32 m0, m0, 4096\n"
    "v_add_u32 v2, 221184, v2\n"
    "global_load_lds_dwordx4 v2, s[54:55]\n"
    "s_add_u32 m0, m0, 4096\n"
    "v_add_u32 v2, 221184, v2\n"
    "global_load_lds_dwordx4 v2, s[54:55]\n"
    "s_load_dwordx4 s[52:55], %[k], 0x40\n"
    "s_load_dwordx2 s[48:49], %[k], 0x50\n"
    "s_load_dwordx2 s[84:85], %[k], 0x68\n"
    "s_barrier\n"
    "s_waitcnt lgkmcnt(0)\n"
    "s_and_b32 s56, %[b], 7\n"
    "s_bfe_u32 s57, %[b], 0x30003\n"
    "s_lshr_b32 s58, s52, 7\n"
    "s_add_u32 s58, s58, s56\n"
    "s_sub_u32 s58, s57, s58\n"
    "s_add_u32 s58, s58, 4\n"
    "s_and_b32 s58, s58, 7\n"
    "s_mul_i32 s59, s56, 9\n"
    "s_add_u32 s58, s58, s59\n"
    "s_lshl_b32 s58, s58, 6\n"
    "s_add_u32 s59, s59, 8\n"
    "s_lshl_b32 s59, s59, 6\n"
    "s_lshl3_add_u32 s59, s57, s59\n"
    "s_lshl3_add_u32 s57, s56, s57\n"
    "v_mov_b32 v2, s52\n"
    "v_mov_b32 v3, s53\n"
    "v_mov_b32 v4, s54\n"
    "v_mov_b32 v5, s55\n"
    "s_mov_b32 s46, 294912\n"
    "s_mov_b32 s47, 0\n"
    "v_bfe_u32 v7, %[t], 2, 2\n"
    "s_mov_b32 vcc_lo, 0xff00ff00\n"
    "s_mov_b32 vcc_hi, 0xff00ff00\n"
    "v_cndmask_b32 v2, v2, v4, vcc\n"
    "v_cndmask_b32 v3, v3, v5, vcc\n"
    "v_lshrrev_b32 v4, 4, %[t]\n"
    "v_mul_u32_u24 v4, 9216, v4\n"
    "s_lshl_b32 s51, %[w], 1\n"
    "s_and_b32 s51, s51, 4\n"
    "v_or_b32 v5, s51, v11\n"
    "v_xor_b32 v5, v5, %[t]\n"
    "v_and_b32 v5, 7, v5\n"
    "v_lshl_add_u32 v4, v5, 4, v4\n"
    "s_lshl_b32 s51, s58, 1\n"
    "v_add_u32 v4, s51, v4\n"
    "v_mov_b32 v5, 0\n"
    "v_lshl_add_u64 v[2:3], v[2:3], 0, v[4:5]\n"
    "s_lshl_b32 m0, %[w], 10\n"
    "s_waitcnt vmcnt(0)\n"
    "s_barrier\n"
    "s_sleep 6\n"
    "global_load_lds_dwordx4 v[2:3], off nt\n"
    "s_add_u32 m0, m0, 8192\n"
    "v_lshl_add_u64 v[2:3], v[2:3], 0, s[46:47]\n"
    "global_load_lds_dwordx4 v[2:3], off nt\n"
    "s_add_u32 m0, m0, 8192\n"
    "v_lshl_add_u64 v[2:3], v[2:3], 0, s[46:47]\n"
    "global_load_lds_dwordx4 v[2:3], off nt\n"
    "s_add_u32 m0, m0, 8192\n"
    "v_lshl_add_u64 v[2:3], v[2:3], 0, s[46:47]\n"
    "global_load_lds_dwordx4 v[2:3], off nt\n"
    "s_add_u32 m0, m0, 8192\n"
    "v_lshl_add_u64 v[2:3], v[2:3], 0, s[46:47]\n"
    "global_load_lds_dwordx4 v[2:3], off nt\n"
    "s_add_u32 m0, m0, 8192\n"
    "v_lshl_add_u64 v[2:3], v[2:3], 0, s[46:47]\n"
    "global_load_lds_dwordx4 v[2:3], off nt\n"
    "s_add_u32 m0, m0, 8192\n"
    "v_lshl_add_u64 v[2:3], v[2:3], 0, s[46:47]\n"
    "s_barrier\n"
    "s_barrier\n"
    "v_mov_b32 v8, 158208\n"
    "ds_read_b32 v8, v8\n"
    "s_waitcnt lgkmcnt(0)\n"
    "v_readfirstlane_b32 s44, v8\n"
    "s_branch Ew1%=\n"
    "Em3%=:\n"
    "s_barrier\n"
    "s_sleep 6\n"
    "global_load_lds_dwordx4 v2, s[54:55]\n"
    "s_add_u32 m0, m0, 4096\n"
    "v_add_u32 v2, 221184, v2\n"
    "global_load_lds_dwordx4 v2, s[54:55]\n"
    "s_add_u32 m0, m0, 4096\n"
    "v_add_u32 v2, 221184, v2\n"
    "global_load_lds_dwordx4 v2, s[54:55]\n"
    "s_add_u32 m0, m0, 4096\n"
    "v_add_u32 v2, 221184, v2\n"
    "global_load_lds_dwordx4 v2, s[54:55]\n"
    "s_add_u32 m0, m0, 4096\n"
    "v_add_u32 v2, 221184, v2\n"
    "global_load_lds_dwordx4 v2, s[54:55]\n"
    "s_add_u32 m0, m0, 4096\n"
    "v_add_u32 v2, 221184, v2\n"
    "s_barrier\n"
    "s_sleep 0\n"
    "global_load_lds_dwordx4 v2, s[54:55]\n"
    "s_load_dwordx4 s[52:55], %[k], 0x40\n"
    "s_load_dwordx2 s[48:49], %[k], 0x50\n"
    "s_load_dwordx2 s[84:85], %[k], 0x68\n"
    "s_waitcnt lgkmcnt(0)\n"
    "s_and_b32 s56, %[b], 7\n"
    "s_bfe_u32 s57, %[b], 0x30003\n"
    "s_lshr_b32 s58, s52, 7\n"
    "s_add_u32 s58, s58, s56\n"
    "s_sub_u32 s58, s57, s58\n"
    "s_add_u32 s58, s58, 4\n"
    "s_and_b32 s58, s58, 7\n"
    "s_mul_i32 s59, s56, 9\n"
    "s_add_u32 s58, s58, s59\n"
    "s_lshl_b32 s58, s58, 6\n"
    "s_add_u32 s59, s59, 8\n"
    "s_lshl_b32 s59, s59, 6\n"
    "s_lshl3_add_u32 s59, s57, s59\n"
    "s_lshl3_add_u32 s57, s56, s57\n"
    "v_mov_b32 v2, s52\n"
    "v_mov_b32 v3, s53\n"
    "v_mov_b32 v4, s54\n"
    "v_mov_b32 v5, s55\n"
    "s_mov_b32 s46, 294912\n"
    "s_mov_b32 s47, 0\n"
    "v_bfe_u32 v7, %[t], 2, 2\n"
    "s_mov_b32 vcc_lo, 0xff00ff00\n"
    "s_mov_b32 vcc_hi, 0xff00ff00\n"
    "v_cndmask_b32 v2, v2, v4, vcc\n"
    "v_cndmask_b32 v3, v3, v5, vcc\n"
    "v_lshrrev_b32 v4, 4, %[t]\n"
    "v_mul_u32_u24 v4, 9216, v4\n"
    "s_lshl_b32 s51, %[w], 1\n"
    "s_and_b32 s51, s51, 4\n"
    "v_or_b32 v5, s51, v11\n"
    "v_xor_b32 v5, v5, %[t]\n"
    "v_and_b32 v5, 7, v5\n"
    "v_lshl_add_u32 v4, v5, 4, v4\n"
    "s_lshl_b32 s51, s58, 1\n"
    "v_add_u32 v4, s51, v4\n"
    "v_mov_b32 v5, 0\n"
    "v_lshl_add_u64 v[2:3], v[2:3], 0, v[4:5]\n"
    "s_lshl_b32 m0, %[w], 10\n"
    "v_mov_b32 v8, 158208\n"
    "ds_read_b32 v8, v8\n"
    "s_waitcnt lgkmcnt(0)\n"
    "v_readfirstlane_b32 s44, v8\n"
    "s_waitcnt vmcnt(0)\n"
    "s_barrier\n"
    "global_load_lds_dwordx4 v[2:3], off nt\n"
    "s_add_u32 m0, m0, 8192\n"
    "v_lshl_add_u64 v[2:3], v[2:3], 0, s[46:47]\n"
    "global_load_lds_dwordx4 v[2:3], off nt\n"
    "s_add_u32 m0, m0, 8192\n"
    "v_lshl_add_u64 v[2:3], v[2:3], 0, s[46:47]\n"
    "s_barrier\n"
    "s_branch Ew3%=\n"
    "Em1%=:\n"
    "s_waitcnt lgkmcnt(0)\n"
    "s_barrier\n"
    "s_barrier\n"
    "s_barrier\n"
    "s_branch Ew0%=\n"
    "Em2%=:\n"
    "s_waitcnt lgkmcnt(0)\n"
    "s_sleep 16\n"
    "s_cmpk_lt_u32 %[b], 248\n"
    "s_cbranch_scc1 Ew0%=\n"
    "s_sleep 0\n"
    "Ew0%=:\n"
    "s_setprio 0\n"
    "s_load_dwordx4 s[52:55], %[k], 0x40\n"
    "s_load_dwordx2 s[48:49], %[k], 0x50\n"
    "s_load_dwordx2 s[84:85], %[k], 0x68\n"
    "s_waitcnt lgkmcnt(0)\n"
    "s_barrier\n"
    "Ew2%=:\n"
    "s_and_b32 s56, %[b], 7\n"
    "s_bfe_u32 s57, %[b], 0x30003\n"
    "s_lshr_b32 s58, s52, 7\n"
    "s_add_u32 s58, s58, s56\n"
    "s_sub_u32 s58, s57, s58\n"
    "s_add_u32 s58, s58, 4\n"
    "s_and_b32 s58, s58, 7\n"
    "s_mul_i32 s59, s56, 9\n"
    "s_add_u32 s58, s58, s59\n"
    "s_lshl_b32 s58, s58, 6\n"
    "s_add_u32 s59, s59, 8\n"
    "s_lshl_b32 s59, s59, 6\n"
    "s_lshl3_add_u32 s59, s57, s59\n"
    "s_lshl3_add_u32 s57, s56, s57\n"
    "v_mov_b32 v8, 158208\n"
    "ds_read_b32 v8, v8\n"
    "s_waitcnt lgkmcnt(0)\n"
    "v_readfirstlane_b32 s44, v8\n"
    "s_cmp_gt_u32 %[w], 8\n"
    "s_cbranch_scc1 Ewd%=\n"
    "v_mov_b32 v2, s52\n"
    "v_mov_b32 v3, s53\n"
    "v_mov_b32 v4, s54\n"
    "v_mov_b32 v5, s55\n"
    "s_mov_b32 s46, 294912\n"
    "s_mov_b32 s47, 0\n"
    "v_bfe_u32 v7, %[t], 2, 2\n"
    "s_cmp_eq_u32 %[w], 8\n"
    "s_cbranch_scc1 Ewx%=\n"
    "s_mov_b32 vcc_lo, 0xff00ff00\n"
    "s_mov_b32 vcc_hi, 0xff00ff00\n"
    "v_cndmask_b32 v2, v2, v4, vcc\n"
    "v_cndmask_b32 v3, v3, v5, vcc\n"
    "v_lshrrev_b32 v4, 4, %[t]\n"
    "v_mul_u32_u24 v4, 9216, v4\n"
    "s_lshl_b32 s51, %[w], 1\n"
    "s_and_b32 s51, s51, 4\n"
    "v_or_b32 v5, s51, v11\n"
    "v_xor_b32 v5, v5, %[t]\n"
    "v_and_b32 v5, 7, v5\n"
    "v_lshl_add_u32 v4, v5, 4, v4\n"
    "s_lshl_b32 s51, s58, 1\n"
    "v_add_u32 v4, s51, v4\n"
    "v_mov_b32 v5, 0\n"
    "v_lshl_add_u64 v[2:3], v[2:3], 0, v[4:5]\n"
    "s_lshl_b32 m0, %[w], 10\n"
    "s_nop 0\n"
    "global_load_lds_dwordx4 v[2:3], off nt\n"
    "s_add_u32 m0, m0, 8192\n"
    "v_lshl_add_u64 v[2:3], v[2:3], 0, s[46:47]\n"
    "global_load_lds_dwordx4 v[2:3], off nt\n"
    "s_add_u32 m0, m0, 8192\n"
    "v_lshl_add_u64 v[2:3], v[2:3], 0, s[46:47]\n"
    "Ew3%=:\n"
    "global_load_lds_dwordx4 v[2:3], off nt\n"
    "s_add_u32 m0, m0, 8192\n"
    "v_lshl_add_u64 v[2:3], v[2:3], 0, s[46:47]\n"
    "global_load_lds_dwordx4 v[2:3], off nt\n"
    "s_add_u32 m0, m0, 8192\n"
    "v_lshl_add_u64 v[2:3], v[2:3], 0, s[46:47]\n"
    "global_load_lds_dwordx4 v[2:3], off nt\n"
    "s_add_u32 m0, m0, 8192\n"
    "v_lshl_add_u64 v[2:3], v[2:3], 0, s[46:47]\n"
    "global_load_lds_dwordx4 v[2:3], off nt\n"
    "s_add_u32 m0, m0, 8192\n"
    "v_lshl_add_u64 v[2:3], v[2:3], 0, s[46:47]\n"
    "Ew1%=:\n"
    "global_load_lds_dwordx4 v[2:3], off nt\n"
    "s_add_u32 m0, m0, 8192\n"
    "v_lshl_add_u64 v[2:3], v[2:3], 0, s[46:47]\n"
    "global_load_lds_dwordx4 v[2:3], off nt\n"
    "s_add_u32 m0, m0, 8192\n"
    "v_lshl_add_u64 v[2:3], v[2:3], 0, s[46:47]\n"
    "global_load_lds_dwordx4 v[2:3], off nt\n"
    "s_add_u32 m0, m0, 8192\n"
    "v_lshl_add_u64 v[2:3], v[2:3], 0, s[46:47]\n"
    "s_barrier\n"
    "global_load_lds_dwordx4 v[2:3], off nt\n"
    "s_add_u32 m0, m0, 8192\n"
    "v_lshl_add_u64 v[2:3], v[2:3], 0, s[46:47]\n"
    "global_load_lds_dwordx4 v[2:3], off nt\n"
    "s_add_u32 m0, m0, 8192\n"
    "v_lshl_add_u64 v[2:3], v[2:3], 0, s[46:47]\n"
    "global_load_lds_dwordx4 v[2:3], off nt\n"
    "s_add_u32 m0, m0, 8192\n"
    "v_lshl_add_u64 v[2:3], v[2:3], 0, s[46:47]\n"
    "global_load_lds_dwordx4 v[2:3], off nt\n"
    "s_add_u32 m0, m0, 8192\n"
    "v_lshl_add_u64 v[2:3], v[2:3], 0, s[46:47]\n"
    "global_load_lds_dwordx4 v[2:3], off nt\n"
    "s_add_u32 m0, m0, 8192\n"
    "v_lshl_add_u64 v[2:3], v[2:3], 0, s[46:47]\n"
    "s_waitcnt vmcnt(5)\n"
    "s_barrier\n"
    "global_load_lds_dwordx4 v[2:3], off nt\n"
    "s_add_u32 m0, m0, 8192\n"
    "v_lshl_add_u64 v[2:3], v[2:3], 0, s[46:47]\n"
    "global_load_lds_dwordx4 v[2:3], off nt\n"
    "s_add_u32 m0, m0, 8192\n"
    "v_lshl_add_u64 v[2:3], v[2:3], 0, s[46:47]\n"
    "global_load_lds_dwordx4 v[2:3], off nt\n"
    "s_add_u32 m0, m0, 8192\n"
    "v_lshl_add_u64 v[2:3], v[2:3], 0, s[46:47]\n"
    "global_load_lds_dwordx4 v[2:3], off nt\n"
    "v_lshl_add_u32 v1, v11, 3, v7\n"
    "v_lshlrev_b32 v1, 8, v1\n"
    "v_and_b32 v4, 2, %[t]\n"
    "v_lshl_add_u32 v1, v4, 6, v1\n"
    "v_and_b32 v4, 1, v11\n"
    "v_lshl_or_b32 v4, v4, 2, v7\n"
    "v_xor_b32 v4, %[w], v4\n"
    "v_lshl_add_u32 v1, v4, 4, v1\n"
    "v_and_b32 v4, 1, %[t]\n"
    "v_lshl_add_u32 v1, v4, 3, v1\n"
    "ds_read_b64_tr_b16 v[40:41], v1 offset:0\n"
    "ds_read_b64_tr_b16 v[42:43], v1 offset:1024\n"
    "ds_read_b64_tr_b16 v[44:45], v1 offset:8192\n"
    "ds_read_b64_tr_b16 v[46:47], v1 offset:9216\n"
    "ds_read_b64_tr_b16 v[48:49], v1 offset:16384\n"
    "ds_read_b64_tr_b16 v[50:51], v1 offset:17408\n"
    "ds_read_b64_tr_b16 v[52:53], v1 offset:24576\n"
    "ds_read_b64_tr_b16 v[54:55], v1 offset:25600\n"
    "ds_read_b64_tr_b16 v[56:57], v1 offset:32768\n"
    "ds_read_b64_tr_b16 v[58:59], v1 offset:33792\n"
    "ds_read_b64_tr_b16 v[60:61], v1 offset:40960\n"
    "ds_read_b64_tr_b16 v[62:63], v1 offset:41984\n"
    "ds_read_b64_tr_b16 v[64:65], v1 offset:49152\n"
    "ds_read_b64_tr_b16 v[66:67], v1 offset:50176\n"
    "ds_read_b64_tr_b16 v[68:69], v1 offset:57344\n"
    "ds_read_b64_tr_b16 v[70:71], v1 offset:58368\n"
    "v_add_u32 v1, 0x10000, v1\n"
    "ds_read_b64_tr_b16 v[72:73], v1\n"
    "ds_read_b64_tr_b16 v[74:75], v1 offset:1024\n"
    "v_add_u32 v1, 0x2000, v1\n"
    "s_waitcnt vmcnt(0) lgkmcnt(0)\n"
    "s_barrier\n"
    "ds_read_b64_tr_b16 v[76:77], v1 offset:0\n"
    "ds_read_b64_tr_b16 v[78:79], v1 offset:1024\n"
    "ds_read_b64_tr_b16 v[80:81], v1 offset:8192\n"
    "ds_read_b64_tr_b16 v[82:83], v1 offset:9216\n"
    "ds_read_b64_tr_b16 v[84:85], v1 offset:16384\n"
    "ds_read_b64_tr_b16 v[86:87], v1 offset:17408\n"
    "ds_read_b64_tr_b16 v[88:89], v1 offset:24576\n"
    "ds_read_b64_tr_b16 v[90:91], v1 offset:25600\n"
    "ds_read_b64_tr_b16 v[92:93], v1 offset:32768\n"
    "ds_read_b64_tr_b16 v[94:95], v1 offset:33792\n"
    "ds_read_b64_tr_b16 v[96:97], v1 offset:40960\n"
    "ds_read_b64_tr_b16 v[98:99], v1 offset:41984\n"
    "ds_read_b64_tr_b16 v[100:101], v1 offset:49152\n"
    "ds_read_b64_tr_b16 v[102:103], v1 offset:50176\n"
    "ds_read_b64_tr_b16 v[104:105], v1 offset:57344\n"
    "ds_read_b64_tr_b16 v[106:107], v1 offset:58368\n"
    "v_add_u32 v1, 0x10000, v1\n"
    "ds_read_b64_tr_b16 v[108:109], v1\n"
    "ds_read_b64_tr_b16 v[110:111], v1 offset:1024\n"
    "v_add_u32 v1, 0x2000, v1\n"
    "s_waitcnt lgkmcnt(0)\n"
    "s_barrier\n"
    "s_branch Ek0%=\n"
    "Ewx%=:\n"
    "s_mov_b32 vcc_lo, 0xaaaaaaaa\n"
    "s_mov_b32 vcc_hi, 0xaaaaaaaa\n"
    "v_cndmask_b32 v2, v2, v4, vcc\n"
    "v_cndmask_b32 v3, v3, v5, vcc\n"
    "v_lshrrev_b32 v4, 1, v12\n"
    "v_mul_u32_u24 v4, 9216, v4\n"
    "s_lshl_b32 s51, s59, 1\n"
    "v_add_u32 v4, s51, v4\n"
    "v_mov_b32 v5, 0\n"
    "v_lshl_add_u64 v[2:3], v[2:3], 0, v[4:5]\n"
    "s_mov_b32 m0, 46080\n"
    "v_lshlrev_b32 v1, 3, v10\n"
    "v_lshl_add_u32 v1, v11, 8, v1\n"
    "s_barrier\n"
    "s_barrier\n"
    "s_barrier\n"
    "s_waitcnt vmcnt(5)\n"
    "global_load_lds_dwordx4 v[2:3], off\n"
    "s_add_u32 m0, m0, 1024\n"
    "v_lshl_add_u64 v[2:3], v[2:3], 0, s[46:47]\n"
    "s_waitcnt vmcnt(5)\n"
    "global_load_lds_dwordx4 v[2:3], off\n"
    "s_add_u32 m0, m0, 1024\n"
    "v_lshl_add_u64 v[2:3], v[2:3], 0, s[46:47]\n"
    "s_waitcnt vmcnt(5)\n"
    "global_load_lds_dwordx4 v[2:3], off\n"
    "s_add_u32 m0, m0, 1024\n"
    "v_lshl_add_u64 v[2:3], v[2:3], 0, s[46:47]\n"
    "s_barrier\n"
    "s_waitcnt vmcnt(5)\n"
    "global_load_lds_dwordx4 v[2:3], off\n"
    "s_add_u32 m0, m0, 1024\n"
    "v_lshl_add_u64 v[2:3], v[2:3], 0, s[46:47]\n"
    "s_waitcnt vmcnt(5)\n"
    "global_load_lds_dwordx4 v[2:3], off\n"
    "s_add_u32 m0, m0, 1024\n"
    "v_lshl_add_u64 v[2:3], v[2:3], 0, s[46:47]\n"
    "s_waitcnt vmcnt(5)\n"
    "global_load_lds_dwordx4 v[2:3], off\n"
    "s_add_u32 m0, m0, 1024\n"
    "v_lshl_add_u64 v[2:3], v[2:3], 0, s[46:47]\n"
    "s_waitcnt vmcnt(5)\n"
    "global_load_lds_dwordx4 v[2:3], off\n"
    "s_add_u32 m0, m0, 1024\n"
    "v_lshl_add_u64 v[2:3], v[2:3], 0, s[46:47]\n"
    "s_waitcnt vmcnt(5)\n"
    "global_load_lds_dwordx4 v[2:3], off\n"
    "s_add_u32 m0, m0, 1024\n"
    "v_lshl_add_u64 v[2:3], v[2:3], 0, s[46:47]\n"
    "s_waitcnt vmcnt(5)\n"
    "global_load_lds_dwordx4 v[2:3], off\n"
    "s_add_u32 m0, m0, 1024\n"
    "v_lshl_add_u64 v[2:3], v[2:3], 0, s[46:47]\n"
    "s_waitcnt vmcnt(5)\n"
    "global_load_lds_dwordx4 v[2:3], off\n"
    "s_add_u32 m0, m0, 1024\n"
    "v_lshl_add_u64 v[2:3], v[2:3], 0, s[46:47]\n"
    "s_waitcnt vmcnt(5)\n"
    "global_load_lds_dwordx4 v[2:3], off\n"
    "s_add_u32 m0, m0, 1024\n"
    "v_lshl_add_u64 v[2:3], v[2:3], 0, s[46:47]\n"
    "s_waitcnt vmcnt(5)\n"
    "global_load_lds_dwordx4 v[2:3], off\n"
    "s_add_u32 m0, m0, 1024\n"
    "v_lshl_add_u64 v[2:3], v[2:3], 0, s[46:47]\n"
    "s_waitcnt vmcnt(5)\n"
    "global_load_lds_dwordx4 v[2:3], off\n"
    "s_add_u32 m0, m0, 1024\n"
    "v_lshl_add_u64 v[2:3], v[2:3], 0, s[46:47]\n"
    "s_waitcnt vmcnt(5)\n"
    "global_load_lds_dwordx4 v[2:3], off\n"
    "s_add_u32 m0, m0, 1024\n"
    "v_lshl_add_u64 v[2:3], v[2:3], 0, s[46:47]\n"
    "s_waitcnt vmcnt(5)\n"
    "global_load_lds_dwordx4 v[2:3], off\n"
    "s_add_u32 m0, m0, 1024\n"
    "v_lshl_add_u64 v[2:3], v[2:3], 0, s[46:47]\n"
    "s_waitcnt vmcnt(5)\n"
    "global_load_lds_dwordx4 v[2:3], off\n"
    "s_add_u32 m0, m0, 1024\n"
    "v_lshl_add_u64 v[2:3], v[2:3], 0, s[46:47]\n"
    "s_waitcnt vmcnt(5)\n"
    "global_load_lds_dwordx4 v[2:3], off\n"
    "s_add_u32 m0, m0, 1024\n"
    "v_lshl_add_u64 v[2:3], v[2:3], 0, s[46:47]\n"
    "s_waitcnt vmcnt(5)\n"
    "global_load_lds_dwordx4 v[2:3], off\n"
    "s_add_u32 m0, m0, 1024\n"
    "v_lshl_add_u64 v[2:3], v[2:3], 0, s[46:47]\n"
    "s_barrier\n"
    "s_barrier\n"
    "s_barrier\n"
    "s_waitcnt vmcnt(17)\n"
    "ds_read_b64_tr_b16 v[40:41], v1 offset:46080\n"
    "ds_read_b64_tr_b16 v[42:43], v1 offset:46208\n"
    "s_waitcnt vmcnt(16)\n"
    "ds_read_b64_tr_b16 v[44:45], v1 offset:47104\n"
    "ds_read_b64_tr_b16 v[46:47], v1 offset:47232\n"
    "s_waitcnt vmcnt(15)\n"
    "ds_read_b64_tr_b16 v[48:49], v1 offset:48128\n"
    "ds_read_b64_tr_b16 v[50:51], v1 offset:48256\n"
    "s_waitcnt vmcnt(14)\n"
    "ds_read_b64_tr_b16 v[52:53], v1 offset:49152\n"
    "ds_read_b64_tr_b16 v[54:55], v1 offset:49280\n"
    "s_waitcnt vmcnt(13)\n"
    "ds_read_b64_tr_b16 v[56:57], v1 offset:50176\n"
    "ds_read_b64_tr_b16 v[58:59], v1 offset:50304\n"
    "s_waitcnt vmcnt(12)\n"
    "ds_read_b64_tr_b16 v[60:61], v1 offset:51200\n"
    "ds_read_b64_tr_b16 v[62:63], v1 offset:51328\n"
    "s_waitcnt vmcnt(11)\n"
    "ds_read_b64_tr_b16 v[64:65], v1 offset:52224\n"
    "ds_read_b64_tr_b16 v[66:67], v1 offset:52352\n"
    "s_waitcnt vmcnt(10)\n"
    "ds_read_b64_tr_b16 v[68:69], v1 offset:53248\n"
    "ds_read_b64_tr_b16 v[70:71], v1 offset:53376\n"
    "s_waitcnt vmcnt(9)\n"
    "ds_read_b64_tr_b16 v[72:73], v1 offset:54272\n"
    "ds_read_b64_tr_b16 v[74:75], v1 offset:54400\n"
    "s_waitcnt vmcnt(8)\n"
    "ds_read_b64_tr_b16 v[76:77], v1 offset:55296\n"
    "ds_read_b64_tr_b16 v[78:79], v1 offset:55424\n"
    "s_waitcnt vmcnt(7)\n"
    "ds_read_b64_tr_b16 v[80:81], v1 offset:56320\n"
    "ds_read_b64_tr_b16 v[82:83], v1 offset:56448\n"
    "s_waitcnt vmcnt(6)\n"
    "ds_read_b64_tr_b16 v[84:85], v1 offset:57344\n"
    "ds_read_b64_tr_b16 v[86:87], v1 offset:57472\n"
    "s_waitcnt vmcnt(5)\n"
    "ds_read_b64_tr_b16 v[88:89], v1 offset:58368\n"
    "ds_read_b64_tr_b16 v[90:91], v1 offset:58496\n"
    "s_waitcnt vmcnt(4)\n"
    "ds_read_b64_tr_b16 v[92:93], v1 offset:59392\n"
    "ds_read_b64_tr_b16 v[94:95], v1 offset:59520\n"
    "s_waitcnt vmcnt(3)\n"
    "ds_read_b64_tr_b16 v[96:97], v1 offset:60416\n"
    "ds_read_b64_tr_b16 v[98:99], v1 offset:60544\n"
    "s_waitcnt vmcnt(2)\n"
    "ds_read_b64_tr_b16 v[100:101], v1 offset:61440\n"
    "ds_read_b64_tr_b16 v[102:103], v1 offset:61568\n"
    "s_waitcnt vmcnt(1)\n"
    "ds_read_b64_tr_b16 v[104:105], v1 offset:62464\n"
    "ds_read_b64_tr_b16 v[106:107], v1 offset:62592\n"
    "s_waitcnt vmcnt(0)\n"
    "ds_read_b64_tr_b16 v[108:109], v1 offset:63488\n"
    "ds_read_b64_tr_b16 v[110:111], v1 offset:63616\n"
    "v_mul_u32_u24 v1, 1184, v10\n"
    "v_lshl_add_u32 v1, v11, 4, v1\n"
    "v_add_u32 v1, 120832, v1\n"
    "v_lshrrev_b32 v2, 3, v12\n"
    "v_mul_u32_u24 v2, 320, v2\n"
    "v_and_b32 v4, 7, %[t]\n"
    "v_lshl_add_u32 v2, v4, 1, v2\n"
    "s_lshl4_add_u32 s46, %[w], 141312\n"
    "v_add_u32 v2, s46, v2\n"
    "s_branch Eg0%=\n"
    "Ewd%=:\n"
    "s_cmp_eq_u32 %[w], 15\n"
    "s_cbranch_scc1 Ewz%=\n"
    "s_sub_u32 s56, %[w], 9\n"
    "s_mul_i32 s51, s56, 192\n"
    "s_mul_i32 s46, s58, 1152\n"
    "s_add_u32 s51, s51, s46\n"
    "s_sub_u32 s47, s59, s58\n"
    "s_mul_i32 s47, s47, 1152\n"
    "s_sub_u32 s47, s47, 55296\n"
    "v_add_u32 v4, 0, v12\n"
    "v_mul_u32_u24 v5, 171, v4\n"
    "v_lshrrev_b32 v5, 11, v5\n"
    "v_lshrrev_b32 v6, 2, v5\n"
    "v_and_b32 v6, 2, v6\n"
    "v_xor_b32 v4, v4, v6\n"
    "v_mul_u32_u24 v5, 960, v5\n"
    "v_lshl_add_u32 v4, v4, 4, v5\n"
    "v_add_u32 v13, s51, v4\n"
    "v_add_u32 v4, 64, v12\n"
    "v_mul_u32_u24 v5, 171, v4\n"
    "v_lshrrev_b32 v5, 11, v5\n"
    "v_lshrrev_b32 v6, 2, v5\n"
    "v_and_b32 v6, 2, v6\n"
    "v_xor_b32 v4, v4, v6\n"
    "v_mul_u32_u24 v5, 960, v5\n"
    "v_lshl_add_u32 v4, v4, 4, v5\n"
    "v_add_u32 v14, s51, v4\n"
    "v_add_u32 v4, 128, v12\n"
    "v_mul_u32_u24 v5, 171, v4\n"
    "v_lshrrev_b32 v5, 11, v5\n"
    "v_lshrrev_b32 v6, 2, v5\n"
    "v_and_b32 v6, 2, v6\n"
    "v_xor_b32 v4, v4, v6\n"
    "v_mul_u32_u24 v5, 960, v5\n"
    "v_lshl_add_u32 v4, v4, 4, v5\n"
    "v_add_u32 v15, s51, v4\n"
    "s_mul_i32 s51, s56, 15360\n"
    "s_cmp_ge_u32 s56, 3\n"
    "s_cselect_b32 s46, 27648, 0\n"
    "s_add_u32 s51, s51, s46\n"
    "s_mov_b32 m0, s51\n"
    "v_and_b32 v4, 1, v11\n"
    "v_bfe_u32 v7, %[t], 2, 2\n"
    "v_mul_u32_u24 v7, 192, v7\n"
    "v_lshrrev_b32 v5, 1, v11\n"
    "v_and_b32 v6, 3, %[t]\n"
    "v_lshl_add_u32 v7, v6, 3, v7\n"
    "v_add_u32 v7, s51, v7\n"
    "v_lshl_add_u32 v2, v5, 5, v7\n"
    "v_xor_b32 v5, v5, v4\n"
    "v_lshl_add_u32 v1, v5, 5, v7\n"
    "v_mul_u32_u24 v5, 1536, v4\n"
    "v_add_u32 v1, v1, v5\n"
    "s_barrier\n"
    "s_barrier\n"
    "s_barrier\n"
    "s_cmp_lt_u32 s56, 3\n"
    "s_cbranch_scc1 Ewe%=\n"
    "s_barrier\n"
    "Ewe%=:\n"
    "s_waitcnt vmcnt(2)\n"
    "global_load_lds_dwordx4 v13, s[48:49]\n"
    "s_add_u32 m0, m0, 1024\n"
    "v_add_u32 v13, 18432, v13\n"
    "s_waitcnt vmcnt(2)\n"
    "global_load_lds_dwordx4 v14, s[48:49]\n"
    "s_add_u32 m0, m0, 1024\n"
    "v_add_u32 v14, 18432, v14\n"
    "s_waitcnt vmcnt(2)\n"
    "global_load_lds_dwordx4 v15, s[48:49]\n"
    "s_add_u32 m0, m0, 1024\n"
    "v_add_u32 v15, 18432, v15\n"
    "s_cmp_ge_u32 s56, 3\n"
    "s_cbranch_scc1 Ewf%=\n"
    "s_barrier\n"
    "Ewf%=:\n"
    "s_waitcnt vmcnt(2)\n"
    "global_load_lds_dwordx4 v13, s[48:49]\n"
    "s_add_u32 m0, m0, 1024\n"
    "v_add_u32 v13, 18432, v13\n"
    "s_waitcnt vmcnt(2)\n"
    "global_load_lds_dwordx4 v14, s[48:49]\n"
    "s_add_u32 m0, m0, 1024\n"
    "v_add_u32 v14, 18432, v14\n"
    "s_waitcnt vmcnt(2)\n"
    "global_load_lds_dwordx4 v15, s[48:49]\n"
    "s_add_u32 m0, m0, 1024\n"
    "v_add_u32 v15, 18432, v15\n"
    "s_waitcnt vmcnt(2)\n"
    "global_load_lds_dwordx4 v13, s[48:49]\n"
    "s_add_u32 m0, m0, 1024\n"
    "v_add_u32 v13, 18432, v13\n"
    "s_waitcnt vmcnt(2)\n"
    "global_load_lds_dwordx4 v14, s[48:49]\n"
    "s_add_u32 m0, m0, 1024\n"
    "v_add_u32 v14, 18432, v14\n"
    "s_waitcnt vmcnt(2)\n"
    "global_load_lds_dwordx4 v15, s[48:49]\n"
    "s_add_u32 m0, m0, 1024\n"
    "v_add_u32 v15, 18432, v15\n"
    "s_waitcnt vmcnt(2)\n"
    "global_load_lds_dwordx4 v13, s[48:49]\n"
    "s_add_u32 m0, m0, 1024\n"
    "v_add_u32 v13, s47, v13\n"
    "s_waitcnt vmcnt(2)\n"
    "global_load_lds_dwordx4 v14, s[48:49]\n"
    "s_add_u32 m0, m0, 1024\n"
    "v_add_u32 v14, s47, v14\n"
    "s_waitcnt vmcnt(2)\n"
    "global_load_lds_dwordx4 v15, s[48:49]\n"
    "s_add_u32 m0, m0, 1024\n"
    "v_add_u32 v15, s47, v15\n"
    "s_waitcnt vmcnt(2)\n"
    "global_load_lds_dwordx4 v13, s[48:49]\n"
    "s_add_u32 m0, m0, 1024\n"
    "v_add_u32 v13, s47, v13\n"
    "s_mov_b32 exec_hi, 0\n"
    "s_waitcnt vmcnt(2)\n"
    "global_load_lds_dwordx4 v14, s[48:49]\n"
    "s_add_u32 m0, m0, 1024\n"
    "v_add_u32 v14, s47, v14\n"
    "s_mov_b64 exec, -1\n"
    "s_barrier\n"
    "s_barrier\n"
    "s_barrier\n"
    "s_waitcnt vmcnt(11)\n"
    "ds_read_b64_tr_b16 v[40:41], v1 offset:0\n"
    "ds_read_b64_tr_b16 v[42:43], v1 offset:768\n"
    "ds_read_b64_tr_b16 v[44:45], v1 offset:64\n"
    "ds_read_b64_tr_b16 v[46:47], v1 offset:832\n"
    "ds_read_b64_tr_b16 v[48:49], v1 offset:128\n"
    "ds_read_b64_tr_b16 v[50:51], v1 offset:896\n"
    "s_waitcnt vmcnt(8)\n"
    "ds_read_b64_tr_b16 v[52:53], v1 offset:3072\n"
    "ds_read_b64_tr_b16 v[54:55], v1 offset:3840\n"
    "ds_read_b64_tr_b16 v[56:57], v1 offset:3136\n"
    "ds_read_b64_tr_b16 v[58:59], v1 offset:3904\n"
    "ds_read_b64_tr_b16 v[60:61], v1 offset:3200\n"
    "ds_read_b64_tr_b16 v[62:63], v1 offset:3968\n"
    "s_waitcnt vmcnt(5)\n"
    "ds_read_b64_tr_b16 v[64:65], v1 offset:6144\n"
    "ds_read_b64_tr_b16 v[66:67], v1 offset:6912\n"
    "ds_read_b64_tr_b16 v[68:69], v1 offset:6208\n"
    "ds_read_b64_tr_b16 v[70:71], v1 offset:6976\n"
    "ds_read_b64_tr_b16 v[72:73], v1 offset:6272\n"
    "ds_read_b64_tr_b16 v[74:75], v1 offset:7040\n"
    "s_waitcnt vmcnt(2)\n"
    "ds_read_b64_tr_b16 v[76:77], v1 offset:9216\n"
    "ds_read_b64_tr_b16 v[78:79], v1 offset:9984\n"
    "ds_read_b64_tr_b16 v[80:81], v1 offset:9280\n"
    "ds_read_b64_tr_b16 v[82:83], v1 offset:10048\n"
    "ds_read_b64_tr_b16 v[84:85], v1 offset:9344\n"
    "ds_read_b64_tr_b16 v[86:87], v1 offset:10112\n"
    "s_waitcnt vmcnt(0)\n"
    "ds_read_b64_tr_b16 v[88:89], v2 offset:12288\n"
    "ds_read_b64_tr_b16 v[90:91], v2 offset:13056\n"
    "ds_read_b64_tr_b16 v[92:93], v2 offset:12352\n"
    "ds_read_b64_tr_b16 v[94:95], v2 offset:13120\n"
    "ds_read_b64_tr_b16 v[96:97], v2 offset:12416\n"
    "ds_read_b64_tr_b16 v[98:99], v2 offset:13184\n"
    "s_lshl_b32 s51, s56, 5\n"
    "v_lshrrev_b32 v5, 1, v11\n"
    "v_lshl_add_u32 v5, v5, 4, v10\n"
    "v_add_u32 v5, s51, v5\n"
    "v_mul_u32_u24 v6, 171, v5\n"
    "v_lshrrev_b32 v6, 11, v6\n"
    "v_mul_u32_u24 v6, 12096, v6\n"
    "v_lshl_add_u32 v6, v5, 4, v6\n"
    "v_mul_u32_u24 v4, 393216, v4\n"
    "s_mul_i32 s51, s57, 192\n"
    "v_add3_u32 v38, v6, v4, s51\n"
    "s_mov_b32 s86, 6455296\n"
    "s_mov_b32 s87, 6651904\n"
    "v_mov_b32 v31, s44\n"
    "v_mov_b32 v35, s44\n"
    "v_mov_b32 v3, 159488\n"
    "s_barrier\n"
    "s_branch Ed0%=\n"
    "Ewz%=:\n"
    "v_mov_b32 v4, 147456\n"
    "v_lshl_add_u32 v4, v12, 4, v4\n"
    "v_mov_b64 v[16:17], 0\n"
    "v_mov_b64 v[18:19], 0\n"
    "ds_write_b128 v4, v[16:19] offset:0\n"
    "ds_write_b128 v4, v[16:19] offset:1024\n"
    "ds_write_b128 v4, v[16:19] offset:2048\n"
    "ds_write_b128 v4, v[16:19] offset:3072\n"
    "ds_write_b128 v4, v[16:19] offset:4096\n"
    "ds_write_b128 v4, v[16:19] offset:5120\n"
    "ds_write_b128 v4, v[16:19] offset:6144\n"
    "ds_write_b128 v4, v[16:19] offset:7168\n"
    "ds_write_b128 v4, v[16:19] offset:8192\n"
    "s_barrier\n"
    "s_barrier\n"
    "s_barrier\n"
    "s_barrier\n"
    "v_add_u32 v4, -6144, v4\n"
    "ds_write_b128 v4, v[16:19] offset:0\n"
    "ds_write_b128 v4, v[16:19] offset:1024\n"
    "ds_write_b128 v4, v[16:19] offset:2048\n"
    "s_setprio 3\n"
    "v_lshlrev_b32 v4, 7, v12\n"
    "s_mov_b32 s46, 87040\n"
    "s_memrealtime s[88:89]\n"
    "s_movk_i32 s97, 0x7fff\n"
    "Etr%=:\n"
    "buffer_load_dwordx2 v[20:21], v4, s[40:43], s46 offen sc1\n"
    "s_waitcnt vmcnt(0)\n"
    "v_cmp_ne_u32 vcc, s44, v21\n"
    "s_cbranch_vccz Etx%=\n"
    "s_sleep 4\n"
    "s_sub_u32 s97, s97, 1\n"
    "s_cbranch_scc0 Etr%=\n"
    "Etx%=:\n"
    "s_waitcnt lgkmcnt(0)\n"
    "v_subrev_u32 v20, s88, v20\n"
    "s_nop 1\n"
    "v_max_i32_dpp v20, v20, v20 quad_perm:[1,0,3,2] row_mask:0xf bank_mask:0xf\n"
    "s_nop 1\n"
    "v_max_i32_dpp v20, v20, v20 quad_perm:[2,3,0,1] row_mask:0xf bank_mask:0xf\n"
    "s_nop 1\n"
    "v_max_i32_dpp v20, v20, v20 row_half_mirror row_mask:0xf bank_mask:0xf\n"
    "s_nop 1\n"
    "v_max_i32_dpp v20, v20, v20 row_mirror row_mask:0xf bank_mask:0xf\n"
    "s_nop 1\n"
    "v_max_i32_dpp v20, v20, v20 row_bcast:15 row_mask:0xa bank_mask:0xf\n"
    "s_nop 1\n"
    "v_max_i32_dpp v20, v20, v20 row_bcast:31 row_mask:0xc bank_mask:0xf\n"
    "v_mov_b32 v3, 159488\n"
    "v_readlane_b32 s46, v20, 63\n"
    "s_add_u32 s46, s46, s88\n"
    "s_or_b32 s46, s46, 1\n"
    "v_mov_b32 v20, s46\n"
    "ds_write_b32 v3, v20\n"
    "v_mov_b32 v13, 160800\n"
    "s_waitcnt lgkmcnt(0)\n"
    "s_barrier\n"
    "s_barrier\n"
    "s_barrier\n"
    "s_branch Eh1%=\n"
    "Ek0%=:\n"
    "v_lshlrev_b32 v1, 4, %[t]\n"
    "s_and_b32 s51, %[b], 7\n"
    "s_mul_i32 s51, s51, 16512\n"
    "s_add_u32 s51, s51, 10403840\n"
    "s_add_u32 s52, s51, 8192\n"
    "v_and_b32 v2, 7, %[t]\n"
    "v_mov_b32 v3, 159488\n"
    "v_lshl_add_u32 v4, v2, 2, v3\n"
    "v_and_b32 v5, 3, %[t]\n"
    "v_lshl_add_u32 v5, v5, 4, v3\n"
    "v_mov_b32 v6, 1\n"
    "v_mov_b32 v7, 147456\n"
    "v_lshrrev_b32 v8, 3, %[t]\n"
    "v_lshl_add_u32 v8, v8, 2, v7\n"
    "v_lshl_add_u32 v9, v2, 4, v7\n"
    "s_lshl_b32 s56, %[w], 3\n"
    "v_mov_b32 v13, 160800\n"
    "v_lshrrev_b32 v14, 5, %[t]\n"
    "v_and_b32 v15, 31, %[t]\n"
    "v_mul_u32_u24 v36, 1184, v14\n"
    "v_mad_u32_u24 v36, v15, 12, v36\n"
    "v_add_u32 v36, 120832, v36\n"
    "v_lshl_add_u32 v14, v14, 2, v13\n"
    "v_lshlrev_b32 v15, 4, v15\n"
    "v_add_u32 v15, 6453760, v15\n"
    "s_mov_b32 s45, -1536\n"
    "s_movk_i32 s97, 0x7fff\n"
    "Ekt%=:\n"
    "ds_read_b32 v16, v3\n"
    "s_memrealtime s[46:47]\n"
    "s_waitcnt lgkmcnt(0)\n"
    "v_readfirstlane_b32 s47, v16\n"
    "s_sub_u32 s46, s46, s47\n"
    "s_cmp_eq_u32 s47, 0\n"
    "s_cselect_b32 s46, -1, s46\n"
    "s_cmp_ge_i32 s46, 0\n"
    "s_cbranch_scc1 Eku%=\n"
    "s_sleep 1\n"
    "s_sub_u32 s97, s97, 1\n"
    "s_cbranch_scc0 Ekt%=\n"
    "Eku%=:\n"
    "s_setprio 2\n"
    "buffer_load_dwordx4 v[20:23], v1, s[40:43], s51 offen sc1\n"
    "buffer_load_dwordx4 v[24:27], v1, s[40:43], s52 offen sc1\n"
    "s_waitcnt vmcnt(1)\n"
    "v_bitop3_b32 v28, v21, v23, s44 bitop3:0x7e\n"
    "v_cmp_ne_u32 vcc, 0, v28\n"
    "s_cbranch_vccz Ekax%=\n"
    "s_movk_i32 s97, 0x7fff\n"
    "Ekar%=:\n"
    "buffer_load_dwordx4 v[20:23], v1, s[40:43], s51 offen sc1\n"
    "s_waitcnt vmcnt(0)\n"
    "v_bitop3_b32 v28, v21, v23, s44 bitop3:0x7e\n"
    "v_cmp_ne_u32 vcc, 0, v28\n"
    "s_cbranch_vccz Ekax%=\n"
    "s_sleep 1\n"
    "s_sub_u32 s97, s97, 1\n"
    "s_cbranch_scc0 Ekar%=\n"
    "Ekax%=:\n"
    "ds_max_u32 v4, v20 offset:96\n"
    "ds_max_u32 v4, v22 offset:128\n"
    "s_waitcnt vmcnt(0)\n"
    "v_bitop3_b32 v28, v25, v27, s44 bitop3:0x7e\n"
    "v_cmp_ne_u32 vcc, 0, v28\n"
    "s_cbranch_vccz Ekbx%=\n"
    "s_movk_i32 s97, 0x7fff\n"
    "Ekbr%=:\n"
    "buffer_load_dwordx4 v[24:27], v1, s[40:43], s52 offen sc1\n"
    "s_waitcnt vmcnt(0)\n"
    "v_bitop3_b32 v28, v25, v27, s44 bitop3:0x7e\n"
    "v_cmp_ne_u32 vcc, 0, v28\n"
    "s_cbranch_vccz Ekbx%=\n"
    "s_sleep 1\n"
    "s_sub_u32 s97, s97, 1\n"
    "s_cbranch_scc0 Ekbr%=\n"
    "Ekbx%=:\n"
    "ds_max_u32 v4, v24 offset:96\n"
    "ds_max_u32 v4, v26 offset:128\n"
    "s_waitcnt lgkmcnt(0)\n"
    "s_barrier\n"
    "ds_read_b128 v[28:31], v5 offset:96\n"
    "s_waitcnt lgkmcnt(0)\n"
    "v_min3_u32 v28, v28, v29, v30\n"
    "v_min_u32 v28, v28, v31\n"
    "s_nop 1\n"
    "v_min_u32_dpp v28, v28, v28 quad_perm:[1,0,3,2] row_mask:0xf bank_mask:0xf\n"
    "s_nop 1\n"
    "v_min_u32_dpp v28, v28, v28 quad_perm:[2,3,0,1] row_mask:0xf bank_mask:0xf\n"
    "v_cmp_ge_u32 s[88:89], v20, v28\n"
    "v_cmp_ge_u32 s[90:91], v22, v28\n"
    "v_cmp_ge_u32 s[94:95], v24, v28\n"
    "v_cmp_ge_u32 s[96:97], v26, v28\n"
    "s_mov_b64 exec, s[88:89]\n"
    "ds_add_rtn_u32 v21, v3, v6 offset:24\n"
    "s_mov_b64 exec, s[90:91]\n"
    "ds_add_rtn_u32 v23, v3, v6 offset:24\n"
    "s_mov_b64 exec, s[94:95]\n"
    "ds_add_rtn_u32 v25, v3, v6 offset:24\n"
    "s_mov_b64 exec, s[96:97]\n"
    "ds_add_rtn_u32 v27, v3, v6 offset:24\n"
    "s_waitcnt lgkmcnt(0)\n"
    "v_lshl_add_u32 v27, v27, 2, v7\n"
    "ds_write_b32 v27, v26\n"
    "s_mov_b64 exec, s[94:95]\n"
    "v_lshl_add_u32 v25, v25, 2, v7\n"
    "ds_write_b32 v25, v24\n"
    "s_mov_b64 exec, s[90:91]\n"
    "v_lshl_add_u32 v23, v23, 2, v7\n"
    "ds_write_b32 v23, v22\n"
    "s_mov_b64 exec, s[88:89]\n"
    "v_lshl_add_u32 v21, v21, 2, v7\n"
    "ds_write_b32 v21, v20\n"
    "s_mov_b64 exec, -1\n"
    "s_waitcnt lgkmcnt(0)\n"
    "s_barrier\n"
    "ds_read_b32 v18, v3 offset:24\n"
    "Ec1%=:\n"
    "ds_read_b32 v17, v8\n"
    "ds_read_b128 v[20:23], v9\n"
    "ds_read_b128 v[24:27], v9 offset:128\n"
    "v_mov_b32 v16, 0\n"
    "s_waitcnt lgkmcnt(3)\n"
    "v_readfirstlane_b32 s46, v18\n"
    "s_cmp_ge_u32 s56, s46\n"
    "s_cbranch_scc1 Ec4%=\n"
    "s_mov_b32 s47, 64\n"
    "v_mov_b32 v19, v9\n"
    "Ec2%=:\n"
    "s_waitcnt lgkmcnt(0)\n"
    "v_cmp_gt_u32 s[88:89], v20, v17\n"
    "v_cmp_gt_u32 s[90:91], v21, v17\n"
    "v_cmp_gt_u32 s[94:95], v22, v17\n"
    "v_cmp_gt_u32 s[96:97], v23, v17\n"
    "v_addc_co_u32 v16, vcc, 0, v16, s[88:89]\n"
    "v_addc_co_u32 v16, vcc, 0, v16, s[90:91]\n"
    "v_addc_co_u32 v16, vcc, 0, v16, s[94:95]\n"
    "v_addc_co_u32 v16, vcc, 0, v16, s[96:97]\n"
    "v_cmp_gt_u32 s[88:89], v24, v17\n"
    "v_cmp_gt_u32 s[90:91], v25, v17\n"
    "v_cmp_gt_u32 s[94:95], v26, v17\n"
    "v_cmp_gt_u32 s[96:97], v27, v17\n"
    "v_addc_co_u32 v16, vcc, 0, v16, s[88:89]\n"
    "v_addc_co_u32 v16, vcc, 0, v16, s[90:91]\n"
    "v_addc_co_u32 v16, vcc, 0, v16, s[94:95]\n"
    "v_addc_co_u32 v16, vcc, 0, v16, s[96:97]\n"
    "s_cmp_ge_u32 s47, s46\n"
    "s_cbranch_scc1 Ec3%=\n"
    "s_add_u32 s47, s47, 64\n"
    "v_add_u32 v19, 256, v19\n"
    "ds_read_b128 v[20:23], v19\n"
    "ds_read_b128 v[24:27], v19 offset:128\n"
    "s_branch Ec2%=\n"
    "Ec3%=:\n"
    "v_add_u32_dpp v16, v16, v16 quad_perm:[1,0,3,2] row_mask:0xf bank_mask:0xf\n"
    "s_nop 1\n"
    "v_add_u32_dpp v16, v16, v16 quad_perm:[2,3,0,1] row_mask:0xf bank_mask:0xf\n"
    "s_addk_i32 s56, 64\n"
    "s_cmp_lt_u32 s56, s46\n"
    "v_add_u32_dpp v16, v16, v16 row_half_mirror row_mask:0xf bank_mask:0xf\n"
    "v_min_u32 v16, 16, v16\n"
    "v_lshl_add_u32 v16, v16, 2, v13\n"
    "ds_write_b32 v16, v17\n"
    "s_cbranch_scc0 Ec4%=\n"
    "v_add_u32 v8, 256, v8\n"
    "s_branch Ec1%=\n"
    "Ec4%=:\n"
    "s_waitcnt lgkmcnt(0)\n"
    "s_barrier\n"
    "ds_read_b32 v16, v14\n"
    "s_waitcnt lgkmcnt(0)\n"
    "v_and_b32 v16, 0x7ff, v16\n"
    "v_mad_i32_i24 v15, v16, s45, v15\n"
    "s_movk_i32 s97, 0x7fff\n"
    "Ehr%=:\n"
    "buffer_load_dwordx4 v[20:23], v15, s[40:43], 0 offen offset:0 sc1\n"
    "buffer_load_dwordx4 v[24:27], v15, s[40:43], 0 offen offset:512 sc1\n"
    "buffer_load_dwordx4 v[28:31], v15, s[40:43], 0 offen offset:1024 sc1\n"
    "v_mul_u32_u24 v1, 1184, v10\n"
    "v_lshl_add_u32 v1, v11, 4, v1\n"
    "v_add_u32 v1, 120832, v1\n"
    "v_lshrrev_b32 v2, 3, v12\n"
    "v_mul_u32_u24 v2, 320, v2\n"
    "v_and_b32 v4, 7, %[t]\n"
    "v_lshl_add_u32 v2, v4, 1, v2\n"
    "s_lshl4_add_u32 s46, %[w], 141312\n"
    "v_add_u32 v2, s46, v2\n"
    "s_waitcnt vmcnt(0)\n"
    "v_bitop3_b32 v16, v23, v27, s44 bitop3:0x7e\n"
    "v_xor_b32 v31, s44, v31\n"
    "v_or_b32 v16, v16, v31\n"
    "v_cmp_ne_u32 vcc, 0, v16\n"
    "s_cbranch_vccz Ehx%=\n"
    "s_sub_u32 s97, s97, 1\n"
    "s_cbranch_scc0 Ehr%=\n"
    "Ehx%=:\n"
    "ds_write2_b32 v36, v20, v24 offset0:0 offset1:96\n"
    "ds_write2_b32 v36, v21, v25 offset0:1 offset1:97\n"
    "ds_write2_b32 v36, v22, v26 offset0:2 offset1:98\n"
    "ds_write2_b32 v36, v28, v29 offset0:192 offset1:193\n"
    "ds_write_b32 v36, v30 offset:776\n"
    "Eg0%=:\n"
    "s_waitcnt lgkmcnt(0)\n"
    "s_barrier\n"
    "ds_read_b128 v[112:115], v1 offset:0\n"
    "ds_read_b128 v[116:119], v1 offset:64\n"
    "ds_read_b128 v[120:123], v1 offset:128\n"
    "ds_read_b128 v[124:127], v1 offset:192\n"
    "ds_read_b128 v[20:23], v1 offset:256\n"
    "ds_read_b128 v[24:27], v1 offset:320\n"
    "ds_read_b128 v[28:31], v1 offset:384\n"
    "ds_read_b128 v[32:35], v1 offset:448\n"
    "s_waitcnt lgkmcnt(7)\n"
    "v_mfma_f32_16x16x32_bf16 v[16:19], v[112:115], v[40:43], 0\n"
    "ds_read_b128 v[112:115], v1 offset:512\n"
    "s_waitcnt lgkmcnt(7)\n"
    "v_mfma_f32_16x16x32_bf16 v[16:19], v[116:119], v[44:47], v[16:19]\n"
    "ds_read_b128 v[116:119], v1 offset:576\n"
    "s_waitcnt lgkmcnt(7)\n"
    "v_mfma_f32_16x16x32_bf16 v[16:19], v[120:123], v[48:51], v[16:19]\n"
    "ds_read_b128 v[120:123], v1 offset:640\n"
    "s_waitcnt lgkmcnt(7)\n"
    "v_mfma_f32_16x16x32_bf16 v[16:19], v[124:127], v[52:55], v[16:19]\n"
    "ds_read_b128 v[124:127], v1 offset:704\n"
    "s_waitcnt lgkmcnt(7)\n"
    "v_mfma_f32_16x16x32_bf16 v[16:19], v[20:23], v[56:59], v[16:19]\n"
    "ds_read_b128 v[20:23], v1 offset:768\n"
    "s_waitcnt lgkmcnt(7)\n"
    "v_mfma_f32_16x16x32_bf16 v[16:19], v[24:27], v[60:63], v[16:19]\n"
    "ds_read_b128 v[24:27], v1 offset:832\n"
    "s_waitcnt lgkmcnt(7)\n"
    "v_mfma_f32_16x16x32_bf16 v[16:19], v[28:31], v[64:67], v[16:19]\n"
    "ds_read_b128 v[28:31], v1 offset:896\n"
    "s_waitcnt lgkmcnt(7)\n"
    "v_mfma_f32_16x16x32_bf16 v[16:19], v[32:35], v[68:71], v[16:19]\n"
    "ds_read_b128 v[32:35], v1 offset:960\n"
    "s_waitcnt lgkmcnt(7)\n"
    "v_mfma_f32_16x16x32_bf16 v[16:19], v[112:115], v[72:75], v[16:19]\n"
    "ds_read_b128 v[112:115], v1 offset:1024\n"
    "s_waitcnt lgkmcnt(7)\n"
    "v_mfma_f32_16x16x32_bf16 v[16:19], v[116:119], v[76:79], v[16:19]\n"
    "ds_read_b128 v[116:119], v1 offset:1088\n"
    "s_waitcnt lgkmcnt(7)\n"
    "v_mfma_f32_16x16x32_bf16 v[16:19], v[120:123], v[80:83], v[16:19]\n"
    "s_waitcnt lgkmcnt(6)\n"
    "v_mfma_f32_16x16x32_bf16 v[16:19], v[124:127], v[84:87], v[16:19]\n"
    "s_waitcnt lgkmcnt(5)\n"
    "v_mfma_f32_16x16x32_bf16 v[16:19], v[20:23], v[88:91], v[16:19]\n"
    "s_waitcnt lgkmcnt(4)\n"
    "v_mfma_f32_16x16x32_bf16 v[16:19], v[24:27], v[92:95], v[16:19]\n"
    "s_waitcnt lgkmcnt(3)\n"
    "v_mfma_f32_16x16x32_bf16 v[16:19], v[28:31], v[96:99], v[16:19]\n"
    "s_waitcnt lgkmcnt(2)\n"
    "v_mfma_f32_16x16x32_bf16 v[16:19], v[32:35], v[100:103], v[16:19]\n"
    "s_waitcnt lgkmcnt(1)\n"
    "v_mfma_f32_16x16x32_bf16 v[16:19], v[112:115], v[104:107], v[16:19]\n"
    "s_waitcnt lgkmcnt(0)\n"
    "v_mfma_f32_16x16x32_bf16 v[16:19], v[116:119], v[108:111], v[16:19]\n"
    "s_nop 7\n"
    "v_mov_b64 v[20:21], v[16:17]\n"
    "v_mov_b32_dpp v16, v18 row_ror:8 row_mask:0xf bank_mask:0xc\n"
    "v_mov_b32_dpp v17, v19 row_ror:8 row_mask:0xf bank_mask:0xc\n"
    "v_mov_b32_dpp v18, v20 row_ror:8 row_mask:0xf bank_mask:0x3\n"
    "v_mov_b32_dpp v19, v21 row_ror:8 row_mask:0xf bank_mask:0x3\n"
    "v_cvt_pk_bf16_f32 v16, 0, v16\n"
    "v_cvt_pk_bf16_f32 v17, 0, v17\n"
    "v_mul_f32 v20, 0xbfb8aa3b, v16\n"
    "v_mul_f32 v21, 0xbfb8aa3b, v17\n"
    "v_exp_f32 v20, v20\n"
    "v_exp_f32 v21, v21\n"
    "v_cvt_pk_bf16_f32 v18, 0, v18\n"
    "v_pk_add_f32 v[20:21], v[20:21], 1.0 op_sel_hi:[1,0]\n"
    "v_rcp_f32 v20, v20\n"
    "v_rcp_f32 v21, v21\n"
    "v_cvt_pk_bf16_f32 v19, 0, v19\n"
    "v_pk_mul_f32 v[16:17], v[16:17], v[20:21]\n"
    "v_cvt_pk_bf16_f32 v16, 0, v16\n"
    "v_cvt_pk_bf16_f32 v17, 0, v17\n"
    "v_pk_mul_f32 v[16:17], v[16:17], v[18:19]\n"
    "v_cvt_pk_bf16_f32 v16, v16, v17\n"
    "ds_write_b16 v2, v16\n"
    "ds_write_b16_d16_hi v2, v16 offset:160\n"
    "s_setprio 0\n"
    "s_waitcnt lgkmcnt(0)\n"
    "s_barrier\n"
    "s_cmp_gt_u32 %[w], 2\n"
    "s_cbranch_scc1 Ez%=\n"
    "s_lshr_b32 s46, s57, 4\n"
    "v_bfe_u32 v1, %[t], 3, 1\n"
    "v_lshl_add_u32 v1, v1, 3, v3\n"
    "s_lshl_b32 s46, s46, 4\n"
    "v_add_u32 v1, s46, v1\n"
    "ds_read_b64 v[4:5], v1 offset:32\n"
    "s_and_b32 s46, s57, 15\n"
    "s_mul_i32 s46, s46, 12\n"
    "s_lshl2_add_u32 s46, %[w], s46\n"
    "v_and_b32 v2, 3, %[t]\n"
    "v_add_u32 v6, s46, v2\n"
    "v_lshrrev_b32 v7, 5, v6\n"
    "v_lshl_add_u32 v6, v7, 6, v6\n"
    "v_min_u32 v7, 2, v11\n"
    "v_lshl_add_u32 v6, v7, 5, v6\n"
    "v_mul_u32_u24 v7, 683, v6\n"
    "v_lshrrev_b32 v7, 11, v7\n"
    "v_mul_u32_u24 v8, 3, v7\n"
    "v_sub_u32 v8, v6, v8\n"
    "v_and_b32 v9, 1, v7\n"
    "v_sub_u32 v8, v8, v9\n"
    "v_min_u32 v8, 2, v8\n"
    "v_mul_u32_u24 v7, 16512, v7\n"
    "v_lshlrev_b32 v9, 3, v6\n"
    "s_waitcnt lgkmcnt(0)\n"
    "v_not_b32 v13, v5\n"
    "v_and_b32 v13, 0x7ff, v13\n"
    "v_lshrrev_b32 v14, 1, v13\n"
    "v_lshl_add_u32 v7, v14, 4, v7\n"
    "v_lshrrev_b32 v14, 4, v13\n"
    "v_lshlrev_b32 v14, 2, v14\n"
    "v_mul_u32_u24 v4, 1184, v4\n"
    "v_lshl_add_u32 v4, v6, 1, v4\n"
    "v_add_u32 v4, 101888, v4\n"
    "v_mov_b32 v15, 0\n"
    "ds_read_u16_d16_hi v15, v4\n"
    "s_mov_b32 s90, 139264\n"
    "s_mov_b32 s91, 136192\n"
    "s_mov_b32 s86, 27136\n"
    "s_mov_b32 s87, 54784\n"
    "s_mov_b32 s88, 82432\n"
    "buffer_load_dwordx4 v[16:19], v7, s[40:43], s90 offen sc1\n"
    "buffer_load_dword v20, v14, s[40:43], s91 offen sc1\n"
    "buffer_load_dwordx2 v[22:23], v9, s[40:43], s86 offen sc1\n"
    "buffer_load_dwordx2 v[24:25], v9, s[40:43], s87 offen sc1\n"
    "buffer_load_dwordx2 v[26:27], v9, s[40:43], s88 offen sc1\n"
    "v_lshrrev_b32 v28, 2, v12\n"
    "v_mul_u32_u24 v28, 192, v28\n"
    "v_lshl_add_u32 v28, v2, 4, v28\n"
    "s_mul_i32 s46, s57, 12288\n"
    "s_lshl_b32 s47, %[w], 6\n"
    "s_add_u32 s46, s46, s47\n"
    "s_add_u32 s46, s46, 6455296\n"
    "s_add_u32 s47, s46, 6144\n"
    "s_sleep 19\n"
    "buffer_load_dwordx4 v[40:43], v28, s[40:43], s46 offen sc1\n"
    "buffer_load_dwordx4 v[44:47], v28, s[40:43], s46 offen offset:3072 sc1\n"
    "buffer_load_dwordx4 v[48:51], v28, s[40:43], s47 offen sc1\n"
    "buffer_load_dwordx4 v[52:55], v28, s[40:43], s47 offen offset:3072 sc1\n"
    "s_waitcnt vmcnt(4)\n"
    "v_bitop3_b32 v21, v19, v20, s44 bitop3:0x7e\n"
    "v_bitop3_b32 v30, v23, v25, s44 bitop3:0x7e\n"
    "v_xor_b32 v29, s44, v27\n"
    "v_or3_b32 v21, v21, v30, v29\n"
    "v_cmp_ne_u32 vcc, 0, v21\n"
    "s_cbranch_vccz Efx%=\n"
    "s_movk_i32 s97, 0x7fff\n"
    "Efr%=:\n"
    "buffer_load_dwordx4 v[16:19], v7, s[40:43], s90 offen sc1\n"
    "buffer_load_dword v20, v14, s[40:43], s91 offen sc1\n"
    "buffer_load_dwordx2 v[22:23], v9, s[40:43], s86 offen sc1\n"
    "buffer_load_dwordx2 v[24:25], v9, s[40:43], s87 offen sc1\n"
    "buffer_load_dwordx2 v[26:27], v9, s[40:43], s88 offen sc1\n"
    "s_waitcnt vmcnt(0)\n"
    "v_bitop3_b32 v21, v19, v20, s44 bitop3:0x7e\n"
    "v_bitop3_b32 v30, v23, v25, s44 bitop3:0x7e\n"
    "v_xor_b32 v29, s44, v27\n"
    "v_or3_b32 v21, v21, v30, v29\n"
    "v_cmp_ne_u32 vcc, 0, v21\n"
    "s_cbranch_vccz Efx%=\n"
    "s_sub_u32 s97, s97, 1\n"
    "s_cbranch_scc0 Efr%=\n"
    "Efx%=:\n"
    "v_add_f32 v22, v22, v24\n"
    "v_add_f32 v22, v22, v26\n"
    "v_cvt_pk_bf16_f32 v22, 0, v22\n"
    "v_and_b32 v20, 1, v13\n"
    "v_cmp_eq_u32 vcc, 1, v20\n"
    "v_cmp_eq_u32 s[94:95], 2, v8\n"
    "v_mul_u32_u24 v13, 1152, v13\n"
    "v_cndmask_b32 v16, v16, v17, vcc\n"
    "v_cndmask_b32 v8, v8, v20, s[94:95]\n"
    "v_cndmask_b32 v16, v16, v18, s[94:95]\n"
    "v_lshlrev_b32 v8, 4, v8\n"
    "v_lshrrev_b32 v16, v8, v16\n"
    "v_lshlrev_b32 v16, 16, v16\n"
    "v_lshl_add_u32 v13, v6, 1, v13\n"
    "v_ashrrev_i32 v17, 31, v5\n"
    "v_not_b32 v17, v17\n"
    "v_or_b32 v17, 0x80000000, v17\n"
    "v_xor_b32 v5, v5, v17\n"
    "v_and_b32 v5, 0xfffff800, v5\n"
    "v_cvt_pk_bf16_f32 v5, 0, v5\n"
    "v_mul_f32 v5, 0xbfb8aa3b, v5\n"
    "v_exp_f32 v5, v5\n"
    "v_and_b32 v17, 2, v10\n"
    "v_add_f32 v5, 1.0, v5\n"
    "v_rcp_f32 v5, v5\n"
    "v_cmp_eq_u32 vcc, 0, v17\n"
    "v_cvt_pk_bf16_f32 v5, 0, v5\n"
    "v_and_b32 v17, 1, v10\n"
    "v_lshlrev_b32 v17, 4, v17\n"
    "v_mov_b32 v18, 0x3f80\n"
    "v_lshlrev_b32 v17, v17, v18\n"
    "v_cndmask_b32 v17, 0, v17, vcc\n"
    "v_lshrrev_b32 v18, 2, v10\n"
    "v_cmp_eq_u32 vcc, 0, v18\n"
    "s_nop 1\n"
    "v_cndmask_b32 v32, 0, v17, vcc\n"
    "v_cmp_eq_u32 vcc, 1, v18\n"
    "s_nop 1\n"
    "v_cndmask_b32 v33, 0, v17, vcc\n"
    "v_cmp_eq_u32 vcc, 2, v18\n"
    "s_nop 1\n"
    "v_cndmask_b32 v34, 0, v17, vcc\n"
    "v_mov_b32 v35, 0\n"
    "s_waitcnt vmcnt(0) lgkmcnt(0)\n"
    "v_xor_b32 v43, s44, v43\n"
    "v_xor_b32 v47, s44, v47\n"
    "v_xor_b32 v51, s44, v51\n"
    "v_xor_b32 v55, s44, v55\n"
    "v_or3_b32 v17, v43, v47, v51\n"
    "v_or_b32 v17, v17, v55\n"
    "v_cmp_ne_u32 vcc, 0, v17\n"
    "s_cbranch_vccnz Eps%=\n"
    "Epb%=:\n"
    "v_mfma_f32_16x16x32_bf16 v[24:27], v[32:35], v[40:43], 0\n"
    "v_mfma_f32_16x16x32_bf16 v[24:27], v[32:35], v[44:47], v[24:27]\n"
    "v_mfma_f32_16x16x32_bf16 v[24:27], v[32:35], v[48:51], v[24:27]\n"
    "v_mfma_f32_16x16x32_bf16 v[24:27], v[32:35], v[52:55], v[24:27]\n"
    "s_mov_b32 s46, 0x0f0f0f0f\n"
    "s_mov_b32 s47, 0x0f0f\n"
    "s_nop 5\n"
    "v_add_f32_dpp v17, v24, v24 row_ror:8 row_mask:0xf bank_mask:0x3\n"
    "v_add_f32_dpp v17, v25, v25 row_ror:8 row_mask:0xf bank_mask:0xc\n"
    "s_nop 1\n"
    "v_add_f32_dpp v17, v17, v17 row_shl:4 row_mask:0xf bank_mask:0xf bound_ctrl:1\n"
    "v_cvt_pk_bf16_f32 v17, 0, v17\n"
    "v_mul_f32 v17, v17, v5\n"
    "v_cvt_pk_bf16_f32 v17, 0, v17\n"
    "v_add_f32 v17, v17, v15\n"
    "v_cvt_pk_bf16_f32 v17, 0, v17\n"
    "v_mul_f32 v17, v17, v22\n"
    "v_cvt_pk_bf16_f32 v17, 0, v17\n"
    "v_add_f32 v17, v17, v16\n"
    "v_cvt_pk_bf16_f32 v17, 0, v17\n"
    "s_mov_b64 exec, s[46:47]\n"
    "global_store_short_d16_hi v13, v17, s[84:85]\n"
    "s_or_b32 s46, s57, %[w]\n"
    "s_cbranch_scc1 Ez%=\n"
    "s_mov_b64 exec, 1\n"
    "v_mov_b32 v17, s44\n"
    "buffer_store_dword v17, off, s[40:43], 0 sc1\n"
    "s_branch Ez%=\n"
    "Eps%=:\n"
    "s_movk_i32 s97, 0x7fff\n"
    "Epr%=:\n"
    "s_mov_b64 exec, vcc\n"
    "buffer_load_dwordx4 v[40:43], v28, s[40:43], s46 offen sc1\n"
    "buffer_load_dwordx4 v[44:47], v28, s[40:43], s46 offen offset:3072 sc1\n"
    "buffer_load_dwordx4 v[48:51], v28, s[40:43], s47 offen sc1\n"
    "buffer_load_dwordx4 v[52:55], v28, s[40:43], s47 offen offset:3072 sc1\n"
    "s_waitcnt vmcnt(0)\n"
    "v_xor_b32 v43, s44, v43\n"
    "v_xor_b32 v47, s44, v47\n"
    "v_xor_b32 v51, s44, v51\n"
    "v_xor_b32 v55, s44, v55\n"
    "v_or3_b32 v17, v43, v47, v51\n"
    "v_or_b32 v17, v17, v55\n"
    "v_cmp_ne_u32 vcc, 0, v17\n"
    "s_mov_b64 exec, -1\n"
    "s_cbranch_vccz Epb%=\n"
    "s_sub_u32 s97, s97, 1\n"
    "s_cbranch_scc0 Epr%=\n"
    "s_branch Epb%=\n"
    "Eh1%=:\n"
    "s_setprio 3\n"
    "v_lshl_add_u32 v1, v10, 2, v13\n"
    "ds_read_b32 v16, v1\n"
    "s_mov_b32 s46, 103424\n"
    "v_lshl_add_u32 v4, v10, 2, v3\n"
    "v_lshl_add_u32 v5, v11, 4, v3\n"
    "v_add_u32 v18, 16, v10\n"
    "s_waitcnt lgkmcnt(0)\n"
    "v_not_b32 v16, v16\n"
    "v_and_b32 v16, 0x7ff, v16\n"
    "v_lshlrev_b32 v1, 3, v16\n"
    "buffer_load_dwordx2 v[16:17], v1, s[40:43], s46 offen sc1\n"
    "s_barrier\n"
    "s_waitcnt vmcnt(0)\n"
    "v_cmp_ne_u32 vcc, s44, v17\n"
    "s_cbranch_vccz Eqx%=\n"
    "s_movk_i32 s97, 0x7fff\n"
    "Eqr%=:\n"
    "buffer_load_dwordx2 v[16:17], v1, s[40:43], s46 offen sc1\n"
    "s_waitcnt vmcnt(0)\n"
    "v_cmp_ne_u32 vcc, s44, v17\n"
    "s_cbranch_vccz Eqx%=\n"
    "s_sub_u32 s97, s97, 1\n"
    "s_cbranch_scc0 Eqr%=\n"
    "Eqx%=:\n"
    "ds_write_b32 v4, v16 offset:96\n"
    "ds_read_b128 v[20:23], v5 offset:96\n"
    "v_mov_b32 v17, v16\n"
    "v_mov_b32 v19, v16\n"
    "v_mov_b32 v16, 0\n"
    "s_waitcnt lgkmcnt(0)\n"
    "v_cmp_gt_u32 s[88:89], v20, v17\n"
    "v_cmp_gt_u32 s[90:91], v21, v17\n"
    "v_cmp_gt_u32 s[94:95], v22, v17\n"
    "v_cmp_gt_u32 s[96:97], v23, v17\n"
    "v_addc_co_u32 v16, vcc, 0, v16, s[88:89]\n"
    "v_addc_co_u32 v16, vcc, 0, v16, s[90:91]\n"
    "v_addc_co_u32 v16, vcc, 0, v16, s[94:95]\n"
    "v_addc_co_u32 v16, vcc, 0, v16, s[96:97]\n"
    "v_mov_b32 v17, v16\n"
    "s_nop 1\n"
    "v_permlane16_swap_b32 v16, v17\n"
    "v_add_u32 v16, v16, v17\n"
    "v_mov_b32 v17, v16\n"
    "s_nop 1\n"
    "v_permlane32_swap_b32 v16, v17\n"
    "v_add_u32 v16, v16, v17\n"
    "v_cmp_gt_u32 vcc, 8, v16\n"
    "v_lshl_add_u32 v16, v16, 3, v3\n"
    "s_mov_b64 exec, vcc\n"
    "ds_write_b64 v16, v[18:19] offset:32\n"
    "s_mov_b64 exec, -1\n"
    "s_waitcnt lgkmcnt(0)\n"
    "s_barrier\n"
    "s_branch Ez%=\n"
    "Ed0%=:\n"
    "v_and_b32 v2, 7, %[t]\n"
    "v_lshl_add_u32 v2, v2, 3, v3\n"
    "s_mov_b32 s97, 0x1fffff\n"
    "Ed1%=:\n"
    "ds_read_b32 v1, v2 offset:32\n"
    "s_waitcnt lgkmcnt(0)\n"
    "v_cmp_eq_u32 vcc, 0, v1\n"
    "s_cbranch_vccz Ed2%=\n"
    "s_sleep 1\n"
    "s_sub_u32 s97, s97, 1\n"
    "s_cbranch_scc0 Ed1%=\n"
    "Ed2%=:\n"
    "s_mov_b32 vcc_lo, 0x00ff00ff\n"
    "s_mov_b32 vcc_hi, 0xff00ff00\n"
    "v_cndmask_b32 v1, 32, v1, vcc\n"
    "v_mul_u32_u24 v1, 160, v1\n"
    "v_and_b32 v2, 16, %[t]\n"
    "v_add_u32 v2, 138752, v2\n"
    "v_add_u32 v1, v1, v2\n"
    "s_setprio 3\n"
    "s_barrier\n"
    "ds_read_b128 v[100:103], v1 offset:0\n"
    "ds_read_b128 v[104:107], v1 offset:32\n"
    "ds_read_b128 v[108:111], v1 offset:64\n"
    "ds_read_b128 v[112:115], v1 offset:96\n"
    "ds_read_b128 v[116:119], v1 offset:128\n"
    "s_waitcnt lgkmcnt(4)\n"
    "v_mfma_f32_16x16x32_bf16 v[16:19], v[100:103], v[40:43], 0\n"
    "v_mfma_f32_16x16x32_bf16 v[20:23], v[100:103], v[44:47], 0\n"
    "v_mfma_f32_16x16x32_bf16 v[24:27], v[100:103], v[48:51], 0\n"
    "s_waitcnt lgkmcnt(3)\n"
    "v_mfma_f32_16x16x32_bf16 v[16:19], v[104:107], v[52:55], v[16:19]\n"
    "v_mfma_f32_16x16x32_bf16 v[20:23], v[104:107], v[56:59], v[20:23]\n"
    "v_mfma_f32_16x16x32_bf16 v[24:27], v[104:107], v[60:63], v[24:27]\n"
    "s_waitcnt lgkmcnt(2)\n"
    "v_mfma_f32_16x16x32_bf16 v[16:19], v[108:111], v[64:67], v[16:19]\n"
    "v_mfma_f32_16x16x32_bf16 v[20:23], v[108:111], v[68:71], v[20:23]\n"
    "v_mfma_f32_16x16x32_bf16 v[24:27], v[108:111], v[72:75], v[24:27]\n"
    "s_waitcnt lgkmcnt(1)\n"
    "v_mfma_f32_16x16x32_bf16 v[16:19], v[112:115], v[76:79], v[16:19]\n"
    "v_mfma_f32_16x16x32_bf16 v[20:23], v[112:115], v[80:83], v[20:23]\n"
    "v_mfma_f32_16x16x32_bf16 v[24:27], v[112:115], v[84:87], v[24:27]\n"
    "s_waitcnt lgkmcnt(0)\n"
    "v_mfma_f32_16x16x32_bf16 v[16:19], v[116:119], v[88:91], v[16:19]\n"
    "v_mfma_f32_16x16x32_bf16 v[20:23], v[116:119], v[92:95], v[20:23]\n"
    "v_mfma_f32_16x16x32_bf16 v[24:27], v[116:119], v[96:99], v[24:27]\n"
    "s_nop 5\n"
    "v_cvt_pk_bf16_f32 v28, v16, v17\n"
    "v_cvt_pk_bf16_f32 v32, v18, v19\n"
    "v_cvt_pk_bf16_f32 v29, v20, v21\n"
    "v_cvt_pk_bf16_f32 v33, v22, v23\n"
    "v_cvt_pk_bf16_f32 v30, v24, v25\n"
    "v_cvt_pk_bf16_f32 v34, v26, v27\n"
    "buffer_store_dwordx4 v[28:31], v38, s[40:43], s86 offen sc1\n"
    "buffer_store_dwordx4 v[32:35], v38, s[40:43], s87 offen sc1\n"
    "Ez%=:\n"
    "s_mov_b64 exec, -1\n"
    "Lce%=:\n"
    "s_waitcnt lgkmcnt(0)\n"
    : [w] "=&s"(wave) : [t] "{v0}"(tid), [b] "s"(blk), [k] "s"(ka) : WAVE_CLOB);
}

extern "C" void kernel_launch(void* const* d_in, const int* in_sizes, int n_in, void* d_out, int out_size, void* d_ws, size_t ws_size, hipStream_t stream) {
  (void)hipFuncSetAttribute((const void*)mega, hipFuncAttributeMaxDynamicSharedMemorySize, LDS_BYTES);
  mega<<<256, 1024, LDS_BYTES, stream>>>((const u16*)d_in[0], (const u16*)d_in[1], (const u16*)d_in[2], (const u16*)d_in[3], (const u16*)d_in[4],
                                         (const u16*)d_in[5], (const u16*)d_in[6], (const u16*)d_in[8], (const u16*)d_in[9], (const u16*)d_in[10],
                                         (const u16*)d_in[11], (const u16*)d_in[12], (const u16*)d_in[13], (u16*)d_out, (char*)d_ws);
}
```
